```python
import torch

def _softmax(x, axis=-1):
    return torch.softmax(x, dim=axis)
def _gelu(x):
    return torch.nn.functional.gelu(x, approximate="tanh")
def _silu(x):
    return torch.nn.functional.silu(x)
def _sigmoid(x):
    return torch.sigmoid(x)
def _layernorm(x, axis=-1, eps=1e-5):
    m = x.mean(dim=axis, keepdim=True)
    v = x.var(dim=axis, keepdim=True, unbiased=False)
    return (x - m) / torch.sqrt(v + eps)
def _scatter_add(tgt, idx, src, axis=0):
    return tgt.clone().index_add_(axis, idx, src)
def _segment_sum(src, seg, n):
    out = torch.zeros((n,) + src.shape[1:], dtype=src.dtype)
    return out.index_add_(0, seg, src)
def _pad(x, pw):
    flat = tuple(v for pair in reversed(pw) for v in pair)
    return torch.nn.functional.pad(x, flat)

kernel_name = 'parallel_sum__grouped_kv_mqa__moe_arch_mod__prompt'

def setup_inputs():

    _w = (lambda a: (lambda t: t.to(torch.bfloat16) if t.is_floating_point() else t)(torch.as_tensor(a)))
    def _A_setup():
        B, S, D, H = 1, 2048, 512, 32
        Dh = D // H
        return {
            "x": _w((lambda *s: torch.randn(*s, dtype=torch.bfloat16) * 0.05)(B, S, D)),
            "wq": _w((lambda *s: torch.randn(*s, dtype=torch.bfloat16) * 0.2)(D, H * Dh)),
            "wk": _w((lambda *s: torch.randn(*s, dtype=torch.bfloat16) * 0.2)(D, Dh)),
            "wv": _w((lambda *s: torch.randn(*s, dtype=torch.bfloat16) * 0.1)(D, Dh)),
            "wo": _w((lambda *s: torch.randn(*s, dtype=torch.bfloat16) * 0.1)(H * Dh, D)),
        }
    _A_in = _A_setup()

    def _B_setup():
        B, S, D, F, C = 1, 2048, 512, 1376, 2
        return {
            "x": _w((lambda *s: torch.randn(*s, dtype=torch.bfloat16) * 0.05)(B, S, D)),
            "w_r": _w((lambda *s: torch.randn(*s, dtype=torch.bfloat16) * 0.2)(D)),
            "w_gate": _w((lambda *s: torch.randn(*s, dtype=torch.bfloat16) * 0.1)(D, F)),
            "w_up": _w((lambda *s: torch.randn(*s, dtype=torch.bfloat16) * 0.1)(D, F)),
            "w_down": _w((lambda *s: torch.randn(*s, dtype=torch.bfloat16) * 0.1)(F, D)),
        }
    _B_in = _B_setup()

    return {
        "A_x": _A_in["x"],
        "A_wq": _A_in["wq"],
        "A_wk": _A_in["wk"],
        "A_wv": _A_in["wv"],
        "A_wo": _A_in["wo"],
        "B_x": _B_in["x"],
        "B_w_r": _B_in["w_r"],
        "B_w_gate": _B_in["w_gate"],
        "B_w_up": _B_in["w_up"],
        "B_w_down": _B_in["w_down"],
    }

def reference(A_x, A_wq, A_wk, A_wv, A_wo, B_x, B_w_r, B_w_gate, B_w_up, B_w_down):
    def _A_ref(x, wq, wk, wv, wo):
        B, S, D = x.shape
        H = 32
        Dh = D // H
        q = (lambda x, *d: x.permute(*d))((x @ wq).reshape(B, S, H, Dh), 0, 2, 1, 3)
        k = x @ wk
        v = x @ wv
        scores = torch.einsum("bhqd,bkd->bhqk", q, k) * (Dh ** -0.5)
        p = _softmax(scores, axis=-1)
        o = torch.einsum("bhqk,bkd->bhqd", p, v)
        return (lambda x, *d: x.permute(*d))(o, 0, 2, 1, 3).reshape(B, S, H * Dh) @ wo

    def _B_ref(x, w_r, w_gate, w_up, w_down):
        B, S, D = x.shape
        C = 2
        r = x @ w_r
        idx = torch.argsort(-r, axis=-1)[:, :C]
        idx_e = (lambda x, n, axis: torch.repeat_interleave(x, n, dim=axis))(idx.reshape(B, C, 1), D, axis=2)
        x_sel = (lambda x, i, axis: torch.take_along_dim(x, i, dim=axis))(x, idx_e, axis=1)
        g = _silu(x_sel @ w_gate)
        u = x_sel @ w_up
        y = (g * u) @ w_down
        r_sel = _sigmoid((lambda x, i, axis: torch.take_along_dim(x, i, dim=axis))(r, idx, axis=1))
        y = y * r_sel.reshape(B, C, 1)
        b_off = (torch.arange(B) * S).reshape(B, 1)
        flat_idx = (idx + b_off).reshape(B * C)
        zero = (x * 0.0).reshape(B * S, D)
        upd = _scatter_add(zero, flat_idx, y.reshape(B * C, D), axis=0)
        return x + upd.reshape(B, S, D)

    x = A_x
    xn = _layernorm(x, axis=-1)
    return x + _A_ref(xn, A_wq, A_wk, A_wv, A_wo) + _B_ref(xn, B_w_r, B_w_gate, B_w_up, B_w_down)
```

```cpp
#include <hip/hip_runtime.h>
#include <hip/hip_bf16.h>

typedef __attribute__((ext_vector_type(8))) __bf16 bf16x8;
typedef __attribute__((ext_vector_type(4))) __bf16 bf16x4;
typedef __attribute__((ext_vector_type(2))) __bf16 bf16x2;
typedef __attribute__((ext_vector_type(4))) short s16x4;
typedef __attribute__((ext_vector_type(8))) short s16x8;
typedef __attribute__((ext_vector_type(16))) float f32x16;
typedef __attribute__((ext_vector_type(4))) float f32x4;
typedef __attribute__((ext_vector_type(4))) unsigned u32x4;
typedef __attribute__((ext_vector_type(2))) unsigned u32x2;
typedef __bf16 bf16;

#define LDS_AS __attribute__((address_space(3)))
#define DI __device__ __forceinline__

constexpr int S_ = 2048, D_ = 512, H_ = 32, DH_ = 16, F_ = 1376;

constexpr size_t WS_XN = 0;
constexpr size_t WS_Q = 2u << 20;
constexpr size_t WS_K = 4u << 20;
constexpr size_t WS_V = WS_K + 65536;
constexpr size_t WS_R = WS_V + 65536;
constexpr size_t WS_O = 5u << 20;
constexpr size_t WS_YP = 7u << 20;
constexpr size_t WS_SEL = 8u << 20;

constexpr int NFFN = 43;

DI float bf2f(bf16 v) { return (float)v; }
DI float rbf(float v) { return (float)(bf16)v; }

DI float wave_sum(float v) {
#pragma unroll
  for (int o = 32; o > 0; o >>= 1) v += __shfl_xor(v, o);
  return v;
}

DI s16x4 tr_read(const char* p) {
  return __builtin_amdgcn_ds_read_tr16_b64_v4i16((LDS_AS s16x4*)p);
}

DI int wimg_off(int row, int ch) { return row * 128 + ((ch ^ (((row >> 1) & 1) << 2)) << 4); }

constexpr int XS_STRIDE = 1040;

DI f32x16 gemm_tile(const char* wimg, const char* ximg, int n0, int t0, int lane) {
  const int r = lane & 31, h = lane >> 5;
  const int q = (lane & 15) >> 2, p = lane & 3, blk = (lane >> 4) & 1;
  f32x16 acc;
#pragma unroll
  for (int i = 0; i < 16; ++i) acc[i] = 0.f;
  const char* xb = ximg + (t0 + r) * XS_STRIDE + 16 * h;
  const int col = n0 + 16 * blk + 4 * p;
  const int ch = col >> 3, sub = (col & 7) * 2;
#pragma unroll 8
  for (int ks = 0; ks < 32; ++ks) {
    const int row0 = ks * 16 + 8 * h + q;
    s16x4 lo = tr_read(wimg + wimg_off(row0, ch) + sub);
    s16x4 hi = tr_read(wimg + wimg_off(row0 + 4, ch) + sub);
    s16x8 a = __builtin_shufflevector(lo, hi, 0, 1, 2, 3, 4, 5, 6, 7);
    bf16x8 b = *(const bf16x8*)(xb + ks * 32);
    acc = __builtin_amdgcn_mfma_f32_32x32x16_bf16(__builtin_bit_cast(bf16x8, a), b, acc, 0, 0, 0);
  }
  return acc;
}

__global__ __launch_bounds__(256) void k1_ln_qkv(const bf16* __restrict__ x, const bf16* __restrict__ wq,
                                                 const bf16* __restrict__ wk, const bf16* __restrict__ wv,
                                                 const bf16* __restrict__ wr, bf16* __restrict__ xn,
                                                 bf16* __restrict__ Q, bf16* __restrict__ Kb,
                                                 bf16* __restrict__ Vf, float* __restrict__ rsc) {
  extern __shared__ __attribute__((aligned(16))) char smem[];
  char* ximg = smem;
  char* wimg = smem + 64 * XS_STRIDE;
  const int cb = blockIdx.x % 9, tb = blockIdx.x / 9;
  const int tid = threadIdx.x, lane = tid & 63, wave = tid >> 6;
  const int tok0 = tb * 64;

  if (cb < 8) {
#pragma unroll
    for (int i = 0; i < 16; ++i) {
      const int c = tid + i * 256, row = c >> 3, ch = c & 7;
      u32x4 v = *(const u32x4*)(wq + row * 512 + cb * 64 + ch * 8);
      *(u32x4*)(wimg + wimg_off(row, ch)) = v;
    }
  } else {
#pragma unroll
    for (int i = 0; i < 8; ++i) {
      const int c = tid + i * 256, row = c >> 2, ch = c & 3;
      const bf16* src = (ch < 2) ? (wk + row * 16 + ch * 8) : (wv + row * 16 + (ch - 2) * 8);
      u32x4 v = *(const u32x4*)src;
      *(u32x4*)(wimg + wimg_off(row, ch)) = v;
    }
  }

  float wrv[8];
  if (cb == 8) {
    bf16x8 w = *(const bf16x8*)(wr + lane * 8);
#pragma unroll
    for (int j = 0; j < 8; ++j) wrv[j] = bf2f(w[j]);
  }
#pragma unroll 4
  for (int i = 0; i < 16; ++i) {
    const int tl = wave * 16 + i, tok = tok0 + tl;
    bf16x8 xv = *(const bf16x8*)(x + tok * 512 + lane * 8);
    float f[8];
    float s = 0.f;
#pragma unroll
    for (int j = 0; j < 8; ++j) { f[j] = bf2f(xv[j]); s += f[j]; }
    s = wave_sum(s);
    const float mean = s * (1.f / 512.f);
    float vs = 0.f;
#pragma unroll
    for (int j = 0; j < 8; ++j) { const float d = f[j] - mean; vs += d * d; }
    vs = wave_sum(vs);
    const float mb = rbf(mean);
    const float vb = rbf(vs * (1.f / 512.f));
    const float den = rbf(sqrtf(rbf(vb + 1e-5f)));
    bf16x8 o;
#pragma unroll
    for (int j = 0; j < 8; ++j) o[j] = (bf16)(rbf(f[j] - mb) / den);
    *(bf16x8*)(ximg + tl * XS_STRIDE + lane * 16) = o;
    if (cb == 0) *(bf16x8*)(xn + tok * 512 + lane * 8) = o;
    if (cb == 8) {
      float d = 0.f;
#pragma unroll
      for (int j = 0; j < 8; ++j) d += bf2f(o[j]) * wrv[j];
      d = wave_sum(d);
      if (lane == 0) rsc[tok] = rbf(d);
    }
  }
  __syncthreads();

  const int r = lane & 31, h = lane >> 5;
  if (cb < 8) {
    const int n0 = (wave & 1) * 32, t0 = (wave >> 1) * 32;
    f32x16 acc = gemm_tile(wimg, ximg, n0, t0, lane);
    const float sc = 0.25f * 1.4426950408889634f;
    bf16* qrow = Q + (tok0 + t0 + r) * 512 + cb * 64 + n0;
#pragma unroll
    for (int g = 0; g < 4; ++g) {
      bf16x4 o;
#pragma unroll
      for (int j = 0; j < 4; ++j) o[j] = (bf16)(rbf(acc[4 * g + j]) * sc);
      *(bf16x4*)(qrow + 8 * g + 4 * h) = o;
    }
  } else if (wave < 2) {
    const int t0 = wave * 32;
    f32x16 acc = gemm_tile(wimg, ximg, 0, t0, lane);
    const int key = tok0 + t0 + r;
#pragma unroll
    for (int g = 0; g < 2; ++g) {
      bf16x4 o;
#pragma unroll
      for (int j = 0; j < 4; ++j) o[j] = (bf16)acc[4 * g + j];
      *(bf16x4*)(Kb + key * 16 + 8 * g + 4 * h) = o;
    }
    const int t = key >> 5, kk = key & 31, s = kk >> 4, rem = kk & 15;
    const int hh = (rem >> 2) & 1, jj = ((rem >> 3) << 2) | (rem & 3);
#pragma unroll
    for (int g = 2; g < 4; ++g) {
#pragma unroll
      for (int j = 0; j < 4; ++j) {
        const int d = 8 * (g - 2) + 4 * h + j;
        Vf[(((t * 2 + s) * 2 + hh) * 16 + d) * 8 + jj] = (bf16)acc[4 * g + j];
      }
    }
  }
}

__global__ __launch_bounds__(256) void k_ffn(const bf16* __restrict__ xn, const float* __restrict__ rsc,
                                             const bf16* __restrict__ wg, const bf16* __restrict__ wu,
                                             const bf16* __restrict__ wd, float* __restrict__ ypart,
                                             int* __restrict__ sel) {
  __shared__ float xs[2][512];
  __shared__ float red[4][2][2][32];
  __shared__ float hs[2][32];
  __shared__ float bv[4][2];
  __shared__ int bi[4][2];
  __shared__ int sidx[2];
  const int tid = threadIdx.x, lane = tid & 63, wave = tid >> 6;
  const int f = blockIdx.x;
  float v1 = -INFINITY, v2 = -INFINITY; int i1 = 0x7fffffff, i2 = 0x7fffffff;
#pragma unroll
  for (int i = 0; i < 8; ++i) {
    const int idx = tid * 8 + i;
    const float v = rsc[idx];
    if (v > v1 || (v == v1 && idx < i1)) { v2 = v1; i2 = i1; v1 = v; i1 = idx; }
    else if (v > v2 || (v == v2 && idx < i2)) { v2 = v; i2 = idx; }
  }
#pragma unroll
  for (int o = 32; o > 0; o >>= 1) {
    const float ov1 = __shfl_xor(v1, o), ov2 = __shfl_xor(v2, o);
    const int oi1 = __shfl_xor(i1, o), oi2 = __shfl_xor(i2, o);
    const bool a_first = (v1 > ov1) || (v1 == ov1 && i1 < oi1);
    float n1, n2; int m1, m2;
    if (a_first) {
      n1 = v1; m1 = i1;
      const bool s = (v2 > ov1) || (v2 == ov1 && i2 < oi1);
      n2 = s ? v2 : ov1; m2 = s ? i2 : oi1;
    } else {
      n1 = ov1; m1 = oi1;
      const bool s = (ov2 > v1) || (ov2 == v1 && oi2 < i1);
      n2 = s ? ov2 : v1; m2 = s ? oi2 : i1;
    }
    v1 = n1; i1 = m1; v2 = n2; i2 = m2;
  }
  if (lane == 0) { bv[wave][0] = v1; bv[wave][1] = v2; bi[wave][0] = i1; bi[wave][1] = i2; }
  __syncthreads();
  if (tid == 0) {
    float a1 = -INFINITY, a2 = -INFINITY; int b1 = 0x7fffffff, b2 = 0x7fffffff;
    for (int w = 0; w < 4; ++w)
      for (int k = 0; k < 2; ++k) {
        const float v = bv[w][k]; const int idx = bi[w][k];
        if (v > a1 || (v == a1 && idx < b1)) { a2 = a1; b2 = b1; a1 = v; b1 = idx; }
        else if (v > a2 || (v == a2 && idx < b2)) { a2 = v; b2 = idx; }
      }
    sidx[0] = b1; sidx[1] = b2;
    if (f == 0) {
      sel[0] = b1; sel[1] = b2;
      ((float*)sel)[2] = rbf(1.f / (1.f + __expf(-a1)));
      ((float*)sel)[3] = rbf(1.f / (1.f + __expf(-a2)));
    }
  }
  __syncthreads();
#pragma unroll
  for (int i = 0; i < 4; ++i) {
    const int e = tid + i * 256, t = e >> 9, k = e & 511;
    xs[t][k] = bf2f(xn[sidx[t] * 512 + k]);
  }
  __syncthreads();
  const int c = tid & 3, rg = tid >> 2;
  float ag[2][8], au[2][8];
#pragma unroll
  for (int j = 0; j < 8; ++j) { ag[0][j] = ag[1][j] = au[0][j] = au[1][j] = 0.f; }
#pragma unroll
  for (int i = 0; i < 8; ++i) {
    const int row = rg + 64 * i;
    bf16x8 g8 = *(const bf16x8*)(wg + row * F_ + f * 32 + c * 8);
    bf16x8 u8 = *(const bf16x8*)(wu + row * F_ + f * 32 + c * 8);
    const float x0 = xs[0][row], x1 = xs[1][row];
#pragma unroll
    for (int j = 0; j < 8; ++j) {
      const float gw = bf2f(g8[j]), uw = bf2f(u8[j]);
      ag[0][j] += x0 * gw; ag[1][j] += x1 * gw; au[0][j] += x0 * uw; au[1][j] += x1 * uw;
    }
  }
#pragma unroll
  for (int j = 0; j < 8; ++j) {
#pragma unroll
    for (int o = 4; o < 64; o <<= 1) {
      ag[0][j] += __shfl_xor(ag[0][j], o); ag[1][j] += __shfl_xor(ag[1][j], o);
      au[0][j] += __shfl_xor(au[0][j], o); au[1][j] += __shfl_xor(au[1][j], o);
    }
  }
  if (lane < 4) {
#pragma unroll
    for (int j = 0; j < 8; ++j) {
      red[wave][0][0][lane * 8 + j] = ag[0][j]; red[wave][0][1][lane * 8 + j] = ag[1][j];
      red[wave][1][0][lane * 8 + j] = au[0][j]; red[wave][1][1][lane * 8 + j] = au[1][j];
    }
  }
  __syncthreads();
  if (tid < 64) {
    const int t = tid >> 5, col = tid & 31;
    float g = red[0][0][t][col] + red[1][0][t][col] + red[2][0][t][col] + red[3][0][t][col];
    float u = red[0][1][t][col] + red[1][1][t][col] + red[2][1][t][col] + red[3][1][t][col];
    g = rbf(g); u = rbf(u);
    const float sg = rbf(g / (1.f + __expf(-g)));
    hs[t][col] = rbf(sg * u);
  }
  __syncthreads();
  float y00 = 0.f, y01 = 0.f, y10 = 0.f, y11 = 0.f;
#pragma unroll 8
  for (int j = 0; j < 32; ++j) {
    bf16x2 w = *(const bf16x2*)(wd + (f * 32 + j) * 512 + tid * 2);
    const float w0 = bf2f(w[0]), w1 = bf2f(w[1]);
    const float h0 = hs[0][j], h1 = hs[1][j];
    y00 += h0 * w0; y01 += h0 * w1; y10 += h1 * w0; y11 += h1 * w1;
  }
  float* yp = ypart + f * 1024;
  *(float2*)(yp + tid * 2) = make_float2(y00, y01);
  *(float2*)(yp + 512 + tid * 2) = make_float2(y10, y11);
}

constexpr int ATT_LDS = 65536 * 2 + 4096 * 2;
constexpr float SM_THR = 16.f;

__global__ __launch_bounds__(512, 2) void k2_attn(const bf16* __restrict__ Q, const bf16* __restrict__ Kb,
                                                  const bf16* __restrict__ Vf, bf16* __restrict__ O) {
  extern __shared__ __attribute__((aligned(16))) char smem[];
  char* kl = smem;
  char* vl = smem + 65536;
  char* ones = smem + 131072;
  char* zeros = smem + 131072 + 4096;
  const int tid = threadIdx.x, lane = tid & 63, wave = tid >> 6;
  const int r = lane & 31, h = lane >> 5;
  const int token = blockIdx.x * 8 + wave;

#pragma unroll
  for (int i = 0; i < 8; ++i) {
    const int c = tid + i * 512;
    u32x4 kv = *(const u32x4*)((const char*)Kb + c * 16);
    u32x4 vv = *(const u32x4*)((const char*)Vf + c * 16);
    *(u32x4*)(kl + c * 16) = kv;
    *(u32x4*)(vl + c * 16) = vv;
  }
  {
    if (tid < 256) { u32x4 o1 = {0x3f803f80u, 0x3f803f80u, 0x3f803f80u, 0x3f803f80u}; *(u32x4*)(ones + tid * 16) = o1; }
    else { u32x4 z = {0u, 0u, 0u, 0u}; *(u32x4*)(zeros + (tid - 256) * 16) = z; }
  }
  const bf16x8 qf = *(const bf16x8*)(Q + token * 512 + r * 16 + 8 * h);
  __syncthreads();

  const char* kp = kl + r * 32 + 16 * h;
  const char* vp = (r < 16) ? (vl + (h * 16 + r) * 16) : ((r == 16 || r == 20) ? ones : zeros);
  const int vstep = (r < 16) ? 4096 : 0;

  f32x16 negm, oacc;
#pragma unroll
  for (int i = 0; i < 16; ++i) { negm[i] = 0.f; oacc[i] = 0.f; }
  {
    bf16x8 kf = *(const bf16x8*)(kp);
    f32x16 x = __builtin_amdgcn_mfma_f32_32x32x16_bf16(kf, qf, negm, 0, 0, 0);
    float m = x[0];
#pragma unroll
    for (int i = 1; i < 16; ++i) m = fmaxf(m, x[i]);
    m = fmaxf(m, __shfl_xor(m, 32));
#pragma unroll
    for (int i = 0; i < 16; ++i) negm[i] = -m;
  }

  for (int b = 0; b < 16; ++b) {
    f32x16 x[4];
#pragma unroll
    for (int t = 0; t < 4; ++t) {
      bf16x8 kf = *(const bf16x8*)(kp + t * 1024);
      x[t] = __builtin_amdgcn_mfma_f32_32x32x16_bf16(kf, qf, negm, 0, 0, 0);
    }
    float bm = fmaxf(x[0][0], x[0][1]);
#pragma unroll
    for (int t = 0; t < 4; ++t)
#pragma unroll
      for (int i = (t == 0 ? 2 : 0); i < 16; i += 2) bm = fmaxf(fmaxf(bm, x[t][i]), x[t][i + 1]);
    bm = fmaxf(bm, __shfl_xor(bm, 32));
    if (__any(bm > SM_THR)) {
      const float d = fmaxf(bm, 0.f);
      const float al = __builtin_amdgcn_exp2f(-d);
#pragma unroll
      for (int t = 0; t < 4; ++t)
#pragma unroll
        for (int i = 0; i < 16; ++i) x[t][i] -= d;
#pragma unroll
      for (int i = 0; i < 16; ++i) { negm[i] -= d; oacc[i] *= al; }
    }
#pragma unroll
    for (int t = 0; t < 4; ++t) {
#pragma unroll
      for (int s = 0; s < 2; ++s) {
        bf16x8 pf;
#pragma unroll
        for (int j = 0; j < 8; ++j) pf[j] = (bf16)__builtin_amdgcn_exp2f(x[t][8 * s + j]);
        bf16x8 vf = *(const bf16x8*)(vp + (t * 2 + s) * 512);
        oacc = __builtin_amdgcn_mfma_f32_32x32x16_bf16(vf, pf, oacc, 0, 0, 0);
      }
    }
    kp += 4096;
    vp += vstep;
  }
  const float inv = 1.f / oacc[8];
  bf16* orow = O + token * 512 + r * 16;
#pragma unroll
  for (int g = 0; g < 2; ++g) {
    bf16x4 o;
#pragma unroll
    for (int j = 0; j < 4; ++j) o[j] = (bf16)(oacc[4 * g + j] * inv);
    *(bf16x4*)(orow + 8 * g + 4 * h) = o;
  }
}

__global__ __launch_bounds__(256) void k3_out(const bf16* __restrict__ O, const bf16* __restrict__ wo,
                                              const bf16* __restrict__ x, const bf16* __restrict__ xn,
                                              const float* __restrict__ ypart, const int* __restrict__ sel,
                                              bf16* __restrict__ out) {
  extern __shared__ __attribute__((aligned(16))) char smem[];
  char* ximg = smem;
  char* wimg = smem + 64 * XS_STRIDE;
  const int cb = blockIdx.x & 7, tb = blockIdx.x >> 3;
  const int tid = threadIdx.x, lane = tid & 63, wave = tid >> 6;
  const int tok0 = tb * 64;
#pragma unroll
  for (int i = 0; i < 16; ++i) {
    const int c = tid + i * 256, row = c >> 3, ch = c & 7;
    u32x4 v = *(const u32x4*)(wo + row * 512 + cb * 64 + ch * 8);
    *(u32x4*)(wimg + wimg_off(row, ch)) = v;
  }
#pragma unroll
  for (int i = 0; i < 16; ++i) {
    const int c = tid + i * 256, row = c >> 6, ch = c & 63;
    u32x4 v = *(const u32x4*)(O + (tok0 + row) * 512 + ch * 8);
    *(u32x4*)(ximg + row * XS_STRIDE + ch * 16) = v;
  }
  __syncthreads();
  const int r = lane & 31, h = lane >> 5;
  const int n0 = (wave & 1) * 32, t0 = (wave >> 1) * 32;
  f32x16 acc = gemm_tile(wimg, ximg, n0, t0, lane);
  const int tok = tok0 + t0 + r;
  const int s0 = sel[0], s1 = sel[1];
  const int which = (tok == s0) ? 0 : ((tok == s1) ? 1 : -1);
  const float gate = (which >= 0) ? ((const float*)sel)[2 + which] : 0.f;
#pragma unroll
  for (int g = 0; g < 4; ++g) {
    const int n = cb * 64 + n0 + 8 * g + 4 * h;
    bf16x4 xv = *(const bf16x4*)(x + tok * 512 + n);
    bf16x4 nv = *(const bf16x4*)(xn + tok * 512 + n);
    float upd[4] = {0.f, 0.f, 0.f, 0.f};
    if (which >= 0) {
      for (int f = 0; f < NFFN; ++f) {
        f32x4 p = *(const f32x4*)(ypart + f * 1024 + which * 512 + n);
#pragma unroll
        for (int j = 0; j < 4; ++j) upd[j] += p[j];
      }
#pragma unroll
      for (int j = 0; j < 4; ++j) upd[j] = rbf(rbf(upd[j]) * gate);
    }
    bf16x4 o;
#pragma unroll
    for (int j = 0; j < 4; ++j) {
      const float a = rbf(acc[4 * g + j]);
      const float xa = rbf(bf2f(xv[j]) + a);
      const float bb = rbf(bf2f(nv[j]) + upd[j]);
      o[j] = (bf16)(xa + bb);
    }
    *(bf16x4*)(out + tok * 512 + n) = o;
  }
}

extern "C" void kernel_launch(void* const* d_in, const int* in_sizes, int n_in, void* d_out, int out_size,
                              void* d_ws, size_t ws_size, hipStream_t stream) {
  const bf16* x = (const bf16*)d_in[0];
  const bf16* wq = (const bf16*)d_in[1];
  const bf16* wk = (const bf16*)d_in[2];
  const bf16* wv = (const bf16*)d_in[3];
  const bf16* wo = (const bf16*)d_in[4];
  const bf16* wr = (const bf16*)d_in[6];
  const bf16* wg = (const bf16*)d_in[7];
  const bf16* wu = (const bf16*)d_in[8];
  const bf16* wd = (const bf16*)d_in[9];
  char* ws = (char*)d_ws;
  bf16* xn = (bf16*)(ws + WS_XN);
  bf16* Q = (bf16*)(ws + WS_Q);
  bf16* Kb = (bf16*)(ws + WS_K);
  bf16* Vf = (bf16*)(ws + WS_V);
  float* rsc = (float*)(ws + WS_R);
  bf16* O = (bf16*)(ws + WS_O);
  float* yp = (float*)(ws + WS_YP);
  int* sel = (int*)(ws + WS_SEL);
  bf16* out = (bf16*)d_out;

  const int gemm_lds = 64 * XS_STRIDE + 65536;
  (void)hipFuncSetAttribute((const void*)k1_ln_qkv, hipFuncAttributeMaxDynamicSharedMemorySize, gemm_lds);
  (void)hipFuncSetAttribute((const void*)k2_attn, hipFuncAttributeMaxDynamicSharedMemorySize, ATT_LDS);
  (void)hipFuncSetAttribute((const void*)k3_out, hipFuncAttributeMaxDynamicSharedMemorySize, gemm_lds);

  k1_ln_qkv<<<32 * 9, 256, gemm_lds, stream>>>(x, wq, wk, wv, wr, xn, Q, Kb, Vf, rsc);
  k_ffn<<<NFFN, 256, 0, stream>>>(xn, rsc, wg, wu, wd, yp, sel);
  k2_attn<<<256, 512, ATT_LDS, stream>>>(Q, Kb, Vf, O);
  k3_out<<<256, 256, gemm_lds, stream>>>(O, wo, x, xn, yp, sel, out);
}
```

```cpp
#include <hip/hip_runtime.h>
#include <hip/hip_bf16.h>

typedef __attribute__((ext_vector_type(8))) __bf16 bf16x8;
typedef __attribute__((ext_vector_type(4))) __bf16 bf16x4;
typedef __attribute__((ext_vector_type(2))) __bf16 bf16x2;
typedef __attribute__((ext_vector_type(4))) short s16x4;
typedef __attribute__((ext_vector_type(8))) short s16x8;
typedef __attribute__((ext_vector_type(16))) float f32x16;
typedef __attribute__((ext_vector_type(4))) float f32x4;
typedef __attribute__((ext_vector_type(4))) unsigned u32x4;
typedef __attribute__((ext_vector_type(2))) unsigned u32x2;
typedef __bf16 bf16;

#define LDS_AS __attribute__((address_space(3)))
#define GLB_AS __attribute__((address_space(1)))
#define DI __device__ __forceinline__

constexpr int S_ = 2048, D_ = 512, H_ = 32, DH_ = 16, F_ = 1376;

constexpr size_t WS_XN = 0;
constexpr size_t WS_Q = 2u << 20;
constexpr size_t WS_K = 4u << 20;
constexpr size_t WS_V = WS_K + 65536;
constexpr size_t WS_R = WS_V + 65536;
constexpr size_t WS_O = 5u << 20;
constexpr size_t WS_YP = 7u << 20;
constexpr size_t WS_SEL = 8u << 20;

constexpr int NFFN = 172;

DI float bf2f(bf16 v) { return (float)v; }
DI float rbf(float v) { return (float)(bf16)v; }

DI void glds16(const void* g, void* l) {
  __builtin_amdgcn_global_load_lds((const GLB_AS void*)g, (LDS_AS void*)l, 16, 0, 0);
}

template <int CTRL>
DI float dpp_add(float v) {
  const int t = __builtin_amdgcn_update_dpp(0, __float_as_int(v), CTRL, 0xf, 0xf, true);
  return v + __int_as_float(t);
}
DI float sum8(float v) {
  v = dpp_add<0xB1>(v);
  v = dpp_add<0x4E>(v);
  v = dpp_add<0x141>(v);
  return v;
}

DI s16x4 tr_read(const char* p) {
  return __builtin_amdgcn_ds_read_tr16_b64_v4i16((LDS_AS s16x4*)p);
}

DI int wimg_off(int row, int ch) { return row * 128 + ((ch ^ (((row >> 1) & 1) << 2)) << 4); }

constexpr int XS_STRIDE = 1040;

DI void stage_w(const bf16* w, int col0, char* wimg, int wave, int nwaves, int lane) {
  for (int p = wave; p < 64; p += nwaves) {
    const int row = p * 8 + (lane >> 3), pc = lane & 7;
    const int ch = pc ^ (((row >> 1) & 1) << 2);
    glds16(w + row * 512 + col0 + ch * 8, wimg + p * 1024);
  }
}

DI f32x16 gemm_tile(const char* wimg, const char* ximg, int n0, int t0, int lane) {
  const int r = lane & 31, h = lane >> 5;
  const int q = (lane & 15) >> 2, p = lane & 3, blk = (lane >> 4) & 1;
  f32x16 acc;
#pragma unroll
  for (int i = 0; i < 16; ++i) acc[i] = 0.f;
  const char* xb = ximg + (t0 + r) * XS_STRIDE + 16 * h;
  const int col = n0 + 16 * blk + 4 * p;
  const int ch = col >> 3, sub = (col & 7) * 2;
  const char* wb0 = wimg + wimg_off(8 * h + q, ch) + sub;
  const char* wb1 = wimg + wimg_off(8 * h + q + 4, ch) + sub;
#pragma unroll
  for (int ks = 0; ks < 32; ++ks) {
    s16x4 lo = tr_read(wb0 + ks * 2048);
    s16x4 hi = tr_read(wb1 + ks * 2048);
    s16x8 a = __builtin_shufflevector(lo, hi, 0, 1, 2, 3, 4, 5, 6, 7);
    bf16x8 b = *(const bf16x8*)(xb + ks * 32);
    acc = __builtin_amdgcn_mfma_f32_32x32x16_bf16(__builtin_bit_cast(bf16x8, a), b, acc, 0, 0, 0);
  }
  return acc;
}
DI f32x16 gemm_tile16(const char* wimg, const char* ximg, int t0, int lane) {
  const int r = lane & 31, h = lane >> 5;
  const int q = (lane & 15) >> 2, p = lane & 3;
  f32x16 acc;
#pragma unroll
  for (int i = 0; i < 16; ++i) acc[i] = 0.f;
  const char* xb = ximg + (t0 + r) * XS_STRIDE + 16 * h;
  const char* wb = wimg + (8 * h + q) * 32 + 8 * p;
#pragma unroll
  for (int ks = 0; ks < 32; ++ks) {
    s16x4 lo = tr_read(wb + ks * 512);
    s16x4 hi = tr_read(wb + ks * 512 + 128);
    s16x8 a = __builtin_shufflevector(lo, hi, 0, 1, 2, 3, 4, 5, 6, 7);
    bf16x8 b = *(const bf16x8*)(xb + ks * 32);
    acc = __builtin_amdgcn_mfma_f32_32x32x16_bf16(__builtin_bit_cast(bf16x8, a), b, acc, 0, 0, 0);
  }
  return acc;
}

constexpr int K1_LDS = 64 * XS_STRIDE + 65536 + 16384;

__global__ __launch_bounds__(512) void k1_ln_qkv(const bf16* __restrict__ x, const bf16* __restrict__ wq,
                                                 const bf16* __restrict__ wk, const bf16* __restrict__ wv,
                                                 const bf16* __restrict__ wr, bf16* __restrict__ xn,
                                                 bf16* __restrict__ Q, bf16* __restrict__ Kb,
                                                 bf16* __restrict__ Vf, float* __restrict__ rsc) {
  extern __shared__ __attribute__((aligned(16))) char smem[];
  char* ximg = smem;
  char* wimg = smem + 64 * XS_STRIDE;
  char* kvimg = smem + 64 * XS_STRIDE + 65536;
  const int cb = blockIdx.x & 7, tb = blockIdx.x >> 3;
  const int tid = threadIdx.x, lane = tid & 63, wave = tid >> 6;
  const int tok0 = tb * 64;

  const int tl = wave * 8 + (lane >> 3), c8 = lane & 7;
  bf16x8 xv[8];
#pragma unroll
  for (int i = 0; i < 8; ++i) xv[i] = *(const bf16x8*)(x + (tok0 + tl) * 512 + (c8 + 8 * i) * 8);

  stage_w(wq, cb * 64, wimg, wave, 8, lane);
  if (cb < 2) {
    const bf16* w = cb == 0 ? wk : wv;
#pragma unroll
    for (int i = 0; i < 2; ++i) {
      const int p = wave + 8 * i;
      glds16((const char*)w + p * 1024 + lane * 16, kvimg + p * 1024);
    }
  }

  float s = 0.f;
#pragma unroll
  for (int i = 0; i < 8; ++i)
#pragma unroll
    for (int j = 0; j < 8; ++j) s += bf2f(xv[i][j]);
  s = sum8(s);
  const float mean = s * (1.f / 512.f);
  float vs = 0.f;
#pragma unroll
  for (int i = 0; i < 8; ++i)
#pragma unroll
    for (int j = 0; j < 8; ++j) { const float d = bf2f(xv[i][j]) - mean; vs += d * d; }
  vs = sum8(vs);
  const float mb = rbf(mean);
  const float vb = rbf(vs * (1.f / 512.f));
  const float den = rbf(sqrtf(rbf(vb + 1e-5f)));
  float rd = 0.f;
#pragma unroll
  for (int i = 0; i < 8; ++i) {
    bf16x8 o;
#pragma unroll
    for (int j = 0; j < 8; ++j) o[j] = (bf16)(rbf(bf2f(xv[i][j]) - mb) / den);
    *(bf16x8*)(ximg + tl * XS_STRIDE + (c8 + 8 * i) * 16) = o;
    if (cb == 2) *(bf16x8*)(xn + (tok0 + tl) * 512 + (c8 + 8 * i) * 8) = o;
    if (cb == 3) {
      bf16x8 w = *(const bf16x8*)(wr + (c8 + 8 * i) * 8);
#pragma unroll
      for (int j = 0; j < 8; ++j) rd += bf2f(o[j]) * bf2f(w[j]);
    }
  }
  if (cb == 3) {
    rd = sum8(rd);
    if (c8 == 0) rsc[tok0 + tl] = rbf(rd);
  }
  __syncthreads();

  const int r = lane & 31, h = lane >> 5;
  if (wave < 4) {
    const int n0 = (wave & 1) * 32, t0 = (wave >> 1) * 32;
    f32x16 acc = gemm_tile(wimg, ximg, n0, t0, lane);
    const float sc = 0.25f * 1.4426950408889634f;
    bf16* qrow = Q + (tok0 + t0 + r) * 512 + cb * 64 + n0;
#pragma unroll
    for (int g = 0; g < 4; ++g) {
      bf16x4 o;
#pragma unroll
      for (int j = 0; j < 4; ++j) o[j] = (bf16)(rbf(acc[4 * g + j]) * sc);
      *(bf16x4*)(qrow + 8 * g + 4 * h) = o;
    }
  } else if (wave < 6 && cb < 2) {
    const int t0 = (wave - 4) * 32;
    f32x16 acc = gemm_tile16(kvimg, ximg, t0, lane);
    const int key = tok0 + t0 + r;
    const int t = key >> 5;
    if (cb == 0) {
#pragma unroll
      for (int g = 0; g < 2; ++g) {
        bf16x4 o;
#pragma unroll
        for (int j = 0; j < 4; ++j) o[j] = (bf16)acc[4 * g + j];
        *(bf16x4*)(Kb + ((t * 2 + g) * 32 + r) * 8 + 4 * h) = o;
      }
    } else {
      const int kk = key & 31, sg = kk >> 4, rem = kk & 15;
      const int hh = (rem >> 2) & 1, jj = ((rem >> 3) << 2) | (rem & 3);
#pragma unroll
      for (int g = 0; g < 2; ++g) {
#pragma unroll
        for (int j = 0; j < 4; ++j) {
          const int d = 8 * g + 4 * h + j;
          Vf[(((t * 2 + sg) * 2 + hh) * 16 + d) * 8 + jj] = (bf16)acc[4 * g + j];
        }
      }
    }
  }
}

constexpr int ATT_LDS = 65536 * 2 + 4096 + 4096;

struct Top2 { float v1, v2; int i1, i2; };
DI void top2_push(Top2& t, float v, int idx) {
  if (v > t.v1 || (v == t.v1 && idx < t.i1)) { t.v2 = t.v1; t.i2 = t.i1; t.v1 = v; t.i1 = idx; }
  else if (v > t.v2 || (v == t.v2 && idx < t.i2)) { t.v2 = v; t.i2 = idx; }
}

DI void ffn_slice(int f, const bf16* __restrict__ xn, const float* __restrict__ rsc, const bf16* __restrict__ wg,
                  const bf16* __restrict__ wu, const bf16* __restrict__ wd, float* __restrict__ ypart,
                  int* __restrict__ sel, char* scratch, int tid) {
  const int lane = tid & 63, wave = tid >> 6;
  float* sv = (float*)scratch;
  int* si = (int*)(scratch + 64);
  float* red = (float*)(scratch + 128);
  float* hs = (float*)(scratch + 128 + 1024);
  const bf16x8 g8 = *(const bf16x8*)(wg + tid * F_ + f * 8);
  const bf16x8 u8 = *(const bf16x8*)(wu + tid * F_ + f * 8);
  bf16 dcol[8];
#pragma unroll
  for (int j = 0; j < 8; ++j) dcol[j] = wd[(f * 8 + j) * 512 + tid];
  const f32x4 rv = *(const f32x4*)(rsc + tid * 4);
  Top2 t; t.v1 = -INFINITY; t.v2 = -INFINITY; t.i1 = 0x7fffffff; t.i2 = 0x7fffffff;
#pragma unroll
  for (int i = 0; i < 4; ++i) top2_push(t, rv[i], tid * 4 + i);
#pragma unroll
  for (int o = 32; o > 0; o >>= 1) {
    const float ov1 = __shfl_xor(t.v1, o), ov2 = __shfl_xor(t.v2, o);
    const int oi1 = __shfl_xor(t.i1, o), oi2 = __shfl_xor(t.i2, o);
    top2_push(t, ov1, oi1);
    top2_push(t, ov2, oi2);
  }
  if (lane == 0) { sv[wave * 2] = t.v1; sv[wave * 2 + 1] = t.v2; si[wave * 2] = t.i1; si[wave * 2 + 1] = t.i2; }
  __syncthreads();
  Top2 a; a.v1 = -INFINITY; a.v2 = -INFINITY; a.i1 = 0x7fffffff; a.i2 = 0x7fffffff;
#pragma unroll
  for (int w = 0; w < 16; ++w) top2_push(a, sv[w], si[w]);
  if (f == 0 && tid == 0) {
    sel[0] = a.i1; sel[1] = a.i2;
    ((float*)sel)[2] = rbf(1.f / (1.f + __expf(-a.v1)));
    ((float*)sel)[3] = rbf(1.f / (1.f + __expf(-a.v2)));
  }
  const float x0 = bf2f(xn[a.i1 * 512 + tid]), x1 = bf2f(xn[a.i2 * 512 + tid]);
  float v[32];
#pragma unroll
  for (int j = 0; j < 8; ++j) {
    const float gw = bf2f(g8[j]), uw = bf2f(u8[j]);
    v[j] = x0 * gw; v[8 + j] = x1 * gw; v[16 + j] = x0 * uw; v[24 + j] = x1 * uw;
  }
#pragma unroll
  for (int st = 0; st < 5; ++st) {
    const int o = 32 >> st, n = 16 >> st;
    const bool up = (lane & o) != 0;
#pragma unroll
    for (int i = 0; i < n; ++i) {
      const float keep = up ? v[i + n] : v[i];
      const float send = up ? v[i] : v[i + n];
      v[i] = keep + __shfl_xor(send, o);
    }
  }
  v[0] += __shfl_xor(v[0], 1);
  if ((lane & 1) == 0) red[wave * 32 + (lane >> 1)] = v[0];
  __syncthreads();
  if (tid < 16) {
    float g = 0.f, u = 0.f;
#pragma unroll
    for (int w = 0; w < 8; ++w) { g += red[w * 32 + tid]; u += red[w * 32 + 16 + tid]; }
    g = rbf(g); u = rbf(u);
    const float sg = rbf(g / (1.f + __expf(-g)));
    hs[tid] = rbf(sg * u);
  }
  __syncthreads();
  float y0 = 0.f, y1 = 0.f;
#pragma unroll
  for (int j = 0; j < 8; ++j) { const float w = bf2f(dcol[j]); y0 += hs[j] * w; y1 += hs[8 + j] * w; }
  float* yp = ypart + (size_t)f * 1024;
  yp[tid] = y0;
  yp[512 + tid] = y1;
}

__global__ __launch_bounds__(512, 2) void k2_attn(const bf16* __restrict__ Q, const bf16* __restrict__ Kb,
                                                  bf16* __restrict__ O, const bf16* __restrict__ xn,
                                                  const float* __restrict__ rsc, const bf16* __restrict__ wg,
                                                  const bf16* __restrict__ wu, const bf16* __restrict__ wd,
                                                  float* __restrict__ ypart, int* __restrict__ sel) {
  extern __shared__ __attribute__((aligned(16))) char smem[];
  char* kl = smem;
  char* vl = smem + 65536;
  char* cst = smem + 131072;
  char* scratch = smem + 131072 + 4096;
  const int tid = threadIdx.x, lane = tid & 63, wave = tid >> 6;
  const int r = lane & 31, h = lane >> 5;
  const int token = blockIdx.x * 8 + wave;

#pragma unroll
  for (int i = 0; i < 16; ++i) {
    const int piece = wave + i * 8;
    glds16((const char*)Kb + piece * 1024 + lane * 16, smem + piece * 1024);
  }
  if (tid < 256) {
    const unsigned cv = ((tid & 15) == 0 || (tid & 15) == 4) ? 0x3f803f80u : 0u;
    *(u32x4*)(cst + tid * 16) = u32x4{cv, cv, cv, cv};
  }
  const bf16x8 qf = *(const bf16x8*)(Q + token * 512 + r * 16 + 8 * h);
  if (blockIdx.x < NFFN) ffn_slice(blockIdx.x, xn, rsc, wg, wu, wd, ypart, sel, scratch, tid);
  __syncthreads();

  const char* kp0 = kl + lane * 16;
  const char* vp0 = (r < 16) ? (vl + (h * 16 + r) * 16) : (cst + (h * 16 + r - 16) * 16);
  const int vstep = (r < 16) ? 4096 : 0;

  f32x16 negm, oacc;
#pragma unroll
  for (int i = 0; i < 16; ++i) { negm[i] = 0.f; oacc[i] = 0.f; }
  {
    float m = -INFINITY;
#pragma unroll
    for (int t = 0; t < 4; ++t) {
      bf16x8 kf = *(const bf16x8*)(kp0 + t * 1024);
      f32x16 x = __builtin_amdgcn_mfma_f32_32x32x16_bf16(kf, qf, negm, 0, 0, 0);
#pragma unroll
      for (int i = 0; i < 16; i += 2) m = fmaxf(fmaxf(m, x[i]), x[i + 1]);
    }
    m = fmaxf(m, __shfl_xor(m, 32));
#pragma unroll
    for (int i = 0; i < 16; ++i) negm[i] = -m;
  }
  {
    const char* kp = kp0;
    const char* vp = vp0;
    for (int b = 0; b < 16; ++b) {
#pragma unroll
      for (int t = 0; t < 4; ++t) {
        bf16x8 kf = *(const bf16x8*)(kp + t * 1024);
        f32x16 x = __builtin_amdgcn_mfma_f32_32x32x16_bf16(kf, qf, negm, 0, 0, 0);
#pragma unroll
        for (int s = 0; s < 2; ++s) {
          bf16x8 pf;
#pragma unroll
          for (int j = 0; j < 8; ++j) pf[j] = (bf16)__builtin_amdgcn_exp2f(x[8 * s + j]);
          bf16x8 vf = *(const bf16x8*)(vp + (t * 2 + s) * 512);
          oacc = __builtin_amdgcn_mfma_f32_32x32x16_bf16(vf, pf, oacc, 0, 0, 0);
        }
      }
      kp += 4096;
      vp += vstep;
    }
  }
  {
    float big = fabsf(oacc[8]);
#pragma unroll
    for (int i = 0; i < 8; ++i) big = fmaxf(big, fabsf(oacc[i]));
    const bool bad = !(big < 1e30f);
    if (__any(bad)) {
#pragma unroll
      for (int i = 0; i < 16; ++i) { negm[i] = 0.f; oacc[i] = 0.f; }
      float mrun = -INFINITY;
      const char* kp = kp0;
      const char* vp = vp0;
      for (int t = 0; t < 64; ++t) {
        bf16x8 kf = *(const bf16x8*)(kp);
        f32x16 x = __builtin_amdgcn_mfma_f32_32x32x16_bf16(kf, qf, negm, 0, 0, 0);
        float m = x[0];
#pragma unroll
        for (int i = 1; i < 16; ++i) m = fmaxf(m, x[i]);
        m = fmaxf(m, __shfl_xor(m, 32));
        const float mn = fmaxf(mrun, m);
        const float al = __builtin_amdgcn_exp2f(mrun - mn);
        mrun = mn;
#pragma unroll
        for (int i = 0; i < 16; ++i) oacc[i] *= al;
#pragma unroll
        for (int s = 0; s < 2; ++s) {
          bf16x8 pf;
#pragma unroll
          for (int j = 0; j < 8; ++j) pf[j] = (bf16)__builtin_amdgcn_exp2f(x[8 * s + j] - mn);
          bf16x8 vf = *(const bf16x8*)(vp + ((t & 3) * 2 + s) * 512);
          oacc = __builtin_amdgcn_mfma_f32_32x32x16_bf16(vf, pf, oacc, 0, 0, 0);
        }
        kp += 1024;
        if ((t & 3) == 3) vp += vstep;
      }
    }
  }
  const float inv = 1.f / oacc[8];
  bf16* orow = O + token * 512 + r * 16;
#pragma unroll
  for (int g = 0; g < 2; ++g) {
    bf16x4 o;
#pragma unroll
    for (int j = 0; j < 4; ++j) o[j] = (bf16)(oacc[4 * g + j] * inv);
    *(bf16x4*)(orow + 8 * g + 4 * h) = o;
  }
}

constexpr int K3_LDS = 64 * XS_STRIDE + 65536;

__global__ __launch_bounds__(512) void k3_out(const bf16* __restrict__ O, const bf16* __restrict__ wo,
                                              const bf16* __restrict__ x, const bf16* __restrict__ xn,
                                              const float* __restrict__ ypart, const int* __restrict__ sel,
                                              bf16* __restrict__ out) {
  extern __shared__ __attribute__((aligned(16))) char smem[];
  char* ximg = smem;
  char* wimg = smem + 64 * XS_STRIDE;
  const int cb = blockIdx.x & 7, tb = blockIdx.x >> 3;
  const int tid = threadIdx.x, lane = tid & 63, wave = tid >> 6;
  const int tok0 = tb * 64;
#pragma unroll
  for (int i = 0; i < 8; ++i) {
    const int row = wave + 8 * i;
    glds16(O + (tok0 + row) * 512 + lane * 8, ximg + row * XS_STRIDE);
  }
  stage_w(wo, cb * 64, wimg, wave, 8, lane);
  const int r = lane & 31, h = lane >> 5;
  const int n0 = (wave & 1) * 32, t0 = ((wave >> 1) & 1) * 32;
  const int tok = tok0 + t0 + r;
  bf16x4 xv[4], nv[4];
  int which = -1;
  if (wave < 4) {
#pragma unroll
    for (int g = 0; g < 4; ++g) {
      const int n = cb * 64 + n0 + 8 * g + 4 * h;
      xv[g] = *(const bf16x4*)(x + tok * 512 + n);
      nv[g] = *(const bf16x4*)(xn + tok * 512 + n);
    }
    const int s0 = sel[0], s1 = sel[1];
    which = (tok == s0) ? 0 : ((tok == s1) ? 1 : -1);
  }
  __syncthreads();
  if (wave >= 4) return;
  f32x16 acc = gemm_tile(wimg, ximg, n0, t0, lane);
  float upd[16];
#pragma unroll
  for (int i = 0; i < 16; ++i) upd[i] = 0.f;
  if (which >= 0) {
    const float gate = ((const float*)sel)[2 + which];
    for (int f = 0; f < NFFN; ++f) {
#pragma unroll
      for (int g = 0; g < 4; ++g) {
        const int n = cb * 64 + n0 + 8 * g + 4 * h;
        f32x4 p = *(const f32x4*)(ypart + (size_t)f * 1024 + which * 512 + n);
#pragma unroll
        for (int j = 0; j < 4; ++j) upd[4 * g + j] += p[j];
      }
    }
#pragma unroll
    for (int i = 0; i < 16; ++i) upd[i] = rbf(rbf(upd[i]) * gate);
  }
#pragma unroll
  for (int g = 0; g < 4; ++g) {
    const int n = cb * 64 + n0 + 8 * g + 4 * h;
    bf16x4 o;
#pragma unroll
    for (int j = 0; j < 4; ++j) {
      const float a = rbf(acc[4 * g + j]);
      const float xa = rbf(bf2f(xv[g][j]) + a);
      const float bb = rbf(bf2f(nv[g][j]) + upd[4 * g + j]);
      o[j] = (bf16)(xa + bb);
    }
    *(bf16x4*)(out + tok * 512 + n) = o;
  }
}

extern "C" void kernel_launch(void* const* d_in, const int* in_sizes, int n_in, void* d_out, int out_size,
                              void* d_ws, size_t ws_size, hipStream_t stream) {
  const bf16* x = (const bf16*)d_in[0];
  const bf16* wq = (const bf16*)d_in[1];
  const bf16* wk = (const bf16*)d_in[2];
  const bf16* wv = (const bf16*)d_in[3];
  const bf16* wo = (const bf16*)d_in[4];
  const bf16* wr = (const bf16*)d_in[6];
  const bf16* wg = (const bf16*)d_in[7];
  const bf16* wu = (const bf16*)d_in[8];
  const bf16* wd = (const bf16*)d_in[9];
  char* ws = (char*)d_ws;
  bf16* xn = (bf16*)(ws + WS_XN);
  bf16* Q = (bf16*)(ws + WS_Q);
  bf16* Kb = (bf16*)(ws + WS_K);
  bf16* Vf = (bf16*)(ws + WS_V);
  float* rsc = (float*)(ws + WS_R);
  bf16* O = (bf16*)(ws + WS_O);
  float* yp = (float*)(ws + WS_YP);
  int* sel = (int*)(ws + WS_SEL);
  bf16* out = (bf16*)d_out;

  (void)hipFuncSetAttribute((const void*)k1_ln_qkv, hipFuncAttributeMaxDynamicSharedMemorySize, K1_LDS);
  (void)hipFuncSetAttribute((const void*)k2_attn, hipFuncAttributeMaxDynamicSharedMemorySize, ATT_LDS);
  (void)hipFuncSetAttribute((const void*)k3_out, hipFuncAttributeMaxDynamicSharedMemorySize, K3_LDS);

  k1_ln_qkv<<<256, 512, K1_LDS, stream>>>(x, wq, wk, wv, wr, xn, Q, Kb, Vf, rsc);
  k2_attn<<<256, 512, ATT_LDS, stream>>>(Q, Kb, O, xn, rsc, wg, wu, wd, yp, sel);
  k3_out<<<256, 512, K3_LDS, stream>>>(O, wo, x, xn, yp, sel, out);
}
```

```cpp
#include <hip/hip_runtime.h>
#include <hip/hip_bf16.h>

typedef __attribute__((ext_vector_type(8))) __bf16 bf16x8;
typedef __attribute__((ext_vector_type(4))) __bf16 bf16x4;
typedef __attribute__((ext_vector_type(2))) __bf16 bf16x2;
typedef __attribute__((ext_vector_type(4))) short s16x4;
typedef __attribute__((ext_vector_type(8))) short s16x8;
typedef __attribute__((ext_vector_type(16))) float f32x16;
typedef __attribute__((ext_vector_type(4))) float f32x4;
typedef __attribute__((ext_vector_type(4))) unsigned u32x4;
typedef __attribute__((ext_vector_type(2))) unsigned u32x2;
typedef __attribute__((ext_vector_type(2))) float f32x2;
typedef __bf16 bf16;

#define LDS_AS __attribute__((address_space(3)))
#define GLB_AS __attribute__((address_space(1)))
#define DI __device__ __forceinline__

constexpr int S_ = 2048, D_ = 512, H_ = 32, DH_ = 16, F_ = 1376;

constexpr size_t WS_XN = 0;
constexpr size_t WS_Q = 2u << 20;
constexpr size_t WS_K = 4u << 20;
constexpr size_t WS_V = WS_K + 65536;
constexpr size_t WS_R = WS_V + 65536;
constexpr size_t WS_O = 5u << 20;
constexpr size_t WS_YP = 7u << 20;
constexpr size_t WS_SEL = 8u << 20;

constexpr int NFFN = 172;

DI float bf2f(bf16 v) { return (float)v; }
DI float rbf(float v) { return (float)(bf16)v; }
DI f32x2 unpack2(unsigned u) { return f32x2{__uint_as_float(u << 16), __uint_as_float(u & 0xffff0000u)}; }
DI unsigned pack2(f32x2 v) {
  bf16x2 b = {(bf16)v.x, (bf16)v.y};
  return __builtin_bit_cast(unsigned, b);
}

DI unsigned route_key(float v, int tok) {
  const unsigned u = __float_as_uint(v) >> 16;
  const unsigned o = (u & 0x8000u) ? (~u & 0xffffu) : (u | 0x8000u);
  return (o << 16) | (unsigned)(0xffff - tok);
}
DI int key_tok(unsigned k) { return 0xffff - (int)(k & 0xffffu); }
DI float key_val(unsigned k) {
  const unsigned o = k >> 16;
  const unsigned u = (o & 0x8000u) ? (o & 0x7fffu) : (~o & 0xffffu);
  return __uint_as_float(u << 16);
}
DI void top2_wave(unsigned& k1, unsigned& k2, int width) {
  for (int o = width >> 1; o > 0; o >>= 1) {
    const unsigned a1 = __shfl_xor(k1, o), a2 = __shfl_xor(k2, o);
    k2 = max(min(k1, a1), max(k2, a2));
    k1 = max(k1, a1);
  }
}

DI void glds16(const void* g, void* l) {
  __builtin_amdgcn_global_load_lds((const GLB_AS void*)g, (LDS_AS void*)l, 16, 0, 0);
}

template <int CTRL>
DI float dpp_add(float v) {
  const int t = __builtin_amdgcn_update_dpp(0, __float_as_int(v), CTRL, 0xf, 0xf, true);
  return v + __int_as_float(t);
}
DI float sum8(float v) {
  v = dpp_add<0xB1>(v);
  v = dpp_add<0x4E>(v);
  v = dpp_add<0x141>(v);
  return v;
}

DI s16x4 tr_read(const char* p) {
  return __builtin_amdgcn_ds_read_tr16_b64_v4i16((LDS_AS s16x4*)p);
}

DI int wimg_off(int row, int ch) { return row * 128 + ((ch ^ (((row >> 1) & 1) << 2)) << 4); }

constexpr int XS_STRIDE = 1040;

DI void stage_w(const bf16* w, int col0, char* wimg, int wave, int nwaves, int lane) {
  for (int p = wave; p < 64; p += nwaves) {
    const int row = p * 8 + (lane >> 3), pc = lane & 7;
    const int ch = pc ^ (((row >> 1) & 1) << 2);
    glds16(w + row * 512 + col0 + ch * 8, wimg + p * 1024);
  }
}

DI f32x16 gemm_tile(const char* wimg, const char* ximg, int n0, int t0, int lane) {
  const int r = lane & 31, h = lane >> 5;
  const int q = (lane & 15) >> 2, p = lane & 3, blk = (lane >> 4) & 1;
  f32x16 acc;
#pragma unroll
  for (int i = 0; i < 16; ++i) acc[i] = 0.f;
  const char* xb = ximg + (t0 + r) * XS_STRIDE + 16 * h;
  const int col = n0 + 16 * blk + 4 * p;
  const int ch = col >> 3, sub = (col & 7) * 2;
  const char* wb0 = wimg + wimg_off(8 * h + q, ch) + sub;
  const char* wb1 = wimg + wimg_off(8 * h + q + 4, ch) + sub;
#pragma unroll
  for (int ks = 0; ks < 32; ++ks) {
    s16x4 lo = tr_read(wb0 + ks * 2048);
    s16x4 hi = tr_read(wb1 + ks * 2048);
    s16x8 a = __builtin_shufflevector(lo, hi, 0, 1, 2, 3, 4, 5, 6, 7);
    bf16x8 b = *(const bf16x8*)(xb + ks * 32);
    acc = __builtin_amdgcn_mfma_f32_32x32x16_bf16(__builtin_bit_cast(bf16x8, a), b, acc, 0, 0, 0);
  }
  return acc;
}
DI f32x16 gemm_tile16(const char* wimg, const char* ximg, int t0, int lane) {
  const int r = lane & 31, h = lane >> 5;
  const int q = (lane & 15) >> 2, p = lane & 3;
  f32x16 acc;
#pragma unroll
  for (int i = 0; i < 16; ++i) acc[i] = 0.f;
  const char* xb = ximg + (t0 + r) * XS_STRIDE + 16 * h;
  const char* wb = wimg + (8 * h + q) * 32 + 8 * p;
#pragma unroll
  for (int ks = 0; ks < 32; ++ks) {
    s16x4 lo = tr_read(wb + ks * 512);
    s16x4 hi = tr_read(wb + ks * 512 + 128);
    s16x8 a = __builtin_shufflevector(lo, hi, 0, 1, 2, 3, 4, 5, 6, 7);
    bf16x8 b = *(const bf16x8*)(xb + ks * 32);
    acc = __builtin_amdgcn_mfma_f32_32x32x16_bf16(__builtin_bit_cast(bf16x8, a), b, acc, 0, 0, 0);
  }
  return acc;
}

constexpr int K1_LDS = 64 * XS_STRIDE + 65536 + 16384 + 256;

__global__ __launch_bounds__(512) void k1_ln_qkv(const bf16* __restrict__ x, const bf16* __restrict__ wq,
                                                 const bf16* __restrict__ wk, const bf16* __restrict__ wv,
                                                 const bf16* __restrict__ wr, bf16* __restrict__ xn,
                                                 bf16* __restrict__ Q, bf16* __restrict__ Kb,
                                                 bf16* __restrict__ Vf, unsigned* __restrict__ cand) {
  extern __shared__ __attribute__((aligned(16))) char smem[];
  char* ximg = smem;
  char* wimg = smem + 64 * XS_STRIDE;
  char* kvimg = smem + 64 * XS_STRIDE + 65536;
  float* rs = (float*)(smem + 64 * XS_STRIDE + 65536 + 16384);
  const int cb = blockIdx.x >> 5, tb = blockIdx.x & 31;
  const int tid = threadIdx.x, lane = tid & 63, wave = tid >> 6;
  const int tok0 = tb * 64;

  const int tl = wave * 8 + (lane >> 3), c8 = lane & 7;
  u32x4 xv[8];
#pragma unroll
  for (int i = 0; i < 8; ++i) xv[i] = *(const u32x4*)(x + (tok0 + tl) * 512 + (c8 + 8 * i) * 8);
  u32x4 wrv[8];
  if (cb == 3) {
#pragma unroll
    for (int i = 0; i < 8; ++i) wrv[i] = *(const u32x4*)(wr + (c8 + 8 * i) * 8);
  }

  stage_w(wq, cb * 64, wimg, wave, 8, lane);
  if (cb < 2) {
    const bf16* w = cb == 0 ? wk : wv;
#pragma unroll
    for (int i = 0; i < 2; ++i) {
      const int p = wave + 8 * i;
      glds16((const char*)w + p * 1024 + lane * 16, kvimg + p * 1024);
    }
  }

  f32x2 xf[32];
#pragma unroll
  for (int i = 0; i < 32; ++i) xf[i] = unpack2(xv[i >> 2][i & 3]);
  f32x2 s2 = {0.f, 0.f};
#pragma unroll
  for (int i = 0; i < 32; ++i) s2 += xf[i];
  const float mean = sum8(s2.x + s2.y) * (1.f / 512.f);
  const f32x2 mean2 = {mean, mean};
  f32x2 v2 = {0.f, 0.f};
#pragma unroll
  for (int i = 0; i < 32; ++i) { const f32x2 d = xf[i] - mean2; v2 += d * d; }
  const float vs = sum8(v2.x + v2.y);
  const float mb = rbf(mean);
  const float vb = rbf(vs * (1.f / 512.f));
  const float den = rbf(sqrtf(rbf(vb + 1e-5f)));
  const float rinv = __builtin_amdgcn_rcpf(den);
  const f32x2 mb2 = {mb, mb}, rinv2 = {rinv, rinv};
  u32x4 o[8];
#pragma unroll
  for (int i = 0; i < 8; ++i) {
#pragma unroll
    for (int j = 0; j < 4; ++j) o[i][j] = pack2(unpack2(pack2(xf[4 * i + j] - mb2)) * rinv2);
    *(u32x4*)(ximg + tl * XS_STRIDE + (c8 + 8 * i) * 16) = o[i];
  }
  if (cb == 2) {
#pragma unroll
    for (int i = 0; i < 8; ++i) *(u32x4*)(xn + (tok0 + tl) * 512 + (c8 + 8 * i) * 8) = o[i];
  }
  if (cb == 3) {
    float rd = 0.f;
#pragma unroll
    for (int i = 0; i < 8; ++i) {
#pragma unroll
      for (int j = 0; j < 4; ++j) {
        const unsigned a = o[i][j], b = wrv[i][j];
        rd = __builtin_amdgcn_fdot2_f32_bf16(__builtin_bit_cast(bf16x2, a), __builtin_bit_cast(bf16x2, b), rd, false);
      }
    }
    rd = sum8(rd);
    if (c8 == 0) rs[tl] = rbf(rd);
  }
  __syncthreads();

  if (cb == 3 && wave == 7) {
    unsigned k1 = route_key(rs[lane], tok0 + lane), k2 = 0;
    top2_wave(k1, k2, 64);
    if (lane == 0) *(u32x2*)(cand + tb * 2) = u32x2{k1, k2};
  }
  const int r = lane & 31, h = lane >> 5;
  if (wave < 4) {
    const int n0 = (wave & 1) * 32, t0 = (wave >> 1) * 32;
    f32x16 acc = gemm_tile(wimg, ximg, n0, t0, lane);
    const float sc = 0.25f * 1.4426950408889634f;
    bf16* qrow = Q + (tok0 + t0 + r) * 512 + cb * 64 + n0;
#pragma unroll
    for (int g = 0; g < 4; ++g) {
      bf16x4 o;
#pragma unroll
      for (int j = 0; j < 4; ++j) o[j] = (bf16)(rbf(acc[4 * g + j]) * sc);
      *(bf16x4*)(qrow + 8 * g + 4 * h) = o;
    }
  } else if (wave < 6 && cb < 2) {
    const int t0 = (wave - 4) * 32;
    f32x16 acc = gemm_tile16(kvimg, ximg, t0, lane);
    const int key = tok0 + t0 + r;
    const int t = key >> 5;
    if (cb == 0) {
#pragma unroll
      for (int g = 0; g < 2; ++g) {
        bf16x4 o;
#pragma unroll
        for (int j = 0; j < 4; ++j) o[j] = (bf16)acc[4 * g + j];
        *(bf16x4*)(Kb + ((t * 2 + g) * 32 + r) * 8 + 4 * h) = o;
      }
    } else {
      const int kk = key & 31, sg = kk >> 4, rem = kk & 15;
      const int hh = (rem >> 2) & 1, jj = ((rem >> 3) << 2) | (rem & 3);
#pragma unroll
      for (int g = 0; g < 2; ++g) {
#pragma unroll
        for (int j = 0; j < 4; ++j) {
          const int d = 8 * g + 4 * h + j;
          Vf[(((t * 2 + sg) * 2 + hh) * 16 + d) * 8 + jj] = (bf16)acc[4 * g + j];
        }
      }
    }
  }
}

constexpr int ATT_LDS = 65536 * 2 + 8192 + 64 + 512;
constexpr int NT = 8;
constexpr int NC = 64 / NT;

DI void ffn_slice(int f, int j, const bf16* __restrict__ xn, const unsigned* __restrict__ cand,
                  const bf16* __restrict__ wg, const bf16* __restrict__ wu, const bf16* __restrict__ wd,
                  float* __restrict__ ypart, int* __restrict__ sel, float* red, int* cnt, int lane) {
  const int k0 = j * 128 + lane * 2;
  u32x4 g8[2], u8[2];
  unsigned d2[8];
#pragma unroll
  for (int i = 0; i < 2; ++i) {
    g8[i] = *(const u32x4*)(wg + (k0 + i) * F_ + f * 8);
    u8[i] = *(const u32x4*)(wu + (k0 + i) * F_ + f * 8);
  }
#pragma unroll
  for (int c = 0; c < 8; ++c) d2[c] = *(const unsigned*)(wd + (f * 8 + c) * 512 + k0);
  const u32x2 cv = *(const u32x2*)(cand + (lane & 31) * 2);
  unsigned k1 = cv[0], k2 = cv[1];
  top2_wave(k1, k2, 32);
  const int i1 = key_tok(k1), i2 = key_tok(k2);
  if (f == 0 && j == 0 && lane == 0) {
    const float g0 = rbf(1.f / (1.f + __expf(-key_val(k1)))), g1 = rbf(1.f / (1.f + __expf(-key_val(k2))));
    *(u32x4*)sel = u32x4{(unsigned)i1, (unsigned)i2, __float_as_uint(g0), __float_as_uint(g1)};
  }
  const f32x2 xa = unpack2(*(const unsigned*)(xn + i1 * 512 + k0));
  const f32x2 xb = unpack2(*(const unsigned*)(xn + i2 * 512 + k0));
  f32x2 v2[16];
#pragma unroll
  for (int i = 0; i < 16; ++i) v2[i] = f32x2{0.f, 0.f};
#pragma unroll
  for (int i = 0; i < 2; ++i) {
    const float x0 = i ? xa.y : xa.x, x1 = i ? xb.y : xb.x;
    const f32x2 x02 = {x0, x0}, x12 = {x1, x1};
#pragma unroll
    for (int q = 0; q < 4; ++q) {
      const unsigned gq = g8[i][q], uq = u8[i][q];
      const f32x2 gw = unpack2(gq), uw = unpack2(uq);
      v2[q] += x02 * gw; v2[4 + q] += x12 * gw; v2[8 + q] += x02 * uw; v2[12 + q] += x12 * uw;
    }
  }
  float v[32];
#pragma unroll
  for (int i = 0; i < 16; ++i) { v[2 * i] = v2[i].x; v[2 * i + 1] = v2[i].y; }
#pragma unroll
  for (int st = 0; st < 5; ++st) {
    const int o = 32 >> st, n = 16 >> st;
    const bool up = (lane & o) != 0;
#pragma unroll
    for (int i = 0; i < n; ++i) {
      const float keep = up ? v[i + n] : v[i];
      const float send = up ? v[i] : v[i + n];
      v[i] = keep + __shfl_xor(send, o);
    }
  }
  v[0] += __shfl_xor(v[0], 1);
  if ((lane & 1) == 0) red[j * 32 + (lane >> 1)] = v[0];
  if (lane == 0) __hip_atomic_fetch_add(cnt, 1, __ATOMIC_RELEASE, __HIP_MEMORY_SCOPE_WORKGROUP);
  while (__hip_atomic_load(cnt, __ATOMIC_ACQUIRE, __HIP_MEMORY_SCOPE_WORKGROUP) < 4) __builtin_amdgcn_s_sleep(1);
  const int l = lane & 15;
  const float g = rbf((red[l] + red[32 + l]) + (red[64 + l] + red[96 + l]));
  const float u = rbf((red[16 + l] + red[48 + l]) + (red[80 + l] + red[112 + l]));
  const float hv = rbf(rbf(g / (1.f + __expf(-g))) * u);
  f32x2 y0 = {0.f, 0.f}, y1 = {0.f, 0.f};
#pragma unroll
  for (int c = 0; c < 8; ++c) {
    const float h0 = __shfl(hv, c), h1 = __shfl(hv, 8 + c);
    const f32x2 w = unpack2(d2[c]);
    y0 += f32x2{h0, h0} * w;
    y1 += f32x2{h1, h1} * w;
  }
  *(f32x2*)(ypart + (size_t)f * 1024 + k0) = y0;
  *(f32x2*)(ypart + (size_t)f * 1024 + 512 + k0) = y1;
}

__global__ __launch_bounds__(768) void k2_attn(const bf16* __restrict__ Q, const bf16* __restrict__ Kb,
                                               bf16* __restrict__ O, const bf16* __restrict__ xn,
                                               const unsigned* __restrict__ cand, const bf16* __restrict__ wg,
                                               const bf16* __restrict__ wu, const bf16* __restrict__ wd,
                                               float* __restrict__ ypart, int* __restrict__ sel) {
  extern __shared__ __attribute__((aligned(16))) char smem[];
  char* kl = smem;
  char* vl = smem + 65536;
  char* cst = smem + 131072;
  const int tid = threadIdx.x, lane = tid & 63;
  const int wave = __builtin_amdgcn_readfirstlane(tid >> 6);

  if (wave >= 8) {
    int* cnt = (int*)(smem + 131072 + 8192 + 32);
    if (tid == 512) *cnt = 0;
    __syncthreads();
    if (blockIdx.x < NFFN)
      ffn_slice(blockIdx.x, wave - 8, xn, cand, wg, wu, wd, ypart, sel, (float*)(smem + 131072 + 8192 + 64), cnt, lane);
    return;
  }

  const int r = lane & 31, h = lane >> 5;
  const int token = ((blockIdx.x >> 6) * 8 + (blockIdx.x & 7)) * 64 + ((blockIdx.x >> 3) & 7) * 8 + wave;
#pragma unroll
  for (int i = 0; i < 16; ++i) {
    const int piece = wave + i * 8;
    glds16((const char*)Kb + piece * 1024 + lane * 16, smem + piece * 1024);
  }
  {
    const unsigned cv = ((tid & 15) == 0 || (tid & 15) == 4) ? 0x3f803f80u : 0u;
    *(u32x4*)(cst + tid * 16) = u32x4{cv, cv, cv, cv};
  }
  volatile int* prog = (volatile int*)(smem + 131072 + 8192);
  if (tid < 8) prog[tid] = 0;
  const bf16x8 qf = *(const bf16x8*)(Q + token * 512 + r * 16 + 8 * h);
  __syncthreads();

  const char* kp0 = kl + lane * 16;
  const char* vp0 = (r < 16) ? (vl + (h * 16 + r) * 16) : (cst + (h * 16 + r - 16) * 16);
  const int vstep = (r < 16) ? NT * 1024 : 0;

  f32x16 zero;
#pragma unroll
  for (int i = 0; i < 16; ++i) zero[i] = 0.f;
  float m;
  {
    float mx = -INFINITY;
#pragma unroll
    for (int t = 0; t < 4; ++t) {
      bf16x8 kf = *(const bf16x8*)(kp0 + t * 1024);
      f32x16 x = __builtin_amdgcn_mfma_f32_32x32x16_bf16(kf, qf, zero, 0, 0, 0);
#pragma unroll
      for (int i = 0; i < 16; i += 2) mx = fmaxf(fmaxf(mx, x[i]), x[i + 1]);
    }
    mx = fmaxf(mx, __shfl_xor(mx, 32));
    m = floorf(mx);
  }
  f32x16 negm;
#pragma unroll
  for (int i = 0; i < 16; ++i) negm[i] = -m;
  float tot[9];
#pragma unroll
  for (int i = 0; i < 9; ++i) tot[i] = 0.f;
  {
    const char* kp = kp0;
    const char* vp = vp0;
    int c = 0;
    bool redone = false;
    f32x16 x = __builtin_amdgcn_mfma_f32_32x32x16_bf16(*(const bf16x8*)kp, qf, negm, 0, 0, 0);
    bf16x8 kfn = *(const bf16x8*)(kp + 1024);
    while (c < NC) {
      f32x16 acc = zero;
#pragma unroll
      for (int t = 0; t < NT; ++t) {
        const bf16x8 kf = kfn;
        kfn = *(const bf16x8*)(kp + (t + 2) * 1024);
        const bf16x8 vf0 = *(const bf16x8*)(vp + (t * 2) * 512);
        const bf16x8 vf1 = *(const bf16x8*)(vp + (t * 2 + 1) * 512);
        bf16x8 pf;
#pragma unroll
        for (int j = 0; j < 4; ++j) pf[j] = (bf16)__builtin_amdgcn_exp2f(x[j]);
        __builtin_amdgcn_sched_barrier(0);
        f32x16 xn = __builtin_amdgcn_mfma_f32_32x32x16_bf16(kf, qf, negm, 0, 0, 0);
        asm volatile("" : "+v"(xn));
        __builtin_amdgcn_sched_barrier(0);
#pragma unroll
        for (int j = 4; j < 8; ++j) pf[j] = (bf16)__builtin_amdgcn_exp2f(x[j]);
        acc = __builtin_amdgcn_mfma_f32_32x32x16_bf16(vf0, pf, acc, 0, 0, 0);
#pragma unroll
        for (int j = 0; j < 8; ++j) pf[j] = (bf16)__builtin_amdgcn_exp2f(x[8 + j]);
        acc = __builtin_amdgcn_mfma_f32_32x32x16_bf16(vf1, pf, acc, 0, 0, 0);
        __builtin_amdgcn_sched_barrier(0);
        x = xn;
      }
      if (!redone && __any(!(acc[8] < 7.9e28f))) {
        float mx = -INFINITY;
        for (int t = 0; t < NT; ++t) {
          bf16x8 kf = *(const bf16x8*)(kp + t * 1024);
          f32x16 x = __builtin_amdgcn_mfma_f32_32x32x16_bf16(kf, qf, zero, 0, 0, 0);
#pragma unroll
          for (int i = 0; i < 16; i += 2) mx = fmaxf(fmaxf(mx, x[i]), x[i + 1]);
        }
        mx = fmaxf(mx, __shfl_xor(mx, 32));
        const float mn = fmaxf(m, floorf(mx));
        const int d = (int)fminf(mn - m, 1024.f);
#pragma unroll
        for (int i = 0; i < 9; ++i) tot[i] = __builtin_amdgcn_ldexpf(tot[i], -d);
        m = mn;
#pragma unroll
        for (int i = 0; i < 16; ++i) negm[i] = -m;
        x = __builtin_amdgcn_mfma_f32_32x32x16_bf16(*(const bf16x8*)kp, qf, negm, 0, 0, 0);
        kfn = *(const bf16x8*)(kp + 1024);
        redone = true;
        continue;
      }
      redone = false;
#pragma unroll
      for (int i = 0; i < 9; ++i) tot[i] += acc[i];
      if (__any(tot[8] > 4.3e9f)) {
        const int e = max(__builtin_amdgcn_frexp_expf(tot[8]) - 1, 0);
#pragma unroll
        for (int i = 0; i < 9; ++i) tot[i] = __builtin_amdgcn_ldexpf(tot[i], -e);
        m += (float)e;
#pragma unroll
        for (int i = 0; i < 16; ++i) negm[i] = -m;
        x = __builtin_amdgcn_mfma_f32_32x32x16_bf16(*(const bf16x8*)(kp + NT * 1024), qf, negm, 0, 0, 0);
      }
      ++c;
      kp += NT * 1024;
      vp += vstep;
      if (lane == 0) prog[wave] = c;
      if (c <= __builtin_amdgcn_readfirstlane(prog[wave ^ 4])) __builtin_amdgcn_s_setprio(1);
      else __builtin_amdgcn_s_setprio(0);
    }
  }
  {
    float chk = 0.f;
#pragma unroll
    for (int i = 0; i < 9; ++i) chk = __builtin_fmaf(tot[i], 0.f, chk);
    if (__any(chk != 0.f)) {
      f32x16 oacc = zero;
      float mrun = -INFINITY;
      const char* kp = kp0;
      const char* vp = vp0;
      for (int t = 0; t < 64; ++t) {
        bf16x8 kf = *(const bf16x8*)(kp);
        f32x16 x = __builtin_amdgcn_mfma_f32_32x32x16_bf16(kf, qf, zero, 0, 0, 0);
        float mx = x[0];
#pragma unroll
        for (int i = 1; i < 16; ++i) mx = fmaxf(mx, x[i]);
        mx = fmaxf(mx, __shfl_xor(mx, 32));
        const float mn = fmaxf(mrun, mx);
        const float al = __builtin_amdgcn_exp2f(mrun - mn);
        mrun = mn;
#pragma unroll
        for (int i = 0; i < 16; ++i) oacc[i] *= al;
#pragma unroll
        for (int s = 0; s < 2; ++s) {
          bf16x8 pf;
#pragma unroll
          for (int j = 0; j < 8; ++j) pf[j] = (bf16)__builtin_amdgcn_exp2f(x[8 * s + j] - mn);
          bf16x8 vf = *(const bf16x8*)(vp + ((t & (NT - 1)) * 2 + s) * 512);
          oacc = __builtin_amdgcn_mfma_f32_32x32x16_bf16(vf, pf, oacc, 0, 0, 0);
        }
        kp += 1024;
        if ((t & (NT - 1)) == NT - 1) vp += vstep;
      }
#pragma unroll
      for (int i = 0; i < 9; ++i) tot[i] = oacc[i];
    }
  }
  const float inv = 1.f / tot[8];
  bf16* orow = O + token * 512 + r * 16;
#pragma unroll
  for (int g = 0; g < 2; ++g) {
    bf16x4 o;
#pragma unroll
    for (int j = 0; j < 4; ++j) o[j] = (bf16)(tot[4 * g + j] * inv);
    *(bf16x4*)(orow + 8 * g + 4 * h) = o;
  }
}

constexpr int K3_LDS = 64 * XS_STRIDE + 65536 + 4096;

__global__ __launch_bounds__(512) void k3_out(const bf16* __restrict__ O, const bf16* __restrict__ wo,
                                              const bf16* __restrict__ x, const bf16* __restrict__ xn,
                                              const float* __restrict__ ypart, const int* __restrict__ sel,
                                              bf16* __restrict__ out) {
  extern __shared__ __attribute__((aligned(16))) char smem[];
  char* ximg = smem;
  char* wimg = smem + 64 * XS_STRIDE;
  float* red = (float*)(smem + 64 * XS_STRIDE + 65536);
  const int cb = blockIdx.x >> 5, tb = blockIdx.x & 31;
  const int tid = threadIdx.x, lane = tid & 63, wave = tid >> 6;
  const int tok0 = tb * 64;
  const u32x4 sv = *(const u32x4*)sel;
#pragma unroll
  for (int i = 0; i < 8; ++i) {
    const int row = wave + 8 * i;
    glds16(O + (tok0 + row) * 512 + lane * 8, ximg + row * XS_STRIDE);
  }
  stage_w(wo, cb * 64, wimg, wave, 8, lane);
  const int r = lane & 31, h = lane >> 5;
  const int n0 = (wave & 1) * 32, t0 = ((wave >> 1) & 1) * 32;
  const int tok = tok0 + t0 + r;
  bf16x4 xv[4], nv[4];
  if (wave < 4) {
#pragma unroll
    for (int g = 0; g < 4; ++g) {
      const int n = cb * 64 + n0 + 8 * g + 4 * h;
      xv[g] = *(const bf16x4*)(x + tok * 512 + n);
      nv[g] = *(const bf16x4*)(xn + tok * 512 + n);
    }
  }
  const int s0 = (int)sv[0], s1 = (int)sv[1];
#pragma unroll
  for (int w = 0; w < 2; ++w) {
    if (((w ? s1 : s0) >> 6) == tb) {
      const int pg = tid >> 4, c4 = tid & 15;
      f32x4 a = {0.f, 0.f, 0.f, 0.f};
#pragma unroll
      for (int i = 0; i < 6; ++i) {
        const int pp = pg + 32 * i;
        if (pp < NFFN) a += *(const f32x4*)(ypart + (size_t)pp * 1024 + w * 512 + cb * 64 + c4 * 4);
      }
#pragma unroll
      for (int o = 16; o < 64; o <<= 1) {
#pragma unroll
        for (int j = 0; j < 4; ++j) a[j] += __shfl_xor(a[j], o);
      }
      if (lane < 16) *(f32x4*)(red + wave * 128 + w * 64 + c4 * 4) = a;
    }
  }
  __syncthreads();
  if (wave >= 4) return;
  f32x16 acc = gemm_tile(wimg, ximg, n0, t0, lane);
  float upd[16];
#pragma unroll
  for (int i = 0; i < 16; ++i) upd[i] = 0.f;
  const int which = (tok == s0) ? 0 : ((tok == s1) ? 1 : -1);
  if (which >= 0) {
    const float gate = __uint_as_float(which == 0 ? sv[2] : sv[3]);
#pragma unroll
    for (int w = 0; w < 8; ++w) {
#pragma unroll
      for (int g = 0; g < 4; ++g) {
        const f32x4 p = *(const f32x4*)(red + w * 128 + which * 64 + n0 + 8 * g + 4 * h);
#pragma unroll
        for (int j = 0; j < 4; ++j) upd[4 * g + j] += p[j];
      }
    }
#pragma unroll
    for (int i = 0; i < 16; ++i) upd[i] = rbf(rbf(upd[i]) * gate);
  }
#pragma unroll
  for (int g = 0; g < 4; ++g) {
    const int n = cb * 64 + n0 + 8 * g + 4 * h;
    bf16x4 o;
#pragma unroll
    for (int j = 0; j < 4; ++j) {
      const float a = rbf(acc[4 * g + j]);
      const float xa = rbf(bf2f(xv[g][j]) + a);
      const float bb = rbf(bf2f(nv[g][j]) + upd[4 * g + j]);
      o[j] = (bf16)(xa + bb);
    }
    *(bf16x4*)(out + tok * 512 + n) = o;
  }
}

extern "C" void kernel_launch(void* const* d_in, const int* in_sizes, int n_in, void* d_out, int out_size,
                              void* d_ws, size_t ws_size, hipStream_t stream) {
  const bf16* x = (const bf16*)d_in[0];
  const bf16* wq = (const bf16*)d_in[1];
  const bf16* wk = (const bf16*)d_in[2];
  const bf16* wv = (const bf16*)d_in[3];
  const bf16* wo = (const bf16*)d_in[4];
  const bf16* wr = (const bf16*)d_in[6];
  const bf16* wg = (const bf16*)d_in[7];
  const bf16* wu = (const bf16*)d_in[8];
  const bf16* wd = (const bf16*)d_in[9];
  char* ws = (char*)d_ws;
  bf16* xn = (bf16*)(ws + WS_XN);
  bf16* Q = (bf16*)(ws + WS_Q);
  bf16* Kb = (bf16*)(ws + WS_K);
  bf16* Vf = (bf16*)(ws + WS_V);
  unsigned* cand = (unsigned*)(ws + WS_R);
  bf16* O = (bf16*)(ws + WS_O);
  float* yp = (float*)(ws + WS_YP);
  int* sel = (int*)(ws + WS_SEL);
  bf16* out = (bf16*)d_out;

  (void)hipFuncSetAttribute((const void*)k1_ln_qkv, hipFuncAttributeMaxDynamicSharedMemorySize, K1_LDS);
  (void)hipFuncSetAttribute((const void*)k2_attn, hipFuncAttributeMaxDynamicSharedMemorySize, ATT_LDS);
  (void)hipFuncSetAttribute((const void*)k3_out, hipFuncAttributeMaxDynamicSharedMemorySize, K3_LDS);

  k1_ln_qkv<<<256, 512, K1_LDS, stream>>>(x, wq, wk, wv, wr, xn, Q, Kb, Vf, cand);
  k2_attn<<<256, 768, ATT_LDS, stream>>>(Q, Kb, O, xn, cand, wg, wu, wd, yp, sel);
  k3_out<<<256, 512, K3_LDS, stream>>>(O, wo, x, xn, yp, sel, out);
}
```

```cpp
#include <hip/hip_runtime.h>
#include <hip/hip_bf16.h>

typedef __attribute__((ext_vector_type(8))) __bf16 bf16x8;
typedef __attribute__((ext_vector_type(4))) __bf16 bf16x4;
typedef __attribute__((ext_vector_type(2))) __bf16 bf16x2;
typedef __attribute__((ext_vector_type(4))) short s16x4;
typedef __attribute__((ext_vector_type(8))) short s16x8;
typedef __attribute__((ext_vector_type(16))) float f32x16;
typedef __attribute__((ext_vector_type(4))) float f32x4;
typedef __attribute__((ext_vector_type(4))) unsigned u32x4;
typedef __attribute__((ext_vector_type(2))) unsigned u32x2;
typedef __attribute__((ext_vector_type(2))) float f32x2;
typedef __bf16 bf16;

#define LDS_AS __attribute__((address_space(3)))
#define GLB_AS __attribute__((address_space(1)))
#define DI __device__ __forceinline__

constexpr int S_ = 2048, D_ = 512, H_ = 32, DH_ = 16, F_ = 1376;

constexpr size_t WS_ST = 0;
constexpr size_t WS_Q = 2u << 20;
constexpr size_t WS_K = 4u << 20;
constexpr size_t WS_V = WS_K + 65536;
constexpr size_t WS_R = WS_V + 65536;
constexpr size_t WS_O = 5u << 20;
constexpr size_t WS_YP = 7u << 20;
constexpr size_t WS_SEL = 8u << 20;

constexpr int NFFN = 172;

DI float bf2f(bf16 v) { return (float)v; }
DI float rbf(float v) { return (float)(bf16)v; }
DI f32x2 unpack2(unsigned u) { return f32x2{__uint_as_float(u << 16), __uint_as_float(u & 0xffff0000u)}; }
DI unsigned pack2(f32x2 v) {
  bf16x2 b = {(bf16)v.x, (bf16)v.y};
  return __builtin_bit_cast(unsigned, b);
}

DI unsigned route_key(float v, int tok) {
  const unsigned u = __float_as_uint(v) >> 16;
  const unsigned o = (u & 0x8000u) ? (~u & 0xffffu) : (u | 0x8000u);
  return (o << 16) | (unsigned)(0xffff - tok);
}
DI int key_tok(unsigned k) { return 0xffff - (int)(k & 0xffffu); }
DI float key_val(unsigned k) {
  const unsigned o = k >> 16;
  const unsigned u = (o & 0x8000u) ? (o & 0x7fffu) : (~o & 0xffffu);
  return __uint_as_float(u << 16);
}
DI unsigned ln2(f32x2 x, f32x2 mb2, f32x2 rinv2) { return pack2(unpack2(pack2(x - mb2)) * rinv2); }
DI void top2_wave(unsigned& k1, unsigned& k2, int width) {
  for (int o = width >> 1; o > 0; o >>= 1) {
    const unsigned a1 = __shfl_xor(k1, o), a2 = __shfl_xor(k2, o);
    k2 = max(min(k1, a1), max(k2, a2));
    k1 = max(k1, a1);
  }
}

DI void glds16(const void* g, void* l) {
  __builtin_amdgcn_global_load_lds((const GLB_AS void*)g, (LDS_AS void*)l, 16, 0, 0);
}

DI void glds16_raw(const void* g, unsigned lds_off) {
  asm volatile("s_mov_b32 m0, %0\n s_nop 0\n global_load_lds_dwordx4 %1, off" ::"s"(lds_off), "v"(g) : "memory", "m0");
}

template <int CTRL>
DI float dpp_add(float v) {
  const int t = __builtin_amdgcn_update_dpp(0, __float_as_int(v), CTRL, 0xf, 0xf, true);
  return v + __int_as_float(t);
}
DI float sum8(float v) {
  v = dpp_add<0xB1>(v);
  v = dpp_add<0x4E>(v);
  v = dpp_add<0x141>(v);
  return v;
}

DI s16x4 tr_read(const char* p) {
  return __builtin_amdgcn_ds_read_tr16_b64_v4i16((LDS_AS s16x4*)p);
}

DI int wimg_off(int row, int ch) { return row * 128 + ((ch ^ (((row >> 1) & 1) << 2)) << 4); }

constexpr int XS_STRIDE = 1040;

DI void stage_w(const bf16* w, int col0, char* wimg, int wave, int nwaves, int lane) {
  for (int p = wave; p < 64; p += nwaves) {
    const int row = p * 8 + (lane >> 3), pc = lane & 7;
    const int ch = pc ^ (((row >> 1) & 1) << 2);
    glds16(w + row * 512 + col0 + ch * 8, wimg + p * 1024);
  }
}

DI f32x16 gemm_tile(const char* wimg, const char* ximg, int n0, int t0, int lane) {
  const int r = lane & 31, h = lane >> 5;
  const int q = (lane & 15) >> 2, p = lane & 3, blk = (lane >> 4) & 1;
  f32x16 acc;
#pragma unroll
  for (int i = 0; i < 16; ++i) acc[i] = 0.f;
  const char* xb = ximg + (t0 + r) * XS_STRIDE + 16 * h;
  const int col = n0 + 16 * blk + 4 * p;
  const int ch = col >> 3, sub = (col & 7) * 2;
  const char* wb0 = wimg + wimg_off(8 * h + q, ch) + sub;
  const char* wb1 = wimg + wimg_off(8 * h + q + 4, ch) + sub;
#pragma unroll
  for (int ks = 0; ks < 32; ++ks) {
    s16x4 lo = tr_read(wb0 + ks * 2048);
    s16x4 hi = tr_read(wb1 + ks * 2048);
    s16x8 a = __builtin_shufflevector(lo, hi, 0, 1, 2, 3, 4, 5, 6, 7);
    bf16x8 b = *(const bf16x8*)(xb + ks * 32);
    acc = __builtin_amdgcn_mfma_f32_32x32x16_bf16(__builtin_bit_cast(bf16x8, a), b, acc, 0, 0, 0);
  }
  return acc;
}
DI f32x16 gemm_tile16(const char* wimg, const char* ximg, int t0, int lane) {
  const int r = lane & 31, h = lane >> 5;
  const int q = (lane & 15) >> 2, p = lane & 3;
  f32x16 acc;
#pragma unroll
  for (int i = 0; i < 16; ++i) acc[i] = 0.f;
  const char* xb = ximg + (t0 + r) * XS_STRIDE + 16 * h;
  const char* wb = wimg + (8 * h + q) * 32 + 8 * p;
#pragma unroll
  for (int ks = 0; ks < 32; ++ks) {
    s16x4 lo = tr_read(wb + ks * 512);
    s16x4 hi = tr_read(wb + ks * 512 + 128);
    s16x8 a = __builtin_shufflevector(lo, hi, 0, 1, 2, 3, 4, 5, 6, 7);
    bf16x8 b = *(const bf16x8*)(xb + ks * 32);
    acc = __builtin_amdgcn_mfma_f32_32x32x16_bf16(__builtin_bit_cast(bf16x8, a), b, acc, 0, 0, 0);
  }
  return acc;
}

constexpr int K1_LDS = 64 * XS_STRIDE + 65536 + 16384 + 256;

__global__ __launch_bounds__(768) void k1_ln_qkv(const bf16* __restrict__ x, const bf16* __restrict__ wq,
                                                 const bf16* __restrict__ wk, const bf16* __restrict__ wv,
                                                 const bf16* __restrict__ wr, float* __restrict__ st,
                                                 bf16* __restrict__ Q, bf16* __restrict__ Kb,
                                                 bf16* __restrict__ Vf, unsigned* __restrict__ cand) {
  extern __shared__ __attribute__((aligned(16))) char smem[];
  char* ximg = smem;
  char* wimg = smem + 64 * XS_STRIDE;
  char* kvimg = smem + 64 * XS_STRIDE + 65536;
  float* rs = (float*)(smem + 64 * XS_STRIDE + 65536 + 16384);
  const int cb = blockIdx.x >> 5, tb = blockIdx.x & 31;
  const int tid = threadIdx.x, lane = tid & 63;
  const int wave = __builtin_amdgcn_readfirstlane(tid >> 6);
  const int tok0 = tb * 64;

  if (wave >= 8) {
    stage_w(wq, cb * 64, wimg, wave - 8, 4, lane);
    if (cb < 2) {
      const bf16* w = cb == 0 ? wk : wv;
#pragma unroll
      for (int i = 0; i < 4; ++i) {
        const int p = wave - 8 + 4 * i;
        glds16((const char*)w + p * 1024 + lane * 16, kvimg + p * 1024);
      }
    }
    __syncthreads();
    if (cb == 3 && wave == 8) {
      unsigned k1 = route_key(rs[lane], tok0 + lane), k2 = 0;
      top2_wave(k1, k2, 64);
      if (lane == 0) *(u32x2*)(cand + tb * 2) = u32x2{k1, k2};
    }
    return;
  }

  const int tl = wave * 8 + (lane >> 3), c8 = lane & 7;
  u32x4 xv[8];
#pragma unroll
  for (int i = 0; i < 8; ++i) xv[i] = *(const u32x4*)(x + (tok0 + tl) * 512 + (c8 + 8 * i) * 8);
  u32x4 wrv[8];
  if (cb == 3) {
#pragma unroll
    for (int i = 0; i < 8; ++i) wrv[i] = *(const u32x4*)(wr + (c8 + 8 * i) * 8);
  }

  f32x2 xf[32];
#pragma unroll
  for (int i = 0; i < 32; ++i) xf[i] = unpack2(xv[i >> 2][i & 3]);
  f32x2 s2 = {0.f, 0.f};
#pragma unroll
  for (int i = 0; i < 32; ++i) s2 += xf[i];
  const float mean = sum8(s2.x + s2.y) * (1.f / 512.f);
  const f32x2 mean2 = {mean, mean};
  f32x2 v2 = {0.f, 0.f};
#pragma unroll
  for (int i = 0; i < 32; ++i) { const f32x2 d = xf[i] - mean2; v2 += d * d; }
  const float vs = sum8(v2.x + v2.y);
  const float mb = rbf(mean);
  const float vb = rbf(vs * (1.f / 512.f));
  const float den = rbf(sqrtf(rbf(vb + 1e-5f)));
  const float rinv = __builtin_amdgcn_rcpf(den);
  const f32x2 mb2 = {mb, mb}, rinv2 = {rinv, rinv};
  u32x4 o[8];
#pragma unroll
  for (int i = 0; i < 8; ++i) {
#pragma unroll
    for (int j = 0; j < 4; ++j) o[i][j] = ln2(xf[4 * i + j], mb2, rinv2);
    *(u32x4*)(ximg + tl * XS_STRIDE + (c8 + 8 * i) * 16) = o[i];
  }
  if (cb == 2 && c8 == 0) *(f32x2*)(st + (tok0 + tl) * 2) = f32x2{mb, rinv};
  if (cb == 3) {
    float rd = 0.f;
#pragma unroll
    for (int i = 0; i < 8; ++i) {
#pragma unroll
      for (int j = 0; j < 4; ++j) {
        const unsigned a = o[i][j], b = wrv[i][j];
        rd = __builtin_amdgcn_fdot2_f32_bf16(__builtin_bit_cast(bf16x2, a), __builtin_bit_cast(bf16x2, b), rd, false);
      }
    }
    rd = sum8(rd);
    if (c8 == 0) rs[tl] = rbf(rd);
  }
  __syncthreads();

  const int r = lane & 31, h = lane >> 5;
  if (wave < 4) {
    const int n0 = (wave & 1) * 32, t0 = (wave >> 1) * 32;
    f32x16 acc = gemm_tile(wimg, ximg, n0, t0, lane);
    const float sc = 0.25f * 1.4426950408889634f;
    bf16* qrow = Q + (tok0 + t0 + r) * 512 + cb * 64 + n0;
#pragma unroll
    for (int g = 0; g < 4; ++g) {
      bf16x4 o;
#pragma unroll
      for (int j = 0; j < 4; ++j) o[j] = (bf16)(rbf(acc[4 * g + j]) * sc);
      *(bf16x4*)(qrow + 8 * g + 4 * h) = o;
    }
  } else if (wave < 6 && cb < 2) {
    const int t0 = (wave - 4) * 32;
    f32x16 acc = gemm_tile16(kvimg, ximg, t0, lane);
    const int key = tok0 + t0 + r;
    const int t = key >> 5;
    if (cb == 0) {
#pragma unroll
      for (int g = 0; g < 2; ++g) {
        bf16x4 o;
#pragma unroll
        for (int j = 0; j < 4; ++j) o[j] = (bf16)acc[4 * g + j];
        *(bf16x4*)(Kb + ((t * 2 + g) * 32 + r) * 8 + 4 * h) = o;
      }
    } else {
      const int kk = key & 31, sg = kk >> 4, rem = kk & 15;
      const int hh = (rem >> 2) & 1, jj = ((rem >> 3) << 2) | (rem & 3);
#pragma unroll
      for (int g = 0; g < 2; ++g) {
#pragma unroll
        for (int j = 0; j < 4; ++j) {
          const int d = 8 * g + 4 * h + j;
          Vf[(((t * 2 + sg) * 2 + hh) * 16 + d) * 8 + jj] = (bf16)acc[4 * g + j];
        }
      }
    }
  }
}

constexpr int ATT_LDS = 65536 * 2 + 8192 + 64 + 512;
constexpr int NT = 8;
constexpr int NC = 64 / NT;

DI void ffn_slice(int f, int j, const bf16* __restrict__ x, const float* __restrict__ st,
                  const unsigned* __restrict__ cand,
                  const bf16* __restrict__ wg, const bf16* __restrict__ wu, const bf16* __restrict__ wd,
                  float* __restrict__ ypart, int* __restrict__ sel, float* red, int* cnt, int lane) {
  const int k0 = j * 128 + lane * 2;
  u32x4 g8[2], u8[2];
  unsigned d2[8];
#pragma unroll
  for (int i = 0; i < 2; ++i) {
    g8[i] = *(const u32x4*)(wg + (k0 + i) * F_ + f * 8);
    u8[i] = *(const u32x4*)(wu + (k0 + i) * F_ + f * 8);
  }
#pragma unroll
  for (int c = 0; c < 8; ++c) d2[c] = *(const unsigned*)(wd + (f * 8 + c) * 512 + k0);
  const u32x2 cv = *(const u32x2*)(cand + (lane & 31) * 2);
  unsigned k1 = cv[0], k2 = cv[1];
  top2_wave(k1, k2, 32);
  const int i1 = key_tok(k1), i2 = key_tok(k2);
  if (f == 0 && j == 0 && lane == 0) {
    const float g0 = rbf(1.f / (1.f + __expf(-key_val(k1)))), g1 = rbf(1.f / (1.f + __expf(-key_val(k2))));
    *(u32x4*)sel = u32x4{(unsigned)i1, (unsigned)i2, __float_as_uint(g0), __float_as_uint(g1)};
  }
  const f32x2 sa = *(const f32x2*)(st + i1 * 2), sb = *(const f32x2*)(st + i2 * 2);
  const f32x2 xa = unpack2(ln2(unpack2(*(const unsigned*)(x + i1 * 512 + k0)), f32x2{sa.x, sa.x}, f32x2{sa.y, sa.y}));
  const f32x2 xb = unpack2(ln2(unpack2(*(const unsigned*)(x + i2 * 512 + k0)), f32x2{sb.x, sb.x}, f32x2{sb.y, sb.y}));
  f32x2 v2[16];
#pragma unroll
  for (int i = 0; i < 16; ++i) v2[i] = f32x2{0.f, 0.f};
#pragma unroll
  for (int i = 0; i < 2; ++i) {
    const float x0 = i ? xa.y : xa.x, x1 = i ? xb.y : xb.x;
    const f32x2 x02 = {x0, x0}, x12 = {x1, x1};
#pragma unroll
    for (int q = 0; q < 4; ++q) {
      const unsigned gq = g8[i][q], uq = u8[i][q];
      const f32x2 gw = unpack2(gq), uw = unpack2(uq);
      v2[q] += x02 * gw; v2[4 + q] += x12 * gw; v2[8 + q] += x02 * uw; v2[12 + q] += x12 * uw;
    }
  }
  float v[32];
#pragma unroll
  for (int i = 0; i < 16; ++i) { v[2 * i] = v2[i].x; v[2 * i + 1] = v2[i].y; }
#pragma unroll
  for (int st = 0; st < 5; ++st) {
    const int o = 32 >> st, n = 16 >> st;
    const bool up = (lane & o) != 0;
#pragma unroll
    for (int i = 0; i < n; ++i) {
      const float keep = up ? v[i + n] : v[i];
      const float send = up ? v[i] : v[i + n];
      v[i] = keep + __shfl_xor(send, o);
    }
  }
  v[0] += __shfl_xor(v[0], 1);
  if ((lane & 1) == 0) red[j * 32 + (lane >> 1)] = v[0];
  if (lane == 0) __hip_atomic_fetch_add(cnt, 1, __ATOMIC_RELEASE, __HIP_MEMORY_SCOPE_WORKGROUP);
  while (__hip_atomic_load(cnt, __ATOMIC_ACQUIRE, __HIP_MEMORY_SCOPE_WORKGROUP) < 4) __builtin_amdgcn_s_sleep(1);
  const int l = lane & 15;
  const float g = rbf((red[l] + red[32 + l]) + (red[64 + l] + red[96 + l]));
  const float u = rbf((red[16 + l] + red[48 + l]) + (red[80 + l] + red[112 + l]));
  const float hv = rbf(rbf(g / (1.f + __expf(-g))) * u);
  f32x2 y0 = {0.f, 0.f}, y1 = {0.f, 0.f};
#pragma unroll
  for (int c = 0; c < 8; ++c) {
    const float h0 = __shfl(hv, c), h1 = __shfl(hv, 8 + c);
    const f32x2 w = unpack2(d2[c]);
    y0 += f32x2{h0, h0} * w;
    y1 += f32x2{h1, h1} * w;
  }
  *(f32x2*)(ypart + (size_t)f * 1024 + k0) = y0;
  *(f32x2*)(ypart + (size_t)f * 1024 + 512 + k0) = y1;
}

__global__ __launch_bounds__(768) void k2_attn(const bf16* __restrict__ Q, const bf16* __restrict__ Kb,
                                               bf16* __restrict__ O, const bf16* __restrict__ x,
                                               const float* __restrict__ st, const unsigned* __restrict__ cand,
                                               const bf16* __restrict__ wg, const bf16* __restrict__ wu,
                                               const bf16* __restrict__ wd, float* __restrict__ ypart,
                                               int* __restrict__ sel, const bf16* __restrict__ wo) {
  extern __shared__ __attribute__((aligned(16))) char smem[];
  char* kl = smem;
  char* vl = smem + 65536;
  char* cst = smem + 131072;
  const int tid = threadIdx.x, lane = tid & 63;
  const int wave = __builtin_amdgcn_readfirstlane(tid >> 6);

  if (wave >= 8) {
    int* cnt = (int*)(smem + 131072 + 8192 + 32);
    if (tid == 512) *cnt = 0;
#pragma unroll
    for (int i = 0; i < 26; ++i) {
      const int q = (wave - 8) + 4 * i, piece = q < 48 ? 16 + q : 24 + q;
      glds16_raw((const char*)Kb + piece * 1024 + lane * 16, (unsigned)(size_t)(LDS_AS char*)smem + piece * 1024);
    }
    asm volatile("s_waitcnt lgkmcnt(0)\n s_barrier\n s_waitcnt vmcnt(0)\n s_barrier" ::: "memory");
    if (blockIdx.x < NFFN) {
      ffn_slice(blockIdx.x, wave - 8, x, st, cand, wg, wu, wd, ypart, sel, (float*)(smem + 131072 + 8192 + 64), cnt,
                lane);
    } else if (blockIdx.x >= 176 && blockIdx.x < 240) {
      const char* src = (const char*)wo + ((((blockIdx.x >> 3) - 22) * 4 + (wave - 8)) << 14) + lane * 16;
      u32x4 t[16];
#pragma unroll
      for (int i = 0; i < 16; ++i) t[i] = *(const u32x4*)(src + i * 1024);
#pragma unroll
      for (int i = 0; i < 16; ++i) asm volatile("" ::"v"(t[i]));
    }
    return;
  }

  const int r = lane & 31, h = lane >> 5;
  const int token = ((blockIdx.x >> 6) * 8 + (blockIdx.x & 7)) * 64 + ((blockIdx.x >> 3) & 7) * 8 + wave;
  bf16x8 qf;
  asm volatile("global_load_dwordx4 %0, %1, off" : "=v"(qf) : "v"(Q + token * 512 + r * 16 + 8 * h) : "memory");
  const unsigned lds0 = (unsigned)(size_t)(LDS_AS char*)smem;
#pragma unroll
  for (int i = 0; i < 3; ++i) {
    const int piece = (i < 2) ? (wave + i * 8) : (64 + wave);
    glds16_raw((const char*)Kb + piece * 1024 + lane * 16, lds0 + piece * 1024);
  }
  {
    const unsigned cv = ((tid & 15) == 0 || (tid & 15) == 4) ? 0x3f803f80u : 0u;
    *(u32x4*)(cst + tid * 16) = u32x4{cv, cv, cv, cv};
  }
  volatile LDS_AS int* prog = (volatile LDS_AS int*)(smem + 131072 + 8192);
  if (tid < 8) prog[tid] = 0;
  asm volatile("s_waitcnt vmcnt(0) lgkmcnt(0)\n s_barrier" ::: "memory");

  const char* kp0 = kl + lane * 16;
  const char* vp0 = (r < 16) ? (vl + (h * 16 + r) * 16) : (cst + (h * 16 + r - 16) * 16);
  const int vstep = (r < 16) ? NT * 1024 : 0;

  f32x16 zero;
#pragma unroll
  for (int i = 0; i < 16; ++i) zero[i] = 0.f;
  float m;
  {
    float mx = -INFINITY;
#pragma unroll
    for (int t = 0; t < 4; ++t) {
      bf16x8 kf = *(const bf16x8*)(kp0 + t * 1024);
      f32x16 x = __builtin_amdgcn_mfma_f32_32x32x16_bf16(kf, qf, zero, 0, 0, 0);
#pragma unroll
      for (int i = 0; i < 16; i += 2) mx = fmaxf(fmaxf(mx, x[i]), x[i + 1]);
    }
    mx = fmaxf(mx, __shfl_xor(mx, 32));
    m = floorf(mx);
  }
  f32x16 negm;
#pragma unroll
  for (int i = 0; i < 16; ++i) negm[i] = -m;
  float tot[9];
#pragma unroll
  for (int i = 0; i < 9; ++i) tot[i] = 0.f;
  {
    const char* kp = kp0;
    const char* vp = vp0;
    int c = 0;
    bool redone = false;
    f32x16 x = __builtin_amdgcn_mfma_f32_32x32x16_bf16(*(const bf16x8*)kp, qf, negm, 0, 0, 0);
    bf16x8 kfn = *(const bf16x8*)(kp + 1024);
    while (c < NC) {
      f32x16 acc = zero;
#pragma unroll
      for (int t = 0; t < NT; ++t) {
        const bf16x8 kf = kfn;
        kfn = *(const bf16x8*)(kp + (t + 2) * 1024);
        const bf16x8 vf0 = *(const bf16x8*)(vp + (t * 2) * 512);
        const bf16x8 vf1 = *(const bf16x8*)(vp + (t * 2 + 1) * 512);
        bf16x8 pf;
#pragma unroll
        for (int j = 0; j < 4; ++j) pf[j] = (bf16)__builtin_amdgcn_exp2f(x[j]);
        __builtin_amdgcn_sched_barrier(0);
        f32x16 xn = __builtin_amdgcn_mfma_f32_32x32x16_bf16(kf, qf, negm, 0, 0, 0);
        asm volatile("" : "+v"(xn));
        __builtin_amdgcn_sched_barrier(0);
#pragma unroll
        for (int j = 4; j < 8; ++j) pf[j] = (bf16)__builtin_amdgcn_exp2f(x[j]);
        acc = __builtin_amdgcn_mfma_f32_32x32x16_bf16(vf0, pf, acc, 0, 0, 0);
#pragma unroll
        for (int j = 0; j < 8; ++j) pf[j] = (bf16)__builtin_amdgcn_exp2f(x[8 + j]);
        acc = __builtin_amdgcn_mfma_f32_32x32x16_bf16(vf1, pf, acc, 0, 0, 0);
        __builtin_amdgcn_sched_barrier(0);
        x = xn;
      }
      if (!redone && __any(!(acc[8] < 7.9e28f))) {
        float mx = -INFINITY;
        for (int t = 0; t < NT; ++t) {
          bf16x8 kf = *(const bf16x8*)(kp + t * 1024);
          f32x16 x = __builtin_amdgcn_mfma_f32_32x32x16_bf16(kf, qf, zero, 0, 0, 0);
#pragma unroll
          for (int i = 0; i < 16; i += 2) mx = fmaxf(fmaxf(mx, x[i]), x[i + 1]);
        }
        mx = fmaxf(mx, __shfl_xor(mx, 32));
        const float mn = fmaxf(m, floorf(mx));
        const int d = (int)fminf(mn - m, 1024.f);
#pragma unroll
        for (int i = 0; i < 9; ++i) tot[i] = __builtin_amdgcn_ldexpf(tot[i], -d);
        m = mn;
#pragma unroll
        for (int i = 0; i < 16; ++i) negm[i] = -m;
        x = __builtin_amdgcn_mfma_f32_32x32x16_bf16(*(const bf16x8*)kp, qf, negm, 0, 0, 0);
        kfn = *(const bf16x8*)(kp + 1024);
        redone = true;
        continue;
      }
      redone = false;
#pragma unroll
      for (int i = 0; i < 9; ++i) tot[i] += acc[i];
      if (__any(tot[8] > 4.3e9f)) {
        const int e = max(__builtin_amdgcn_frexp_expf(tot[8]) - 1, 0);
#pragma unroll
        for (int i = 0; i < 9; ++i) tot[i] = __builtin_amdgcn_ldexpf(tot[i], -e);
        m += (float)e;
#pragma unroll
        for (int i = 0; i < 16; ++i) negm[i] = -m;
        x = __builtin_amdgcn_mfma_f32_32x32x16_bf16(*(const bf16x8*)(kp + NT * 1024), qf, negm, 0, 0, 0);
      }
      ++c;
      kp += NT * 1024;
      vp += vstep;
      if (c == 1) asm volatile("s_barrier" ::: "memory");
      if (lane == 0) prog[wave] = c;
      if (c <= __builtin_amdgcn_readfirstlane(prog[wave ^ 4])) __builtin_amdgcn_s_setprio(1);
      else __builtin_amdgcn_s_setprio(0);
    }
  }
  {
    float chk = 0.f;
#pragma unroll
    for (int i = 0; i < 9; ++i) chk = __builtin_fmaf(tot[i], 0.f, chk);
    if (__any(chk != 0.f)) {
      f32x16 oacc = zero;
      float mrun = -INFINITY;
      const char* kp = kp0;
      const char* vp = vp0;
      for (int t = 0; t < 64; ++t) {
        bf16x8 kf = *(const bf16x8*)(kp);
        f32x16 x = __builtin_amdgcn_mfma_f32_32x32x16_bf16(kf, qf, zero, 0, 0, 0);
        float mx = x[0];
#pragma unroll
        for (int i = 1; i < 16; ++i) mx = fmaxf(mx, x[i]);
        mx = fmaxf(mx, __shfl_xor(mx, 32));
        const float mn = fmaxf(mrun, mx);
        const float al = __builtin_amdgcn_exp2f(mrun - mn);
        mrun = mn;
#pragma unroll
        for (int i = 0; i < 16; ++i) oacc[i] *= al;
#pragma unroll
        for (int s = 0; s < 2; ++s) {
          bf16x8 pf;
#pragma unroll
          for (int j = 0; j < 8; ++j) pf[j] = (bf16)__builtin_amdgcn_exp2f(x[8 * s + j] - mn);
          bf16x8 vf = *(const bf16x8*)(vp + ((t & (NT - 1)) * 2 + s) * 512);
          oacc = __builtin_amdgcn_mfma_f32_32x32x16_bf16(vf, pf, oacc, 0, 0, 0);
        }
        kp += 1024;
        if ((t & (NT - 1)) == NT - 1) vp += vstep;
      }
#pragma unroll
      for (int i = 0; i < 9; ++i) tot[i] = oacc[i];
    }
  }
  const float inv = 1.f / tot[8];
  bf16* orow = O + token * 512 + r * 16;
#pragma unroll
  for (int g = 0; g < 2; ++g) {
    bf16x4 o;
#pragma unroll
    for (int j = 0; j < 4; ++j) o[j] = (bf16)(tot[4 * g + j] * inv);
    *(bf16x4*)(orow + 8 * g + 4 * h) = o;
  }
}

constexpr int K3_LDS = 64 * XS_STRIDE + 65536 + 4096;

__global__ __launch_bounds__(768) void k3_out(const bf16* __restrict__ O, const bf16* __restrict__ wo,
                                              const bf16* __restrict__ x, const float* __restrict__ st,
                                              const float* __restrict__ ypart, const int* __restrict__ sel,
                                              bf16* __restrict__ out) {
  extern __shared__ __attribute__((aligned(16))) char smem[];
  char* ximg = smem;
  char* wimg = smem + 64 * XS_STRIDE;
  float* red = (float*)(smem + 64 * XS_STRIDE + 65536);
  const int cb = blockIdx.x >> 5, tb = blockIdx.x & 31;
  const int tid = threadIdx.x, lane = tid & 63;
  const int wave = __builtin_amdgcn_readfirstlane(tid >> 6);
  const int tok0 = tb * 64;
  const u32x4 sv = *(const u32x4*)sel;
  const int s0 = (int)sv[0], s1 = (int)sv[1];
  if (wave >= 8) {
#pragma unroll
    for (int w = 0; w < 2; ++w) {
      if (((w ? s1 : s0) >> 6) == tb) {
        const int pg = (tid - 512) >> 4, c4 = tid & 15;
        f32x4 a = {0.f, 0.f, 0.f, 0.f};
#pragma unroll
        for (int i = 0; i < 11; ++i) {
          const int pp = pg + 16 * i;
          if (pp < NFFN) a += *(const f32x4*)(ypart + (size_t)pp * 1024 + w * 512 + cb * 64 + c4 * 4);
        }
#pragma unroll
        for (int o = 16; o < 64; o <<= 1) {
#pragma unroll
          for (int j = 0; j < 4; ++j) a[j] += __shfl_xor(a[j], o);
        }
        if (lane < 16) *(f32x4*)(red + (wave - 8) * 128 + w * 64 + c4 * 4) = a;
      }
    }
    __syncthreads();
    return;
  }
#pragma unroll
  for (int i = 0; i < 8; ++i) {
    const int row = wave + 8 * i;
    glds16(O + (tok0 + row) * 512 + lane * 8, ximg + row * XS_STRIDE);
  }
  stage_w(wo, cb * 64, wimg, wave, 8, lane);
  const int r = lane & 31, h = lane >> 5;
  const int n0 = (wave & 1) * 32, t0 = ((wave >> 1) & 1) * 32;
  const int tok = tok0 + t0 + r;
  u32x2 xv[4];
  f32x2 sx = {0.f, 0.f};
  if (wave < 4) {
#pragma unroll
    for (int g = 0; g < 4; ++g) xv[g] = *(const u32x2*)(x + tok * 512 + cb * 64 + n0 + 8 * g + 4 * h);
    sx = *(const f32x2*)(st + tok * 2);
  }
  __syncthreads();
  if (wave >= 4) return;
  f32x16 acc = gemm_tile(wimg, ximg, n0, t0, lane);
  float upd[16];
#pragma unroll
  for (int i = 0; i < 16; ++i) upd[i] = 0.f;
  const int which = (tok == s0) ? 0 : ((tok == s1) ? 1 : -1);
  if (which >= 0) {
    const float gate = __uint_as_float(which == 0 ? sv[2] : sv[3]);
#pragma unroll
    for (int w = 0; w < 4; ++w) {
#pragma unroll
      for (int g = 0; g < 4; ++g) {
        const f32x4 p = *(const f32x4*)(red + w * 128 + which * 64 + n0 + 8 * g + 4 * h);
#pragma unroll
        for (int j = 0; j < 4; ++j) upd[4 * g + j] += p[j];
      }
    }
#pragma unroll
    for (int i = 0; i < 16; ++i) upd[i] = rbf(rbf(upd[i]) * gate);
  }
#pragma unroll
  for (int g = 0; g < 4; ++g) {
    const int n = cb * 64 + n0 + 8 * g + 4 * h;
    bf16x4 o;
#pragma unroll
    for (int jj = 0; jj < 2; ++jj) {
      const unsigned xr = xv[g][jj];
      const f32x2 xf = unpack2(xr), nf = unpack2(ln2(xf, f32x2{sx.x, sx.x}, f32x2{sx.y, sx.y}));
#pragma unroll
      for (int e = 0; e < 2; ++e) {
        const int j = 2 * jj + e;
        const float a = rbf(acc[4 * g + j]);
        const float xa = rbf((e ? xf.y : xf.x) + a);
        const float bb = rbf((e ? nf.y : nf.x) + upd[4 * g + j]);
        o[j] = (bf16)(xa + bb);
      }
    }
    *(bf16x4*)(out + tok * 512 + n) = o;
  }
}

extern "C" void kernel_launch(void* const* d_in, const int* in_sizes, int n_in, void* d_out, int out_size,
                              void* d_ws, size_t ws_size, hipStream_t stream) {
  const bf16* x = (const bf16*)d_in[0];
  const bf16* wq = (const bf16*)d_in[1];
  const bf16* wk = (const bf16*)d_in[2];
  const bf16* wv = (const bf16*)d_in[3];
  const bf16* wo = (const bf16*)d_in[4];
  const bf16* wr = (const bf16*)d_in[6];
  const bf16* wg = (const bf16*)d_in[7];
  const bf16* wu = (const bf16*)d_in[8];
  const bf16* wd = (const bf16*)d_in[9];
  char* ws = (char*)d_ws;
  float* st = (float*)(ws + WS_ST);
  bf16* Q = (bf16*)(ws + WS_Q);
  bf16* Kb = (bf16*)(ws + WS_K);
  bf16* Vf = (bf16*)(ws + WS_V);
  unsigned* cand = (unsigned*)(ws + WS_R);
  bf16* O = (bf16*)(ws + WS_O);
  float* yp = (float*)(ws + WS_YP);
  int* sel = (int*)(ws + WS_SEL);
  bf16* out = (bf16*)d_out;

  (void)hipFuncSetAttribute((const void*)k1_ln_qkv, hipFuncAttributeMaxDynamicSharedMemorySize, K1_LDS);
  (void)hipFuncSetAttribute((const void*)k2_attn, hipFuncAttributeMaxDynamicSharedMemorySize, ATT_LDS);
  (void)hipFuncSetAttribute((const void*)k3_out, hipFuncAttributeMaxDynamicSharedMemorySize, K3_LDS);

  k1_ln_qkv<<<256, 768, K1_LDS, stream>>>(x, wq, wk, wv, wr, st, Q, Kb, Vf, cand);
  k2_attn<<<256, 768, ATT_LDS, stream>>>(Q, Kb, O, x, st, cand, wg, wu, wd, yp, sel, wo);
  k3_out<<<256, 768, K3_LDS, stream>>>(O, wo, x, st, yp, sel, out);
}
```

```cpp
#include <hip/hip_runtime.h>
#include <hip/hip_bf16.h>

typedef __attribute__((ext_vector_type(8))) __bf16 bf16x8;
typedef __attribute__((ext_vector_type(4))) __bf16 bf16x4;
typedef __attribute__((ext_vector_type(2))) __bf16 bf16x2;
typedef __attribute__((ext_vector_type(4))) short s16x4;
typedef __attribute__((ext_vector_type(8))) short s16x8;
typedef __attribute__((ext_vector_type(16))) float f32x16;
typedef __attribute__((ext_vector_type(4))) float f32x4;
typedef __attribute__((ext_vector_type(4))) unsigned u32x4;
typedef __attribute__((ext_vector_type(2))) unsigned u32x2;
typedef __attribute__((ext_vector_type(2))) float f32x2;
typedef __bf16 bf16;

#define LDS_AS __attribute__((address_space(3)))
#define GLB_AS __attribute__((address_space(1)))
#define DI __device__ __forceinline__

constexpr int S_ = 2048, D_ = 512, H_ = 32, DH_ = 16, F_ = 1376;

constexpr size_t WS_ST = 0;
constexpr size_t WS_Q = 2u << 20;
constexpr size_t WS_K = 4u << 20;
constexpr size_t WS_V = WS_K + 65536;
constexpr size_t WS_R = WS_V + 65536;
constexpr size_t WS_O = 5u << 20;
constexpr size_t WS_YP = 7u << 20;
constexpr size_t WS_SEL = 8u << 20;
constexpr size_t WS_CNT = WS_SEL + 4096;
constexpr size_t WS_UPD = WS_SEL + 16384;

constexpr int NFFN = 172;
constexpr int GSZ = 12, NGRP = 15;

DI float bf2f(bf16 v) { return (float)v; }
DI float rbf(float v) { return (float)(bf16)v; }
DI f32x2 unpack2(unsigned u) { return f32x2{__uint_as_float(u << 16), __uint_as_float(u & 0xffff0000u)}; }
DI unsigned pack2(f32x2 v) {
  unsigned r = __builtin_bit_cast(unsigned, __builtin_convertvector(v, bf16x2));
  asm("" : "+v"(r));
  return r;
}

DI unsigned route_key(float v, int tok) {
  const unsigned u = __float_as_uint(v) >> 16;
  const unsigned o = (u & 0x8000u) ? (~u & 0xffffu) : (u | 0x8000u);
  return (o << 16) | (unsigned)(0xffff - tok);
}
DI int key_tok(unsigned k) { return 0xffff - (int)(k & 0xffffu); }
DI float key_val(unsigned k) {
  const unsigned o = k >> 16;
  const unsigned u = (o & 0x8000u) ? (o & 0x7fffu) : (~o & 0xffffu);
  return __uint_as_float(u << 16);
}
DI unsigned ln2(f32x2 x, f32x2 mb2, f32x2 rinv2) { return pack2(unpack2(pack2(x - mb2)) * rinv2); }
DI void top2_wave(unsigned& k1, unsigned& k2, int width) {
  for (int o = width >> 1; o > 0; o >>= 1) {
    const unsigned a1 = __shfl_xor(k1, o), a2 = __shfl_xor(k2, o);
    k2 = max(min(k1, a1), max(k2, a2));
    k1 = max(k1, a1);
  }
}

DI void glds16(const void* g, void* l) {
  __builtin_amdgcn_global_load_lds((const GLB_AS void*)g, (LDS_AS void*)l, 16, 0, 0);
}

DI void glds16_raw(const void* g, unsigned lds_off) {
  asm volatile("s_mov_b32 m0, %0\n s_nop 0\n global_load_lds_dwordx4 %1, off" ::"s"(lds_off), "v"(g) : "memory", "m0");
}

template <int CTRL>
DI float dpp_add(float v) {
  const int t = __builtin_amdgcn_update_dpp(0, __float_as_int(v), CTRL, 0xf, 0xf, true);
  return v + __int_as_float(t);
}
DI float sum8(float v) {
  v = dpp_add<0xB1>(v);
  v = dpp_add<0x4E>(v);
  v = dpp_add<0x141>(v);
  return v;
}

DI s16x4 tr_read(const char* p) {
  return __builtin_amdgcn_ds_read_tr16_b64_v4i16((LDS_AS s16x4*)p);
}

DI int wimg_off(int row, int ch) { return row * 128 + ((ch ^ (((row >> 1) & 1) << 2)) << 4); }

constexpr int XS_STRIDE = 1040;

DI void stage_w(const bf16* w, int col0, char* wimg, int wave, int nwaves, int lane) {
  for (int p = wave; p < 64; p += nwaves) {
    const int row = p * 8 + (lane >> 3), pc = lane & 7;
    const int ch = pc ^ (((row >> 1) & 1) << 2);
    glds16(w + row * 512 + col0 + ch * 8, wimg + p * 1024);
  }
}

constexpr int GP = 4;
DI f32x16 gemm_tile(const char* wimg, const char* ximg, int n0, int t0, int lane) {
  const int r = lane & 31, h = lane >> 5;
  const int q = (lane & 15) >> 2, p = lane & 3, blk = (lane >> 4) & 1;
  f32x16 acc;
#pragma unroll
  for (int i = 0; i < 16; ++i) acc[i] = 0.f;
  const char* xb = ximg + (t0 + r) * XS_STRIDE + 16 * h;
  const int col = n0 + 16 * blk + 4 * p;
  const int ch = col >> 3, sub = (col & 7) * 2;
  const char* wb0 = wimg + wimg_off(8 * h + q, ch) + sub;
  const char* wb1 = wimg + wimg_off(8 * h + q + 4, ch) + sub;
  s16x4 lo[32], hi[32];
  bf16x8 b[32];
#pragma unroll
  for (int ks = 0; ks < 32 + GP; ++ks) {
    if (ks < 32) {
      lo[ks] = tr_read(wb0 + ks * 2048);
      hi[ks] = tr_read(wb1 + ks * 2048);
      b[ks] = *(const bf16x8*)(xb + ks * 32);
    }
    if (ks >= GP) {
      const s16x8 a = __builtin_shufflevector(lo[ks - GP], hi[ks - GP], 0, 1, 2, 3, 4, 5, 6, 7);
      acc = __builtin_amdgcn_mfma_f32_32x32x16_bf16(__builtin_bit_cast(bf16x8, a), b[ks - GP], acc, 0, 0, 0);
      asm volatile("" : "+v"(acc));
    }
    __builtin_amdgcn_sched_barrier(0);
  }
  return acc;
}
DI f32x16 gemm_tile16(const char* wimg, const char* ximg, int t0, int lane) {
  const int r = lane & 31, h = lane >> 5;
  const int q = (lane & 15) >> 2, p = lane & 3;
  f32x16 acc;
#pragma unroll
  for (int i = 0; i < 16; ++i) acc[i] = 0.f;
  const char* xb = ximg + (t0 + r) * XS_STRIDE + 16 * h;
  const char* wb = wimg + (8 * h + q) * 32 + 8 * p;
  s16x4 lo[32], hi[32];
  bf16x8 b[32];
#pragma unroll
  for (int ks = 0; ks < 32 + GP; ++ks) {
    if (ks < 32) {
      lo[ks] = tr_read(wb + ks * 512);
      hi[ks] = tr_read(wb + ks * 512 + 128);
      b[ks] = *(const bf16x8*)(xb + ks * 32);
    }
    if (ks >= GP) {
      const s16x8 a = __builtin_shufflevector(lo[ks - GP], hi[ks - GP], 0, 1, 2, 3, 4, 5, 6, 7);
      acc = __builtin_amdgcn_mfma_f32_32x32x16_bf16(__builtin_bit_cast(bf16x8, a), b[ks - GP], acc, 0, 0, 0);
      asm volatile("" : "+v"(acc));
    }
    __builtin_amdgcn_sched_barrier(0);
  }
  return acc;
}

constexpr int K1_LDS = 64 * XS_STRIDE + 65536 + 16384 + 256;

__global__ __launch_bounds__(768) void k1_ln_qkv(const bf16* __restrict__ x, const bf16* __restrict__ wq,
                                                 const bf16* __restrict__ wk, const bf16* __restrict__ wv,
                                                 const bf16* __restrict__ wr, float* __restrict__ st,
                                                 bf16* __restrict__ Q, bf16* __restrict__ Kb,
                                                 bf16* __restrict__ Vf, unsigned* __restrict__ cand,
                                                 unsigned* __restrict__ gcnt) {
  extern __shared__ __attribute__((aligned(16))) char smem[];
  char* ximg = smem;
  char* wimg = smem + 64 * XS_STRIDE;
  char* kvimg = smem + 64 * XS_STRIDE + 65536;
  float* rs = (float*)(smem + 64 * XS_STRIDE + 65536 + 16384);
  const int cb = blockIdx.x >> 5, tb = blockIdx.x & 31;
  const int tid = threadIdx.x, lane = tid & 63;
  const int wave = __builtin_amdgcn_readfirstlane(tid >> 6);
  const int tok0 = tb * 64;

  if (wave >= 8) {
    if (blockIdx.x == 255 && tid >= 768 - (NGRP + 1)) gcnt[(767 - tid) * 64] = 0;
    stage_w(wq, cb * 64, wimg, wave - 8, 4, lane);
    if (cb < 2) {
      const bf16* w = cb == 0 ? wk : wv;
#pragma unroll
      for (int i = 0; i < 4; ++i) {
        const int p = wave - 8 + 4 * i;
        glds16((const char*)w + p * 1024 + lane * 16, kvimg + p * 1024);
      }
    }
    __syncthreads();
    if (cb == 3 && wave == 8) {
      unsigned k1 = route_key(rs[lane], tok0 + lane), k2 = 0;
      top2_wave(k1, k2, 64);
      if (lane == 0) *(u32x2*)(cand + tb * 2) = u32x2{k1, k2};
    }
    return;
  }

  const int tl = wave * 8 + (lane >> 3), c8 = lane & 7;
  u32x4 xv[8];
#pragma unroll
  for (int i = 0; i < 8; ++i) xv[i] = *(const u32x4*)(x + (tok0 + tl) * 512 + (c8 + 8 * i) * 8);
  u32x4 wrv[8];
  if (cb == 3) {
#pragma unroll
    for (int i = 0; i < 8; ++i) wrv[i] = *(const u32x4*)(wr + (c8 + 8 * i) * 8);
  }

  f32x2 xf[32];
#pragma unroll
  for (int i = 0; i < 32; ++i) xf[i] = unpack2(xv[i >> 2][i & 3]);
  f32x2 s2 = {0.f, 0.f};
#pragma unroll
  for (int i = 0; i < 32; ++i) s2 += xf[i];
  const float mean = sum8(s2.x + s2.y) * (1.f / 512.f);
  const f32x2 mean2 = {mean, mean};
  f32x2 v2 = {0.f, 0.f};
#pragma unroll
  for (int i = 0; i < 32; ++i) { const f32x2 d = xf[i] - mean2; v2 += d * d; }
  const float vs = sum8(v2.x + v2.y);
  const float mb = rbf(mean);
  const float vb = rbf(vs * (1.f / 512.f));
  const float den = rbf(sqrtf(rbf(vb + 1e-5f)));
  const float rinv = __builtin_amdgcn_rcpf(den);
  const f32x2 mb2 = {mb, mb}, rinv2 = {rinv, rinv};
  u32x4 o[8];
#pragma unroll
  for (int i = 0; i < 8; ++i) {
#pragma unroll
    for (int j = 0; j < 4; ++j) o[i][j] = ln2(xf[4 * i + j], mb2, rinv2);
    *(u32x4*)(ximg + tl * XS_STRIDE + (c8 + 8 * i) * 16) = o[i];
  }
  if (cb == 2 && c8 == 0) *(f32x2*)(st + (tok0 + tl) * 2) = f32x2{mb, rinv};
  if (cb == 3) {
    float rd = 0.f;
#pragma unroll
    for (int i = 0; i < 8; ++i) {
#pragma unroll
      for (int j = 0; j < 4; ++j) {
        const unsigned a = o[i][j], b = wrv[i][j];
        rd = __builtin_amdgcn_fdot2_f32_bf16(__builtin_bit_cast(bf16x2, a), __builtin_bit_cast(bf16x2, b), rd, false);
      }
    }
    rd = sum8(rd);
    if (c8 == 0) rs[tl] = rbf(rd);
  }
  __syncthreads();

  const int r = lane & 31, h = lane >> 5;
  if (wave < 4) {
    const int n0 = (wave & 1) * 32, t0 = (wave >> 1) * 32;
    f32x16 acc = gemm_tile(wimg, ximg, n0, t0, lane);
    const float sc = 0.25f * 1.4426950408889634f;
    bf16* qrow = Q + (tok0 + t0 + r) * 512 + cb * 64 + n0;
#pragma unroll
    for (int g = 0; g < 4; ++g) {
      u32x2 o;
#pragma unroll
      for (int jj = 0; jj < 2; ++jj)
        o[jj] = pack2(unpack2(pack2(f32x2{acc[4 * g + 2 * jj], acc[4 * g + 2 * jj + 1]})) * f32x2{sc, sc});
      *(u32x2*)(qrow + 8 * g + 4 * h) = o;
    }
  } else if (wave < 6 && cb < 2) {
    const int t0 = (wave - 4) * 32;
    f32x16 acc = gemm_tile16(kvimg, ximg, t0, lane);
    const int key = tok0 + t0 + r;
    const int t = key >> 5;
    if (cb == 0) {
#pragma unroll
      for (int g = 0; g < 2; ++g) {
        bf16x4 o;
#pragma unroll
        for (int j = 0; j < 4; ++j) o[j] = (bf16)acc[4 * g + j];
        *(bf16x4*)(Kb + ((t * 2 + g) * 32 + r) * 8 + 4 * h) = o;
      }
    } else {
      const int kk = key & 31, sg = kk >> 4, rem = kk & 15;
      const int hh = (rem >> 2) & 1, jj = ((rem >> 3) << 2) | (rem & 3);
#pragma unroll
      for (int g = 0; g < 2; ++g) {
#pragma unroll
        for (int j = 0; j < 4; ++j) {
          const int d = 8 * g + 4 * h + j;
          Vf[(((t * 2 + sg) * 2 + hh) * 16 + d) * 8 + jj] = (bf16)acc[4 * g + j];
        }
      }
    }
  }
}

constexpr int NT = 16;
constexpr int CST = NT * 1024;
constexpr int ATT_LDS = 65536 * 2 + CST + 64 + 512;
constexpr int NC = 64 / NT;

DI void st_sc1(float* p, f32x2 v) {
  __hip_atomic_store((GLB_AS unsigned long long*)p, __builtin_bit_cast(unsigned long long, v), __ATOMIC_RELAXED,
                     __HIP_MEMORY_SCOPE_AGENT);
}
DI bool last_arrival(int* w, unsigned* gc, int n, int lane) {
  asm volatile("s_waitcnt vmcnt(0)" ::: "memory");
  if (lane == 0) {
    if (__hip_atomic_fetch_add(w, 1, __ATOMIC_RELAXED, __HIP_MEMORY_SCOPE_WORKGROUP) == 3) {
      const unsigned t = __hip_atomic_fetch_add((GLB_AS unsigned*)gc, 1u, __ATOMIC_RELAXED, __HIP_MEMORY_SCOPE_AGENT);
      __hip_atomic_store(w + 1, t == (unsigned)(n - 1) ? 2 : 1, __ATOMIC_RELAXED, __HIP_MEMORY_SCOPE_WORKGROUP);
    }
  }
  int r;
  while ((r = __hip_atomic_load(w + 1, __ATOMIC_RELAXED, __HIP_MEMORY_SCOPE_WORKGROUP)) == 0) __builtin_amdgcn_s_sleep(2);
  asm volatile("" ::: "memory");
  return r == 2;
}

DI void ffn_slice(int f, int j, const bf16* __restrict__ x, const float* __restrict__ st,
                  const unsigned* __restrict__ cand,
                  const bf16* __restrict__ wg, const bf16* __restrict__ wu, const bf16* __restrict__ wd,
                  float* __restrict__ ypart, int* __restrict__ sel, unsigned* __restrict__ gcnt,
                  float* __restrict__ upd, float* red, int* cnt, int lane) {
  const int k0 = j * 128 + lane * 2;
  u32x4 g8[2], u8[2];
  unsigned d2[8];
#pragma unroll
  for (int i = 0; i < 2; ++i) {
    g8[i] = *(const u32x4*)(wg + (k0 + i) * F_ + f * 8);
    u8[i] = *(const u32x4*)(wu + (k0 + i) * F_ + f * 8);
  }
#pragma unroll
  for (int c = 0; c < 8; ++c) d2[c] = *(const unsigned*)(wd + (f * 8 + c) * 512 + k0);
  const u32x2 cv = *(const u32x2*)(cand + (lane & 31) * 2);
  unsigned k1 = cv[0], k2 = cv[1];
  top2_wave(k1, k2, 32);
  const int i1 = key_tok(k1), i2 = key_tok(k2);
  if (f == 0 && j == 0 && lane == 0) *(u32x2*)sel = u32x2{(unsigned)i1, (unsigned)i2};
  const f32x2 sa = *(const f32x2*)(st + i1 * 2), sb = *(const f32x2*)(st + i2 * 2);
  const f32x2 xa = unpack2(ln2(unpack2(*(const unsigned*)(x + i1 * 512 + k0)), f32x2{sa.x, sa.x}, f32x2{sa.y, sa.y}));
  const f32x2 xb = unpack2(ln2(unpack2(*(const unsigned*)(x + i2 * 512 + k0)), f32x2{sb.x, sb.x}, f32x2{sb.y, sb.y}));
  f32x2 v2[16];
#pragma unroll
  for (int i = 0; i < 16; ++i) v2[i] = f32x2{0.f, 0.f};
#pragma unroll
  for (int i = 0; i < 2; ++i) {
    const float x0 = i ? xa.y : xa.x, x1 = i ? xb.y : xb.x;
    const f32x2 x02 = {x0, x0}, x12 = {x1, x1};
#pragma unroll
    for (int q = 0; q < 4; ++q) {
      const unsigned gq = g8[i][q], uq = u8[i][q];
      const f32x2 gw = unpack2(gq), uw = unpack2(uq);
      v2[q] += x02 * gw; v2[4 + q] += x12 * gw; v2[8 + q] += x02 * uw; v2[12 + q] += x12 * uw;
    }
  }
  float v[32];
#pragma unroll
  for (int i = 0; i < 16; ++i) { v[2 * i] = v2[i].x; v[2 * i + 1] = v2[i].y; }
#pragma unroll
  for (int st = 0; st < 5; ++st) {
    const int o = 32 >> st, n = 16 >> st;
    const bool up = (lane & o) != 0;
#pragma unroll
    for (int i = 0; i < n; ++i) {
      const float keep = up ? v[i + n] : v[i];
      const float send = up ? v[i] : v[i + n];
      v[i] = keep + __shfl_xor(send, o);
    }
  }
  v[0] += __shfl_xor(v[0], 1);
  if ((lane & 1) == 0) red[j * 32 + (lane >> 1)] = v[0];
  if (lane == 0) __hip_atomic_fetch_add(cnt, 1, __ATOMIC_RELEASE, __HIP_MEMORY_SCOPE_WORKGROUP);
  while (__hip_atomic_load(cnt, __ATOMIC_ACQUIRE, __HIP_MEMORY_SCOPE_WORKGROUP) < 4) __builtin_amdgcn_s_sleep(1);
  const int l = lane & 15;
  const float g = rbf((red[l] + red[32 + l]) + (red[64 + l] + red[96 + l]));
  const float u = rbf((red[16 + l] + red[48 + l]) + (red[80 + l] + red[112 + l]));
  const float hv = rbf(rbf(g / (1.f + __expf(-g))) * u);
  f32x2 y0 = {0.f, 0.f}, y1 = {0.f, 0.f};
#pragma unroll
  for (int c = 0; c < 8; ++c) {
    const float h0 = __shfl(hv, c), h1 = __shfl(hv, 8 + c);
    const f32x2 w = unpack2(d2[c]);
    y0 += f32x2{h0, h0} * w;
    y1 += f32x2{h1, h1} * w;
  }
  st_sc1(ypart + (size_t)f * 1024 + k0, y0);
  st_sc1(ypart + (size_t)f * 1024 + 512 + k0, y1);
  const int grp = f / GSZ, gs = min(GSZ, NFFN - grp * GSZ);
  if (!last_arrival(cnt + 1, gcnt + grp * 64, gs, lane)) return;
  const __amdgpu_buffer_rsrc_t rs = __builtin_amdgcn_make_buffer_rsrc(ypart, 0, (NFFN + NGRP) * 4096, 0x00020000);
  const int voff = j * 1024 + lane * 16;
  f32x4 a = {0.f, 0.f, 0.f, 0.f};
  {
    u32x4 t[GSZ];
#pragma unroll
    for (int i = 0; i < GSZ; ++i) t[i] = __builtin_amdgcn_raw_buffer_load_b128(rs, voff, (grp * GSZ + min(i, gs - 1)) * 4096, 16);
#pragma unroll
    for (int i = 0; i < GSZ; ++i)
      if (i < gs) a += __builtin_bit_cast(f32x4, t[i]);
  }
  __builtin_amdgcn_raw_buffer_store_b128(__builtin_bit_cast(u32x4, a), rs, voff, (NFFN + grp) * 4096, 16);
  if (!last_arrival(cnt + 3, gcnt + NGRP * 64, NGRP, lane)) return;
  a = f32x4{0.f, 0.f, 0.f, 0.f};
  {
    u32x4 t[NGRP];
#pragma unroll
    for (int i = 0; i < NGRP; ++i) t[i] = __builtin_amdgcn_raw_buffer_load_b128(rs, voff, (NFFN + i) * 4096, 16);
#pragma unroll
    for (int i = 0; i < NGRP; ++i) a += __builtin_bit_cast(f32x4, t[i]);
  }
  const float gate = rbf(1.f / (1.f + __expf(-key_val(j < 2 ? k1 : k2))));
#pragma unroll
  for (int i = 0; i < 4; ++i) a[i] = rbf(rbf(a[i]) * gate);
  *(f32x4*)(upd + j * 256 + lane * 4) = a;
}

__global__ __launch_bounds__(768) void k2_attn(const bf16* __restrict__ Q, const bf16* __restrict__ Kb,
                                               bf16* __restrict__ O, const bf16* __restrict__ x,
                                               const float* __restrict__ st, const unsigned* __restrict__ cand,
                                               const bf16* __restrict__ wg, const bf16* __restrict__ wu,
                                               const bf16* __restrict__ wd, float* __restrict__ ypart,
                                               int* __restrict__ sel, unsigned* __restrict__ gcnt,
                                               float* __restrict__ upd, const bf16* __restrict__ wo) {
  extern __shared__ __attribute__((aligned(16))) char smem[];
  char* kl = smem;
  char* vl = smem + 65536;
  char* cst = smem + 131072;
  const int tid = threadIdx.x, lane = tid & 63;
  const int wave = __builtin_amdgcn_readfirstlane(tid >> 6);

  if (wave >= 8) {
    int* cnt = (int*)(smem + 131072 + CST + 32);
    if (tid < 512 + 5) cnt[tid - 512] = 0;
#pragma unroll
    for (int i = 0; i < 26; ++i) {
      const int q = (wave - 8) + 4 * i, piece = q < 48 ? 16 + q : 24 + q;
      glds16_raw((const char*)Kb + piece * 1024 + lane * 16, (unsigned)(size_t)(LDS_AS char*)smem + piece * 1024);
    }
    asm volatile("s_waitcnt lgkmcnt(0)\n s_barrier\n s_waitcnt vmcnt(0)\n s_barrier" ::: "memory");
    if (blockIdx.x < NFFN) {
      __builtin_amdgcn_s_setprio(3);
      ffn_slice(blockIdx.x, wave - 8, x, st, cand, wg, wu, wd, ypart, sel, gcnt, upd,
                (float*)(smem + 131072 + CST + 64), cnt, lane);
    } else if (blockIdx.x >= 176 && blockIdx.x < 240) {
      const char* src = (const char*)wo + ((((blockIdx.x >> 3) - 22) * 4 + (wave - 8)) << 14) + lane * 16;
      u32x4 t[16];
#pragma unroll
      for (int i = 0; i < 16; ++i) t[i] = *(const u32x4*)(src + i * 1024);
#pragma unroll
      for (int i = 0; i < 16; ++i) asm volatile("" ::"v"(t[i]));
    }
    return;
  }

  const int r = lane & 31, h = lane >> 5;
  const int token = ((blockIdx.x >> 6) * 8 + (blockIdx.x & 7)) * 64 + ((blockIdx.x >> 3) & 7) * 8 + wave;
  bf16x8 qf;
  asm volatile("global_load_dwordx4 %0, %1, off" : "=v"(qf) : "v"(Q + token * 512 + r * 16 + 8 * h) : "memory");
  const unsigned lds0 = (unsigned)(size_t)(LDS_AS char*)smem;
#pragma unroll
  for (int i = 0; i < 3; ++i) {
    const int piece = (i < 2) ? (wave + i * 8) : (64 + wave);
    glds16_raw((const char*)Kb + piece * 1024 + lane * 16, lds0 + piece * 1024);
  }
  {
    const unsigned cv = ((tid & 15) == 0 || (tid & 15) == 4) ? 0x3f803f80u : 0u;
#pragma unroll
    for (int i = 0; i < CST / 8192; ++i) *(u32x4*)(cst + i * 8192 + tid * 16) = u32x4{cv, cv, cv, cv};
  }
  asm volatile("s_waitcnt vmcnt(0) lgkmcnt(0)\n s_barrier" ::: "memory");

  const char* kp0 = kl + lane * 16;
  const char* vp0 = (r < 16) ? (vl + (h * 16 + r) * 16) : (cst + (h * 16 + r - 16) * 16);
  const int vstep = (r < 16) ? NT * 1024 : 0;

  f32x16 zero;
#pragma unroll
  for (int i = 0; i < 16; ++i) zero[i] = 0.f;
  constexpr float HEAD = 40.f;
  float m;
  {
    float mx = -INFINITY;
#pragma unroll
    for (int t = 0; t < 4; ++t) {
      bf16x8 kf = *(const bf16x8*)(kp0 + t * 1024);
      f32x16 x = __builtin_amdgcn_mfma_f32_32x32x16_bf16(kf, qf, zero, 0, 0, 0);
#pragma unroll
      for (int i = 0; i < 16; i += 2) mx = fmaxf(fmaxf(mx, x[i]), x[i + 1]);
    }
    mx = fmaxf(mx, __shfl_xor(mx, 32));
    m = floorf(mx) + HEAD;
  }
  f32x16 negm;
#pragma unroll
  for (int i = 0; i < 16; ++i) negm[i] = -m;
  asm volatile("" : "+v"(negm));
  float tot[9];
#pragma unroll
  for (int i = 0; i < 9; ++i) tot[i] = 0.f;
  {
    const char* kp = kp0;
    const char* vp = vp0;
    int c = 0;
    bool redone = false, first = true;
    if (wave < 4) __builtin_amdgcn_s_setprio(2);
    else __builtin_amdgcn_s_setprio(1);
    f32x16 x = __builtin_amdgcn_mfma_f32_32x32x16_bf16(*(const bf16x8*)kp, qf, negm, 0, 0, 0);
    bf16x8 kfn = *(const bf16x8*)(kp + 1024);
    while (true) {
      f32x16 acc;
      while (true) {
        acc = zero;
#pragma unroll
        for (int t = 0; t < NT; ++t) {
          const bf16x8 kf = kfn;
          kfn = *(const bf16x8*)(kp + (t + 2) * 1024);
          const bf16x8 vf0 = *(const bf16x8*)(vp + (t * 2) * 512);
          const bf16x8 vf1 = *(const bf16x8*)(vp + (t * 2 + 1) * 512);
          bf16x8 pf;
#pragma unroll
          for (int j = 0; j < 4; ++j) pf[j] = (bf16)__builtin_amdgcn_exp2f(x[j]);
          __builtin_amdgcn_sched_barrier(0);
          f32x16 xn = __builtin_amdgcn_mfma_f32_32x32x16_bf16(kf, qf, negm, 0, 0, 0);
          asm volatile("" : "+v"(xn));
          __builtin_amdgcn_sched_barrier(0);
#pragma unroll
          for (int j = 4; j < 8; ++j) pf[j] = (bf16)__builtin_amdgcn_exp2f(x[j]);
          acc = __builtin_amdgcn_mfma_f32_32x32x16_bf16(vf0, pf, acc, 0, 0, 0);
#pragma unroll
          for (int j = 0; j < 8; ++j) pf[j] = (bf16)__builtin_amdgcn_exp2f(x[8 + j]);
          acc = __builtin_amdgcn_mfma_f32_32x32x16_bf16(vf1, pf, acc, 0, 0, 0);
          if (t == 7 && first) {
            asm volatile("s_barrier" ::: "memory");
            first = false;
          }
          if (c * NT + t == 53 && wave < 4) __builtin_amdgcn_s_setprio(0);
          __builtin_amdgcn_sched_barrier(0);
          x = xn;
        }
        if (__any(!(tot[8] + acc[8] <= 4.3e9f))) break;
        redone = false;
#pragma unroll
        for (int i = 0; i < 9; ++i) tot[i] += acc[i];
        ++c;
        kp += NT * 1024;
        vp += vstep;
        if (c == NC) break;
      }
      if (c == NC) break;
      if (!redone && __any(!(acc[8] < 7.9e28f))) {
        float mx = -INFINITY;
        for (int t = 0; t < NT; ++t) {
          bf16x8 kf = *(const bf16x8*)(kp + t * 1024);
          f32x16 x = __builtin_amdgcn_mfma_f32_32x32x16_bf16(kf, qf, zero, 0, 0, 0);
#pragma unroll
          for (int i = 0; i < 16; i += 2) mx = fmaxf(fmaxf(mx, x[i]), x[i + 1]);
        }
        mx = fmaxf(mx, __shfl_xor(mx, 32));
        const float mn = fmaxf(m, floorf(mx) + HEAD);
        const int d = (int)fminf(mn - m, 1024.f);
#pragma unroll
        for (int i = 0; i < 9; ++i) tot[i] = __builtin_amdgcn_ldexpf(tot[i], -d);
        m = mn;
        redone = true;
      } else {
        redone = false;
#pragma unroll
        for (int i = 0; i < 9; ++i) tot[i] += acc[i];
        const int e = max(__builtin_amdgcn_frexp_expf(tot[8]) - 1 + (int)HEAD, 0);
#pragma unroll
        for (int i = 0; i < 9; ++i) tot[i] = __builtin_amdgcn_ldexpf(tot[i], -e);
        m += (float)e;
        ++c;
        kp += NT * 1024;
        vp += vstep;
        if (c == NC) break;
      }
#pragma unroll
      for (int i = 0; i < 16; ++i) negm[i] = -m;
      asm volatile("" : "+v"(negm));
      x = __builtin_amdgcn_mfma_f32_32x32x16_bf16(*(const bf16x8*)kp, qf, negm, 0, 0, 0);
      kfn = *(const bf16x8*)(kp + 1024);
    }
  }
  {
    float chk = 0.f;
#pragma unroll
    for (int i = 0; i < 9; ++i) chk = __builtin_fmaf(tot[i], 0.f, chk);
    if (__any(chk != 0.f)) {
      f32x16 oacc = zero;
      float mrun = -INFINITY;
      const char* kp = kp0;
      const char* vp = vp0;
      for (int t = 0; t < 64; ++t) {
        bf16x8 kf = *(const bf16x8*)(kp);
        f32x16 x = __builtin_amdgcn_mfma_f32_32x32x16_bf16(kf, qf, zero, 0, 0, 0);
        float mx = x[0];
#pragma unroll
        for (int i = 1; i < 16; ++i) mx = fmaxf(mx, x[i]);
        mx = fmaxf(mx, __shfl_xor(mx, 32));
        const float mn = fmaxf(mrun, mx);
        const float al = __builtin_amdgcn_exp2f(mrun - mn);
        mrun = mn;
#pragma unroll
        for (int i = 0; i < 16; ++i) oacc[i] *= al;
#pragma unroll
        for (int s = 0; s < 2; ++s) {
          bf16x8 pf;
#pragma unroll
          for (int j = 0; j < 8; ++j) pf[j] = (bf16)__builtin_amdgcn_exp2f(x[8 * s + j] - mn);
          bf16x8 vf = *(const bf16x8*)(vp + ((t & (NT - 1)) * 2 + s) * 512);
          oacc = __builtin_amdgcn_mfma_f32_32x32x16_bf16(vf, pf, oacc, 0, 0, 0);
        }
        kp += 1024;
        if ((t & (NT - 1)) == NT - 1) vp += vstep;
      }
#pragma unroll
      for (int i = 0; i < 9; ++i) tot[i] = oacc[i];
    }
  }
  const float inv = 1.f / tot[8];
  bf16* orow = O + token * 512 + r * 16;
#pragma unroll
  for (int g = 0; g < 2; ++g) {
    bf16x4 o;
#pragma unroll
    for (int j = 0; j < 4; ++j) o[j] = (bf16)(tot[4 * g + j] * inv);
    *(bf16x4*)(orow + 8 * g + 4 * h) = o;
  }
}

constexpr int K3_LDS = 64 * XS_STRIDE + 65536;

__global__ __launch_bounds__(512) void k3_out(const bf16* __restrict__ O, const bf16* __restrict__ wo,
                                              const bf16* __restrict__ x, const float* __restrict__ st,
                                              const float* __restrict__ upd, const int* __restrict__ sel,
                                              bf16* __restrict__ out) {
  extern __shared__ __attribute__((aligned(16))) char smem[];
  char* ximg = smem;
  char* wimg = smem + 64 * XS_STRIDE;
  const int cb = blockIdx.x >> 5, tb = blockIdx.x & 31;
  const int tid = threadIdx.x, lane = tid & 63;
  const int wave = __builtin_amdgcn_readfirstlane(tid >> 6);
  const int tok0 = tb * 64;
  const int s0 = sel[0], s1 = sel[1];
#pragma unroll
  for (int i = 0; i < 8; ++i) {
    const int row = wave + 8 * i;
    glds16(O + (tok0 + row) * 512 + lane * 8, ximg + row * XS_STRIDE);
  }
  stage_w(wo, cb * 64, wimg, wave, 8, lane);
  const int r = lane & 31, h = lane >> 5;
  const int n0 = (wave & 1) * 32, t0 = ((wave >> 1) & 1) * 32;
  const int tok = tok0 + t0 + r;
  u32x2 xv[4], nv[4];
  if (wave < 4) {
#pragma unroll
    for (int g = 0; g < 4; ++g) xv[g] = *(const u32x2*)(x + tok * 512 + cb * 64 + n0 + 8 * g + 4 * h);
    const f32x2 sx = *(const f32x2*)(st + tok * 2);
    f32x4 u[4];
#pragma unroll
    for (int g = 0; g < 4; ++g) u[g] = f32x4{0.f, 0.f, 0.f, 0.f};
    if (tok == s0 || tok == s1) {
#pragma unroll
      for (int g = 0; g < 4; ++g) u[g] = *(const f32x4*)(upd + (tok == s0 ? 0 : 512) + cb * 64 + n0 + 8 * g + 4 * h);
    }
#pragma unroll
    for (int g = 0; g < 4; ++g) {
#pragma unroll
      for (int jj = 0; jj < 2; ++jj) {
        const unsigned xr = xv[g][jj];
        unsigned nr = ln2(unpack2(xr), f32x2{sx.x, sx.x}, f32x2{sx.y, sx.y});
        nr = pack2(unpack2(nr) + (jj ? f32x2{u[g][2], u[g][3]} : f32x2{u[g][0], u[g][1]}));
        asm volatile("" : "+v"(nr));
        nv[g][jj] = nr;
      }
    }
  }
  __syncthreads();
  if (wave >= 4) return;
  f32x16 acc = gemm_tile(wimg, ximg, n0, t0, lane);
#pragma unroll
  for (int g = 0; g < 4; ++g) {
    u32x2 o;
#pragma unroll
    for (int jj = 0; jj < 2; ++jj) {
      const unsigned xr = xv[g][jj], nr = nv[g][jj];
      const f32x2 a = unpack2(pack2(f32x2{acc[4 * g + 2 * jj], acc[4 * g + 2 * jj + 1]}));
      o[jj] = pack2(unpack2(pack2(unpack2(xr) + a)) + unpack2(nr));
    }
    *(u32x2*)(out + tok * 512 + cb * 64 + n0 + 8 * g + 4 * h) = o;
  }
}

extern "C" void kernel_launch(void* const* d_in, const int* in_sizes, int n_in, void* d_out, int out_size,
                              void* d_ws, size_t ws_size, hipStream_t stream) {
  const bf16* x = (const bf16*)d_in[0];
  const bf16* wq = (const bf16*)d_in[1];
  const bf16* wk = (const bf16*)d_in[2];
  const bf16* wv = (const bf16*)d_in[3];
  const bf16* wo = (const bf16*)d_in[4];
  const bf16* wr = (const bf16*)d_in[6];
  const bf16* wg = (const bf16*)d_in[7];
  const bf16* wu = (const bf16*)d_in[8];
  const bf16* wd = (const bf16*)d_in[9];
  char* ws = (char*)d_ws;
  float* st = (float*)(ws + WS_ST);
  bf16* Q = (bf16*)(ws + WS_Q);
  bf16* Kb = (bf16*)(ws + WS_K);
  bf16* Vf = (bf16*)(ws + WS_V);
  unsigned* cand = (unsigned*)(ws + WS_R);
  bf16* O = (bf16*)(ws + WS_O);
  float* yp = (float*)(ws + WS_YP);
  int* sel = (int*)(ws + WS_SEL);
  unsigned* gcnt = (unsigned*)(ws + WS_CNT);
  float* upd = (float*)(ws + WS_UPD);
  bf16* out = (bf16*)d_out;

  (void)hipFuncSetAttribute((const void*)k1_ln_qkv, hipFuncAttributeMaxDynamicSharedMemorySize, K1_LDS);
  (void)hipFuncSetAttribute((const void*)k2_attn, hipFuncAttributeMaxDynamicSharedMemorySize, ATT_LDS);
  (void)hipFuncSetAttribute((const void*)k3_out, hipFuncAttributeMaxDynamicSharedMemorySize, K3_LDS);

  k1_ln_qkv<<<256, 768, K1_LDS, stream>>>(x, wq, wk, wv, wr, st, Q, Kb, Vf, cand, gcnt);
  k2_attn<<<256, 768, ATT_LDS, stream>>>(Q, Kb, O, x, st, cand, wg, wu, wd, yp, sel, gcnt, upd, wo);
  k3_out<<<256, 512, K3_LDS, stream>>>(O, wo, x, st, upd, sel, out);
}
```

```cpp
#include <hip/hip_runtime.h>
#include <hip/hip_bf16.h>

typedef __attribute__((ext_vector_type(8))) __bf16 bf16x8;
typedef __attribute__((ext_vector_type(4))) __bf16 bf16x4;
typedef __attribute__((ext_vector_type(2))) __bf16 bf16x2;
typedef __attribute__((ext_vector_type(4))) short s16x4;
typedef __attribute__((ext_vector_type(8))) short s16x8;
typedef __attribute__((ext_vector_type(16))) float f32x16;
typedef __attribute__((ext_vector_type(4))) float f32x4;
typedef __attribute__((ext_vector_type(4))) unsigned u32x4;
typedef __attribute__((ext_vector_type(2))) unsigned u32x2;
typedef __attribute__((ext_vector_type(2))) float f32x2;
typedef __bf16 bf16;

#define LDS_AS __attribute__((address_space(3)))
#define GLB_AS __attribute__((address_space(1)))
#define DI __device__ __forceinline__

constexpr int S_ = 2048, D_ = 512, H_ = 32, DH_ = 16, F_ = 1376;

constexpr size_t WS_Q = 0;
constexpr size_t WS_O = WS_Q;
constexpr size_t WS_K = 2u << 20;
constexpr size_t WS_V = WS_K + 65536;
constexpr size_t WS_ST = WS_V + 65536;
constexpr size_t WS_R = WS_ST + 16384;
constexpr size_t WS_SEL = WS_R + 4096;
constexpr size_t WS_CNT = WS_SEL + 4096;
constexpr size_t WS_UPD = WS_CNT + 4096;
constexpr size_t WS_YP = WS_UPD + 4096;

constexpr int NFFN = 172;
constexpr int GSZ = 12, NGRP = 15;

DI float bf2f(bf16 v) { return (float)v; }
DI float rbf(float v) { return (float)(bf16)v; }
DI f32x2 unpack2(unsigned u) { return f32x2{__uint_as_float(u << 16), __uint_as_float(u & 0xffff0000u)}; }
DI unsigned pack2(f32x2 v) {
  unsigned r = __builtin_bit_cast(unsigned, __builtin_convertvector(v, bf16x2));
  asm("" : "+v"(r));
  return r;
}

DI unsigned route_key(float v, int tok) {
  const unsigned u = __float_as_uint(v) >> 16;
  const unsigned o = (u & 0x8000u) ? (~u & 0xffffu) : (u | 0x8000u);
  return (o << 16) | (unsigned)(0xffff - tok);
}
DI int key_tok(unsigned k) { return 0xffff - (int)(k & 0xffffu); }
DI float key_val(unsigned k) {
  const unsigned o = k >> 16;
  const unsigned u = (o & 0x8000u) ? (o & 0x7fffu) : (~o & 0xffffu);
  return __uint_as_float(u << 16);
}
DI unsigned ln2(f32x2 x, f32x2 mb2, f32x2 rinv2) { return pack2(unpack2(pack2(x - mb2)) * rinv2); }
DI void top2_wave(unsigned& k1, unsigned& k2, int width) {
  for (int o = width >> 1; o > 0; o >>= 1) {
    const unsigned a1 = __shfl_xor(k1, o), a2 = __shfl_xor(k2, o);
    k2 = max(min(k1, a1), max(k2, a2));
    k1 = max(k1, a1);
  }
}

DI void glds16(const void* g, void* l) {
  __builtin_amdgcn_global_load_lds((const GLB_AS void*)g, (LDS_AS void*)l, 16, 0, 0);
}

DI void glds16_raw(const void* g, unsigned lds_off) {
  asm volatile("s_mov_b32 m0, %0\n s_nop 0\n global_load_lds_dwordx4 %1, off" ::"s"(lds_off), "v"(g) : "memory", "m0");
}

template <int CTRL>
DI float dpp_add(float v) {
  const int t = __builtin_amdgcn_update_dpp(0, __float_as_int(v), CTRL, 0xf, 0xf, true);
  return v + __int_as_float(t);
}
DI float sum8(float v) {
  v = dpp_add<0xB1>(v);
  v = dpp_add<0x4E>(v);
  v = dpp_add<0x141>(v);
  return v;
}

DI s16x4 tr_read(const char* p) {
  return __builtin_amdgcn_ds_read_tr16_b64_v4i16((LDS_AS s16x4*)p);
}

DI int wimg_off(int row, int ch) { return row * 128 + ((ch ^ (((row >> 1) & 1) << 2)) << 4); }

constexpr int XS_STRIDE = 1040;

DI void stage_w(const bf16* w, int col0, char* wimg, int wave, int nwaves, int lane) {
  for (int p = wave; p < 64; p += nwaves) {
    const int row = p * 8 + (lane >> 3), pc = lane & 7;
    const int ch = pc ^ (((row >> 1) & 1) << 2);
    glds16(w + row * 512 + col0 + ch * 8, wimg + p * 1024);
  }
}

constexpr int GP = 4;
DI f32x16 gemm_tile(const char* wimg, const char* ximg, int n0, int t0, int lane) {
  const int r = lane & 31, h = lane >> 5;
  const int q = (lane & 15) >> 2, p = lane & 3, blk = (lane >> 4) & 1;
  f32x16 acc;
#pragma unroll
  for (int i = 0; i < 16; ++i) acc[i] = 0.f;
  const char* xb = ximg + (t0 + r) * XS_STRIDE + 16 * h;
  const int col = n0 + 16 * blk + 4 * p;
  const int ch = col >> 3, sub = (col & 7) * 2;
  const char* wb0 = wimg + wimg_off(8 * h + q, ch) + sub;
  const char* wb1 = wimg + wimg_off(8 * h + q + 4, ch) + sub;
  s16x4 lo[32], hi[32];
  bf16x8 b[32];
#pragma unroll
  for (int ks = 0; ks < 32 + GP; ++ks) {
    if (ks < 32) {
      lo[ks] = tr_read(wb0 + ks * 2048);
      hi[ks] = tr_read(wb1 + ks * 2048);
      b[ks] = *(const bf16x8*)(xb + ks * 32);
    }
    if (ks >= GP) {
      const s16x8 a = __builtin_shufflevector(lo[ks - GP], hi[ks - GP], 0, 1, 2, 3, 4, 5, 6, 7);
      acc = __builtin_amdgcn_mfma_f32_32x32x16_bf16(__builtin_bit_cast(bf16x8, a), b[ks - GP], acc, 0, 0, 0);
      asm volatile("" : "+v"(acc));
    }
    __builtin_amdgcn_sched_barrier(0);
  }
  return acc;
}
DI f32x16 gemm_tile16(const char* wimg, const char* ximg, int t0, int lane) {
  const int r = lane & 31, h = lane >> 5;
  const int q = (lane & 15) >> 2, p = lane & 3;
  f32x16 acc;
#pragma unroll
  for (int i = 0; i < 16; ++i) acc[i] = 0.f;
  const char* xb = ximg + (t0 + r) * XS_STRIDE + 16 * h;
  const char* wb = wimg + (8 * h + q) * 32 + 8 * p;
  s16x4 lo[32], hi[32];
  bf16x8 b[32];
#pragma unroll
  for (int ks = 0; ks < 32 + GP; ++ks) {
    if (ks < 32) {
      lo[ks] = tr_read(wb + ks * 512);
      hi[ks] = tr_read(wb + ks * 512 + 128);
      b[ks] = *(const bf16x8*)(xb + ks * 32);
    }
    if (ks >= GP) {
      const s16x8 a = __builtin_shufflevector(lo[ks - GP], hi[ks - GP], 0, 1, 2, 3, 4, 5, 6, 7);
      acc = __builtin_amdgcn_mfma_f32_32x32x16_bf16(__builtin_bit_cast(bf16x8, a), b[ks - GP], acc, 0, 0, 0);
      asm volatile("" : "+v"(acc));
    }
    __builtin_amdgcn_sched_barrier(0);
  }
  return acc;
}

constexpr int K1_LDS = 64 * XS_STRIDE + 65536 + 16384 + 256;

__global__ __launch_bounds__(768) void k1_ln_qkv(const bf16* __restrict__ x, const bf16* __restrict__ wq,
                                                 const bf16* __restrict__ wk, const bf16* __restrict__ wv,
                                                 const bf16* __restrict__ wr, float* __restrict__ st,
                                                 bf16* __restrict__ Q, bf16* __restrict__ Kb,
                                                 bf16* __restrict__ Vf, unsigned* __restrict__ cand,
                                                 unsigned* __restrict__ gcnt) {
  extern __shared__ __attribute__((aligned(16))) char smem[];
  char* ximg = smem;
  char* wimg = smem + 64 * XS_STRIDE;
  char* kvimg = smem + 64 * XS_STRIDE + 65536;
  float* rs = (float*)(smem + 64 * XS_STRIDE + 65536 + 16384);
  const int cb = blockIdx.x >> 5, tb = blockIdx.x & 31;
  const int tid = threadIdx.x, lane = tid & 63;
  const int wave = __builtin_amdgcn_readfirstlane(tid >> 6);
  const int tok0 = tb * 64;

  if (wave >= 8) {
    if (blockIdx.x == 255 && tid >= 768 - (NGRP + 1)) gcnt[(767 - tid) * 64] = 0;
    stage_w(wq, cb * 64, wimg, wave - 8, 4, lane);
    if (cb < 2) {
      const bf16* w = cb == 0 ? wk : wv;
#pragma unroll
      for (int i = 0; i < 4; ++i) {
        const int p = wave - 8 + 4 * i;
        glds16((const char*)w + p * 1024 + lane * 16, kvimg + p * 1024);
      }
    }
    __syncthreads();
    if (cb == 3 && wave == 8) {
      unsigned k1 = route_key(rs[lane], tok0 + lane), k2 = 0;
      top2_wave(k1, k2, 64);
      if (lane == 0) *(u32x2*)(cand + tb * 2) = u32x2{k1, k2};
    }
    return;
  }

  const int tl = wave * 8 + (lane >> 3), c8 = lane & 7;
  u32x4 xv[8];
#pragma unroll
  for (int i = 0; i < 8; ++i) xv[i] = *(const u32x4*)(x + (tok0 + tl) * 512 + (c8 + 8 * i) * 8);
  u32x4 wrv[8];
  if (cb == 3) {
#pragma unroll
    for (int i = 0; i < 8; ++i) wrv[i] = *(const u32x4*)(wr + (c8 + 8 * i) * 8);
  }

  f32x2 xf[32];
#pragma unroll
  for (int i = 0; i < 32; ++i) xf[i] = unpack2(xv[i >> 2][i & 3]);
  f32x2 s2 = {0.f, 0.f};
#pragma unroll
  for (int i = 0; i < 32; ++i) s2 += xf[i];
  const float mean = sum8(s2.x + s2.y) * (1.f / 512.f);
  const f32x2 mean2 = {mean, mean};
  f32x2 v2 = {0.f, 0.f};
#pragma unroll
  for (int i = 0; i < 32; ++i) { const f32x2 d = xf[i] - mean2; v2 += d * d; }
  const float vs = sum8(v2.x + v2.y);
  const float mb = rbf(mean);
  const float vb = rbf(vs * (1.f / 512.f));
  const float den = rbf(sqrtf(rbf(vb + 1e-5f)));
  const float rinv = __builtin_amdgcn_rcpf(den);
  const f32x2 mb2 = {mb, mb}, rinv2 = {rinv, rinv};
  u32x4 o[8];
#pragma unroll
  for (int i = 0; i < 8; ++i) {
#pragma unroll
    for (int j = 0; j < 4; ++j) o[i][j] = ln2(xf[4 * i + j], mb2, rinv2);
    *(u32x4*)(ximg + tl * XS_STRIDE + (c8 + 8 * i) * 16) = o[i];
  }
  if (cb == 2 && c8 == 0) *(f32x2*)(st + (tok0 + tl) * 2) = f32x2{mb, rinv};
  if (cb == 3) {
    float rd = 0.f;
#pragma unroll
    for (int i = 0; i < 8; ++i) {
#pragma unroll
      for (int j = 0; j < 4; ++j) {
        const unsigned a = o[i][j], b = wrv[i][j];
        rd = __builtin_amdgcn_fdot2_f32_bf16(__builtin_bit_cast(bf16x2, a), __builtin_bit_cast(bf16x2, b), rd, false);
      }
    }
    rd = sum8(rd);
    if (c8 == 0) rs[tl] = rbf(rd);
  }
  __syncthreads();

  const int r = lane & 31, h = lane >> 5;
  if (wave < 4) {
    const int n0 = (wave & 1) * 32, t0 = (wave >> 1) * 32;
    f32x16 acc = gemm_tile(wimg, ximg, n0, t0, lane);
    const float sc = 0.25f * 1.4426950408889634f;
    bf16* qrow = Q + (tok0 + t0 + r) * 512 + cb * 64 + n0;
#pragma unroll
    for (int g = 0; g < 4; ++g) {
      u32x2 o;
#pragma unroll
      for (int jj = 0; jj < 2; ++jj)
        o[jj] = pack2(unpack2(pack2(f32x2{acc[4 * g + 2 * jj], acc[4 * g + 2 * jj + 1]})) * f32x2{sc, sc});
      *(u32x2*)(qrow + 8 * g + 4 * h) = o;
    }
  } else if (wave < 6 && cb < 2) {
    const int t0 = (wave - 4) * 32;
    f32x16 acc = gemm_tile16(kvimg, ximg, t0, lane);
    const int key = tok0 + t0 + r;
    const int t = key >> 5;
    if (cb == 0) {
#pragma unroll
      for (int g = 0; g < 2; ++g) {
        bf16x4 o;
#pragma unroll
        for (int j = 0; j < 4; ++j) o[j] = (bf16)acc[4 * g + j];
        *(bf16x4*)(Kb + ((t * 2 + g) * 32 + r) * 8 + 4 * h) = o;
      }
    } else {
      const int kk = key & 31, sg = kk >> 4, rem = kk & 15;
      const int hh = (rem >> 2) & 1, jj = ((rem >> 3) << 2) | (rem & 3);
#pragma unroll
      for (int g = 0; g < 2; ++g) {
#pragma unroll
        for (int j = 0; j < 4; ++j) {
          const int d = 8 * g + 4 * h + j;
          Vf[(((t * 2 + sg) * 2 + hh) * 16 + d) * 8 + jj] = (bf16)acc[4 * g + j];
        }
      }
    }
  }
}

constexpr int NT = 16;
constexpr int CST = NT * 1024;
constexpr int ATT_LDS = 65536 * 2 + CST + 64 + 512;
constexpr int NC = 64 / NT;

DI void st_sc1(float* p, f32x2 v) {
  __hip_atomic_store((GLB_AS unsigned long long*)p, __builtin_bit_cast(unsigned long long, v), __ATOMIC_RELAXED,
                     __HIP_MEMORY_SCOPE_AGENT);
}
DI bool last_arrival(int* w, unsigned* gc, int n, int lane) {
  asm volatile("s_waitcnt vmcnt(0)" ::: "memory");
  if (lane == 0) {
    if (__hip_atomic_fetch_add(w, 1, __ATOMIC_RELAXED, __HIP_MEMORY_SCOPE_WORKGROUP) == 3) {
      const unsigned t = __hip_atomic_fetch_add((GLB_AS unsigned*)gc, 1u, __ATOMIC_RELAXED, __HIP_MEMORY_SCOPE_AGENT);
      __hip_atomic_store(w + 1, t == (unsigned)(n - 1) ? 2 : 1, __ATOMIC_RELAXED, __HIP_MEMORY_SCOPE_WORKGROUP);
    }
  }
  int r;
  while ((r = __hip_atomic_load(w + 1, __ATOMIC_RELAXED, __HIP_MEMORY_SCOPE_WORKGROUP)) == 0) __builtin_amdgcn_s_sleep(2);
  asm volatile("" ::: "memory");
  return r == 2;
}

DI void ffn_slice(int f, int j, const bf16* __restrict__ x, const float* __restrict__ st,
                  const unsigned* __restrict__ cand,
                  const bf16* __restrict__ wg, const bf16* __restrict__ wu, const bf16* __restrict__ wd,
                  float* __restrict__ ypart, int* __restrict__ sel, unsigned* __restrict__ gcnt,
                  float* __restrict__ upd, float* red, int* cnt, int lane) {
  const int k0 = j * 128 + lane * 2;
  u32x4 g8[2], u8[2];
  unsigned d2[8];
#pragma unroll
  for (int i = 0; i < 2; ++i) {
    g8[i] = *(const u32x4*)(wg + (k0 + i) * F_ + f * 8);
    u8[i] = *(const u32x4*)(wu + (k0 + i) * F_ + f * 8);
  }
#pragma unroll
  for (int c = 0; c < 8; ++c) d2[c] = *(const unsigned*)(wd + (f * 8 + c) * 512 + k0);
  const u32x2 cv = *(const u32x2*)(cand + (lane & 31) * 2);
  unsigned k1 = cv[0], k2 = cv[1];
  top2_wave(k1, k2, 32);
  const int i1 = key_tok(k1), i2 = key_tok(k2);
  if (f == 0 && j == 0 && lane == 0) *(u32x2*)sel = u32x2{(unsigned)i1, (unsigned)i2};
  const f32x2 sa = *(const f32x2*)(st + i1 * 2), sb = *(const f32x2*)(st + i2 * 2);
  const f32x2 xa = unpack2(ln2(unpack2(*(const unsigned*)(x + i1 * 512 + k0)), f32x2{sa.x, sa.x}, f32x2{sa.y, sa.y}));
  const f32x2 xb = unpack2(ln2(unpack2(*(const unsigned*)(x + i2 * 512 + k0)), f32x2{sb.x, sb.x}, f32x2{sb.y, sb.y}));
  f32x2 v2[16];
#pragma unroll
  for (int i = 0; i < 16; ++i) v2[i] = f32x2{0.f, 0.f};
#pragma unroll
  for (int i = 0; i < 2; ++i) {
    const float x0 = i ? xa.y : xa.x, x1 = i ? xb.y : xb.x;
    const f32x2 x02 = {x0, x0}, x12 = {x1, x1};
#pragma unroll
    for (int q = 0; q < 4; ++q) {
      const unsigned gq = g8[i][q], uq = u8[i][q];
      const f32x2 gw = unpack2(gq), uw = unpack2(uq);
      v2[q] += x02 * gw; v2[4 + q] += x12 * gw; v2[8 + q] += x02 * uw; v2[12 + q] += x12 * uw;
    }
  }
  float v[32];
#pragma unroll
  for (int i = 0; i < 16; ++i) { v[2 * i] = v2[i].x; v[2 * i + 1] = v2[i].y; }
#pragma unroll
  for (int st = 0; st < 5; ++st) {
    const int o = 32 >> st, n = 16 >> st;
    const bool up = (lane & o) != 0;
#pragma unroll
    for (int i = 0; i < n; ++i) {
      const float keep = up ? v[i + n] : v[i];
      const float send = up ? v[i] : v[i + n];
      v[i] = keep + __shfl_xor(send, o);
    }
  }
  v[0] += __shfl_xor(v[0], 1);
  if ((lane & 1) == 0) red[j * 32 + (lane >> 1)] = v[0];
  if (lane == 0) __hip_atomic_fetch_add(cnt, 1, __ATOMIC_RELEASE, __HIP_MEMORY_SCOPE_WORKGROUP);
  while (__hip_atomic_load(cnt, __ATOMIC_ACQUIRE, __HIP_MEMORY_SCOPE_WORKGROUP) < 4) __builtin_amdgcn_s_sleep(1);
  const int l = lane & 15;
  const float g = rbf((red[l] + red[32 + l]) + (red[64 + l] + red[96 + l]));
  const float u = rbf((red[16 + l] + red[48 + l]) + (red[80 + l] + red[112 + l]));
  const float hv = rbf(rbf(g / (1.f + __expf(-g))) * u);
  f32x2 y0 = {0.f, 0.f}, y1 = {0.f, 0.f};
#pragma unroll
  for (int c = 0; c < 8; ++c) {
    const float h0 = __shfl(hv, c), h1 = __shfl(hv, 8 + c);
    const f32x2 w = unpack2(d2[c]);
    y0 += f32x2{h0, h0} * w;
    y1 += f32x2{h1, h1} * w;
  }
  st_sc1(ypart + (size_t)f * 1024 + k0, y0);
  st_sc1(ypart + (size_t)f * 1024 + 512 + k0, y1);
  const int grp = f / GSZ, gs = min(GSZ, NFFN - grp * GSZ);
  if (!last_arrival(cnt + 1, gcnt + grp * 64, gs, lane)) return;
  const __amdgpu_buffer_rsrc_t rs = __builtin_amdgcn_make_buffer_rsrc(ypart, 0, (NFFN + NGRP) * 4096, 0x00020000);
  const int voff = j * 1024 + lane * 16;
  f32x4 a = {0.f, 0.f, 0.f, 0.f};
  {
    u32x4 t[GSZ];
#pragma unroll
    for (int i = 0; i < GSZ; ++i) t[i] = __builtin_amdgcn_raw_buffer_load_b128(rs, voff, (grp * GSZ + min(i, gs - 1)) * 4096, 16);
#pragma unroll
    for (int i = 0; i < GSZ; ++i)
      if (i < gs) a += __builtin_bit_cast(f32x4, t[i]);
  }
  __builtin_amdgcn_raw_buffer_store_b128(__builtin_bit_cast(u32x4, a), rs, voff, (NFFN + grp) * 4096, 16);
  if (!last_arrival(cnt + 3, gcnt + NGRP * 64, NGRP, lane)) return;
  a = f32x4{0.f, 0.f, 0.f, 0.f};
  {
    u32x4 t[NGRP];
#pragma unroll
    for (int i = 0; i < NGRP; ++i) t[i] = __builtin_amdgcn_raw_buffer_load_b128(rs, voff, (NFFN + i) * 4096, 16);
#pragma unroll
    for (int i = 0; i < NGRP; ++i) a += __builtin_bit_cast(f32x4, t[i]);
  }
  const float gate = rbf(1.f / (1.f + __expf(-key_val(j < 2 ? k1 : k2))));
#pragma unroll
  for (int i = 0; i < 4; ++i) a[i] = rbf(rbf(a[i]) * gate);
  *(f32x4*)(upd + j * 256 + lane * 4) = a;
}

__global__ __launch_bounds__(768) void k2_attn(const bf16* Q, const bf16* __restrict__ Kb,
                                               bf16* O, const bf16* __restrict__ x,
                                               const float* __restrict__ st, const unsigned* __restrict__ cand,
                                               const bf16* __restrict__ wg, const bf16* __restrict__ wu,
                                               const bf16* __restrict__ wd, float* __restrict__ ypart,
                                               int* __restrict__ sel, unsigned* __restrict__ gcnt,
                                               float* __restrict__ upd, const bf16* __restrict__ wo) {
  extern __shared__ __attribute__((aligned(16))) char smem[];
  char* kl = smem;
  char* vl = smem + 65536;
  char* cst = smem + 131072;
  const int tid = threadIdx.x, lane = tid & 63;
  const int wave = __builtin_amdgcn_readfirstlane(tid >> 6);

  if (wave >= 8) {
    int* cnt = (int*)(smem + 131072 + CST + 32);
    if (tid < 512 + 5) cnt[tid - 512] = 0;
#pragma unroll
    for (int i = 0; i < 26; ++i) {
      const int q = (wave - 8) + 4 * i, piece = q < 48 ? 16 + q : 24 + q;
      glds16_raw((const char*)Kb + piece * 1024 + lane * 16, (unsigned)(size_t)(LDS_AS char*)smem + piece * 1024);
    }
    asm volatile("s_waitcnt lgkmcnt(0)\n s_barrier\n s_waitcnt vmcnt(0)\n s_barrier" ::: "memory");
    if (blockIdx.x < NFFN) {
      __builtin_amdgcn_s_setprio(3);
      ffn_slice(blockIdx.x, wave - 8, x, st, cand, wg, wu, wd, ypart, sel, gcnt, upd,
                (float*)(smem + 131072 + CST + 64), cnt, lane);
    } else if (blockIdx.x >= 176 && blockIdx.x < 240) {
      const char* src = (const char*)wo + ((((blockIdx.x >> 3) - 22) * 4 + (wave - 8)) << 14) + lane * 16;
      u32x4 t[16];
#pragma unroll
      for (int i = 0; i < 16; ++i) t[i] = *(const u32x4*)(src + i * 1024);
#pragma unroll
      for (int i = 0; i < 16; ++i) asm volatile("" ::"v"(t[i]));
    }
    return;
  }

  const int r = lane & 31, h = lane >> 5;
  const int token = ((blockIdx.x >> 6) * 8 + (blockIdx.x & 7)) * 64 + ((blockIdx.x >> 3) & 7) * 8 + wave;
  bf16x8 qf;
  asm volatile("global_load_dwordx4 %0, %1, off" : "=v"(qf) : "v"(Q + token * 512 + r * 16 + 8 * h) : "memory");
  const unsigned lds0 = (unsigned)(size_t)(LDS_AS char*)smem;
#pragma unroll
  for (int i = 0; i < 3; ++i) {
    const int piece = (i < 2) ? (wave + i * 8) : (64 + wave);
    glds16_raw((const char*)Kb + piece * 1024 + lane * 16, lds0 + piece * 1024);
  }
  {
    const unsigned cv = ((tid & 15) == 0 || (tid & 15) == 4) ? 0x3f803f80u : 0u;
#pragma unroll
    for (int i = 0; i < CST / 8192; ++i) *(u32x4*)(cst + i * 8192 + tid * 16) = u32x4{cv, cv, cv, cv};
  }
  asm volatile("s_waitcnt vmcnt(0) lgkmcnt(0)\n s_barrier" ::: "memory");

  const char* kp0 = kl + lane * 16;
  const char* vp0 = (r < 16) ? (vl + (h * 16 + r) * 16) : (cst + (h * 16 + r - 16) * 16);
  const int vstep = (r < 16) ? NT * 1024 : 0;

  f32x16 zero;
#pragma unroll
  for (int i = 0; i < 16; ++i) zero[i] = 0.f;
  constexpr float HEAD = 40.f;
  float m;
  {
    float mx = -INFINITY;
#pragma unroll
    for (int t = 0; t < 4; ++t) {
      bf16x8 kf = *(const bf16x8*)(kp0 + t * 1024);
      f32x16 x = __builtin_amdgcn_mfma_f32_32x32x16_bf16(kf, qf, zero, 0, 0, 0);
#pragma unroll
      for (int i = 0; i < 16; i += 2) mx = fmaxf(fmaxf(mx, x[i]), x[i + 1]);
    }
    mx = fmaxf(mx, __shfl_xor(mx, 32));
    m = floorf(mx) + HEAD;
  }
  f32x16 negm;
#pragma unroll
  for (int i = 0; i < 16; ++i) negm[i] = -m;
  asm volatile("" : "+v"(negm));
  float tot[9];
#pragma unroll
  for (int i = 0; i < 9; ++i) tot[i] = 0.f;
  {
    const char* kp = kp0;
    const char* vp = vp0;
    int c = 0;
    bool redone = false, first = true;
    if (wave < 4) __builtin_amdgcn_s_setprio(2);
    else __builtin_amdgcn_s_setprio(1);
    f32x16 x = __builtin_amdgcn_mfma_f32_32x32x16_bf16(*(const bf16x8*)kp, qf, negm, 0, 0, 0);
    bf16x8 kfn = *(const bf16x8*)(kp + 1024);
    while (true) {
      f32x16 acc;
      while (true) {
        acc = zero;
#pragma unroll
        for (int t = 0; t < NT; ++t) {
          const bf16x8 kf = kfn;
          kfn = *(const bf16x8*)(kp + (t + 2) * 1024);
          const bf16x8 vf0 = *(const bf16x8*)(vp + (t * 2) * 512);
          const bf16x8 vf1 = *(const bf16x8*)(vp + (t * 2 + 1) * 512);
          bf16x8 pf;
#pragma unroll
          for (int j = 0; j < 4; ++j) pf[j] = (bf16)__builtin_amdgcn_exp2f(x[j]);
          __builtin_amdgcn_sched_barrier(0);
          f32x16 xn = __builtin_amdgcn_mfma_f32_32x32x16_bf16(kf, qf, negm, 0, 0, 0);
          asm volatile("" : "+v"(xn));
          __builtin_amdgcn_sched_barrier(0);
#pragma unroll
          for (int j = 4; j < 8; ++j) pf[j] = (bf16)__builtin_amdgcn_exp2f(x[j]);
          acc = __builtin_amdgcn_mfma_f32_32x32x16_bf16(vf0, pf, acc, 0, 0, 0);
#pragma unroll
          for (int j = 0; j < 8; ++j) pf[j] = (bf16)__builtin_amdgcn_exp2f(x[8 + j]);
          acc = __builtin_amdgcn_mfma_f32_32x32x16_bf16(vf1, pf, acc, 0, 0, 0);
          if (t == 7 && first) {
            asm volatile("s_barrier" ::: "memory");
            first = false;
          }
          if (c * NT + t == 53 && wave < 4) __builtin_amdgcn_s_setprio(0);
          __builtin_amdgcn_sched_barrier(0);
          x = xn;
        }
        if (__any(!(tot[8] + acc[8] <= 4.3e9f))) break;
        redone = false;
#pragma unroll
        for (int i = 0; i < 9; ++i) tot[i] += acc[i];
        ++c;
        kp += NT * 1024;
        vp += vstep;
        if (c == NC) break;
      }
      if (c == NC) break;
      if (!redone && __any(!(acc[8] < 7.9e28f))) {
        float mx = -INFINITY;
        for (int t = 0; t < NT; ++t) {
          bf16x8 kf = *(const bf16x8*)(kp + t * 1024);
          f32x16 x = __builtin_amdgcn_mfma_f32_32x32x16_bf16(kf, qf, zero, 0, 0, 0);
#pragma unroll
          for (int i = 0; i < 16; i += 2) mx = fmaxf(fmaxf(mx, x[i]), x[i + 1]);
        }
        mx = fmaxf(mx, __shfl_xor(mx, 32));
        const float mn = fmaxf(m, floorf(mx) + HEAD);
        const int d = (int)fminf(mn - m, 1024.f);
#pragma unroll
        for (int i = 0; i < 9; ++i) tot[i] = __builtin_amdgcn_ldexpf(tot[i], -d);
        m = mn;
        redone = true;
      } else {
        redone = false;
#pragma unroll
        for (int i = 0; i < 9; ++i) tot[i] += acc[i];
        const int e = max(__builtin_amdgcn_frexp_expf(tot[8]) - 1 + (int)HEAD, 0);
#pragma unroll
        for (int i = 0; i < 9; ++i) tot[i] = __builtin_amdgcn_ldexpf(tot[i], -e);
        m += (float)e;
        ++c;
        kp += NT * 1024;
        vp += vstep;
        if (c == NC) break;
      }
#pragma unroll
      for (int i = 0; i < 16; ++i) negm[i] = -m;
      asm volatile("" : "+v"(negm));
      x = __builtin_amdgcn_mfma_f32_32x32x16_bf16(*(const bf16x8*)kp, qf, negm, 0, 0, 0);
      kfn = *(const bf16x8*)(kp + 1024);
    }
  }
  {
    float chk = 0.f;
#pragma unroll
    for (int i = 0; i < 9; ++i) chk = __builtin_fmaf(tot[i], 0.f, chk);
    if (__any(chk != 0.f)) {
      f32x16 oacc = zero;
      float mrun = -INFINITY;
      const char* kp = kp0;
      const char* vp = vp0;
      for (int t = 0; t < 64; ++t) {
        bf16x8 kf = *(const bf16x8*)(kp);
        f32x16 x = __builtin_amdgcn_mfma_f32_32x32x16_bf16(kf, qf, zero, 0, 0, 0);
        float mx = x[0];
#pragma unroll
        for (int i = 1; i < 16; ++i) mx = fmaxf(mx, x[i]);
        mx = fmaxf(mx, __shfl_xor(mx, 32));
        const float mn = fmaxf(mrun, mx);
        const float al = __builtin_amdgcn_exp2f(mrun - mn);
        mrun = mn;
#pragma unroll
        for (int i = 0; i < 16; ++i) oacc[i] *= al;
#pragma unroll
        for (int s = 0; s < 2; ++s) {
          bf16x8 pf;
#pragma unroll
          for (int j = 0; j < 8; ++j) pf[j] = (bf16)__builtin_amdgcn_exp2f(x[8 * s + j] - mn);
          bf16x8 vf = *(const bf16x8*)(vp + ((t & (NT - 1)) * 2 + s) * 512);
          oacc = __builtin_amdgcn_mfma_f32_32x32x16_bf16(vf, pf, oacc, 0, 0, 0);
        }
        kp += 1024;
        if ((t & (NT - 1)) == NT - 1) vp += vstep;
      }
#pragma unroll
      for (int i = 0; i < 9; ++i) tot[i] = oacc[i];
    }
  }
  const float inv = 1.f / tot[8];
  bf16* orow = O + token * 512 + r * 16;
#pragma unroll
  for (int g = 0; g < 2; ++g) {
    bf16x4 o;
#pragma unroll
    for (int j = 0; j < 4; ++j) o[j] = (bf16)(tot[4 * g + j] * inv);
    *(bf16x4*)(orow + 8 * g + 4 * h) = o;
  }
}

constexpr int K3_LDS = 64 * XS_STRIDE + 65536;

__global__ __launch_bounds__(512) void k3_out(const bf16* __restrict__ O, const bf16* __restrict__ wo,
                                              const bf16* __restrict__ x, const float* __restrict__ st,
                                              const float* __restrict__ upd, const int* __restrict__ sel,
                                              bf16* __restrict__ out) {
  extern __shared__ __attribute__((aligned(16))) char smem[];
  char* ximg = smem;
  char* wimg = smem + 64 * XS_STRIDE;
  const int cb = blockIdx.x >> 5, tb = blockIdx.x & 31;
  const int tid = threadIdx.x, lane = tid & 63;
  const int wave = __builtin_amdgcn_readfirstlane(tid >> 6);
  const int tok0 = tb * 64;
  const int s0 = sel[0], s1 = sel[1];
#pragma unroll
  for (int i = 0; i < 8; ++i) {
    const int row = wave + 8 * i;
    glds16(O + (tok0 + row) * 512 + lane * 8, ximg + row * XS_STRIDE);
  }
  stage_w(wo, cb * 64, wimg, wave, 8, lane);
  const int r = lane & 31, h = lane >> 5;
  const int n0 = (wave & 1) * 32, t0 = ((wave >> 1) & 1) * 32;
  const int tok = tok0 + t0 + r;
  u32x2 xv[4], nv[4];
  if (wave < 4) {
#pragma unroll
    for (int g = 0; g < 4; ++g) xv[g] = *(const u32x2*)(x + tok * 512 + cb * 64 + n0 + 8 * g + 4 * h);
    const f32x2 sx = *(const f32x2*)(st + tok * 2);
    f32x4 u[4];
#pragma unroll
    for (int g = 0; g < 4; ++g) u[g] = f32x4{0.f, 0.f, 0.f, 0.f};
    if (tok == s0 || tok == s1) {
#pragma unroll
      for (int g = 0; g < 4; ++g) u[g] = *(const f32x4*)(upd + (tok == s0 ? 0 : 512) + cb * 64 + n0 + 8 * g + 4 * h);
    }
#pragma unroll
    for (int g = 0; g < 4; ++g) {
#pragma unroll
      for (int jj = 0; jj < 2; ++jj) {
        const unsigned xr = xv[g][jj];
        unsigned nr = ln2(unpack2(xr), f32x2{sx.x, sx.x}, f32x2{sx.y, sx.y});
        nr = pack2(unpack2(nr) + (jj ? f32x2{u[g][2], u[g][3]} : f32x2{u[g][0], u[g][1]}));
        asm volatile("" : "+v"(nr));
        nv[g][jj] = nr;
      }
    }
  }
  __syncthreads();
  if (wave >= 4) return;
  f32x16 acc = gemm_tile(wimg, ximg, n0, t0, lane);
#pragma unroll
  for (int g = 0; g < 4; ++g) {
    u32x2 o;
#pragma unroll
    for (int jj = 0; jj < 2; ++jj) {
      const unsigned xr = xv[g][jj], nr = nv[g][jj];
      const f32x2 a = unpack2(pack2(f32x2{acc[4 * g + 2 * jj], acc[4 * g + 2 * jj + 1]}));
      o[jj] = pack2(unpack2(pack2(unpack2(xr) + a)) + unpack2(nr));
    }
    *(u32x2*)(out + tok * 512 + cb * 64 + n0 + 8 * g + 4 * h) = o;
  }
}

extern "C" void kernel_launch(void* const* d_in, const int* in_sizes, int n_in, void* d_out, int out_size,
                              void* d_ws, size_t ws_size, hipStream_t stream) {
  const bf16* x = (const bf16*)d_in[0];
  const bf16* wq = (const bf16*)d_in[1];
  const bf16* wk = (const bf16*)d_in[2];
  const bf16* wv = (const bf16*)d_in[3];
  const bf16* wo = (const bf16*)d_in[4];
  const bf16* wr = (const bf16*)d_in[6];
  const bf16* wg = (const bf16*)d_in[7];
  const bf16* wu = (const bf16*)d_in[8];
  const bf16* wd = (const bf16*)d_in[9];
  char* ws = (char*)d_ws;
  float* st = (float*)(ws + WS_ST);
  bf16* Q = (bf16*)(ws + WS_Q);
  bf16* Kb = (bf16*)(ws + WS_K);
  bf16* Vf = (bf16*)(ws + WS_V);
  unsigned* cand = (unsigned*)(ws + WS_R);
  bf16* O = (bf16*)(ws + WS_O);
  float* yp = (float*)(ws + WS_YP);
  int* sel = (int*)(ws + WS_SEL);
  unsigned* gcnt = (unsigned*)(ws + WS_CNT);
  float* upd = (float*)(ws + WS_UPD);
  bf16* out = (bf16*)d_out;

  (void)hipFuncSetAttribute((const void*)k1_ln_qkv, hipFuncAttributeMaxDynamicSharedMemorySize, K1_LDS);
  (void)hipFuncSetAttribute((const void*)k2_attn, hipFuncAttributeMaxDynamicSharedMemorySize, ATT_LDS);
  (void)hipFuncSetAttribute((const void*)k3_out, hipFuncAttributeMaxDynamicSharedMemorySize, K3_LDS);

  k1_ln_qkv<<<256, 768, K1_LDS, stream>>>(x, wq, wk, wv, wr, st, Q, Kb, Vf, cand, gcnt);
  k2_attn<<<256, 768, ATT_LDS, stream>>>(Q, Kb, O, x, st, cand, wg, wu, wd, yp, sel, gcnt, upd, wo);
  k3_out<<<256, 512, K3_LDS, stream>>>(O, wo, x, st, upd, sel, out);
}
```

```cpp
#include <hip/hip_runtime.h>
#include <hip/hip_bf16.h>

typedef __attribute__((ext_vector_type(8))) __bf16 bf16x8;
typedef __attribute__((ext_vector_type(4))) __bf16 bf16x4;
typedef __attribute__((ext_vector_type(2))) __bf16 bf16x2;
typedef __attribute__((ext_vector_type(4))) short s16x4;
typedef __attribute__((ext_vector_type(8))) short s16x8;
typedef __attribute__((ext_vector_type(16))) float f32x16;
typedef __attribute__((ext_vector_type(4))) float f32x4;
typedef __attribute__((ext_vector_type(4))) unsigned u32x4;
typedef __attribute__((ext_vector_type(2))) unsigned u32x2;
typedef __attribute__((ext_vector_type(2))) float f32x2;
typedef __bf16 bf16;

#define LDS_AS __attribute__((address_space(3)))
#define GLB_AS __attribute__((address_space(1)))
#define DI __device__ __forceinline__

constexpr int S_ = 2048, D_ = 512, H_ = 32, DH_ = 16, F_ = 1376;

constexpr size_t WS_Q = 0;
constexpr size_t WS_O = WS_Q;
constexpr size_t WS_K = 2u << 20;
constexpr size_t WS_V = WS_K + 65536;
constexpr size_t WS_ST = WS_V + 65536;
constexpr size_t WS_R = WS_ST + 16384;
constexpr size_t WS_SEL = WS_R + 4096;
constexpr size_t WS_CNT = WS_SEL + 4096;
constexpr size_t WS_UPD = WS_CNT + 4096;
constexpr size_t WS_YP = WS_UPD + 4096;

constexpr int NFFN = 172;
constexpr int GSZ = 12, NGRP = 15;

DI float bf2f(bf16 v) { return (float)v; }
DI float rbf(float v) { return (float)(bf16)v; }
DI f32x2 unpack2(unsigned u) { return f32x2{__uint_as_float(u << 16), __uint_as_float(u & 0xffff0000u)}; }
DI unsigned pack2(f32x2 v) {
  unsigned r = __builtin_bit_cast(unsigned, __builtin_convertvector(v, bf16x2));
  asm("" : "+v"(r));
  return r;
}

DI unsigned route_key(float v, int tok) {
  const unsigned u = __float_as_uint(v) >> 16;
  const unsigned o = (u & 0x8000u) ? (~u & 0xffffu) : (u | 0x8000u);
  return (o << 16) | (unsigned)(0xffff - tok);
}
DI int key_tok(unsigned k) { return 0xffff - (int)(k & 0xffffu); }
DI float key_val(unsigned k) {
  const unsigned o = k >> 16;
  const unsigned u = (o & 0x8000u) ? (o & 0x7fffu) : (~o & 0xffffu);
  return __uint_as_float(u << 16);
}
DI unsigned ln2(f32x2 x, f32x2 mb2, f32x2 rinv2) { return pack2(unpack2(pack2(x - mb2)) * rinv2); }
DI void top2_wave(unsigned& k1, unsigned& k2, int width) {
  for (int o = width >> 1; o > 0; o >>= 1) {
    const unsigned a1 = __shfl_xor(k1, o), a2 = __shfl_xor(k2, o);
    k2 = max(min(k1, a1), max(k2, a2));
    k1 = max(k1, a1);
  }
}

DI void glds16(const void* g, void* l) {
  __builtin_amdgcn_global_load_lds((const GLB_AS void*)g, (LDS_AS void*)l, 16, 0, 0);
}

DI void glds16_raw(const void* g, unsigned lds_off) {
  asm volatile("s_mov_b32 m0, %0\n s_nop 0\n global_load_lds_dwordx4 %1, off" ::"s"(lds_off), "v"(g) : "memory", "m0");
}

template <int CTRL>
DI float dpp_add(float v) {
  const int t = __builtin_amdgcn_update_dpp(0, __float_as_int(v), CTRL, 0xf, 0xf, true);
  return v + __int_as_float(t);
}
DI float sum8(float v) {
  v = dpp_add<0xB1>(v);
  v = dpp_add<0x4E>(v);
  v = dpp_add<0x141>(v);
  return v;
}

DI s16x4 tr_read(const char* p) {
  return __builtin_amdgcn_ds_read_tr16_b64_v4i16((LDS_AS s16x4*)p);
}

DI int wimg_off(int row, int ch) { return row * 128 + ((ch ^ (((row >> 1) & 1) << 2)) << 4); }

constexpr int XS_STRIDE = 1040;

DI void stage_w(const bf16* w, int col0, char* wimg, int wave, int nwaves, int lane) {
  for (int p = wave; p < 64; p += nwaves) {
    const int row = p * 8 + (lane >> 3), pc = lane & 7;
    const int ch = pc ^ (((row >> 1) & 1) << 2);
    glds16(w + row * 512 + col0 + ch * 8, wimg + p * 1024);
  }
}

constexpr int GP = 4;
DI f32x16 gemm_tile(const char* wimg, const char* ximg, int n0, int t0, int lane) {
  const int r = lane & 31, h = lane >> 5;
  const int q = (lane & 15) >> 2, p = lane & 3, blk = (lane >> 4) & 1;
  f32x16 acc;
#pragma unroll
  for (int i = 0; i < 16; ++i) acc[i] = 0.f;
  const char* xb = ximg + (t0 + r) * XS_STRIDE + 16 * h;
  const int col = n0 + 16 * blk + 4 * p;
  const int ch = col >> 3, sub = (col & 7) * 2;
  const char* wb0 = wimg + wimg_off(8 * h + q, ch) + sub;
  const char* wb1 = wimg + wimg_off(8 * h + q + 4, ch) + sub;
  s16x4 lo[32], hi[32];
  bf16x8 b[32];
#pragma unroll
  for (int ks = 0; ks < 32 + GP; ++ks) {
    if (ks < 32) {
      lo[ks] = tr_read(wb0 + ks * 2048);
      hi[ks] = tr_read(wb1 + ks * 2048);
      b[ks] = *(const bf16x8*)(xb + ks * 32);
    }
    if (ks >= GP) {
      const s16x8 a = __builtin_shufflevector(lo[ks - GP], hi[ks - GP], 0, 1, 2, 3, 4, 5, 6, 7);
      acc = __builtin_amdgcn_mfma_f32_32x32x16_bf16(__builtin_bit_cast(bf16x8, a), b[ks - GP], acc, 0, 0, 0);
      asm volatile("" : "+v"(acc));
    }
    __builtin_amdgcn_sched_barrier(0);
  }
  return acc;
}
DI f32x16 gemm_tile16(const char* wimg, const char* ximg, int t0, int lane) {
  const int r = lane & 31, h = lane >> 5;
  const int q = (lane & 15) >> 2, p = lane & 3;
  f32x16 acc;
#pragma unroll
  for (int i = 0; i < 16; ++i) acc[i] = 0.f;
  const char* xb = ximg + (t0 + r) * XS_STRIDE + 16 * h;
  const char* wb = wimg + (8 * h + q) * 32 + 8 * p;
  s16x4 lo[32], hi[32];
  bf16x8 b[32];
#pragma unroll
  for (int ks = 0; ks < 32 + GP; ++ks) {
    if (ks < 32) {
      lo[ks] = tr_read(wb + ks * 512);
      hi[ks] = tr_read(wb + ks * 512 + 128);
      b[ks] = *(const bf16x8*)(xb + ks * 32);
    }
    if (ks >= GP) {
      const s16x8 a = __builtin_shufflevector(lo[ks - GP], hi[ks - GP], 0, 1, 2, 3, 4, 5, 6, 7);
      acc = __builtin_amdgcn_mfma_f32_32x32x16_bf16(__builtin_bit_cast(bf16x8, a), b[ks - GP], acc, 0, 0, 0);
      asm volatile("" : "+v"(acc));
    }
    __builtin_amdgcn_sched_barrier(0);
  }
  return acc;
}

constexpr int K1_LDS = 64 * XS_STRIDE + 65536 + 16384 + 256;

__global__ __launch_bounds__(768) void k1_ln_qkv(const bf16* __restrict__ x, const bf16* __restrict__ wq,
                                                 const bf16* __restrict__ wk, const bf16* __restrict__ wv,
                                                 const bf16* __restrict__ wr, char* __restrict__ ws) {
  float* st = (float*)(ws + WS_ST);
  bf16* Q = (bf16*)(ws + WS_Q);
  bf16* Kb = (bf16*)(ws + WS_K);
  bf16* Vf = (bf16*)(ws + WS_V);
  unsigned* cand = (unsigned*)(ws + WS_R);
  unsigned* gcnt = (unsigned*)(ws + WS_CNT);
  extern __shared__ __attribute__((aligned(16))) char smem[];
  char* ximg = smem;
  char* wimg = smem + 64 * XS_STRIDE;
  char* kvimg = smem + 64 * XS_STRIDE + 65536;
  float* rs = (float*)(smem + 64 * XS_STRIDE + 65536 + 16384);
  const int cb = blockIdx.x >> 5, tb = blockIdx.x & 31;
  const int tid = threadIdx.x, lane = tid & 63;
  const int wave = __builtin_amdgcn_readfirstlane(tid >> 6);
  const int tok0 = tb * 64;

  if (wave >= 8) {
    if (blockIdx.x == 255 && tid >= 768 - (NGRP + 1)) gcnt[(767 - tid) * 64] = 0;
    stage_w(wq, cb * 64, wimg, wave - 8, 4, lane);
    if (cb < 2) {
      const bf16* w = cb == 0 ? wk : wv;
#pragma unroll
      for (int i = 0; i < 4; ++i) {
        const int p = wave - 8 + 4 * i;
        glds16((const char*)w + p * 1024 + lane * 16, kvimg + p * 1024);
      }
    }
    __syncthreads();
    if (cb == 3 && wave == 8) {
      unsigned k1 = route_key(rs[lane], tok0 + lane), k2 = 0;
      top2_wave(k1, k2, 64);
      if (lane == 0) *(u32x2*)(cand + tb * 2) = u32x2{k1, k2};
    }
    return;
  }

  const int tl = wave * 8 + (lane >> 3), c8 = lane & 7;
  u32x4 xv[8];
#pragma unroll
  for (int i = 0; i < 8; ++i) xv[i] = *(const u32x4*)(x + (tok0 + tl) * 512 + (c8 + 8 * i) * 8);
  u32x4 wrv[8];
  if (cb == 3) {
#pragma unroll
    for (int i = 0; i < 8; ++i) wrv[i] = *(const u32x4*)(wr + (c8 + 8 * i) * 8);
  }

  f32x2 xf[32];
#pragma unroll
  for (int i = 0; i < 32; ++i) xf[i] = unpack2(xv[i >> 2][i & 3]);
  f32x2 s2 = {0.f, 0.f};
#pragma unroll
  for (int i = 0; i < 32; ++i) s2 += xf[i];
  const float mean = sum8(s2.x + s2.y) * (1.f / 512.f);
  const f32x2 mean2 = {mean, mean};
  f32x2 v2 = {0.f, 0.f};
#pragma unroll
  for (int i = 0; i < 32; ++i) { const f32x2 d = xf[i] - mean2; v2 += d * d; }
  const float vs = sum8(v2.x + v2.y);
  const float mb = rbf(mean);
  const float vb = rbf(vs * (1.f / 512.f));
  const float den = rbf(sqrtf(rbf(vb + 1e-5f)));
  const float rinv = __builtin_amdgcn_rcpf(den);
  const f32x2 mb2 = {mb, mb}, rinv2 = {rinv, rinv};
  u32x4 o[8];
#pragma unroll
  for (int i = 0; i < 8; ++i) {
#pragma unroll
    for (int j = 0; j < 4; ++j) o[i][j] = ln2(xf[4 * i + j], mb2, rinv2);
    *(u32x4*)(ximg + tl * XS_STRIDE + (c8 + 8 * i) * 16) = o[i];
  }
  if (cb == 2 && c8 == 0) *(f32x2*)(st + (tok0 + tl) * 2) = f32x2{mb, rinv};
  if (cb == 3) {
    float rd = 0.f;
#pragma unroll
    for (int i = 0; i < 8; ++i) {
#pragma unroll
      for (int j = 0; j < 4; ++j) {
        const unsigned a = o[i][j], b = wrv[i][j];
        rd = __builtin_amdgcn_fdot2_f32_bf16(__builtin_bit_cast(bf16x2, a), __builtin_bit_cast(bf16x2, b), rd, false);
      }
    }
    rd = sum8(rd);
    if (c8 == 0) rs[tl] = rbf(rd);
  }
  __syncthreads();

  const int r = lane & 31, h = lane >> 5;
  if (wave < 4) {
    const int n0 = (wave & 1) * 32, t0 = (wave >> 1) * 32;
    f32x16 acc = gemm_tile(wimg, ximg, n0, t0, lane);
    const float sc = 0.25f * 1.4426950408889634f;
    bf16* qrow = Q + (tok0 + t0 + r) * 512 + cb * 64 + n0;
#pragma unroll
    for (int g = 0; g < 4; ++g) {
      u32x2 o;
#pragma unroll
      for (int jj = 0; jj < 2; ++jj)
        o[jj] = pack2(unpack2(pack2(f32x2{acc[4 * g + 2 * jj], acc[4 * g + 2 * jj + 1]})) * f32x2{sc, sc});
      *(u32x2*)(qrow + 8 * g + 4 * h) = o;
    }
  } else if (wave < 6 && cb < 2) {
    const int t0 = (wave - 4) * 32;
    f32x16 acc = gemm_tile16(kvimg, ximg, t0, lane);
    const int key = tok0 + t0 + r;
    const int t = key >> 5;
    if (cb == 0) {
#pragma unroll
      for (int g = 0; g < 2; ++g) {
        bf16x4 o;
#pragma unroll
        for (int j = 0; j < 4; ++j) o[j] = (bf16)acc[4 * g + j];
        *(bf16x4*)(Kb + ((t * 2 + g) * 32 + r) * 8 + 4 * h) = o;
      }
    } else {
      const int kk = key & 31, sg = kk >> 4, rem = kk & 15;
      const int hh = (rem >> 2) & 1, jj = ((rem >> 3) << 2) | (rem & 3);
#pragma unroll
      for (int g = 0; g < 2; ++g) {
#pragma unroll
        for (int j = 0; j < 4; ++j) {
          const int d = 8 * g + 4 * h + j;
          Vf[(((t * 2 + sg) * 2 + hh) * 16 + d) * 8 + jj] = (bf16)acc[4 * g + j];
        }
      }
    }
  }
}

constexpr int NT = 16;
constexpr int CST = NT * 1024;
constexpr int ATT_LDS = 65536 * 2 + CST + 64 + 512;
constexpr int NC = 64 / NT;

DI void st_sc1(float* p, f32x2 v) {
  __hip_atomic_store((GLB_AS unsigned long long*)p, __builtin_bit_cast(unsigned long long, v), __ATOMIC_RELAXED,
                     __HIP_MEMORY_SCOPE_AGENT);
}
DI bool last_arrival(int* w, unsigned* gc, int n, int lane) {
  asm volatile("s_waitcnt vmcnt(0)" ::: "memory");
  if (lane == 0) {
    if (__hip_atomic_fetch_add(w, 1, __ATOMIC_RELAXED, __HIP_MEMORY_SCOPE_WORKGROUP) == 3) {
      const unsigned t = __hip_atomic_fetch_add((GLB_AS unsigned*)gc, 1u, __ATOMIC_RELAXED, __HIP_MEMORY_SCOPE_AGENT);
      __hip_atomic_store(w + 1, t == (unsigned)(n - 1) ? 2 : 1, __ATOMIC_RELAXED, __HIP_MEMORY_SCOPE_WORKGROUP);
    }
  }
  int r;
  while ((r = __hip_atomic_load(w + 1, __ATOMIC_RELAXED, __HIP_MEMORY_SCOPE_WORKGROUP)) == 0) __builtin_amdgcn_s_sleep(2);
  asm volatile("" ::: "memory");
  return r == 2;
}

DI void ffn_slice(int f, int j, const bf16* __restrict__ x, const float* __restrict__ st,
                  const unsigned* __restrict__ cand,
                  const bf16* __restrict__ wg, const bf16* __restrict__ wu, const bf16* __restrict__ wd,
                  float* __restrict__ ypart, int* __restrict__ sel, unsigned* __restrict__ gcnt,
                  float* __restrict__ upd, float* red, int* cnt, int lane) {
  const int k0 = j * 128 + lane * 2;
  u32x4 g8[2], u8[2];
  unsigned d2[8];
#pragma unroll
  for (int i = 0; i < 2; ++i) {
    g8[i] = *(const u32x4*)(wg + (k0 + i) * F_ + f * 8);
    u8[i] = *(const u32x4*)(wu + (k0 + i) * F_ + f * 8);
  }
#pragma unroll
  for (int c = 0; c < 8; ++c) d2[c] = *(const unsigned*)(wd + (f * 8 + c) * 512 + k0);
  const u32x2 cv = *(const u32x2*)(cand + (lane & 31) * 2);
  unsigned k1 = cv[0], k2 = cv[1];
  top2_wave(k1, k2, 32);
  const int i1 = key_tok(k1), i2 = key_tok(k2);
  if (f == 0 && j == 0 && lane == 0) *(u32x2*)sel = u32x2{(unsigned)i1, (unsigned)i2};
  const f32x2 sa = *(const f32x2*)(st + i1 * 2), sb = *(const f32x2*)(st + i2 * 2);
  const f32x2 xa = unpack2(ln2(unpack2(*(const unsigned*)(x + i1 * 512 + k0)), f32x2{sa.x, sa.x}, f32x2{sa.y, sa.y}));
  const f32x2 xb = unpack2(ln2(unpack2(*(const unsigned*)(x + i2 * 512 + k0)), f32x2{sb.x, sb.x}, f32x2{sb.y, sb.y}));
  f32x2 v2[16];
#pragma unroll
  for (int i = 0; i < 16; ++i) v2[i] = f32x2{0.f, 0.f};
#pragma unroll
  for (int i = 0; i < 2; ++i) {
    const float x0 = i ? xa.y : xa.x, x1 = i ? xb.y : xb.x;
    const f32x2 x02 = {x0, x0}, x12 = {x1, x1};
#pragma unroll
    for (int q = 0; q < 4; ++q) {
      const unsigned gq = g8[i][q], uq = u8[i][q];
      const f32x2 gw = unpack2(gq), uw = unpack2(uq);
      v2[q] += x02 * gw; v2[4 + q] += x12 * gw; v2[8 + q] += x02 * uw; v2[12 + q] += x12 * uw;
    }
  }
  float v[32];
#pragma unroll
  for (int i = 0; i < 16; ++i) { v[2 * i] = v2[i].x; v[2 * i + 1] = v2[i].y; }
#pragma unroll
  for (int st = 0; st < 5; ++st) {
    const int o = 32 >> st, n = 16 >> st;
    const bool up = (lane & o) != 0;
#pragma unroll
    for (int i = 0; i < n; ++i) {
      const float keep = up ? v[i + n] : v[i];
      const float send = up ? v[i] : v[i + n];
      v[i] = keep + __shfl_xor(send, o);
    }
  }
  v[0] += __shfl_xor(v[0], 1);
  if ((lane & 1) == 0) red[j * 32 + (lane >> 1)] = v[0];
  if (lane == 0) __hip_atomic_fetch_add(cnt, 1, __ATOMIC_RELEASE, __HIP_MEMORY_SCOPE_WORKGROUP);
  while (__hip_atomic_load(cnt, __ATOMIC_ACQUIRE, __HIP_MEMORY_SCOPE_WORKGROUP) < 4) __builtin_amdgcn_s_sleep(1);
  const int l = lane & 15;
  const float g = rbf((red[l] + red[32 + l]) + (red[64 + l] + red[96 + l]));
  const float u = rbf((red[16 + l] + red[48 + l]) + (red[80 + l] + red[112 + l]));
  const float hv = rbf(rbf(g / (1.f + __expf(-g))) * u);
  f32x2 y0 = {0.f, 0.f}, y1 = {0.f, 0.f};
#pragma unroll
  for (int c = 0; c < 8; ++c) {
    const float h0 = __shfl(hv, c), h1 = __shfl(hv, 8 + c);
    const f32x2 w = unpack2(d2[c]);
    y0 += f32x2{h0, h0} * w;
    y1 += f32x2{h1, h1} * w;
  }
  st_sc1(ypart + (size_t)f * 1024 + k0, y0);
  st_sc1(ypart + (size_t)f * 1024 + 512 + k0, y1);
  const int grp = f / GSZ, gs = min(GSZ, NFFN - grp * GSZ);
  if (!last_arrival(cnt + 1, gcnt + grp * 64, gs, lane)) return;
  const __amdgpu_buffer_rsrc_t rs = __builtin_amdgcn_make_buffer_rsrc(ypart, 0, (NFFN + NGRP) * 4096, 0x00020000);
  const int voff = j * 1024 + lane * 16;
  f32x4 a = {0.f, 0.f, 0.f, 0.f};
  {
    u32x4 t[GSZ];
#pragma unroll
    for (int i = 0; i < GSZ; ++i) t[i] = __builtin_amdgcn_raw_buffer_load_b128(rs, voff, (grp * GSZ + min(i, gs - 1)) * 4096, 16);
#pragma unroll
    for (int i = 0; i < GSZ; ++i)
      if (i < gs) a += __builtin_bit_cast(f32x4, t[i]);
  }
  __builtin_amdgcn_raw_buffer_store_b128(__builtin_bit_cast(u32x4, a), rs, voff, (NFFN + grp) * 4096, 16);
  if (!last_arrival(cnt + 3, gcnt + NGRP * 64, NGRP, lane)) return;
  a = f32x4{0.f, 0.f, 0.f, 0.f};
  {
    u32x4 t[NGRP];
#pragma unroll
    for (int i = 0; i < NGRP; ++i) t[i] = __builtin_amdgcn_raw_buffer_load_b128(rs, voff, (NFFN + i) * 4096, 16);
#pragma unroll
    for (int i = 0; i < NGRP; ++i) a += __builtin_bit_cast(f32x4, t[i]);
  }
  const float gate = rbf(1.f / (1.f + __expf(-key_val(j < 2 ? k1 : k2))));
#pragma unroll
  for (int i = 0; i < 4; ++i) a[i] = rbf(rbf(a[i]) * gate);
  *(f32x4*)(upd + j * 256 + lane * 4) = a;
}

__global__ __launch_bounds__(768) void k2_attn(char* __restrict__ ws, const bf16* __restrict__ x,
                                               const bf16* __restrict__ wg, const bf16* __restrict__ wu,
                                               const bf16* __restrict__ wd, const bf16* __restrict__ wo) {
  const bf16* Q = (const bf16*)(ws + WS_Q);
  const bf16* Kb = (const bf16*)(ws + WS_K);
  bf16* O = (bf16*)(ws + WS_O);
  const float* st = (const float*)(ws + WS_ST);
  const unsigned* cand = (const unsigned*)(ws + WS_R);
  float* ypart = (float*)(ws + WS_YP);
  int* sel = (int*)(ws + WS_SEL);
  unsigned* gcnt = (unsigned*)(ws + WS_CNT);
  float* upd = (float*)(ws + WS_UPD);
  extern __shared__ __attribute__((aligned(16))) char smem[];
  char* kl = smem;
  char* vl = smem + 65536;
  char* cst = smem + 131072;
  const int tid = threadIdx.x, lane = tid & 63;
  const int wave = __builtin_amdgcn_readfirstlane(tid >> 6);

  if (wave >= 8) {
    int* cnt = (int*)(smem + 131072 + CST + 32);
    if (tid < 512 + 5) cnt[tid - 512] = 0;
#pragma unroll
    for (int i = 0; i < 26; ++i) {
      const int q = (wave - 8) + 4 * i, piece = q < 48 ? 16 + q : 24 + q;
      glds16_raw((const char*)Kb + piece * 1024 + lane * 16, (unsigned)(size_t)(LDS_AS char*)smem + piece * 1024);
    }
    asm volatile("s_waitcnt lgkmcnt(0)\n s_barrier\n s_waitcnt vmcnt(0)\n s_barrier" ::: "memory");
    if (blockIdx.x < NFFN) {
      __builtin_amdgcn_s_setprio(3);
      ffn_slice(blockIdx.x, wave - 8, x, st, cand, wg, wu, wd, ypart, sel, gcnt, upd,
                (float*)(smem + 131072 + CST + 64), cnt, lane);
    } else if (blockIdx.x >= 176 && blockIdx.x < 240) {
      const char* src = (const char*)wo + ((((blockIdx.x >> 3) - 22) * 4 + (wave - 8)) << 14) + lane * 16;
      u32x4 t[16];
#pragma unroll
      for (int i = 0; i < 16; ++i) t[i] = *(const u32x4*)(src + i * 1024);
#pragma unroll
      for (int i = 0; i < 16; ++i) asm volatile("" ::"v"(t[i]));
    }
    return;
  }

  const int r = lane & 31, h = lane >> 5;
  const int token = ((blockIdx.x >> 6) * 8 + (blockIdx.x & 7)) * 64 + ((blockIdx.x >> 3) & 7) * 8 + wave;
  bf16x8 qf;
  asm volatile("global_load_dwordx4 %0, %1, off" : "=v"(qf) : "v"(Q + token * 512 + r * 16 + 8 * h) : "memory");
  const unsigned lds0 = (unsigned)(size_t)(LDS_AS char*)smem;
#pragma unroll
  for (int i = 0; i < 3; ++i) {
    const int piece = (i < 2) ? (wave + i * 8) : (64 + wave);
    glds16_raw((const char*)Kb + piece * 1024 + lane * 16, lds0 + piece * 1024);
  }
  {
    const unsigned cv = ((tid & 15) == 0 || (tid & 15) == 4) ? 0x3f803f80u : 0u;
#pragma unroll
    for (int i = 0; i < CST / 8192; ++i) *(u32x4*)(cst + i * 8192 + tid * 16) = u32x4{cv, cv, cv, cv};
  }
  asm volatile("s_waitcnt vmcnt(0) lgkmcnt(0)\n s_barrier" ::: "memory");

  const char* kp0 = kl + lane * 16;
  const char* vp0 = (r < 16) ? (vl + (h * 16 + r) * 16) : (cst + (h * 16 + r - 16) * 16);
  const int vstep = (r < 16) ? NT * 1024 : 0;

  f32x16 zero;
#pragma unroll
  for (int i = 0; i < 16; ++i) zero[i] = 0.f;
  constexpr float HEAD = 40.f;
  float m;
  {
    float mx = -INFINITY;
#pragma unroll
    for (int t = 0; t < 4; ++t) {
      bf16x8 kf = *(const bf16x8*)(kp0 + t * 1024);
      f32x16 x = __builtin_amdgcn_mfma_f32_32x32x16_bf16(kf, qf, zero, 0, 0, 0);
#pragma unroll
      for (int i = 0; i < 16; i += 2) mx = fmaxf(fmaxf(mx, x[i]), x[i + 1]);
    }
    mx = fmaxf(mx, __shfl_xor(mx, 32));
    m = floorf(mx) + HEAD;
  }
  f32x16 negm;
#pragma unroll
  for (int i = 0; i < 16; ++i) negm[i] = -m;
  asm volatile("" : "+v"(negm));
  float tot[9];
#pragma unroll
  for (int i = 0; i < 9; ++i) tot[i] = 0.f;
  {
    const char* kp = kp0;
    const char* vp = vp0;
    int c = 0;
    bool redone = false, first = true;
    if (wave < 4) __builtin_amdgcn_s_setprio(2);
    else __builtin_amdgcn_s_setprio(1);
    f32x16 x = __builtin_amdgcn_mfma_f32_32x32x16_bf16(*(const bf16x8*)kp, qf, negm, 0, 0, 0);
    bf16x8 kfn = *(const bf16x8*)(kp + 1024);
    while (true) {
      f32x16 acc;
      while (true) {
        acc = zero;
#pragma unroll
        for (int t = 0; t < NT; ++t) {
          const bf16x8 kf = kfn;
          kfn = *(const bf16x8*)(kp + (t + 2) * 1024);
          const bf16x8 vf0 = *(const bf16x8*)(vp + (t * 2) * 512);
          const bf16x8 vf1 = *(const bf16x8*)(vp + (t * 2 + 1) * 512);
          bf16x8 pf;
#pragma unroll
          for (int j = 0; j < 4; ++j) pf[j] = (bf16)__builtin_amdgcn_exp2f(x[j]);
          __builtin_amdgcn_sched_barrier(0);
          f32x16 xn = __builtin_amdgcn_mfma_f32_32x32x16_bf16(kf, qf, negm, 0, 0, 0);
          asm volatile("" : "+v"(xn));
          __builtin_amdgcn_sched_barrier(0);
#pragma unroll
          for (int j = 4; j < 8; ++j) pf[j] = (bf16)__builtin_amdgcn_exp2f(x[j]);
          acc = __builtin_amdgcn_mfma_f32_32x32x16_bf16(vf0, pf, acc, 0, 0, 0);
#pragma unroll
          for (int j = 0; j < 8; ++j) pf[j] = (bf16)__builtin_amdgcn_exp2f(x[8 + j]);
          acc = __builtin_amdgcn_mfma_f32_32x32x16_bf16(vf1, pf, acc, 0, 0, 0);
          if (t == 7 && first) {
            asm volatile("s_barrier" ::: "memory");
            first = false;
          }
          if (c * NT + t == 53 && wave < 4) __builtin_amdgcn_s_setprio(0);
          __builtin_amdgcn_sched_barrier(0);
          x = xn;
        }
        if (__any(!(tot[8] + acc[8] <= 4.3e9f))) break;
        redone = false;
#pragma unroll
        for (int i = 0; i < 9; ++i) tot[i] += acc[i];
        ++c;
        kp += NT * 1024;
        vp += vstep;
        if (c == NC) break;
      }
      if (c == NC) break;
      if (!redone && __any(!(acc[8] < 7.9e28f))) {
        float mx = -INFINITY;
        for (int t = 0; t < NT; ++t) {
          bf16x8 kf = *(const bf16x8*)(kp + t * 1024);
          f32x16 x = __builtin_amdgcn_mfma_f32_32x32x16_bf16(kf, qf, zero, 0, 0, 0);
#pragma unroll
          for (int i = 0; i < 16; i += 2) mx = fmaxf(fmaxf(mx, x[i]), x[i + 1]);
        }
        mx = fmaxf(mx, __shfl_xor(mx, 32));
        const float mn = fmaxf(m, floorf(mx) + HEAD);
        const int d = (int)fminf(mn - m, 1024.f);
#pragma unroll
        for (int i = 0; i < 9; ++i) tot[i] = __builtin_amdgcn_ldexpf(tot[i], -d);
        m = mn;
        redone = true;
      } else {
        redone = false;
#pragma unroll
        for (int i = 0; i < 9; ++i) tot[i] += acc[i];
        const int e = max(__builtin_amdgcn_frexp_expf(tot[8]) - 1 + (int)HEAD, 0);
#pragma unroll
        for (int i = 0; i < 9; ++i) tot[i] = __builtin_amdgcn_ldexpf(tot[i], -e);
        m += (float)e;
        ++c;
        kp += NT * 1024;
        vp += vstep;
        if (c == NC) break;
      }
#pragma unroll
      for (int i = 0; i < 16; ++i) negm[i] = -m;
      asm volatile("" : "+v"(negm));
      x = __builtin_amdgcn_mfma_f32_32x32x16_bf16(*(const bf16x8*)kp, qf, negm, 0, 0, 0);
      kfn = *(const bf16x8*)(kp + 1024);
    }
  }
  {
    float chk = 0.f;
#pragma unroll
    for (int i = 0; i < 9; ++i) chk = __builtin_fmaf(tot[i], 0.f, chk);
    if (__any(chk != 0.f)) {
      f32x16 oacc = zero;
      float mrun = -INFINITY;
      const char* kp = kp0;
      const char* vp = vp0;
      for (int t = 0; t < 64; ++t) {
        bf16x8 kf = *(const bf16x8*)(kp);
        f32x16 x = __builtin_amdgcn_mfma_f32_32x32x16_bf16(kf, qf, zero, 0, 0, 0);
        float mx = x[0];
#pragma unroll
        for (int i = 1; i < 16; ++i) mx = fmaxf(mx, x[i]);
        mx = fmaxf(mx, __shfl_xor(mx, 32));
        const float mn = fmaxf(mrun, mx);
        const float al = __builtin_amdgcn_exp2f(mrun - mn);
        mrun = mn;
#pragma unroll
        for (int i = 0; i < 16; ++i) oacc[i] *= al;
#pragma unroll
        for (int s = 0; s < 2; ++s) {
          bf16x8 pf;
#pragma unroll
          for (int j = 0; j < 8; ++j) pf[j] = (bf16)__builtin_amdgcn_exp2f(x[8 * s + j] - mn);
          bf16x8 vf = *(const bf16x8*)(vp + ((t & (NT - 1)) * 2 + s) * 512);
          oacc = __builtin_amdgcn_mfma_f32_32x32x16_bf16(vf, pf, oacc, 0, 0, 0);
        }
        kp += 1024;
        if ((t & (NT - 1)) == NT - 1) vp += vstep;
      }
#pragma unroll
      for (int i = 0; i < 9; ++i) tot[i] = oacc[i];
    }
  }
  const float inv = 1.f / tot[8];
  bf16* orow = O + token * 512 + r * 16;
#pragma unroll
  for (int g = 0; g < 2; ++g) {
    bf16x4 o;
#pragma unroll
    for (int j = 0; j < 4; ++j) o[j] = (bf16)(tot[4 * g + j] * inv);
    *(bf16x4*)(orow + 8 * g + 4 * h) = o;
  }
}

constexpr int K3_LDS = 64 * XS_STRIDE + 65536;

__global__ __launch_bounds__(512) void k3_out(const char* __restrict__ ws, const bf16* __restrict__ wo,
                                              const bf16* __restrict__ x, bf16* __restrict__ out) {
  const bf16* O = (const bf16*)(ws + WS_O);
  const float* st = (const float*)(ws + WS_ST);
  const float* upd = (const float*)(ws + WS_UPD);
  const int* sel = (const int*)(ws + WS_SEL);
  extern __shared__ __attribute__((aligned(16))) char smem[];
  char* ximg = smem;
  char* wimg = smem + 64 * XS_STRIDE;
  const int cb = blockIdx.x >> 5, tb = blockIdx.x & 31;
  const int tid = threadIdx.x, lane = tid & 63;
  const int wave = __builtin_amdgcn_readfirstlane(tid >> 6);
  const int tok0 = tb * 64;
  const int s0 = sel[0], s1 = sel[1];
#pragma unroll
  for (int i = 0; i < 8; ++i) {
    const int row = wave + 8 * i;
    glds16(O + (tok0 + row) * 512 + lane * 8, ximg + row * XS_STRIDE);
  }
  stage_w(wo, cb * 64, wimg, wave, 8, lane);
  const int r = lane & 31, h = lane >> 5;
  const int n0 = (wave & 1) * 32, t0 = ((wave >> 1) & 1) * 32;
  const int tok = tok0 + t0 + r;
  u32x2 xv[4], nv[4];
  if (wave < 4) {
#pragma unroll
    for (int g = 0; g < 4; ++g) xv[g] = *(const u32x2*)(x + tok * 512 + cb * 64 + n0 + 8 * g + 4 * h);
    const f32x2 sx = *(const f32x2*)(st + tok * 2);
    f32x4 u[4];
#pragma unroll
    for (int g = 0; g < 4; ++g) u[g] = f32x4{0.f, 0.f, 0.f, 0.f};
    if (tok == s0 || tok == s1) {
#pragma unroll
      for (int g = 0; g < 4; ++g) u[g] = *(const f32x4*)(upd + (tok == s0 ? 0 : 512) + cb * 64 + n0 + 8 * g + 4 * h);
    }
#pragma unroll
    for (int g = 0; g < 4; ++g) {
#pragma unroll
      for (int jj = 0; jj < 2; ++jj) {
        const unsigned xr = xv[g][jj];
        unsigned nr = ln2(unpack2(xr), f32x2{sx.x, sx.x}, f32x2{sx.y, sx.y});
        nr = pack2(unpack2(nr) + (jj ? f32x2{u[g][2], u[g][3]} : f32x2{u[g][0], u[g][1]}));
        asm volatile("" : "+v"(nr));
        nv[g][jj] = nr;
      }
    }
  }
  __syncthreads();
  if (wave >= 4) return;
  f32x16 acc = gemm_tile(wimg, ximg, n0, t0, lane);
#pragma unroll
  for (int g = 0; g < 4; ++g) {
    u32x2 o;
#pragma unroll
    for (int jj = 0; jj < 2; ++jj) {
      const unsigned xr = xv[g][jj], nr = nv[g][jj];
      const f32x2 a = unpack2(pack2(f32x2{acc[4 * g + 2 * jj], acc[4 * g + 2 * jj + 1]}));
      o[jj] = pack2(unpack2(pack2(unpack2(xr) + a)) + unpack2(nr));
    }
    *(u32x2*)(out + tok * 512 + cb * 64 + n0 + 8 * g + 4 * h) = o;
  }
}

extern "C" void kernel_launch(void* const* d_in, const int* in_sizes, int n_in, void* d_out, int out_size,
                              void* d_ws, size_t ws_size, hipStream_t stream) {
  const bf16* x = (const bf16*)d_in[0];
  const bf16* wq = (const bf16*)d_in[1];
  const bf16* wk = (const bf16*)d_in[2];
  const bf16* wv = (const bf16*)d_in[3];
  const bf16* wo = (const bf16*)d_in[4];
  const bf16* wr = (const bf16*)d_in[6];
  const bf16* wg = (const bf16*)d_in[7];
  const bf16* wu = (const bf16*)d_in[8];
  const bf16* wd = (const bf16*)d_in[9];
  char* ws = (char*)d_ws;
  bf16* out = (bf16*)d_out;

  (void)hipFuncSetAttribute((const void*)k1_ln_qkv, hipFuncAttributeMaxDynamicSharedMemorySize, K1_LDS);
  (void)hipFuncSetAttribute((const void*)k2_attn, hipFuncAttributeMaxDynamicSharedMemorySize, ATT_LDS);
  (void)hipFuncSetAttribute((const void*)k3_out, hipFuncAttributeMaxDynamicSharedMemorySize, K3_LDS);

  k1_ln_qkv<<<256, 768, K1_LDS, stream>>>(x, wq, wk, wv, wr, ws);
  k2_attn<<<256, 768, ATT_LDS, stream>>>(ws, x, wg, wu, wd, wo);
  k3_out<<<256, 512, K3_LDS, stream>>>(ws, wo, x, out);
}
```

```cpp
#include <hip/hip_runtime.h>
#include <hip/hip_bf16.h>

typedef __attribute__((ext_vector_type(8))) __bf16 bf16x8;
typedef __attribute__((ext_vector_type(4))) __bf16 bf16x4;
typedef __attribute__((ext_vector_type(2))) __bf16 bf16x2;
typedef __attribute__((ext_vector_type(4))) short s16x4;
typedef __attribute__((ext_vector_type(8))) short s16x8;
typedef __attribute__((ext_vector_type(16))) float f32x16;
typedef __attribute__((ext_vector_type(4))) float f32x4;
typedef __attribute__((ext_vector_type(4))) unsigned u32x4;
typedef __attribute__((ext_vector_type(2))) unsigned u32x2;
typedef __attribute__((ext_vector_type(2))) float f32x2;
typedef __bf16 bf16;

#define LDS_AS __attribute__((address_space(3)))
#define GLB_AS __attribute__((address_space(1)))
#define DI __device__ __forceinline__

constexpr int S_ = 2048, D_ = 512, H_ = 32, DH_ = 16, F_ = 1376;

constexpr size_t WS_Q = 0;
constexpr size_t WS_O = WS_Q;
constexpr size_t WS_K = 2u << 20;
constexpr size_t WS_V = WS_K + 65536;
constexpr size_t WS_ST = WS_V + 65536;
constexpr size_t WS_R = WS_ST + 16384;
constexpr size_t WS_SEL = WS_R + 4096;
constexpr size_t WS_CNT = WS_SEL + 4096;
constexpr size_t WS_UPD = WS_CNT + 4096;
constexpr size_t WS_YP = WS_UPD + 4096;

constexpr int NFFN = 172;
constexpr int GSZ = 12, NGRP = 15;

DI float bf2f(bf16 v) { return (float)v; }
DI float rbf(float v) { return (float)(bf16)v; }
DI f32x2 unpack2(unsigned u) { return f32x2{__uint_as_float(u << 16), __uint_as_float(u & 0xffff0000u)}; }
DI unsigned pack2(f32x2 v) {
  unsigned r = __builtin_bit_cast(unsigned, __builtin_convertvector(v, bf16x2));
  asm("" : "+v"(r));
  return r;
}

DI unsigned route_key(float v, int tok) {
  const unsigned u = __float_as_uint(v) >> 16;
  const unsigned o = (u & 0x8000u) ? (~u & 0xffffu) : (u | 0x8000u);
  return (o << 16) | (unsigned)(0xffff - tok);
}
DI int key_tok(unsigned k) { return 0xffff - (int)(k & 0xffffu); }
DI float key_val(unsigned k) {
  const unsigned o = k >> 16;
  const unsigned u = (o & 0x8000u) ? (o & 0x7fffu) : (~o & 0xffffu);
  return __uint_as_float(u << 16);
}
DI unsigned ln2(f32x2 x, f32x2 mb2, f32x2 rinv2) { return pack2(unpack2(pack2(x - mb2)) * rinv2); }
DI void top2_wave(unsigned& k1, unsigned& k2, int width) {
  for (int o = width >> 1; o > 0; o >>= 1) {
    const unsigned a1 = __shfl_xor(k1, o), a2 = __shfl_xor(k2, o);
    k2 = max(min(k1, a1), max(k2, a2));
    k1 = max(k1, a1);
  }
}

template <int O>
DI void top2_step(unsigned& k1, unsigned& k2) {
  const unsigned a1 = __builtin_amdgcn_ds_swizzle(k1, (O << 10) | 31), a2 = __builtin_amdgcn_ds_swizzle(k2, (O << 10) | 31);
  k2 = max(min(k1, a1), max(k2, a2));
  k1 = max(k1, a1);
}

DI void glds16(const void* g, void* l) {
  __builtin_amdgcn_global_load_lds((const GLB_AS void*)g, (LDS_AS void*)l, 16, 0, 0);
}

DI void glds16_raw(const void* g, unsigned lds_off) {
  asm volatile("s_mov_b32 m0, %0\n s_nop 0\n global_load_lds_dwordx4 %1, off" ::"s"(lds_off), "v"(g) : "memory", "m0");
}

template <int CTRL>
DI float dpp_add(float v) {
  const int t = __builtin_amdgcn_update_dpp(0, __float_as_int(v), CTRL, 0xf, 0xf, true);
  return v + __int_as_float(t);
}
DI float sum8(float v) {
  v = dpp_add<0xB1>(v);
  v = dpp_add<0x4E>(v);
  v = dpp_add<0x141>(v);
  return v;
}

DI s16x4 tr_read(const char* p) {
  return __builtin_amdgcn_ds_read_tr16_b64_v4i16((LDS_AS s16x4*)p);
}

DI int wimg_off(int row, int ch) { return row * 128 + ((ch ^ (((row >> 1) & 1) << 2)) << 4); }

constexpr int XS_STRIDE = 1040;

DI void stage_w(const bf16* w, int col0, char* wimg, int wave, int nwaves, int lane) {
  for (int p = wave; p < 64; p += nwaves) {
    const int row = p * 8 + (lane >> 3), pc = lane & 7;
    const int ch = pc ^ (((row >> 1) & 1) << 2);
    glds16(w + row * 512 + col0 + ch * 8, wimg + p * 1024);
  }
}

constexpr int GP = 4;
DI f32x16 gemm_tile(const char* wimg, const char* ximg, int n0, int t0, int lane) {
  const int r = lane & 31, h = lane >> 5;
  const int q = (lane & 15) >> 2, p = lane & 3, blk = (lane >> 4) & 1;
  f32x16 acc;
#pragma unroll
  for (int i = 0; i < 16; ++i) acc[i] = 0.f;
  const char* xb = ximg + (t0 + r) * XS_STRIDE + 16 * h;
  const int col = n0 + 16 * blk + 4 * p;
  const int ch = col >> 3, sub = (col & 7) * 2;
  const char* wb0 = wimg + wimg_off(8 * h + q, ch) + sub;
  const char* wb1 = wimg + wimg_off(8 * h + q + 4, ch) + sub;
  s16x4 lo[32], hi[32];
  bf16x8 b[32];
#pragma unroll
  for (int ks = 0; ks < 32 + GP; ++ks) {
    if (ks < 32) {
      lo[ks] = tr_read(wb0 + ks * 2048);
      hi[ks] = tr_read(wb1 + ks * 2048);
      b[ks] = *(const bf16x8*)(xb + ks * 32);
    }
    if (ks >= GP) {
      const s16x8 a = __builtin_shufflevector(lo[ks - GP], hi[ks - GP], 0, 1, 2, 3, 4, 5, 6, 7);
      acc = __builtin_amdgcn_mfma_f32_32x32x16_bf16(__builtin_bit_cast(bf16x8, a), b[ks - GP], acc, 0, 0, 0);
      asm volatile("" : "+v"(acc));
    }
    __builtin_amdgcn_sched_barrier(0);
  }
  return acc;
}
DI f32x16 gemm_tile16(const char* wimg, const char* ximg, int t0, int lane) {
  const int r = lane & 31, h = lane >> 5;
  const int q = (lane & 15) >> 2, p = lane & 3;
  f32x16 acc;
#pragma unroll
  for (int i = 0; i < 16; ++i) acc[i] = 0.f;
  const char* xb = ximg + (t0 + r) * XS_STRIDE + 16 * h;
  const char* wb = wimg + (8 * h + q) * 32 + 8 * p;
  s16x4 lo[32], hi[32];
  bf16x8 b[32];
#pragma unroll
  for (int ks = 0; ks < 32 + GP; ++ks) {
    if (ks < 32) {
      lo[ks] = tr_read(wb + ks * 512);
      hi[ks] = tr_read(wb + ks * 512 + 128);
      b[ks] = *(const bf16x8*)(xb + ks * 32);
    }
    if (ks >= GP) {
      const s16x8 a = __builtin_shufflevector(lo[ks - GP], hi[ks - GP], 0, 1, 2, 3, 4, 5, 6, 7);
      acc = __builtin_amdgcn_mfma_f32_32x32x16_bf16(__builtin_bit_cast(bf16x8, a), b[ks - GP], acc, 0, 0, 0);
      asm volatile("" : "+v"(acc));
    }
    __builtin_amdgcn_sched_barrier(0);
  }
  return acc;
}

#define CODE_ADDR(sym, out)                                                                               \
  asm volatile("s_getpc_b64 s[20:21]\n s_add_u32 s20, s20, " #sym "@rel32@lo+4\n s_addc_u32 s21, s21, " #sym \
               "@rel32@hi+12\n s_mov_b64 %0, s[20:21]"                                                    \
               : "=s"(out)::"s20", "s21", "scc")

constexpr int NT = 16;
constexpr int CST = NT * 1024;
constexpr int ATT_LDS = 65536 * 2 + CST + 64 + 512 + 4 * 640;
constexpr int NC = 64 / NT;

DI bool last_arrival(int* w, unsigned* gc, int n, int lane) {
  asm volatile("s_waitcnt vmcnt(0)" ::: "memory");
  if (lane == 0) {
    if (__hip_atomic_fetch_add(w, 1, __ATOMIC_RELAXED, __HIP_MEMORY_SCOPE_WORKGROUP) == 3) {
      const unsigned t = __hip_atomic_fetch_add((GLB_AS unsigned*)gc, 1u, __ATOMIC_RELAXED, __HIP_MEMORY_SCOPE_AGENT);
      __hip_atomic_store(w + 1, t == (unsigned)(n - 1) ? 2 : 1, __ATOMIC_RELAXED, __HIP_MEMORY_SCOPE_WORKGROUP);
    }
  }
  int r;
  while ((r = __hip_atomic_load(w + 1, __ATOMIC_RELAXED, __HIP_MEMORY_SCOPE_WORKGROUP)) == 0) __builtin_amdgcn_s_sleep(2);
  asm volatile("" ::: "memory");
  return r == 2;
}

DI void ffn_slice(int f, int j, const bf16* __restrict__ x, const float* __restrict__ st,
                  const unsigned* __restrict__ cand,
                  const bf16* __restrict__ wg, const bf16* __restrict__ wu, const bf16* __restrict__ wd,
                  float* __restrict__ ypart, int* __restrict__ sel, unsigned* __restrict__ gcnt,
                  float* __restrict__ upd, float* red, int* cnt, char* scr, int lane) {
  const int k0 = j * 128 + lane * 2;
  const int r = lane & 31, h = lane >> 5;
  const int kr = (lane & 51) | ((lane & 4) << 1) | ((lane & 8) >> 1);
  u32x4 wf[4];
  unsigned d2[8];
  {
    const __amdgpu_buffer_rsrc_t rg = __builtin_amdgcn_make_buffer_rsrc((void*)wg, 0, 512 * F_ * 2, 0x00020000);
    const __amdgpu_buffer_rsrc_t ru = __builtin_amdgcn_make_buffer_rsrc((void*)wu, 0, 512 * F_ * 2, 0x00020000);
    const __amdgpu_buffer_rsrc_t rd = __builtin_amdgcn_make_buffer_rsrc((void*)wd, 0, 512 * F_ * 2, 0x00020000);
    const int wo_ = kr * (F_ * 2), so = (j * 128 * F_ + f * 8) * 2;
#pragma unroll
    for (int t = 0; t < 2; ++t) {
      wf[2 * t] = __builtin_amdgcn_raw_buffer_load_b128(rg, wo_, so + t * (64 * F_ * 2), 0);
      wf[2 * t + 1] = __builtin_amdgcn_raw_buffer_load_b128(ru, wo_, so + t * (64 * F_ * 2), 0);
    }
#pragma unroll
    for (int c = 0; c < 8; ++c)
      d2[c] = __builtin_amdgcn_raw_buffer_load_b32(rd, 8 * r + 4 * h + c * 1024, (f * 8 * 512 + j * 128) * 2, 0);
  }
  const u32x2 cv = *(const u32x2*)(cand + (lane & 31) * 2);
  unsigned k1 = cv[0], k2 = cv[1];
  top2_step<16>(k1, k2);
  top2_step<8>(k1, k2);
  top2_step<4>(k1, k2);
  top2_step<2>(k1, k2);
  top2_step<1>(k1, k2);
  k1 = __builtin_amdgcn_readfirstlane(k1);
  k2 = __builtin_amdgcn_readfirstlane(k2);
  const int i1 = key_tok(k1), i2 = key_tok(k2);
  if (f == 0 && j == 0 && lane == 0) *(u32x2*)sel = u32x2{(unsigned)i1, (unsigned)i2};
  const f32x2 sa = *(const f32x2*)(st + i1 * 2), sb = *(const f32x2*)(st + i2 * 2);
  *(unsigned*)(scr + lane * 4) = ln2(unpack2(*(const unsigned*)(x + i1 * 512 + k0)), f32x2{sa.x, sa.x}, f32x2{sa.y, sa.y});
  *(unsigned*)(scr + 256 + lane * 4) = ln2(unpack2(*(const unsigned*)(x + i2 * 512 + k0)), f32x2{sb.x, sb.x}, f32x2{sb.y, sb.y});
  f32x16 zero;
#pragma unroll
  for (int i = 0; i < 16; ++i) zero[i] = 0.f;
  u32x4 ida, idb;
  {
    const int d = r - 8 * h;
    const unsigned one = 0x3f80u << ((d & 1) << 4);
#pragma unroll
    for (int q = 0; q < 4; ++q) {
      ida[q] = (d >> 1) == q ? one : 0u;
      idb[q] = (d >> 1) == q + 8 ? one : 0u;
    }
  }
  f32x16 gu = zero;
#pragma unroll
  for (int t = 0; t < 2; ++t) {
    f32x16 wt = __builtin_amdgcn_mfma_f32_32x32x16_bf16(__builtin_bit_cast(bf16x8, wf[2 * t]),
                                                        __builtin_bit_cast(bf16x8, ida), zero, 0, 0, 0);
    wt = __builtin_amdgcn_mfma_f32_32x32x16_bf16(__builtin_bit_cast(bf16x8, wf[2 * t + 1]),
                                                 __builtin_bit_cast(bf16x8, idb), wt, 0, 0, 0);
#pragma unroll
    for (int q = 0; q < 2; ++q) {
      u32x4 bw;
#pragma unroll
      for (int i = 0; i < 4; ++i) bw[i] = pack2(f32x2{wt[8 * q + 2 * i], wt[8 * q + 2 * i + 1]});
      const bf16x8 xf = *(const bf16x8*)(scr + (r & 1) * 256 + (r & 2) * 32 + (64 * t + 16 * q + 8 * h) * 2);
      gu = __builtin_amdgcn_mfma_f32_32x32x16_bf16(xf, __builtin_bit_cast(bf16x8, bw), gu, 0, 0, 0);
    }
  }
  {
    const float g0 = dpp_add<0x128>((lane & 8) ? gu[2] : gu[0]), g1 = dpp_add<0x128>((lane & 8) ? gu[3] : gu[1]);
    if (lane < 32) red[j * 32 + (lane & 16) + (lane & 15)] = (lane & 8) ? g1 : g0;
  }
  if (lane == 0) __hip_atomic_fetch_add(cnt, 1, __ATOMIC_RELEASE, __HIP_MEMORY_SCOPE_WORKGROUP);
  while (__hip_atomic_load(cnt, __ATOMIC_ACQUIRE, __HIP_MEMORY_SCOPE_WORKGROUP) < 4) __builtin_amdgcn_s_sleep(1);
  const int l = lane & 15;
  const float g = rbf((red[l] + red[32 + l]) + (red[64 + l] + red[96 + l]));
  const float u = rbf((red[16 + l] + red[48 + l]) + (red[80 + l] + red[112 + l]));
  const float hv = rbf(g * __builtin_amdgcn_rcpf(1.f + __expf(-g))) * u;
  if (lane < 24) *(bf16*)(scr + 512 + lane * 2) = (bf16)(lane < 16 ? hv : 0.f);
  const bf16x8 hf = *(const bf16x8*)(scr + 512 + ((r >> 1) == h ? (r & 1) * 16 : 32));
  u32x4 da, db;
#pragma unroll
  for (int c = 0; c < 4; ++c) {
    da[c] = __builtin_amdgcn_perm(d2[2 * c + 1], d2[2 * c], 0x05040100u);
    db[c] = __builtin_amdgcn_perm(d2[2 * c + 1], d2[2 * c], 0x07060302u);
  }
  const f32x16 ya = __builtin_amdgcn_mfma_f32_32x32x16_bf16(hf, __builtin_bit_cast(bf16x8, da), zero, 0, 0, 0);
  const f32x16 yb = __builtin_amdgcn_mfma_f32_32x32x16_bf16(hf, __builtin_bit_cast(bf16x8, db), zero, 0, 0, 0);
  const __amdgpu_buffer_rsrc_t rs = __builtin_amdgcn_make_buffer_rsrc(ypart, 0, (NFFN + NGRP) * 4096, 0x00020000);
  if (lane < 32) {
#pragma unroll
    for (int t = 0; t < 2; ++t)
      __builtin_amdgcn_raw_buffer_store_b128(__builtin_bit_cast(u32x4, f32x4{ya[t], yb[t], ya[2 + t], yb[2 + t]}), rs,
                                             t * 2048 + j * 512 + lane * 16, f * 4096, 16);
  }
  const int grp = f / GSZ, gs = min(GSZ, NFFN - grp * GSZ);
  if (!last_arrival(cnt + 1, gcnt + grp * 64, gs, lane)) return;
  const int voff = j * 1024 + lane * 16;
  f32x4 a = {0.f, 0.f, 0.f, 0.f};
  {
    u32x4 t[GSZ];
#pragma unroll
    for (int i = 0; i < GSZ; ++i) t[i] = __builtin_amdgcn_raw_buffer_load_b128(rs, voff, (grp * GSZ + min(i, gs - 1)) * 4096, 16);
#pragma unroll
    for (int i = 0; i < GSZ; ++i)
      if (i < gs) a += __builtin_bit_cast(f32x4, t[i]);
  }
  __builtin_amdgcn_raw_buffer_store_b128(__builtin_bit_cast(u32x4, a), rs, voff, (NFFN + grp) * 4096, 16);
  if (!last_arrival(cnt + 3, gcnt + NGRP * 64, NGRP, lane)) return;
  a = f32x4{0.f, 0.f, 0.f, 0.f};
  {
    u32x4 t[NGRP];
#pragma unroll
    for (int i = 0; i < NGRP; ++i) t[i] = __builtin_amdgcn_raw_buffer_load_b128(rs, voff, (NFFN + i) * 4096, 16);
#pragma unroll
    for (int i = 0; i < NGRP; ++i) a += __builtin_bit_cast(f32x4, t[i]);
  }
  const float gate = rbf(1.f / (1.f + __expf(-key_val(j < 2 ? k1 : k2))));
#pragma unroll
  for (int i = 0; i < 4; ++i) a[i] = rbf(rbf(a[i]) * gate);
  *(f32x4*)(upd + j * 256 + lane * 4) = a;
}

extern "C" __global__ __launch_bounds__(768) void k2_attn(char* __restrict__ ws, const bf16* __restrict__ x,
                                               const bf16* __restrict__ wg, const bf16* __restrict__ wu,
                                               const bf16* __restrict__ wd, const bf16* __restrict__ wo,
                                               const bf16* __restrict__ out) {
  const bf16* Q = (const bf16*)(ws + WS_Q);
  const bf16* Kb = (const bf16*)(ws + WS_K);
  bf16* O = (bf16*)(ws + WS_O);
  const float* st = (const float*)(ws + WS_ST);
  const unsigned* cand = (const unsigned*)(ws + WS_R);
  float* ypart = (float*)(ws + WS_YP);
  int* sel = (int*)(ws + WS_SEL);
  unsigned* gcnt = (unsigned*)(ws + WS_CNT);
  float* upd = (float*)(ws + WS_UPD);
  extern __shared__ __attribute__((aligned(16))) char smem[];
  char* kl = smem;
  char* vl = smem + 65536;
  char* cst = smem + 131072;
  const int tid = threadIdx.x, lane = tid & 63;
  const int wave = __builtin_amdgcn_readfirstlane(tid >> 6);

  if (wave >= 8) {
    int* cnt = (int*)(smem + 131072 + CST + 32);
    if (tid < 512 + 5) cnt[tid - 512] = 0;
#pragma unroll
    for (int i = 0; i < 26; ++i) {
      const int q = (wave - 8) + 4 * i, piece = q < 48 ? 16 + q : 24 + q;
      glds16_raw((const char*)Kb + piece * 1024 + lane * 16, (unsigned)(size_t)(LDS_AS char*)smem + piece * 1024);
    }
    asm volatile("s_waitcnt lgkmcnt(0)\n s_barrier\n s_waitcnt vmcnt(0)\n s_barrier" ::: "memory");
    unsigned tch = 0;
    if (wave == 11 && lane < 3) {
      const size_t o = (size_t)(blockIdx.x & 31) * 65536;
      tch = *(const volatile unsigned*)(lane == 0 ? (const char*)out + o : lane == 1 ? (const char*)x + o : (const char*)wo + (o >> 2));
    }
    const int fs = (blockIdx.x & 7) * 22 + (blockIdx.x >> 3);
    if (blockIdx.x < 176 && fs < NFFN) {
      __builtin_amdgcn_s_setprio(3);
      ffn_slice(fs, wave - 8, x, st, cand, wg, wu, wd, ypart, sel, gcnt, upd,
                (float*)(smem + 131072 + CST + 64), cnt, smem + 131072 + CST + 576 + (wave - 8) * 640, lane);
    } else if (blockIdx.x >= 176 && blockIdx.x < 240) {
      const char* src = (const char*)wo + ((((blockIdx.x >> 3) - 22) * 4 + (wave - 8)) << 14) + lane * 16;
      u32x4 t[16];
#pragma unroll
      for (int i = 0; i < 16; ++i) t[i] = *(const u32x4*)(src + i * 1024);
#pragma unroll
      for (int i = 0; i < 16; ++i) asm volatile("" ::"v"(t[i]));
    }
    asm volatile("" ::"v"(tch));
    return;
  }

  const int r = lane & 31, h = lane >> 5;
  const int token = ((blockIdx.x >> 6) * 8 + (blockIdx.x & 7)) * 64 + ((blockIdx.x >> 3) & 7) * 8 + wave;
  bf16x8 qf;
  asm volatile("global_load_dwordx4 %0, %1, off" : "=v"(qf) : "v"(Q + token * 512 + r * 16 + 8 * h) : "memory");
  const unsigned lds0 = (unsigned)(size_t)(LDS_AS char*)smem;
#pragma unroll
  for (int i = 0; i < 3; ++i) {
    const int piece = (i < 2) ? (wave + i * 8) : (64 + wave);
    glds16_raw((const char*)Kb + piece * 1024 + lane * 16, lds0 + piece * 1024);
  }
  {
    const unsigned cv = ((tid & 15) == 0 || (tid & 15) == 4) ? 0x3f803f80u : 0u;
#pragma unroll
    for (int i = 0; i < CST / 8192; ++i) *(u32x4*)(cst + i * 8192 + tid * 16) = u32x4{cv, cv, cv, cv};
  }
  asm volatile("s_waitcnt vmcnt(0) lgkmcnt(0)\n s_barrier" ::: "memory");

  const char* kp0 = kl + lane * 16;
  const char* vp0 = (r < 16) ? (vl + (h * 16 + r) * 16) : (cst + (h * 16 + r - 16) * 16);
  const int vstep = (r < 16) ? NT * 1024 : 0;

  f32x16 zero;
#pragma unroll
  for (int i = 0; i < 16; ++i) zero[i] = 0.f;
  constexpr float HEAD = 40.f;
  float m;
  {
    float mx = -INFINITY;
#pragma unroll
    for (int t = 0; t < 4; ++t) {
      bf16x8 kf = *(const bf16x8*)(kp0 + t * 1024);
      f32x16 x = __builtin_amdgcn_mfma_f32_32x32x16_bf16(kf, qf, zero, 0, 0, 0);
#pragma unroll
      for (int i = 0; i < 16; i += 2) mx = fmaxf(fmaxf(mx, x[i]), x[i + 1]);
    }
    mx = fmaxf(mx, __shfl_xor(mx, 32));
    m = floorf(mx) + HEAD;
  }
  f32x16 negm;
#pragma unroll
  for (int i = 0; i < 16; ++i) negm[i] = -m;
  asm volatile("" : "+v"(negm));
  float tot[9];
#pragma unroll
  for (int i = 0; i < 9; ++i) tot[i] = 0.f;
  {
    const char* kp = kp0;
    const char* vp = vp0;
    int c = 0;
    bool redone = false, first = true;
    if (wave < 4) __builtin_amdgcn_s_setprio(2);
    else __builtin_amdgcn_s_setprio(1);
    f32x16 x = __builtin_amdgcn_mfma_f32_32x32x16_bf16(*(const bf16x8*)kp, qf, negm, 0, 0, 0);
    bf16x8 kfn = *(const bf16x8*)(kp + 1024);
    while (true) {
      f32x16 acc;
      while (true) {
        acc = zero;
#pragma unroll
        for (int t = 0; t < NT; ++t) {
          const bf16x8 kf = kfn;
          kfn = *(const bf16x8*)(kp + (t + 2) * 1024);
          const bf16x8 vf0 = *(const bf16x8*)(vp + (t * 2) * 512);
          const bf16x8 vf1 = *(const bf16x8*)(vp + (t * 2 + 1) * 512);
          bf16x8 pf;
#pragma unroll
          for (int j = 0; j < 4; ++j) pf[j] = (bf16)__builtin_amdgcn_exp2f(x[j]);
          __builtin_amdgcn_sched_barrier(0);
          f32x16 xn = __builtin_amdgcn_mfma_f32_32x32x16_bf16(kf, qf, negm, 0, 0, 0);
          asm volatile("" : "+v"(xn));
          __builtin_amdgcn_sched_barrier(0);
#pragma unroll
          for (int j = 4; j < 8; ++j) pf[j] = (bf16)__builtin_amdgcn_exp2f(x[j]);
          acc = __builtin_amdgcn_mfma_f32_32x32x16_bf16(vf0, pf, acc, 0, 0, 0);
#pragma unroll
          for (int j = 0; j < 8; ++j) pf[j] = (bf16)__builtin_amdgcn_exp2f(x[8 + j]);
          acc = __builtin_amdgcn_mfma_f32_32x32x16_bf16(vf1, pf, acc, 0, 0, 0);
          if (t == 7 && first) {
            asm volatile("s_barrier" ::: "memory");
            first = false;
          }
          if (c * NT + t == 53 && wave < 4) __builtin_amdgcn_s_setprio(0);
          __builtin_amdgcn_sched_barrier(0);
          x = xn;
        }
        if (__any(!(tot[8] + acc[8] <= 4.3e9f))) break;
        redone = false;
#pragma unroll
        for (int i = 0; i < 9; ++i) tot[i] += acc[i];
        ++c;
        kp += NT * 1024;
        vp += vstep;
        if (c == NC) break;
      }
      if (c == NC) break;
      if (!redone && __any(!(acc[8] < 7.9e28f))) {
        float mx = -INFINITY;
        for (int t = 0; t < NT; ++t) {
          bf16x8 kf = *(const bf16x8*)(kp + t * 1024);
          f32x16 x = __builtin_amdgcn_mfma_f32_32x32x16_bf16(kf, qf, zero, 0, 0, 0);
#pragma unroll
          for (int i = 0; i < 16; i += 2) mx = fmaxf(fmaxf(mx, x[i]), x[i + 1]);
        }
        mx = fmaxf(mx, __shfl_xor(mx, 32));
        const float mn = fmaxf(m, floorf(mx) + HEAD);
        const int d = (int)fminf(mn - m, 1024.f);
#pragma unroll
        for (int i = 0; i < 9; ++i) tot[i] = __builtin_amdgcn_ldexpf(tot[i], -d);
        m = mn;
        redone = true;
      } else {
        redone = false;
#pragma unroll
        for (int i = 0; i < 9; ++i) tot[i] += acc[i];
        const int e = max(__builtin_amdgcn_frexp_expf(tot[8]) - 1 + (int)HEAD, 0);
#pragma unroll
        for (int i = 0; i < 9; ++i) tot[i] = __builtin_amdgcn_ldexpf(tot[i], -e);
        m += (float)e;
        ++c;
        kp += NT * 1024;
        vp += vstep;
        if (c == NC) break;
      }
#pragma unroll
      for (int i = 0; i < 16; ++i) negm[i] = -m;
      asm volatile("" : "+v"(negm));
      x = __builtin_amdgcn_mfma_f32_32x32x16_bf16(*(const bf16x8*)kp, qf, negm, 0, 0, 0);
      kfn = *(const bf16x8*)(kp + 1024);
    }
  }
  {
    float chk = 0.f;
#pragma unroll
    for (int i = 0; i < 9; ++i) chk = __builtin_fmaf(tot[i], 0.f, chk);
    if (__any(chk != 0.f)) {
      f32x16 oacc = zero;
      float mrun = -INFINITY;
      const char* kp = kp0;
      const char* vp = vp0;
      for (int t = 0; t < 64; ++t) {
        bf16x8 kf = *(const bf16x8*)(kp);
        f32x16 x = __builtin_amdgcn_mfma_f32_32x32x16_bf16(kf, qf, zero, 0, 0, 0);
        float mx = x[0];
#pragma unroll
        for (int i = 1; i < 16; ++i) mx = fmaxf(mx, x[i]);
        mx = fmaxf(mx, __shfl_xor(mx, 32));
        const float mn = fmaxf(mrun, mx);
        const float al = __builtin_amdgcn_exp2f(mrun - mn);
        mrun = mn;
#pragma unroll
        for (int i = 0; i < 16; ++i) oacc[i] *= al;
#pragma unroll
        for (int s = 0; s < 2; ++s) {
          bf16x8 pf;
#pragma unroll
          for (int j = 0; j < 8; ++j) pf[j] = (bf16)__builtin_amdgcn_exp2f(x[8 * s + j] - mn);
          bf16x8 vf = *(const bf16x8*)(vp + ((t & (NT - 1)) * 2 + s) * 512);
          oacc = __builtin_amdgcn_mfma_f32_32x32x16_bf16(vf, pf, oacc, 0, 0, 0);
        }
        kp += 1024;
        if ((t & (NT - 1)) == NT - 1) vp += vstep;
      }
#pragma unroll
      for (int i = 0; i < 9; ++i) tot[i] = oacc[i];
    }
  }
  const float inv = 1.f / tot[8];
  bf16* orow = O + token * 512 + r * 16;
#pragma unroll
  for (int g = 0; g < 2; ++g) {
    bf16x4 o;
#pragma unroll
    for (int j = 0; j < 4; ++j) o[j] = (bf16)(tot[4 * g + j] * inv);
    *(bf16x4*)(orow + 8 * g + 4 * h) = o;
  }
}

constexpr int K3_LDS = 64 * XS_STRIDE + 65536;

extern "C" __global__ __launch_bounds__(512) void k3_out(const char* __restrict__ ws, const bf16* __restrict__ wo,
                                              const bf16* __restrict__ x, bf16* __restrict__ out) {
  const bf16* O = (const bf16*)(ws + WS_O);
  const float* st = (const float*)(ws + WS_ST);
  const float* upd = (const float*)(ws + WS_UPD);
  const int* sel = (const int*)(ws + WS_SEL);
  extern __shared__ __attribute__((aligned(16))) char smem[];
  char* ximg = smem;
  char* wimg = smem + 64 * XS_STRIDE;
  const int cb = blockIdx.x >> 5, tb = blockIdx.x & 31;
  const int tid = threadIdx.x, lane = tid & 63;
  const int wave = __builtin_amdgcn_readfirstlane(tid >> 6);
  const int tok0 = tb * 64;
  const int s0 = sel[0], s1 = sel[1];
#pragma unroll
  for (int i = 0; i < 8; ++i) {
    const int row = wave + 8 * i;
    glds16(O + (tok0 + row) * 512 + lane * 8, ximg + row * XS_STRIDE);
  }
  stage_w(wo, cb * 64, wimg, wave, 8, lane);
  const int r = lane & 31, h = lane >> 5;
  const int n0 = (wave & 1) * 32, t0 = ((wave >> 1) & 1) * 32;
  const int tok = tok0 + t0 + r;
  u32x2 xv[4], nv[4];
  if (wave < 4) {
#pragma unroll
    for (int g = 0; g < 4; ++g) xv[g] = *(const u32x2*)(x + tok * 512 + cb * 64 + n0 + 8 * g + 4 * h);
    const f32x2 sx = *(const f32x2*)(st + tok * 2);
    f32x4 u[4];
#pragma unroll
    for (int g = 0; g < 4; ++g) u[g] = f32x4{0.f, 0.f, 0.f, 0.f};
    if (tok == s0 || tok == s1) {
#pragma unroll
      for (int g = 0; g < 4; ++g) u[g] = *(const f32x4*)(upd + (tok == s0 ? 0 : 512) + cb * 64 + n0 + 8 * g + 4 * h);
    }
#pragma unroll
    for (int g = 0; g < 4; ++g) {
#pragma unroll
      for (int jj = 0; jj < 2; ++jj) {
        const unsigned xr = xv[g][jj];
        unsigned nr = ln2(unpack2(xr), f32x2{sx.x, sx.x}, f32x2{sx.y, sx.y});
        nr = pack2(unpack2(nr) + (jj ? f32x2{u[g][2], u[g][3]} : f32x2{u[g][0], u[g][1]}));
        asm volatile("" : "+v"(nr));
        nv[g][jj] = nr;
      }
    }
  }
  __syncthreads();
  if (wave >= 4) {
    if (tid == 256) {
      const unsigned t = *(const volatile unsigned*)(out + tok0 * 512 + cb * 64);
      asm volatile("" ::"v"(t));
    }
    return;
  }
  f32x16 acc = gemm_tile(wimg, ximg, n0, t0, lane);
#pragma unroll
  for (int g = 0; g < 4; ++g) {
    u32x2 o;
#pragma unroll
    for (int jj = 0; jj < 2; ++jj) {
      const unsigned xr = xv[g][jj], nr = nv[g][jj];
      const f32x2 a = unpack2(pack2(f32x2{acc[4 * g + 2 * jj], acc[4 * g + 2 * jj + 1]}));
      o[jj] = pack2(unpack2(pack2(unpack2(xr) + a)) + unpack2(nr));
    }
    *(u32x2*)(out + tok * 512 + cb * 64 + n0 + 8 * g + 4 * h) = o;
  }
}

constexpr int K1_LDS = 64 * XS_STRIDE + 65536 + 16384 + 256;

extern "C" __global__ __launch_bounds__(768) void k1_ln_qkv(const bf16* __restrict__ x, const bf16* __restrict__ wq,
                                                 const bf16* __restrict__ wk, const bf16* __restrict__ wv,
                                                 const bf16* __restrict__ wr, char* __restrict__ ws) {
  float* st = (float*)(ws + WS_ST);
  bf16* Q = (bf16*)(ws + WS_Q);
  bf16* Kb = (bf16*)(ws + WS_K);
  bf16* Vf = (bf16*)(ws + WS_V);
  unsigned* cand = (unsigned*)(ws + WS_R);
  unsigned* gcnt = (unsigned*)(ws + WS_CNT);
  extern __shared__ __attribute__((aligned(16))) char smem[];
  char* ximg = smem;
  char* wimg = smem + 64 * XS_STRIDE;
  char* kvimg = smem + 64 * XS_STRIDE + 65536;
  float* rs = (float*)(smem + 64 * XS_STRIDE + 65536 + 16384);
  const int cb = blockIdx.x >> 5, tb = blockIdx.x & 31;
  const int tid = threadIdx.x, lane = tid & 63;
  const int wave = __builtin_amdgcn_readfirstlane(tid >> 6);
  const int tok0 = tb * 64;

  if (wave >= 8) {
    if (blockIdx.x == 255 && tid >= 768 - (NGRP + 1)) gcnt[(767 - tid) * 64] = 0;
    stage_w(wq, cb * 64, wimg, wave - 8, 4, lane);
    if (cb < 2) {
      const bf16* w = cb == 0 ? wk : wv;
#pragma unroll
      for (int i = 0; i < 4; ++i) {
        const int p = wave - 8 + 4 * i;
        glds16((const char*)w + p * 1024 + lane * 16, kvimg + p * 1024);
      }
    }
    __syncthreads();
    if (cb == 3 && wave == 8) {
      unsigned k1 = route_key(rs[lane], tok0 + lane), k2 = 0;
      top2_wave(k1, k2, 64);
      if (lane == 0) *(u32x2*)(cand + tb * 2) = u32x2{k1, k2};
    }
    return;
  }

  const int tl = wave * 8 + (lane >> 3), c8 = lane & 7;
  u32x4 xv[8];
#pragma unroll
  for (int i = 0; i < 8; ++i) xv[i] = *(const u32x4*)(x + (tok0 + tl) * 512 + (c8 + 8 * i) * 8);
  u32x4 wrv[8];
  if (cb == 3) {
#pragma unroll
    for (int i = 0; i < 8; ++i) wrv[i] = *(const u32x4*)(wr + (c8 + 8 * i) * 8);
  }

  f32x2 xf[32];
#pragma unroll
  for (int i = 0; i < 32; ++i) xf[i] = unpack2(xv[i >> 2][i & 3]);
  f32x2 s2 = {0.f, 0.f};
#pragma unroll
  for (int i = 0; i < 32; ++i) s2 += xf[i];
  const float mean = sum8(s2.x + s2.y) * (1.f / 512.f);
  const f32x2 mean2 = {mean, mean};
  f32x2 v2 = {0.f, 0.f};
#pragma unroll
  for (int i = 0; i < 32; ++i) { const f32x2 d = xf[i] - mean2; v2 += d * d; }
  const float vs = sum8(v2.x + v2.y);
  const float mb = rbf(mean);
  const float vb = rbf(vs * (1.f / 512.f));
  const float den = rbf(sqrtf(rbf(vb + 1e-5f)));
  const float rinv = __builtin_amdgcn_rcpf(den);
  const f32x2 mb2 = {mb, mb}, rinv2 = {rinv, rinv};
  u32x4 o[8];
#pragma unroll
  for (int i = 0; i < 8; ++i) {
#pragma unroll
    for (int j = 0; j < 4; ++j) o[i][j] = ln2(xf[4 * i + j], mb2, rinv2);
    *(u32x4*)(ximg + tl * XS_STRIDE + (c8 + 8 * i) * 16) = o[i];
  }
  if (cb == 2 && c8 == 0) *(f32x2*)(st + (tok0 + tl) * 2) = f32x2{mb, rinv};
  if (cb == 3) {
    float rd = 0.f;
#pragma unroll
    for (int i = 0; i < 8; ++i) {
#pragma unroll
      for (int j = 0; j < 4; ++j) {
        const unsigned a = o[i][j], b = wrv[i][j];
        rd = __builtin_amdgcn_fdot2_f32_bf16(__builtin_bit_cast(bf16x2, a), __builtin_bit_cast(bf16x2, b), rd, false);
      }
    }
    rd = sum8(rd);
    if (c8 == 0) rs[tl] = rbf(rd);
  }
  __syncthreads();

  const int r = lane & 31, h = lane >> 5;
  if (wave < 4) {
    const int n0 = (wave & 1) * 32, t0 = (wave >> 1) * 32;
    f32x16 acc = gemm_tile(wimg, ximg, n0, t0, lane);
    const float sc = 0.25f * 1.4426950408889634f;
    bf16* qrow = Q + (tok0 + t0 + r) * 512 + cb * 64 + n0;
#pragma unroll
    for (int g = 0; g < 4; ++g) {
      u32x2 o;
#pragma unroll
      for (int jj = 0; jj < 2; ++jj)
        o[jj] = pack2(unpack2(pack2(f32x2{acc[4 * g + 2 * jj], acc[4 * g + 2 * jj + 1]})) * f32x2{sc, sc});
      *(u32x2*)(qrow + 8 * g + 4 * h) = o;
    }
  } else if (wave == 7) {
    if (lane < 2) {
      const unsigned t = *(const volatile unsigned*)(lane ? ws + WS_K + tb * 2048 : (const char*)(Q + tok0 * 512 + cb * 64));
      asm volatile("" ::"v"(t));
    }
  } else if (wave == 6 && cb == 7 && tb < 8) {
    unsigned long long c0, c1;
    CODE_ADDR(k2_attn, c0);
    CODE_ADDR(k1_ln_qkv, c1);
    const long long n = (long long)(c1 - c0);
    unsigned t[4];
#pragma unroll
    for (int i = 0; i < 4; ++i) {
      const int off = (i * 64 + lane) * 128;
      t[i] = off < n ? *(const volatile unsigned*)(c0 + off) : 0u;
    }
#pragma unroll
    for (int i = 0; i < 4; ++i) asm volatile("" ::"v"(t[i]));
  } else if (wave < 6 && cb < 2) {
    const int t0 = (wave - 4) * 32;
    f32x16 acc = gemm_tile16(kvimg, ximg, t0, lane);
    const int key = tok0 + t0 + r;
    const int t = key >> 5;
    if (cb == 0) {
#pragma unroll
      for (int g = 0; g < 2; ++g) {
        bf16x4 o;
#pragma unroll
        for (int j = 0; j < 4; ++j) o[j] = (bf16)acc[4 * g + j];
        *(bf16x4*)(Kb + ((t * 2 + g) * 32 + r) * 8 + 4 * h) = o;
      }
    } else {
      const int kk = key & 31, sg = kk >> 4, rem = kk & 15;
      const int hh = (rem >> 2) & 1, jj = ((rem >> 3) << 2) | (rem & 3);
#pragma unroll
      for (int g = 0; g < 2; ++g) {
#pragma unroll
        for (int j = 0; j < 4; ++j) {
          const int d = 8 * g + 4 * h + j;
          Vf[(((t * 2 + sg) * 2 + hh) * 16 + d) * 8 + jj] = (bf16)acc[4 * g + j];
        }
      }
    }
  }
}

extern "C" void kernel_launch(void* const* d_in, const int* in_sizes, int n_in, void* d_out, int out_size,
                              void* d_ws, size_t ws_size, hipStream_t stream) {
  const bf16* x = (const bf16*)d_in[0];
  const bf16* wq = (const bf16*)d_in[1];
  const bf16* wk = (const bf16*)d_in[2];
  const bf16* wv = (const bf16*)d_in[3];
  const bf16* wo = (const bf16*)d_in[4];
  const bf16* wr = (const bf16*)d_in[6];
  const bf16* wg = (const bf16*)d_in[7];
  const bf16* wu = (const bf16*)d_in[8];
  const bf16* wd = (const bf16*)d_in[9];
  char* ws = (char*)d_ws;
  bf16* out = (bf16*)d_out;

  (void)hipFuncSetAttribute((const void*)k1_ln_qkv, hipFuncAttributeMaxDynamicSharedMemorySize, K1_LDS);
  (void)hipFuncSetAttribute((const void*)k2_attn, hipFuncAttributeMaxDynamicSharedMemorySize, ATT_LDS);
  (void)hipFuncSetAttribute((const void*)k3_out, hipFuncAttributeMaxDynamicSharedMemorySize, K3_LDS);

  k1_ln_qkv<<<256, 768, K1_LDS, stream>>>(x, wq, wk, wv, wr, ws);
  k2_attn<<<256, 768, ATT_LDS, stream>>>(ws, x, wg, wu, wd, wo, out);
  k3_out<<<256, 512, K3_LDS, stream>>>(ws, wo, x, out);
}
```

```cpp
#include <hip/hip_runtime.h>
#include <hip/hip_bf16.h>

typedef __attribute__((ext_vector_type(8))) __bf16 bf16x8;
typedef __attribute__((ext_vector_type(4))) __bf16 bf16x4;
typedef __attribute__((ext_vector_type(2))) __bf16 bf16x2;
typedef __attribute__((ext_vector_type(4))) short s16x4;
typedef __attribute__((ext_vector_type(8))) short s16x8;
typedef __attribute__((ext_vector_type(16))) float f32x16;
typedef __attribute__((ext_vector_type(4))) float f32x4;
typedef __attribute__((ext_vector_type(4))) unsigned u32x4;
typedef __attribute__((ext_vector_type(2))) unsigned u32x2;
typedef __attribute__((ext_vector_type(2))) float f32x2;
typedef __bf16 bf16;

#define LDS_AS __attribute__((address_space(3)))
#define GLB_AS __attribute__((address_space(1)))
#define DI __device__ __forceinline__

constexpr int S_ = 2048, D_ = 512, H_ = 32, DH_ = 16, F_ = 1376;

constexpr size_t WS_Q = 0;
constexpr size_t WS_O = WS_Q;
constexpr size_t WS_K = 2u << 20;
constexpr size_t WS_V = WS_K + 65536;
constexpr size_t WS_ST = WS_V + 65536;
constexpr size_t WS_R = WS_ST + 16384;
constexpr size_t WS_SEL = WS_R + 4096;
constexpr size_t WS_CNT = WS_SEL + 4096;
constexpr size_t WS_UPD = WS_CNT + 4096;
constexpr size_t WS_YP = WS_UPD + 4096;

constexpr int NFFN = 172;
constexpr int GSZ = 12, NGRP = 15;
constexpr size_t WS_CL = WS_YP + (NFFN + NGRP) * 4096;

DI float bf2f(bf16 v) { return (float)v; }
DI float rbf(float v) { return (float)(bf16)v; }
DI f32x2 unpack2(unsigned u) { return f32x2{__uint_as_float(u << 16), __uint_as_float(u & 0xffff0000u)}; }
DI unsigned pack2(f32x2 v) {
  unsigned r = __builtin_bit_cast(unsigned, __builtin_convertvector(v, bf16x2));
  asm("" : "+v"(r));
  return r;
}

DI unsigned route_key(float v, int tok) {
  const unsigned u = __float_as_uint(v) >> 16;
  const unsigned o = (u & 0x8000u) ? (~u & 0xffffu) : (u | 0x8000u);
  return (o << 16) | (unsigned)(0xffff - tok);
}
DI int key_tok(unsigned k) { return 0xffff - (int)(k & 0xffffu); }
DI float key_val(unsigned k) {
  const unsigned o = k >> 16;
  const unsigned u = (o & 0x8000u) ? (o & 0x7fffu) : (~o & 0xffffu);
  return __uint_as_float(u << 16);
}
DI unsigned ln2(f32x2 x, f32x2 mb2, f32x2 rinv2) { return pack2(unpack2(pack2(x - mb2)) * rinv2); }
DI void top2_wave(unsigned& k1, unsigned& k2, int width) {
  for (int o = width >> 1; o > 0; o >>= 1) {
    const unsigned a1 = __shfl_xor(k1, o), a2 = __shfl_xor(k2, o);
    k2 = max(min(k1, a1), max(k2, a2));
    k1 = max(k1, a1);
  }
}

template <int O>
DI void top2_step(unsigned& k1, unsigned& k2) {
  const unsigned a1 = __builtin_amdgcn_ds_swizzle(k1, (O << 10) | 31), a2 = __builtin_amdgcn_ds_swizzle(k2, (O << 10) | 31);
  k2 = max(min(k1, a1), max(k2, a2));
  k1 = max(k1, a1);
}

DI void glds16(const void* g, void* l) {
  __builtin_amdgcn_global_load_lds((const GLB_AS void*)g, (LDS_AS void*)l, 16, 0, 0);
}

DI void glds16_raw(const void* g, unsigned lds_off) {
  asm volatile("s_mov_b32 m0, %0\n s_nop 0\n global_load_lds_dwordx4 %1, off" ::"s"(lds_off), "v"(g) : "memory", "m0");
}

template <int CTRL>
DI float dpp_add(float v) {
  const int t = __builtin_amdgcn_update_dpp(0, __float_as_int(v), CTRL, 0xf, 0xf, true);
  return v + __int_as_float(t);
}
DI float sum8(float v) {
  v = dpp_add<0xB1>(v);
  v = dpp_add<0x4E>(v);
  v = dpp_add<0x141>(v);
  return v;
}

DI s16x4 tr_read(const char* p) {
  return __builtin_amdgcn_ds_read_tr16_b64_v4i16((LDS_AS s16x4*)p);
}

DI int wimg_off(int row, int ch) { return row * 128 + ((ch ^ (((row >> 1) & 1) << 2)) << 4); }

constexpr int XS_STRIDE = 1040;

DI void stage_w(const bf16* w, int col0, char* wimg, int wave, int nwaves, int lane) {
  for (int p = wave; p < 64; p += nwaves) {
    const int row = p * 8 + (lane >> 3), pc = lane & 7;
    const int ch = pc ^ (((row >> 1) & 1) << 2);
    glds16(w + row * 512 + col0 + ch * 8, wimg + p * 1024);
  }
}

constexpr int GP = 4;
DI f32x16 gemm_tile(const char* wimg, const char* ximg, int n0, int t0, int lane) {
  const int r = lane & 31, h = lane >> 5;
  const int q = (lane & 15) >> 2, p = lane & 3, blk = (lane >> 4) & 1;
  f32x16 acc;
#pragma unroll
  for (int i = 0; i < 16; ++i) acc[i] = 0.f;
  const char* xb = ximg + (t0 + r) * XS_STRIDE + 16 * h;
  const int col = n0 + 16 * blk + 4 * p;
  const int ch = col >> 3, sub = (col & 7) * 2;
  const char* wb0 = wimg + wimg_off(8 * h + q, ch) + sub;
  const char* wb1 = wimg + wimg_off(8 * h + q + 4, ch) + sub;
  s16x4 lo[32], hi[32];
  bf16x8 b[32];
#pragma unroll
  for (int ks = 0; ks < 32 + GP; ++ks) {
    if (ks < 32) {
      lo[ks] = tr_read(wb0 + ks * 2048);
      hi[ks] = tr_read(wb1 + ks * 2048);
      b[ks] = *(const bf16x8*)(xb + ks * 32);
    }
    if (ks >= GP) {
      const s16x8 a = __builtin_shufflevector(lo[ks - GP], hi[ks - GP], 0, 1, 2, 3, 4, 5, 6, 7);
      acc = __builtin_amdgcn_mfma_f32_32x32x16_bf16(__builtin_bit_cast(bf16x8, a), b[ks - GP], acc, 0, 0, 0);
      asm volatile("" : "+v"(acc));
    }
    __builtin_amdgcn_sched_barrier(0);
  }
  return acc;
}
DI f32x16 gemm_tile16(const char* wimg, const char* ximg, int t0, int lane) {
  const int r = lane & 31, h = lane >> 5;
  const int q = (lane & 15) >> 2, p = lane & 3;
  f32x16 acc;
#pragma unroll
  for (int i = 0; i < 16; ++i) acc[i] = 0.f;
  const char* xb = ximg + (t0 + r) * XS_STRIDE + 16 * h;
  const char* wb = wimg + (8 * h + q) * 32 + 8 * p;
  s16x4 lo[32], hi[32];
  bf16x8 b[32];
#pragma unroll
  for (int ks = 0; ks < 32 + GP; ++ks) {
    if (ks < 32) {
      lo[ks] = tr_read(wb + ks * 512);
      hi[ks] = tr_read(wb + ks * 512 + 128);
      b[ks] = *(const bf16x8*)(xb + ks * 32);
    }
    if (ks >= GP) {
      const s16x8 a = __builtin_shufflevector(lo[ks - GP], hi[ks - GP], 0, 1, 2, 3, 4, 5, 6, 7);
      acc = __builtin_amdgcn_mfma_f32_32x32x16_bf16(__builtin_bit_cast(bf16x8, a), b[ks - GP], acc, 0, 0, 0);
      asm volatile("" : "+v"(acc));
    }
    __builtin_amdgcn_sched_barrier(0);
  }
  return acc;
}

#define CODE_ADDR(sym, out)                                                                               \
  asm volatile("s_getpc_b64 s[20:21]\n s_add_u32 s20, s20, " #sym "@rel32@lo+4\n s_addc_u32 s21, s21, " #sym \
               "@rel32@hi+12\n s_mov_b64 %0, s[20:21]"                                                    \
               : "=s"(out)::"s20", "s21", "scc")

constexpr int NT = 16;
constexpr int CST = NT * 1024;
constexpr int ATT_LDS = 65536 * 2 + CST + 64 + 512 + 4 * 640;
constexpr int NC = 64 / NT;

DI bool last_arrival(int* w, unsigned* gc, int n, int lane) {
  asm volatile("s_waitcnt vmcnt(0)" ::: "memory");
  if (lane == 0) {
    if (__hip_atomic_fetch_add(w, 1, __ATOMIC_RELAXED, __HIP_MEMORY_SCOPE_WORKGROUP) == 3) {
      const unsigned t = __hip_atomic_fetch_add((GLB_AS unsigned*)gc, 1u, __ATOMIC_RELAXED, __HIP_MEMORY_SCOPE_AGENT);
      __hip_atomic_store(w + 1, t == (unsigned)(n - 1) ? 2 : 1, __ATOMIC_RELAXED, __HIP_MEMORY_SCOPE_WORKGROUP);
    }
  }
  int r;
  while ((r = __hip_atomic_load(w + 1, __ATOMIC_RELAXED, __HIP_MEMORY_SCOPE_WORKGROUP)) == 0) __builtin_amdgcn_s_sleep(2);
  asm volatile("" ::: "memory");
  return r == 2;
}

DI void ffn_slice(int f, int j, const bf16* __restrict__ x, const float* __restrict__ st,
                  const unsigned* __restrict__ cand, unsigned* __restrict__ fdone,
                  const bf16* __restrict__ wg, const bf16* __restrict__ wu, const bf16* __restrict__ wd,
                  float* __restrict__ ypart, int* __restrict__ sel, unsigned* __restrict__ gcnt,
                  float* __restrict__ upd, float* red, int* cnt, char* scr, int lane) {
  const int k0 = j * 128 + lane * 2;
  const int r = lane & 31, h = lane >> 5;
  const int kr = (lane & 51) | ((lane & 4) << 1) | ((lane & 8) >> 1);
  u32x4 wf[4];
  unsigned d2[8];
  {
    const __amdgpu_buffer_rsrc_t rg = __builtin_amdgcn_make_buffer_rsrc((void*)wg, 0, 512 * F_ * 2, 0x00020000);
    const __amdgpu_buffer_rsrc_t ru = __builtin_amdgcn_make_buffer_rsrc((void*)wu, 0, 512 * F_ * 2, 0x00020000);
    const __amdgpu_buffer_rsrc_t rd = __builtin_amdgcn_make_buffer_rsrc((void*)wd, 0, 512 * F_ * 2, 0x00020000);
    const int wo_ = kr * (F_ * 2), so = (j * 128 * F_ + f * 8) * 2;
#pragma unroll
    for (int t = 0; t < 2; ++t) {
      wf[2 * t] = __builtin_amdgcn_raw_buffer_load_b128(rg, wo_, so + t * (64 * F_ * 2), 0);
      wf[2 * t + 1] = __builtin_amdgcn_raw_buffer_load_b128(ru, wo_, so + t * (64 * F_ * 2), 0);
    }
#pragma unroll
    for (int c = 0; c < 8; ++c)
      d2[c] = __builtin_amdgcn_raw_buffer_load_b32(rd, 8 * r + 4 * h + c * 1024, (f * 8 * 512 + j * 128) * 2, 0);
  }
  const u32x2 cv = *(const u32x2*)(cand + (lane & 31) * 2);
  unsigned k1 = cv[0], k2 = cv[1];
  top2_step<16>(k1, k2);
  top2_step<8>(k1, k2);
  top2_step<4>(k1, k2);
  top2_step<2>(k1, k2);
  top2_step<1>(k1, k2);
  k1 = __builtin_amdgcn_readfirstlane(k1);
  k2 = __builtin_amdgcn_readfirstlane(k2);
  const int i1 = key_tok(k1), i2 = key_tok(k2);
  const f32x2 sa = *(const f32x2*)(st + i1 * 2), sb = *(const f32x2*)(st + i2 * 2);
  *(unsigned*)(scr + lane * 4) = ln2(unpack2(*(const unsigned*)(x + i1 * 512 + k0)), f32x2{sa.x, sa.x}, f32x2{sa.y, sa.y});
  *(unsigned*)(scr + 256 + lane * 4) = ln2(unpack2(*(const unsigned*)(x + i2 * 512 + k0)), f32x2{sb.x, sb.x}, f32x2{sb.y, sb.y});
  f32x16 zero;
#pragma unroll
  for (int i = 0; i < 16; ++i) zero[i] = 0.f;
  u32x4 ida, idb;
  {
    const int d = r - 8 * h;
    const unsigned one = 0x3f80u << ((d & 1) << 4);
#pragma unroll
    for (int q = 0; q < 4; ++q) {
      ida[q] = (d >> 1) == q ? one : 0u;
      idb[q] = (d >> 1) == q + 8 ? one : 0u;
    }
  }
  f32x16 gu = zero;
#pragma unroll
  for (int t = 0; t < 2; ++t) {
    f32x16 wt = __builtin_amdgcn_mfma_f32_32x32x16_bf16(__builtin_bit_cast(bf16x8, wf[2 * t]),
                                                        __builtin_bit_cast(bf16x8, ida), zero, 0, 0, 0);
    wt = __builtin_amdgcn_mfma_f32_32x32x16_bf16(__builtin_bit_cast(bf16x8, wf[2 * t + 1]),
                                                 __builtin_bit_cast(bf16x8, idb), wt, 0, 0, 0);
#pragma unroll
    for (int q = 0; q < 2; ++q) {
      u32x4 bw;
#pragma unroll
      for (int i = 0; i < 4; ++i) bw[i] = pack2(f32x2{wt[8 * q + 2 * i], wt[8 * q + 2 * i + 1]});
      const bf16x8 xf = *(const bf16x8*)(scr + (r & 1) * 256 + (r & 2) * 32 + (64 * t + 16 * q + 8 * h) * 2);
      gu = __builtin_amdgcn_mfma_f32_32x32x16_bf16(xf, __builtin_bit_cast(bf16x8, bw), gu, 0, 0, 0);
    }
  }
  {
    const float g0 = dpp_add<0x128>((lane & 8) ? gu[2] : gu[0]), g1 = dpp_add<0x128>((lane & 8) ? gu[3] : gu[1]);
    if (lane < 32) red[j * 32 + (lane & 16) + (lane & 15)] = (lane & 8) ? g1 : g0;
  }
  if (lane == 0) __hip_atomic_fetch_add(cnt, 1, __ATOMIC_RELEASE, __HIP_MEMORY_SCOPE_WORKGROUP);
  while (__hip_atomic_load(cnt, __ATOMIC_ACQUIRE, __HIP_MEMORY_SCOPE_WORKGROUP) < 4) __builtin_amdgcn_s_sleep(1);
  const int l = lane & 15;
  const float g = rbf((red[l] + red[32 + l]) + (red[64 + l] + red[96 + l]));
  const float u = rbf((red[16 + l] + red[48 + l]) + (red[80 + l] + red[112 + l]));
  const float hv = rbf(g * __builtin_amdgcn_rcpf(1.f + __expf(-g))) * u;
  if (lane < 24) *(bf16*)(scr + 512 + lane * 2) = (bf16)(lane < 16 ? hv : 0.f);
  const bf16x8 hf = *(const bf16x8*)(scr + 512 + ((r >> 1) == h ? (r & 1) * 16 : 32));
  u32x4 da, db;
#pragma unroll
  for (int c = 0; c < 4; ++c) {
    da[c] = __builtin_amdgcn_perm(d2[2 * c + 1], d2[2 * c], 0x05040100u);
    db[c] = __builtin_amdgcn_perm(d2[2 * c + 1], d2[2 * c], 0x07060302u);
  }
  const f32x16 ya = __builtin_amdgcn_mfma_f32_32x32x16_bf16(hf, __builtin_bit_cast(bf16x8, da), zero, 0, 0, 0);
  const f32x16 yb = __builtin_amdgcn_mfma_f32_32x32x16_bf16(hf, __builtin_bit_cast(bf16x8, db), zero, 0, 0, 0);
  const __amdgpu_buffer_rsrc_t rs = __builtin_amdgcn_make_buffer_rsrc(ypart, 0, (NFFN + NGRP) * 4096, 0x00020000);
  if (lane < 32) {
#pragma unroll
    for (int t = 0; t < 2; ++t)
      __builtin_amdgcn_raw_buffer_store_b128(__builtin_bit_cast(u32x4, f32x4{ya[t], yb[t], ya[2 + t], yb[2 + t]}), rs,
                                             t * 2048 + j * 512 + lane * 16, f * 4096, 16);
  }
  const int grp = f / GSZ, gs = min(GSZ, NFFN - grp * GSZ);
  if (!last_arrival(cnt + 1, gcnt + grp * 64, gs, lane)) return;
  const int voff = j * 1024 + lane * 16;
  f32x4 a = {0.f, 0.f, 0.f, 0.f};
  {
    u32x4 t[GSZ];
#pragma unroll
    for (int i = 0; i < GSZ; ++i) t[i] = __builtin_amdgcn_raw_buffer_load_b128(rs, voff, (grp * GSZ + min(i, gs - 1)) * 4096, 16);
#pragma unroll
    for (int i = 0; i < GSZ; ++i)
      if (i < gs) a += __builtin_bit_cast(f32x4, t[i]);
  }
  __builtin_amdgcn_raw_buffer_store_b128(__builtin_bit_cast(u32x4, a), rs, voff, (NFFN + grp) * 4096, 16);
  if (!last_arrival(cnt + 3, gcnt + NGRP * 64, NGRP, lane)) return;
  a = f32x4{0.f, 0.f, 0.f, 0.f};
  {
    u32x4 t[NGRP];
#pragma unroll
    for (int i = 0; i < NGRP; ++i) t[i] = __builtin_amdgcn_raw_buffer_load_b128(rs, voff, (NFFN + i) * 4096, 16);
#pragma unroll
    for (int i = 0; i < NGRP; ++i) a += __builtin_bit_cast(f32x4, t[i]);
  }
  const float gate = rbf(1.f / (1.f + __expf(-key_val(j < 2 ? k1 : k2))));
#pragma unroll
  for (int i = 0; i < 4; ++i) a[i] = rbf(rbf(a[i]) * gate);
  {
    const __amdgpu_buffer_rsrc_t ru = __builtin_amdgcn_make_buffer_rsrc(upd, 0, 4096, 0x00020000);
    __builtin_amdgcn_raw_buffer_store_b128(__builtin_bit_cast(u32x4, a), ru, voff, 0, 16);
  }
  asm volatile("s_waitcnt vmcnt(0)" ::: "memory");
  if (lane == 0 && __hip_atomic_fetch_add(cnt + 5, 1, __ATOMIC_RELAXED, __HIP_MEMORY_SCOPE_WORKGROUP) == 3)
    __hip_atomic_store((GLB_AS unsigned*)fdone, 1u, __ATOMIC_RELAXED, __HIP_MEMORY_SCOPE_AGENT);
}

extern "C" __global__ __launch_bounds__(768) void k2_attn(char* __restrict__ ws, const bf16* __restrict__ x,
                                               const bf16* __restrict__ wg, const bf16* __restrict__ wu,
                                               const bf16* __restrict__ wd, const bf16* __restrict__ wo,
                                               bf16* __restrict__ out) {
  const bf16* Q = (const bf16*)(ws + WS_Q);
  const bf16* Kb = (const bf16*)(ws + WS_K);
  bf16* O = (bf16*)(ws + WS_O);
  const float* st = (const float*)(ws + WS_ST);
  const unsigned* cand = (const unsigned*)(ws + WS_R);
  float* ypart = (float*)(ws + WS_YP);
  int* sel = (int*)(ws + WS_SEL);
  unsigned* gcnt = (unsigned*)(ws + WS_CNT);
  float* upd = (float*)(ws + WS_UPD);
  extern __shared__ __attribute__((aligned(16))) char smem[];
  char* kl = smem;
  char* vl = smem + 65536;
  char* cst = smem + 131072;
  const int tid = threadIdx.x, lane = tid & 63;
  const int wave = __builtin_amdgcn_readfirstlane(tid >> 6);

  if (wave >= 8) {
    int* cnt = (int*)(smem + 131072 + CST + 32);
    if (tid < 512 + 6) cnt[tid - 512] = 0;
#pragma unroll
    for (int i = 0; i < 26; ++i) {
      const int q = (wave - 8) + 4 * i, piece = q < 48 ? 16 + q : 24 + q;
      glds16_raw((const char*)Kb + piece * 1024 + lane * 16, (unsigned)(size_t)(LDS_AS char*)smem + piece * 1024);
    }
    asm volatile("s_waitcnt lgkmcnt(0)\n s_barrier\n s_waitcnt vmcnt(0)\n s_barrier" ::: "memory");
    unsigned* clc = (unsigned*)(ws + WS_CL);
    const int fs = (blockIdx.x & 7) * 22 + (blockIdx.x >> 3);
    if (blockIdx.x < 176 && fs < NFFN) {
      __builtin_amdgcn_s_setprio(3);
      ffn_slice(fs, wave - 8, x, st, cand, clc + 32 * 64, wg, wu, wd, ypart, sel, gcnt, upd,
                (float*)(smem + 131072 + CST + 64), cnt, smem + 131072 + CST + 576 + (wave - 8) * 640, lane);
    }
    const int tb = (blockIdx.x >> 6) * 8 + (blockIdx.x & 7), cb = (blockIdx.x >> 3) & 7, tok0 = tb * 64;
    const int sw = wave - 8, r = lane & 31, h = lane >> 5;
    char* ximg = smem;
    char* wimg = smem + 64 * XS_STRIDE;
    u32x4 wreg[16];
#pragma unroll
    for (int i = 0; i < 16; ++i) {
      const int row = (sw + 4 * i) * 8 + (lane >> 3);
      wreg[i] = *(const u32x4*)(wo + row * 512 + cb * 64 + (((lane & 7) ^ (((row >> 1) & 1) << 2)) << 3));
    }
    const int n0 = (sw & 1) * 32, t0 = (sw >> 1) * 32;
    const int tok = tok0 + t0 + r;
    u32x2 xv[4], nv[4];
#pragma unroll
    for (int g = 0; g < 4; ++g) xv[g] = *(const u32x2*)(x + tok * 512 + cb * 64 + n0 + 8 * g + 4 * h);
    const f32x2 sx = *(const f32x2*)(st + tok * 2);
    int rsel;
    {
      const u32x2 cv = *(const u32x2*)(cand + (lane & 31) * 2);
      unsigned k1 = cv[0], k2 = cv[1];
      top2_step<16>(k1, k2);
      top2_step<8>(k1, k2);
      top2_step<4>(k1, k2);
      top2_step<2>(k1, k2);
      top2_step<1>(k1, k2);
      rsel = tok == key_tok(k1) ? 0 : tok == key_tok(k2) ? 1 : -1;
    }
#pragma unroll
    for (int g = 0; g < 4; ++g) {
#pragma unroll
      for (int jj = 0; jj < 2; ++jj) {
        unsigned nr = ln2(unpack2(xv[g][jj]), f32x2{sx.x, sx.x}, f32x2{sx.y, sx.y});
        asm volatile("" : "+v"(nr));
        nv[g][jj] = nr;
      }
    }
    if (wave == 11 && lane == 0) {
      const unsigned t = *(const volatile unsigned*)(out + tok0 * 512 + cb * 64);
      asm volatile("" ::"v"(t));
    }
#pragma unroll
    for (int i = 0; i < 16; ++i) asm volatile("" : "+v"(wreg[i]));
    __builtin_amdgcn_s_setprio(3);
    asm volatile("s_barrier" ::: "memory");
    if (tid == 512) __hip_atomic_fetch_add((GLB_AS unsigned*)(clc + tb * 64), 1u, __ATOMIC_RELAXED, __HIP_MEMORY_SCOPE_AGENT);
#pragma unroll
    for (int i = 0; i < 16; ++i) *(u32x4*)(wimg + (sw + 4 * i) * 1024 + lane * 16) = wreg[i];
    if (__any(rsel >= 0)) {
      int spin = 0;
      while (__hip_atomic_load((GLB_AS unsigned*)(clc + 32 * 64), __ATOMIC_RELAXED, __HIP_MEMORY_SCOPE_AGENT) == 0 && ++spin < (1 << 20))
        __builtin_amdgcn_s_sleep(1);
      asm volatile("" ::: "memory");
      const __amdgpu_buffer_rsrc_t ru = __builtin_amdgcn_make_buffer_rsrc(upd, 0, 4096, 0x00020000);
      if (rsel >= 0) {
#pragma unroll
        for (int g = 0; g < 4; ++g) {
          const f32x4 u = __builtin_bit_cast(f32x4, __builtin_amdgcn_raw_buffer_load_b128(ru, (rsel * 512 + cb * 64 + n0 + 8 * g + 4 * h) * 4, 0, 16));
          nv[g][0] = pack2(unpack2(nv[g][0]) + f32x2{u[0], u[1]});
          nv[g][1] = pack2(unpack2(nv[g][1]) + f32x2{u[2], u[3]});
        }
      }
    }
    if (wave == 8) {
      int spin = 0;
      while (__hip_atomic_load((GLB_AS unsigned*)(clc + tb * 64), __ATOMIC_RELAXED, __HIP_MEMORY_SCOPE_AGENT) < 8 && ++spin < (1 << 20))
        __builtin_amdgcn_s_sleep(1);
    }
    asm volatile("s_barrier" ::: "memory");
    {
      const __amdgpu_buffer_rsrc_t ro = __builtin_amdgcn_make_buffer_rsrc(O + tok0 * 512, 0, 65536, 0x00020000);
      u32x4 t[16];
#pragma unroll
      for (int i = 0; i < 16; ++i) t[i] = __builtin_amdgcn_raw_buffer_load_b128(ro, lane * 16, (sw * 16 + i) * 1024, 16);
#pragma unroll
      for (int i = 0; i < 16; ++i) *(u32x4*)(ximg + (sw * 16 + i) * XS_STRIDE + lane * 16) = t[i];
    }
    asm volatile("s_waitcnt lgkmcnt(0)\n s_barrier" ::: "memory");
    f32x16 acc = gemm_tile(wimg, ximg, n0, t0, lane);
#pragma unroll
    for (int g = 0; g < 4; ++g) {
      u32x2 o;
#pragma unroll
      for (int jj = 0; jj < 2; ++jj) {
        const unsigned xr = xv[g][jj], nr = nv[g][jj];
        const f32x2 a = unpack2(pack2(f32x2{acc[4 * g + 2 * jj], acc[4 * g + 2 * jj + 1]}));
        o[jj] = pack2(unpack2(pack2(unpack2(xr) + a)) + unpack2(nr));
      }
      *(u32x2*)(out + tok * 512 + cb * 64 + n0 + 8 * g + 4 * h) = o;
    }
    return;
  }

  const int r = lane & 31, h = lane >> 5;
  const int token = ((blockIdx.x >> 6) * 8 + (blockIdx.x & 7)) * 64 + ((blockIdx.x >> 3) & 7) * 8 + wave;
  bf16x8 qf;
  asm volatile("global_load_dwordx4 %0, %1, off" : "=v"(qf) : "v"(Q + token * 512 + r * 16 + 8 * h) : "memory");
  const unsigned lds0 = (unsigned)(size_t)(LDS_AS char*)smem;
#pragma unroll
  for (int i = 0; i < 3; ++i) {
    const int piece = (i < 2) ? (wave + i * 8) : (64 + wave);
    glds16_raw((const char*)Kb + piece * 1024 + lane * 16, lds0 + piece * 1024);
  }
  {
    const unsigned cv = ((tid & 15) == 0 || (tid & 15) == 4) ? 0x3f803f80u : 0u;
#pragma unroll
    for (int i = 0; i < CST / 8192; ++i) *(u32x4*)(cst + i * 8192 + tid * 16) = u32x4{cv, cv, cv, cv};
  }
  asm volatile("s_waitcnt vmcnt(0) lgkmcnt(0)\n s_barrier" ::: "memory");

  const char* kp0 = kl + lane * 16;
  const char* vp0 = (r < 16) ? (vl + (h * 16 + r) * 16) : (cst + (h * 16 + r - 16) * 16);
  const int vstep = (r < 16) ? NT * 1024 : 0;

  f32x16 zero;
#pragma unroll
  for (int i = 0; i < 16; ++i) zero[i] = 0.f;
  constexpr float HEAD = 40.f;
  float m;
  {
    float mx = -INFINITY;
#pragma unroll
    for (int t = 0; t < 4; ++t) {
      bf16x8 kf = *(const bf16x8*)(kp0 + t * 1024);
      f32x16 x = __builtin_amdgcn_mfma_f32_32x32x16_bf16(kf, qf, zero, 0, 0, 0);
#pragma unroll
      for (int i = 0; i < 16; i += 2) mx = fmaxf(fmaxf(mx, x[i]), x[i + 1]);
    }
    mx = fmaxf(mx, __shfl_xor(mx, 32));
    m = floorf(mx) + HEAD;
  }
  f32x16 negm;
#pragma unroll
  for (int i = 0; i < 16; ++i) negm[i] = -m;
  asm volatile("" : "+v"(negm));
  float tot[9];
#pragma unroll
  for (int i = 0; i < 9; ++i) tot[i] = 0.f;
  {
    const char* kp = kp0;
    const char* vp = vp0;
    int c = 0;
    bool redone = false, first = true;
    if (wave < 4) __builtin_amdgcn_s_setprio(2);
    else __builtin_amdgcn_s_setprio(1);
    f32x16 x = __builtin_amdgcn_mfma_f32_32x32x16_bf16(*(const bf16x8*)kp, qf, negm, 0, 0, 0);
    bf16x8 kfn = *(const bf16x8*)(kp + 1024);
    while (true) {
      f32x16 acc;
      while (true) {
        acc = zero;
#pragma unroll
        for (int t = 0; t < NT; ++t) {
          const bf16x8 kf = kfn;
          kfn = *(const bf16x8*)(kp + (t + 2) * 1024);
          const bf16x8 vf0 = *(const bf16x8*)(vp + (t * 2) * 512);
          const bf16x8 vf1 = *(const bf16x8*)(vp + (t * 2 + 1) * 512);
          bf16x8 pf;
#pragma unroll
          for (int j = 0; j < 4; ++j) pf[j] = (bf16)__builtin_amdgcn_exp2f(x[j]);
          __builtin_amdgcn_sched_barrier(0);
          f32x16 xn = __builtin_amdgcn_mfma_f32_32x32x16_bf16(kf, qf, negm, 0, 0, 0);
          asm volatile("" : "+v"(xn));
          __builtin_amdgcn_sched_barrier(0);
#pragma unroll
          for (int j = 4; j < 8; ++j) pf[j] = (bf16)__builtin_amdgcn_exp2f(x[j]);
          acc = __builtin_amdgcn_mfma_f32_32x32x16_bf16(vf0, pf, acc, 0, 0, 0);
#pragma unroll
          for (int j = 0; j < 8; ++j) pf[j] = (bf16)__builtin_amdgcn_exp2f(x[8 + j]);
          acc = __builtin_amdgcn_mfma_f32_32x32x16_bf16(vf1, pf, acc, 0, 0, 0);
          if (t == 7 && first) {
            asm volatile("s_barrier" ::: "memory");
            first = false;
          }
          if (c * NT + t == 53 && wave < 4) __builtin_amdgcn_s_setprio(0);
          __builtin_amdgcn_sched_barrier(0);
          x = xn;
        }
        if (__any(!(tot[8] + acc[8] <= 4.3e9f))) break;
        redone = false;
#pragma unroll
        for (int i = 0; i < 9; ++i) tot[i] += acc[i];
        ++c;
        kp += NT * 1024;
        vp += vstep;
        if (c == NC) break;
      }
      if (c == NC) break;
      if (!redone && __any(!(acc[8] < 7.9e28f))) {
        float mx = -INFINITY;
        for (int t = 0; t < NT; ++t) {
          bf16x8 kf = *(const bf16x8*)(kp + t * 1024);
          f32x16 x = __builtin_amdgcn_mfma_f32_32x32x16_bf16(kf, qf, zero, 0, 0, 0);
#pragma unroll
          for (int i = 0; i < 16; i += 2) mx = fmaxf(fmaxf(mx, x[i]), x[i + 1]);
        }
        mx = fmaxf(mx, __shfl_xor(mx, 32));
        const float mn = fmaxf(m, floorf(mx) + HEAD);
        const int d = (int)fminf(mn - m, 1024.f);
#pragma unroll
        for (int i = 0; i < 9; ++i) tot[i] = __builtin_amdgcn_ldexpf(tot[i], -d);
        m = mn;
        redone = true;
      } else {
        redone = false;
#pragma unroll
        for (int i = 0; i < 9; ++i) tot[i] += acc[i];
        const int e = max(__builtin_amdgcn_frexp_expf(tot[8]) - 1 + (int)HEAD, 0);
#pragma unroll
        for (int i = 0; i < 9; ++i) tot[i] = __builtin_amdgcn_ldexpf(tot[i], -e);
        m += (float)e;
        ++c;
        kp += NT * 1024;
        vp += vstep;
        if (c == NC) break;
      }
#pragma unroll
      for (int i = 0; i < 16; ++i) negm[i] = -m;
      asm volatile("" : "+v"(negm));
      x = __builtin_amdgcn_mfma_f32_32x32x16_bf16(*(const bf16x8*)kp, qf, negm, 0, 0, 0);
      kfn = *(const bf16x8*)(kp + 1024);
    }
  }
  {
    float chk = 0.f;
#pragma unroll
    for (int i = 0; i < 9; ++i) chk = __builtin_fmaf(tot[i], 0.f, chk);
    if (__any(chk != 0.f)) {
      f32x16 oacc = zero;
      float mrun = -INFINITY;
      const char* kp = kp0;
      const char* vp = vp0;
      for (int t = 0; t < 64; ++t) {
        bf16x8 kf = *(const bf16x8*)(kp);
        f32x16 x = __builtin_amdgcn_mfma_f32_32x32x16_bf16(kf, qf, zero, 0, 0, 0);
        float mx = x[0];
#pragma unroll
        for (int i = 1; i < 16; ++i) mx = fmaxf(mx, x[i]);
        mx = fmaxf(mx, __shfl_xor(mx, 32));
        const float mn = fmaxf(mrun, mx);
        const float al = __builtin_amdgcn_exp2f(mrun - mn);
        mrun = mn;
#pragma unroll
        for (int i = 0; i < 16; ++i) oacc[i] *= al;
#pragma unroll
        for (int s = 0; s < 2; ++s) {
          bf16x8 pf;
#pragma unroll
          for (int j = 0; j < 8; ++j) pf[j] = (bf16)__builtin_amdgcn_exp2f(x[8 * s + j] - mn);
          bf16x8 vf = *(const bf16x8*)(vp + ((t & (NT - 1)) * 2 + s) * 512);
          oacc = __builtin_amdgcn_mfma_f32_32x32x16_bf16(vf, pf, oacc, 0, 0, 0);
        }
        kp += 1024;
        if ((t & (NT - 1)) == NT - 1) vp += vstep;
      }
#pragma unroll
      for (int i = 0; i < 9; ++i) tot[i] = oacc[i];
    }
  }
  const float inv = 1.f / tot[8];
  {
    unsigned a0 = pack2(f32x2{tot[0] * inv, tot[1] * inv}), a1 = pack2(f32x2{tot[2] * inv, tot[3] * inv});
    unsigned b0 = pack2(f32x2{tot[4] * inv, tot[5] * inv}), b1 = pack2(f32x2{tot[6] * inv, tot[7] * inv});
    const unsigned s0 = __shfl_xor(h ? a0 : b0, 32), s1 = __shfl_xor(h ? a1 : b1, 32);
    const u32x4 o = h ? u32x4{s0, s1, b0, b1} : u32x4{a0, a1, s0, s1};
    const __amdgpu_buffer_rsrc_t ro = __builtin_amdgcn_make_buffer_rsrc(O + token * 512, 0, 1024, 0x00020000);
    __builtin_amdgcn_raw_buffer_store_b128(o, ro, r * 32 + h * 16, 0, 16);
  }
  asm volatile("s_waitcnt vmcnt(0)\n s_barrier" ::: "memory");
}

constexpr int K1_LDS = 64 * XS_STRIDE + 65536 + 16384 + 256;

extern "C" __global__ __launch_bounds__(768) void k1_ln_qkv(const bf16* __restrict__ x, const bf16* __restrict__ wq,
                                                 const bf16* __restrict__ wk, const bf16* __restrict__ wv,
                                                 const bf16* __restrict__ wr, char* __restrict__ ws) {
  float* st = (float*)(ws + WS_ST);
  bf16* Q = (bf16*)(ws + WS_Q);
  bf16* Kb = (bf16*)(ws + WS_K);
  bf16* Vf = (bf16*)(ws + WS_V);
  unsigned* cand = (unsigned*)(ws + WS_R);
  unsigned* gcnt = (unsigned*)(ws + WS_CNT);
  extern __shared__ __attribute__((aligned(16))) char smem[];
  char* ximg = smem;
  char* wimg = smem + 64 * XS_STRIDE;
  char* kvimg = smem + 64 * XS_STRIDE + 65536;
  float* rs = (float*)(smem + 64 * XS_STRIDE + 65536 + 16384);
  const int cb = blockIdx.x >> 5, tb = blockIdx.x & 31;
  const int tid = threadIdx.x, lane = tid & 63;
  const int wave = __builtin_amdgcn_readfirstlane(tid >> 6);
  const int tok0 = tb * 64;

  if (wave >= 8) {
    if (blockIdx.x == 255 && tid >= 768 - (NGRP + 1)) gcnt[(767 - tid) * 64] = 0;
    if (blockIdx.x == 254 && tid >= 768 - 33) ((unsigned*)(ws + WS_CL))[(767 - tid) * 64] = 0;
    stage_w(wq, cb * 64, wimg, wave - 8, 4, lane);
    if (cb < 2) {
      const bf16* w = cb == 0 ? wk : wv;
#pragma unroll
      for (int i = 0; i < 4; ++i) {
        const int p = wave - 8 + 4 * i;
        glds16((const char*)w + p * 1024 + lane * 16, kvimg + p * 1024);
      }
    }
    __syncthreads();
    if (cb == 3 && wave == 8) {
      unsigned k1 = route_key(rs[lane], tok0 + lane), k2 = 0;
      top2_wave(k1, k2, 64);
      if (lane == 0) *(u32x2*)(cand + tb * 2) = u32x2{k1, k2};
    }
    return;
  }

  const int tl = wave * 8 + (lane >> 3), c8 = lane & 7;
  u32x4 xv[8];
#pragma unroll
  for (int i = 0; i < 8; ++i) xv[i] = *(const u32x4*)(x + (tok0 + tl) * 512 + (c8 + 8 * i) * 8);
  u32x4 wrv[8];
  if (cb == 3) {
#pragma unroll
    for (int i = 0; i < 8; ++i) wrv[i] = *(const u32x4*)(wr + (c8 + 8 * i) * 8);
  }

  f32x2 xf[32];
#pragma unroll
  for (int i = 0; i < 32; ++i) xf[i] = unpack2(xv[i >> 2][i & 3]);
  f32x2 s2 = {0.f, 0.f};
#pragma unroll
  for (int i = 0; i < 32; ++i) s2 += xf[i];
  const float mean = sum8(s2.x + s2.y) * (1.f / 512.f);
  const f32x2 mean2 = {mean, mean};
  f32x2 v2 = {0.f, 0.f};
#pragma unroll
  for (int i = 0; i < 32; ++i) { const f32x2 d = xf[i] - mean2; v2 += d * d; }
  const float vs = sum8(v2.x + v2.y);
  const float mb = rbf(mean);
  const float vb = rbf(vs * (1.f / 512.f));
  const float den = rbf(sqrtf(rbf(vb + 1e-5f)));
  const float rinv = __builtin_amdgcn_rcpf(den);
  const f32x2 mb2 = {mb, mb}, rinv2 = {rinv, rinv};
  u32x4 o[8];
#pragma unroll
  for (int i = 0; i < 8; ++i) {
#pragma unroll
    for (int j = 0; j < 4; ++j) o[i][j] = ln2(xf[4 * i + j], mb2, rinv2);
    *(u32x4*)(ximg + tl * XS_STRIDE + (c8 + 8 * i) * 16) = o[i];
  }
  if (cb == 2 && c8 == 0) *(f32x2*)(st + (tok0 + tl) * 2) = f32x2{mb, rinv};
  if (cb == 3) {
    float rd = 0.f;
#pragma unroll
    for (int i = 0; i < 8; ++i) {
#pragma unroll
      for (int j = 0; j < 4; ++j) {
        const unsigned a = o[i][j], b = wrv[i][j];
        rd = __builtin_amdgcn_fdot2_f32_bf16(__builtin_bit_cast(bf16x2, a), __builtin_bit_cast(bf16x2, b), rd, false);
      }
    }
    rd = sum8(rd);
    if (c8 == 0) rs[tl] = rbf(rd);
  }
  __syncthreads();

  const int r = lane & 31, h = lane >> 5;
  if (wave < 4) {
    const int n0 = (wave & 1) * 32, t0 = (wave >> 1) * 32;
    f32x16 acc = gemm_tile(wimg, ximg, n0, t0, lane);
    const float sc = 0.25f * 1.4426950408889634f;
    bf16* qrow = Q + (tok0 + t0 + r) * 512 + cb * 64 + n0;
#pragma unroll
    for (int g = 0; g < 4; ++g) {
      u32x2 o;
#pragma unroll
      for (int jj = 0; jj < 2; ++jj)
        o[jj] = pack2(unpack2(pack2(f32x2{acc[4 * g + 2 * jj], acc[4 * g + 2 * jj + 1]})) * f32x2{sc, sc});
      *(u32x2*)(qrow + 8 * g + 4 * h) = o;
    }
  } else if (wave == 7) {
    if (lane < 2) {
      const unsigned t = *(const volatile unsigned*)(lane ? ws + WS_K + tb * 2048 : (const char*)(Q + tok0 * 512 + cb * 64));
      asm volatile("" ::"v"(t));
    }
  } else if (wave == 6 && cb == 7 && tb < 8) {
    unsigned long long c0, c1;
    CODE_ADDR(k2_attn, c0);
    CODE_ADDR(k1_ln_qkv, c1);
    const long long n = (long long)(c1 - c0);
    unsigned t[4];
#pragma unroll
    for (int i = 0; i < 4; ++i) {
      const int off = (i * 64 + lane) * 128;
      t[i] = off < n ? *(const volatile unsigned*)(c0 + off) : 0u;
    }
#pragma unroll
    for (int i = 0; i < 4; ++i) asm volatile("" ::"v"(t[i]));
  } else if (wave < 6 && cb < 2) {
    const int t0 = (wave - 4) * 32;
    f32x16 acc = gemm_tile16(kvimg, ximg, t0, lane);
    const int key = tok0 + t0 + r;
    const int t = key >> 5;
    if (cb == 0) {
#pragma unroll
      for (int g = 0; g < 2; ++g) {
        bf16x4 o;
#pragma unroll
        for (int j = 0; j < 4; ++j) o[j] = (bf16)acc[4 * g + j];
        *(bf16x4*)(Kb + ((t * 2 + g) * 32 + r) * 8 + 4 * h) = o;
      }
    } else {
      const int kk = key & 31, sg = kk >> 4, rem = kk & 15;
      const int hh = (rem >> 2) & 1, jj = ((rem >> 3) << 2) | (rem & 3);
#pragma unroll
      for (int g = 0; g < 2; ++g) {
#pragma unroll
        for (int j = 0; j < 4; ++j) {
          const int d = 8 * g + 4 * h + j;
          Vf[(((t * 2 + sg) * 2 + hh) * 16 + d) * 8 + jj] = (bf16)acc[4 * g + j];
        }
      }
    }
  }
}

extern "C" void kernel_launch(void* const* d_in, const int* in_sizes, int n_in, void* d_out, int out_size,
                              void* d_ws, size_t ws_size, hipStream_t stream) {
  const bf16* x = (const bf16*)d_in[0];
  const bf16* wq = (const bf16*)d_in[1];
  const bf16* wk = (const bf16*)d_in[2];
  const bf16* wv = (const bf16*)d_in[3];
  const bf16* wo = (const bf16*)d_in[4];
  const bf16* wr = (const bf16*)d_in[6];
  const bf16* wg = (const bf16*)d_in[7];
  const bf16* wu = (const bf16*)d_in[8];
  const bf16* wd = (const bf16*)d_in[9];
  char* ws = (char*)d_ws;
  bf16* out = (bf16*)d_out;

  (void)hipFuncSetAttribute((const void*)k1_ln_qkv, hipFuncAttributeMaxDynamicSharedMemorySize, K1_LDS);
  (void)hipFuncSetAttribute((const void*)k2_attn, hipFuncAttributeMaxDynamicSharedMemorySize, ATT_LDS);

  k1_ln_qkv<<<256, 768, K1_LDS, stream>>>(x, wq, wk, wv, wr, ws);
  k2_attn<<<256, 768, ATT_LDS, stream>>>(ws, x, wg, wu, wd, wo, out);
}
```

```cpp
#include <hip/hip_runtime.h>
#include <hip/hip_bf16.h>

typedef __attribute__((ext_vector_type(8))) __bf16 bf16x8;
typedef __attribute__((ext_vector_type(4))) __bf16 bf16x4;
typedef __attribute__((ext_vector_type(2))) __bf16 bf16x2;
typedef __attribute__((ext_vector_type(4))) short s16x4;
typedef __attribute__((ext_vector_type(8))) short s16x8;
typedef __attribute__((ext_vector_type(16))) float f32x16;
typedef __attribute__((ext_vector_type(4))) float f32x4;
typedef __attribute__((ext_vector_type(4))) unsigned u32x4;
typedef __attribute__((ext_vector_type(2))) unsigned u32x2;
typedef __attribute__((ext_vector_type(2))) float f32x2;
typedef __bf16 bf16;

#define LDS_AS __attribute__((address_space(3)))
#define GLB_AS __attribute__((address_space(1)))
#define DI __device__ __forceinline__

constexpr int S_ = 2048, D_ = 512, H_ = 32, DH_ = 16, F_ = 1376;

constexpr size_t WS_Q = 0;
constexpr size_t WS_O = WS_Q;
constexpr size_t WS_K = 2u << 20;
constexpr size_t WS_V = WS_K + 65536;
constexpr size_t WS_ST = WS_V + 65536;
constexpr size_t WS_R = WS_ST + 16384;
constexpr size_t WS_CNT = WS_R + 4096;
constexpr size_t WS_UPD = WS_CNT + 4096;
constexpr size_t WS_YP = WS_UPD + 4096;

constexpr int NFFN = 172;
constexpr int GSZ = 12, NGRP = 15;
constexpr size_t WS_CL = WS_YP + (NFFN + NGRP) * 4096;

DI float bf2f(bf16 v) { return (float)v; }
DI float rbf(float v) { return (float)(bf16)v; }
DI f32x2 unpack2(unsigned u) { return f32x2{__uint_as_float(u << 16), __uint_as_float(u & 0xffff0000u)}; }
DI unsigned pack2(f32x2 v) {
  unsigned r = __builtin_bit_cast(unsigned, __builtin_convertvector(v, bf16x2));
  asm("" : "+v"(r));
  return r;
}

DI unsigned route_key(float v, int tok) {
  const unsigned u = __float_as_uint(v) >> 16;
  const unsigned o = (u & 0x8000u) ? (~u & 0xffffu) : (u | 0x8000u);
  return (o << 16) | (unsigned)(0xffff - tok);
}
DI int key_tok(unsigned k) { return 0xffff - (int)(k & 0xffffu); }
DI float key_val(unsigned k) {
  const unsigned o = k >> 16;
  const unsigned u = (o & 0x8000u) ? (o & 0x7fffu) : (~o & 0xffffu);
  return __uint_as_float(u << 16);
}
DI unsigned ln2(f32x2 x, f32x2 mb2, f32x2 rinv2) { return pack2(unpack2(pack2(x - mb2)) * rinv2); }
DI void top2_wave(unsigned& k1, unsigned& k2, int width) {
  for (int o = width >> 1; o > 0; o >>= 1) {
    const unsigned a1 = __shfl_xor(k1, o), a2 = __shfl_xor(k2, o);
    k2 = max(min(k1, a1), max(k2, a2));
    k1 = max(k1, a1);
  }
}

template <int O>
DI void top2_step(unsigned& k1, unsigned& k2) {
  const unsigned a1 = __builtin_amdgcn_ds_swizzle(k1, (O << 10) | 31), a2 = __builtin_amdgcn_ds_swizzle(k2, (O << 10) | 31);
  k2 = max(min(k1, a1), max(k2, a2));
  k1 = max(k1, a1);
}

DI void glds16(const void* g, void* l) {
  __builtin_amdgcn_global_load_lds((const GLB_AS void*)g, (LDS_AS void*)l, 16, 0, 0);
}

DI void glds16_raw(const void* g, unsigned lds_off) {
  asm volatile("s_mov_b32 m0, %0\n s_nop 0\n global_load_lds_dwordx4 %1, off" ::"s"(lds_off), "v"(g) : "memory", "m0");
}

template <int CTRL>
DI float dpp_add(float v) {
  const int t = __builtin_amdgcn_update_dpp(0, __float_as_int(v), CTRL, 0xf, 0xf, true);
  return v + __int_as_float(t);
}
DI float sum8(float v) {
  v = dpp_add<0xB1>(v);
  v = dpp_add<0x4E>(v);
  v = dpp_add<0x141>(v);
  return v;
}

DI s16x4 tr_read(const char* p) {
  return __builtin_amdgcn_ds_read_tr16_b64_v4i16((LDS_AS s16x4*)p);
}

DI int wimg_off(int row, int ch) { return row * 128 + ((ch ^ (((row >> 1) & 1) << 2)) << 4); }

constexpr int XS_STRIDE = 1040;

DI void stage_w(const bf16* w, int col0, char* wimg, int wave, int nwaves, int lane) {
  for (int p = wave; p < 64; p += nwaves) {
    const int row = p * 8 + (lane >> 3), pc = lane & 7;
    const int ch = pc ^ (((row >> 1) & 1) << 2);
    glds16(w + row * 512 + col0 + ch * 8, wimg + p * 1024);
  }
}

constexpr int GP = 4;
DI f32x16 gemm_tile(const char* wimg, const char* ximg, int n0, int t0, int lane) {
  const int r = lane & 31, h = lane >> 5;
  const int q = (lane & 15) >> 2, p = lane & 3, blk = (lane >> 4) & 1;
  f32x16 acc;
#pragma unroll
  for (int i = 0; i < 16; ++i) acc[i] = 0.f;
  const char* xb = ximg + (t0 + r) * XS_STRIDE + 16 * h;
  const int col = n0 + 16 * blk + 4 * p;
  const int ch = col >> 3, sub = (col & 7) * 2;
  const char* wb0 = wimg + wimg_off(8 * h + q, ch) + sub;
  const char* wb1 = wimg + wimg_off(8 * h + q + 4, ch) + sub;
  s16x4 lo[32], hi[32];
  bf16x8 b[32];
#pragma unroll
  for (int ks = 0; ks < 32 + GP; ++ks) {
    if (ks < 32) {
      lo[ks] = tr_read(wb0 + ks * 2048);
      hi[ks] = tr_read(wb1 + ks * 2048);
      b[ks] = *(const bf16x8*)(xb + ks * 32);
    }
    if (ks >= GP) {
      const s16x8 a = __builtin_shufflevector(lo[ks - GP], hi[ks - GP], 0, 1, 2, 3, 4, 5, 6, 7);
      acc = __builtin_amdgcn_mfma_f32_32x32x16_bf16(__builtin_bit_cast(bf16x8, a), b[ks - GP], acc, 0, 0, 0);
      asm volatile("" : "+v"(acc));
    }
    __builtin_amdgcn_sched_barrier(0);
  }
  return acc;
}
DI f32x16 gemm_tile16(const char* wimg, const char* ximg, int t0, int lane) {
  const int r = lane & 31, h = lane >> 5;
  const int q = (lane & 15) >> 2, p = lane & 3;
  f32x16 acc;
#pragma unroll
  for (int i = 0; i < 16; ++i) acc[i] = 0.f;
  const char* xb = ximg + (t0 + r) * XS_STRIDE + 16 * h;
  const char* wb = wimg + (8 * h + q) * 32 + 8 * p;
  s16x4 lo[32], hi[32];
  bf16x8 b[32];
#pragma unroll
  for (int ks = 0; ks < 32 + GP; ++ks) {
    if (ks < 32) {
      lo[ks] = tr_read(wb + ks * 512);
      hi[ks] = tr_read(wb + ks * 512 + 128);
      b[ks] = *(const bf16x8*)(xb + ks * 32);
    }
    if (ks >= GP) {
      const s16x8 a = __builtin_shufflevector(lo[ks - GP], hi[ks - GP], 0, 1, 2, 3, 4, 5, 6, 7);
      acc = __builtin_amdgcn_mfma_f32_32x32x16_bf16(__builtin_bit_cast(bf16x8, a), b[ks - GP], acc, 0, 0, 0);
      asm volatile("" : "+v"(acc));
    }
    __builtin_amdgcn_sched_barrier(0);
  }
  return acc;
}

#define CODE_ADDR(sym, out)                                                                               \
  asm volatile("s_getpc_b64 s[20:21]\n s_add_u32 s20, s20, " #sym "@rel32@lo+4\n s_addc_u32 s21, s21, " #sym \
               "@rel32@hi+12\n s_mov_b64 %0, s[20:21]"                                                    \
               : "=s"(out)::"s20", "s21", "scc")

constexpr int NT = 16;
constexpr int CST = NT * 1024;
constexpr int ATT_LDS = 65536 * 2 + CST + 64 + 512 + 4 * 640;
constexpr int NC = 64 / NT;
constexpr int SPIN_MAX = 1 << 22;

DI bool last_arrival(int* w, unsigned* gc, int n, int lane) {
  asm volatile("s_waitcnt vmcnt(0)" ::: "memory");
  if (lane == 0) {
    if (__hip_atomic_fetch_add(w, 1, __ATOMIC_RELAXED, __HIP_MEMORY_SCOPE_WORKGROUP) == 3) {
      const unsigned t = __hip_atomic_fetch_add((GLB_AS unsigned*)gc, 1u, __ATOMIC_RELAXED, __HIP_MEMORY_SCOPE_AGENT);
      __hip_atomic_store(w + 1, t == (unsigned)(n - 1) ? 2 : 1, __ATOMIC_RELAXED, __HIP_MEMORY_SCOPE_WORKGROUP);
    }
  }
  int r;
  while ((r = __hip_atomic_load(w + 1, __ATOMIC_RELAXED, __HIP_MEMORY_SCOPE_WORKGROUP)) == 0) __builtin_amdgcn_s_sleep(2);
  asm volatile("" ::: "memory");
  return r == 2;
}

DI void ffn_slice(int f, int j, const bf16* __restrict__ x, const float* __restrict__ st,
                  const unsigned* __restrict__ cand, unsigned* __restrict__ fdone,
                  const bf16* __restrict__ wg, const bf16* __restrict__ wu, const bf16* __restrict__ wd,
                  float* __restrict__ ypart, unsigned* __restrict__ gcnt,
                  float* __restrict__ upd, float* red, int* cnt, char* scr, int lane) {
  const int k0 = j * 128 + lane * 2;
  const int r = lane & 31, h = lane >> 5;
  const int kr = (lane & 51) | ((lane & 4) << 1) | ((lane & 8) >> 1);
  u32x4 wf[4];
  unsigned d2[8];
  {
    const __amdgpu_buffer_rsrc_t rg = __builtin_amdgcn_make_buffer_rsrc((void*)wg, 0, 512 * F_ * 2, 0x00020000);
    const __amdgpu_buffer_rsrc_t ru = __builtin_amdgcn_make_buffer_rsrc((void*)wu, 0, 512 * F_ * 2, 0x00020000);
    const __amdgpu_buffer_rsrc_t rd = __builtin_amdgcn_make_buffer_rsrc((void*)wd, 0, 512 * F_ * 2, 0x00020000);
    const int wo_ = kr * (F_ * 2), so = (j * 128 * F_ + f * 8) * 2;
#pragma unroll
    for (int t = 0; t < 2; ++t) {
      wf[2 * t] = __builtin_amdgcn_raw_buffer_load_b128(rg, wo_, so + t * (64 * F_ * 2), 0);
      wf[2 * t + 1] = __builtin_amdgcn_raw_buffer_load_b128(ru, wo_, so + t * (64 * F_ * 2), 0);
    }
#pragma unroll
    for (int c = 0; c < 8; ++c)
      d2[c] = __builtin_amdgcn_raw_buffer_load_b32(rd, 8 * r + 4 * h + c * 1024, (f * 8 * 512 + j * 128) * 2, 0);
  }
  const u32x2 cv = *(const u32x2*)(cand + (lane & 31) * 2);
  unsigned k1 = cv[0], k2 = cv[1];
  top2_step<16>(k1, k2);
  top2_step<8>(k1, k2);
  top2_step<4>(k1, k2);
  top2_step<2>(k1, k2);
  top2_step<1>(k1, k2);
  k1 = __builtin_amdgcn_readfirstlane(k1);
  k2 = __builtin_amdgcn_readfirstlane(k2);
  const int i1 = key_tok(k1), i2 = key_tok(k2);
  const f32x2 sa = *(const f32x2*)(st + i1 * 2), sb = *(const f32x2*)(st + i2 * 2);
  *(unsigned*)(scr + lane * 4) = ln2(unpack2(*(const unsigned*)(x + i1 * 512 + k0)), f32x2{sa.x, sa.x}, f32x2{sa.y, sa.y});
  *(unsigned*)(scr + 256 + lane * 4) = ln2(unpack2(*(const unsigned*)(x + i2 * 512 + k0)), f32x2{sb.x, sb.x}, f32x2{sb.y, sb.y});
  f32x16 zero;
#pragma unroll
  for (int i = 0; i < 16; ++i) zero[i] = 0.f;
  u32x4 ida, idb;
  {
    const int d = r - 8 * h;
    const unsigned one = 0x3f80u << ((d & 1) << 4);
#pragma unroll
    for (int q = 0; q < 4; ++q) {
      ida[q] = (d >> 1) == q ? one : 0u;
      idb[q] = (d >> 1) == q + 8 ? one : 0u;
    }
  }
  f32x16 gu = zero;
#pragma unroll
  for (int t = 0; t < 2; ++t) {
    f32x16 wt = __builtin_amdgcn_mfma_f32_32x32x16_bf16(__builtin_bit_cast(bf16x8, wf[2 * t]),
                                                        __builtin_bit_cast(bf16x8, ida), zero, 0, 0, 0);
    wt = __builtin_amdgcn_mfma_f32_32x32x16_bf16(__builtin_bit_cast(bf16x8, wf[2 * t + 1]),
                                                 __builtin_bit_cast(bf16x8, idb), wt, 0, 0, 0);
#pragma unroll
    for (int q = 0; q < 2; ++q) {
      u32x4 bw;
#pragma unroll
      for (int i = 0; i < 4; ++i) bw[i] = pack2(f32x2{wt[8 * q + 2 * i], wt[8 * q + 2 * i + 1]});
      const bf16x8 xf = *(const bf16x8*)(scr + (r & 1) * 256 + (r & 2) * 32 + (64 * t + 16 * q + 8 * h) * 2);
      gu = __builtin_amdgcn_mfma_f32_32x32x16_bf16(xf, __builtin_bit_cast(bf16x8, bw), gu, 0, 0, 0);
    }
  }
  {
    const float g0 = dpp_add<0x128>((lane & 8) ? gu[2] : gu[0]), g1 = dpp_add<0x128>((lane & 8) ? gu[3] : gu[1]);
    if (lane < 32) red[j * 32 + (lane & 16) + (lane & 15)] = (lane & 8) ? g1 : g0;
  }
  if (lane == 0) __hip_atomic_fetch_add(cnt, 1, __ATOMIC_RELEASE, __HIP_MEMORY_SCOPE_WORKGROUP);
  while (__hip_atomic_load(cnt, __ATOMIC_ACQUIRE, __HIP_MEMORY_SCOPE_WORKGROUP) < 4) __builtin_amdgcn_s_sleep(1);
  const int l = lane & 15;
  const float g = rbf((red[l] + red[32 + l]) + (red[64 + l] + red[96 + l]));
  const float u = rbf((red[16 + l] + red[48 + l]) + (red[80 + l] + red[112 + l]));
  const float hv = rbf(g * __builtin_amdgcn_rcpf(1.f + __expf(-g))) * u;
  if (lane < 24) *(bf16*)(scr + 512 + lane * 2) = (bf16)(lane < 16 ? hv : 0.f);
  const bf16x8 hf = *(const bf16x8*)(scr + 512 + ((r >> 1) == h ? (r & 1) * 16 : 32));
  u32x4 da, db;
#pragma unroll
  for (int c = 0; c < 4; ++c) {
    da[c] = __builtin_amdgcn_perm(d2[2 * c + 1], d2[2 * c], 0x05040100u);
    db[c] = __builtin_amdgcn_perm(d2[2 * c + 1], d2[2 * c], 0x07060302u);
  }
  const f32x16 ya = __builtin_amdgcn_mfma_f32_32x32x16_bf16(hf, __builtin_bit_cast(bf16x8, da), zero, 0, 0, 0);
  const f32x16 yb = __builtin_amdgcn_mfma_f32_32x32x16_bf16(hf, __builtin_bit_cast(bf16x8, db), zero, 0, 0, 0);
  const __amdgpu_buffer_rsrc_t rs = __builtin_amdgcn_make_buffer_rsrc(ypart, 0, (NFFN + NGRP) * 4096, 0x00020000);
  if (lane < 32) {
#pragma unroll
    for (int t = 0; t < 2; ++t)
      __builtin_amdgcn_raw_buffer_store_b128(__builtin_bit_cast(u32x4, f32x4{ya[t], yb[t], ya[2 + t], yb[2 + t]}), rs,
                                             t * 2048 + j * 512 + lane * 16, f * 4096, 16);
  }
  const int grp = f / GSZ, gs = min(GSZ, NFFN - grp * GSZ);
  if (!last_arrival(cnt + 1, gcnt + grp * 64, gs, lane)) return;
  const int voff = j * 1024 + lane * 16;
  f32x4 a = {0.f, 0.f, 0.f, 0.f};
  {
    u32x4 t[GSZ];
#pragma unroll
    for (int i = 0; i < GSZ; ++i) t[i] = __builtin_amdgcn_raw_buffer_load_b128(rs, voff, (grp * GSZ + min(i, gs - 1)) * 4096, 16);
#pragma unroll
    for (int i = 0; i < GSZ; ++i)
      if (i < gs) a += __builtin_bit_cast(f32x4, t[i]);
  }
  __builtin_amdgcn_raw_buffer_store_b128(__builtin_bit_cast(u32x4, a), rs, voff, (NFFN + grp) * 4096, 16);
  if (!last_arrival(cnt + 3, gcnt + NGRP * 64, NGRP, lane)) return;
  a = f32x4{0.f, 0.f, 0.f, 0.f};
  {
    u32x4 t[NGRP];
#pragma unroll
    for (int i = 0; i < NGRP; ++i) t[i] = __builtin_amdgcn_raw_buffer_load_b128(rs, voff, (NFFN + i) * 4096, 16);
#pragma unroll
    for (int i = 0; i < NGRP; ++i) a += __builtin_bit_cast(f32x4, t[i]);
  }
  const float gate = rbf(1.f / (1.f + __expf(-key_val(j < 2 ? k1 : k2))));
#pragma unroll
  for (int i = 0; i < 4; ++i) a[i] = rbf(rbf(a[i]) * gate);
  {
    const __amdgpu_buffer_rsrc_t ru = __builtin_amdgcn_make_buffer_rsrc(upd, 0, 4096, 0x00020000);
    __builtin_amdgcn_raw_buffer_store_b128(__builtin_bit_cast(u32x4, a), ru, voff, 0, 16);
  }
  asm volatile("s_waitcnt vmcnt(0)" ::: "memory");
  if (lane == 0 && __hip_atomic_fetch_add(cnt + 5, 1, __ATOMIC_RELAXED, __HIP_MEMORY_SCOPE_WORKGROUP) == 3)
    __hip_atomic_store((GLB_AS unsigned*)fdone, 1u, __ATOMIC_RELAXED, __HIP_MEMORY_SCOPE_AGENT);
}

extern "C" __global__ __launch_bounds__(768) void k2_attn(char* __restrict__ ws, const bf16* __restrict__ x,
                                               const bf16* __restrict__ wg, const bf16* __restrict__ wu,
                                               const bf16* __restrict__ wd, const bf16* __restrict__ wo,
                                               bf16* __restrict__ out) {
  const bf16* Q = (const bf16*)(ws + WS_Q);
  const bf16* Kb = (const bf16*)(ws + WS_K);
  bf16* O = (bf16*)(ws + WS_O);
  const float* st = (const float*)(ws + WS_ST);
  const unsigned* cand = (const unsigned*)(ws + WS_R);
  float* ypart = (float*)(ws + WS_YP);
  unsigned* gcnt = (unsigned*)(ws + WS_CNT);
  float* upd = (float*)(ws + WS_UPD);
  extern __shared__ __attribute__((aligned(16))) char smem[];
  char* kl = smem;
  char* vl = smem + 65536;
  char* cst = smem + 131072;
  const int tid = threadIdx.x, lane = tid & 63;
  const int wave = __builtin_amdgcn_readfirstlane(tid >> 6);

  if (wave >= 8) {
    int* cnt = (int*)(smem + 131072 + CST + 32);
    if (tid < 512 + 6) cnt[tid - 512] = 0;
#pragma unroll
    for (int i = 0; i < 26; ++i) {
      const int q = (wave - 8) + 4 * i, piece = q < 48 ? 16 + q : 24 + q;
      glds16_raw((const char*)Kb + piece * 1024 + lane * 16, (unsigned)(size_t)(LDS_AS char*)smem + piece * 1024);
    }
    asm volatile("s_waitcnt lgkmcnt(0)\n s_barrier\n s_waitcnt vmcnt(0)\n s_barrier" ::: "memory");
    unsigned* clc = (unsigned*)(ws + WS_CL);
    __builtin_amdgcn_s_setprio(3);
    const int fs = (blockIdx.x & 7) * 22 + (blockIdx.x >> 3);
    if (blockIdx.x < 176 && fs < NFFN) {
      ffn_slice(fs, wave - 8, x, st, cand, clc + 32 * 64, wg, wu, wd, ypart, gcnt, upd,
                (float*)(smem + 131072 + CST + 64), cnt, smem + 131072 + CST + 576 + (wave - 8) * 640, lane);
    }
    const int tb = (blockIdx.x >> 6) * 8 + (blockIdx.x & 7), cb = (blockIdx.x >> 3) & 7, tok0 = tb * 64;
    const int sw = wave - 8, r = lane & 31, h = lane >> 5;
    char* ximg = smem;
    char* wimg = smem + 64 * XS_STRIDE;
    u32x4 wreg[16];
#pragma unroll
    for (int i = 0; i < 16; ++i) {
      const int row = (sw + 4 * i) * 8 + (lane >> 3);
      wreg[i] = *(const u32x4*)(wo + row * 512 + cb * 64 + (((lane & 7) ^ (((row >> 1) & 1) << 2)) << 3));
    }
    const int n0 = (sw & 1) * 32, t0 = (sw >> 1) * 32;
    const int tok = tok0 + t0 + r;
    u32x2 xv[4], nv[4];
#pragma unroll
    for (int g = 0; g < 4; ++g) xv[g] = *(const u32x2*)(x + tok * 512 + cb * 64 + n0 + 8 * g + 4 * h);
    const f32x2 sx = *(const f32x2*)(st + tok * 2);
    int rsel;
    {
      const u32x2 cv = *(const u32x2*)(cand + (lane & 31) * 2);
      unsigned k1 = cv[0], k2 = cv[1];
      top2_step<16>(k1, k2);
      top2_step<8>(k1, k2);
      top2_step<4>(k1, k2);
      top2_step<2>(k1, k2);
      top2_step<1>(k1, k2);
      rsel = tok == key_tok(k1) ? 0 : tok == key_tok(k2) ? 1 : -1;
    }
#pragma unroll
    for (int g = 0; g < 4; ++g) {
#pragma unroll
      for (int jj = 0; jj < 2; ++jj) {
        unsigned nr = ln2(unpack2(xv[g][jj]), f32x2{sx.x, sx.x}, f32x2{sx.y, sx.y});
        asm volatile("" : "+v"(nr));
        nv[g][jj] = nr;
      }
    }
    if (wave == 11 && lane == 0) {
      const unsigned t = *(const volatile unsigned*)(out + tok0 * 512 + cb * 64);
      asm volatile("" ::"v"(t));
    }
#pragma unroll
    for (int i = 0; i < 16; ++i) asm volatile("" : "+v"(wreg[i]));
    asm volatile("s_barrier" ::: "memory");
    if (tid == 512) __hip_atomic_fetch_add((GLB_AS unsigned*)(clc + tb * 64), 1u, __ATOMIC_RELAXED, __HIP_MEMORY_SCOPE_AGENT);
#pragma unroll
    for (int i = 0; i < 16; ++i) *(u32x4*)(wimg + (sw + 4 * i) * 1024 + lane * 16) = wreg[i];
    if (__any(rsel >= 0)) {
      int spin = 0;
      while (__hip_atomic_load((GLB_AS unsigned*)(clc + 32 * 64), __ATOMIC_RELAXED, __HIP_MEMORY_SCOPE_AGENT) == 0 && ++spin < SPIN_MAX)
        __builtin_amdgcn_s_sleep(1);
      asm volatile("" ::: "memory");
      const __amdgpu_buffer_rsrc_t ru = __builtin_amdgcn_make_buffer_rsrc(upd, 0, 4096, 0x00020000);
      if (rsel >= 0) {
#pragma unroll
        for (int g = 0; g < 4; ++g) {
          const f32x4 u = __builtin_bit_cast(f32x4, __builtin_amdgcn_raw_buffer_load_b128(ru, (rsel * 512 + cb * 64 + n0 + 8 * g + 4 * h) * 4, 0, 16));
          nv[g][0] = pack2(unpack2(nv[g][0]) + f32x2{u[0], u[1]});
          nv[g][1] = pack2(unpack2(nv[g][1]) + f32x2{u[2], u[3]});
        }
      }
    }
    if (wave == 8) {
      int spin = 0;
      while (__hip_atomic_load((GLB_AS unsigned*)(clc + tb * 64), __ATOMIC_RELAXED, __HIP_MEMORY_SCOPE_AGENT) < 8 && ++spin < SPIN_MAX)
        __builtin_amdgcn_s_sleep(1);
    }
    asm volatile("s_barrier" ::: "memory");
    {
      const __amdgpu_buffer_rsrc_t ro = __builtin_amdgcn_make_buffer_rsrc(O + tok0 * 512, 0, 65536, 0x00020000);
      u32x4 t[16];
#pragma unroll
      for (int i = 0; i < 16; ++i) t[i] = __builtin_amdgcn_raw_buffer_load_b128(ro, lane * 16, (sw * 16 + i) * 1024, 16);
#pragma unroll
      for (int i = 0; i < 16; ++i) *(u32x4*)(ximg + (sw * 16 + i) * XS_STRIDE + lane * 16) = t[i];
    }
    asm volatile("s_waitcnt lgkmcnt(0)\n s_barrier" ::: "memory");
    f32x16 acc = gemm_tile(wimg, ximg, n0, t0, lane);
#pragma unroll
    for (int g = 0; g < 4; ++g) {
      u32x2 o;
#pragma unroll
      for (int jj = 0; jj < 2; ++jj) {
        const unsigned xr = xv[g][jj], nr = nv[g][jj];
        const f32x2 a = unpack2(pack2(f32x2{acc[4 * g + 2 * jj], acc[4 * g + 2 * jj + 1]}));
        o[jj] = pack2(unpack2(pack2(unpack2(xr) + a)) + unpack2(nr));
      }
      *(u32x2*)(out + tok * 512 + cb * 64 + n0 + 8 * g + 4 * h) = o;
    }
    return;
  }

  const int r = lane & 31, h = lane >> 5;
  const int token = ((blockIdx.x >> 6) * 8 + (blockIdx.x & 7)) * 64 + ((blockIdx.x >> 3) & 7) * 8 + wave;
  bf16x8 qf;
  asm volatile("global_load_dwordx4 %0, %1, off" : "=v"(qf) : "v"(Q + token * 512 + r * 16 + 8 * h) : "memory");
  const unsigned lds0 = (unsigned)(size_t)(LDS_AS char*)smem;
#pragma unroll
  for (int i = 0; i < 3; ++i) {
    const int piece = (i < 2) ? (wave + i * 8) : (64 + wave);
    glds16_raw((const char*)Kb + piece * 1024 + lane * 16, lds0 + piece * 1024);
  }
  {
    const unsigned cv = ((tid & 15) == 0 || (tid & 15) == 4) ? 0x3f803f80u : 0u;
#pragma unroll
    for (int i = 0; i < CST / 8192; ++i) *(u32x4*)(cst + i * 8192 + tid * 16) = u32x4{cv, cv, cv, cv};
  }
  asm volatile("s_waitcnt vmcnt(0) lgkmcnt(0)\n s_barrier" ::: "memory");

  const char* kp0 = kl + lane * 16;
  const char* vp0 = (r < 16) ? (vl + (h * 16 + r) * 16) : (cst + (h * 16 + r - 16) * 16);
  const int vstep = (r < 16) ? NT * 1024 : 0;

  f32x16 zero;
#pragma unroll
  for (int i = 0; i < 16; ++i) zero[i] = 0.f;
  constexpr float HEAD = 40.f;
  float m;
  {
    float mx = -INFINITY;
#pragma unroll
    for (int t = 0; t < 4; ++t) {
      bf16x8 kf = *(const bf16x8*)(kp0 + t * 1024);
      f32x16 x = __builtin_amdgcn_mfma_f32_32x32x16_bf16(kf, qf, zero, 0, 0, 0);
#pragma unroll
      for (int i = 0; i < 16; i += 2) mx = fmaxf(fmaxf(mx, x[i]), x[i + 1]);
    }
    mx = fmaxf(mx, __shfl_xor(mx, 32));
    m = floorf(mx) + HEAD;
  }
  f32x16 negm;
#pragma unroll
  for (int i = 0; i < 16; ++i) negm[i] = -m;
  asm volatile("" : "+v"(negm));
  float tot[9];
#pragma unroll
  for (int i = 0; i < 9; ++i) tot[i] = 0.f;
  {
    const char* kp = kp0;
    const char* vp = vp0;
    int c = 0;
    bool redone = false, first = true;
    if (wave < 4) __builtin_amdgcn_s_setprio(2);
    else __builtin_amdgcn_s_setprio(1);
    f32x16 x = __builtin_amdgcn_mfma_f32_32x32x16_bf16(*(const bf16x8*)kp, qf, negm, 0, 0, 0);
    bf16x8 kfn = *(const bf16x8*)(kp + 1024);
    while (true) {
      f32x16 acc;
      while (true) {
        acc = zero;
#pragma unroll
        for (int t = 0; t < NT; ++t) {
          const bf16x8 kf = kfn;
          kfn = *(const bf16x8*)(kp + (t + 2) * 1024);
          const bf16x8 vf0 = *(const bf16x8*)(vp + (t * 2) * 512);
          const bf16x8 vf1 = *(const bf16x8*)(vp + (t * 2 + 1) * 512);
          bf16x8 pf;
#pragma unroll
          for (int j = 0; j < 4; ++j) pf[j] = (bf16)__builtin_amdgcn_exp2f(x[j]);
          __builtin_amdgcn_sched_barrier(0);
          f32x16 xn = __builtin_amdgcn_mfma_f32_32x32x16_bf16(kf, qf, negm, 0, 0, 0);
          asm volatile("" : "+v"(xn));
          __builtin_amdgcn_sched_barrier(0);
#pragma unroll
          for (int j = 4; j < 8; ++j) pf[j] = (bf16)__builtin_amdgcn_exp2f(x[j]);
          acc = __builtin_amdgcn_mfma_f32_32x32x16_bf16(vf0, pf, acc, 0, 0, 0);
#pragma unroll
          for (int j = 0; j < 8; ++j) pf[j] = (bf16)__builtin_amdgcn_exp2f(x[8 + j]);
          acc = __builtin_amdgcn_mfma_f32_32x32x16_bf16(vf1, pf, acc, 0, 0, 0);
          if (t == 7 && first) {
            asm volatile("s_barrier" ::: "memory");
            first = false;
          }
          if (c * NT + t == 53 && wave < 4) __builtin_amdgcn_s_setprio(0);
          __builtin_amdgcn_sched_barrier(0);
          x = xn;
        }
        if (__any(!(tot[8] + acc[8] <= 4.3e9f))) break;
        redone = false;
#pragma unroll
        for (int i = 0; i < 9; ++i) tot[i] += acc[i];
        ++c;
        kp += NT * 1024;
        vp += vstep;
        if (c == NC) break;
      }
      if (c == NC) break;
      if (!redone && __any(!(acc[8] < 7.9e28f))) {
        float mx = -INFINITY;
        for (int t = 0; t < NT; ++t) {
          bf16x8 kf = *(const bf16x8*)(kp + t * 1024);
          f32x16 x = __builtin_amdgcn_mfma_f32_32x32x16_bf16(kf, qf, zero, 0, 0, 0);
#pragma unroll
          for (int i = 0; i < 16; i += 2) mx = fmaxf(fmaxf(mx, x[i]), x[i + 1]);
        }
        mx = fmaxf(mx, __shfl_xor(mx, 32));
        const float mn = fmaxf(m, floorf(mx) + HEAD);
        const int d = (int)fminf(mn - m, 1024.f);
#pragma unroll
        for (int i = 0; i < 9; ++i) tot[i] = __builtin_amdgcn_ldexpf(tot[i], -d);
        m = mn;
        redone = true;
      } else {
        redone = false;
#pragma unroll
        for (int i = 0; i < 9; ++i) tot[i] += acc[i];
        const int e = max(__builtin_amdgcn_frexp_expf(tot[8]) - 1 + (int)HEAD, 0);
#pragma unroll
        for (int i = 0; i < 9; ++i) tot[i] = __builtin_amdgcn_ldexpf(tot[i], -e);
        m += (float)e;
        ++c;
        kp += NT * 1024;
        vp += vstep;
        if (c == NC) break;
      }
#pragma unroll
      for (int i = 0; i < 16; ++i) negm[i] = -m;
      asm volatile("" : "+v"(negm));
      x = __builtin_amdgcn_mfma_f32_32x32x16_bf16(*(const bf16x8*)kp, qf, negm, 0, 0, 0);
      kfn = *(const bf16x8*)(kp + 1024);
    }
  }
  {
    float chk = 0.f;
#pragma unroll
    for (int i = 0; i < 9; ++i) chk = __builtin_fmaf(tot[i], 0.f, chk);
    if (__any(chk != 0.f)) {
      f32x16 oacc = zero;
      float mrun = -INFINITY;
      const char* kp = kp0;
      const char* vp = vp0;
      for (int t = 0; t < 64; ++t) {
        bf16x8 kf = *(const bf16x8*)(kp);
        f32x16 x = __builtin_amdgcn_mfma_f32_32x32x16_bf16(kf, qf, zero, 0, 0, 0);
        float mx = x[0];
#pragma unroll
        for (int i = 1; i < 16; ++i) mx = fmaxf(mx, x[i]);
        mx = fmaxf(mx, __shfl_xor(mx, 32));
        const float mn = fmaxf(mrun, mx);
        const float al = __builtin_amdgcn_exp2f(mrun - mn);
        mrun = mn;
#pragma unroll
        for (int i = 0; i < 16; ++i) oacc[i] *= al;
#pragma unroll
        for (int s = 0; s < 2; ++s) {
          bf16x8 pf;
#pragma unroll
          for (int j = 0; j < 8; ++j) pf[j] = (bf16)__builtin_amdgcn_exp2f(x[8 * s + j] - mn);
          bf16x8 vf = *(const bf16x8*)(vp + ((t & (NT - 1)) * 2 + s) * 512);
          oacc = __builtin_amdgcn_mfma_f32_32x32x16_bf16(vf, pf, oacc, 0, 0, 0);
        }
        kp += 1024;
        if ((t & (NT - 1)) == NT - 1) vp += vstep;
      }
#pragma unroll
      for (int i = 0; i < 9; ++i) tot[i] = oacc[i];
    }
  }
  const float inv = 1.f / tot[8];
  {
    unsigned a0 = pack2(f32x2{tot[0] * inv, tot[1] * inv}), a1 = pack2(f32x2{tot[2] * inv, tot[3] * inv});
    unsigned b0 = pack2(f32x2{tot[4] * inv, tot[5] * inv}), b1 = pack2(f32x2{tot[6] * inv, tot[7] * inv});
    const unsigned s0 = __shfl_xor(h ? a0 : b0, 32), s1 = __shfl_xor(h ? a1 : b1, 32);
    const u32x4 o = h ? u32x4{s0, s1, b0, b1} : u32x4{a0, a1, s0, s1};
    const __amdgpu_buffer_rsrc_t ro = __builtin_amdgcn_make_buffer_rsrc(O + token * 512, 0, 1024, 0x00020000);
    __builtin_amdgcn_raw_buffer_store_b128(o, ro, r * 32 + h * 16, 0, 16);
  }
  asm volatile("s_waitcnt vmcnt(0)\n s_barrier" ::: "memory");
}

constexpr int K1_LDS = 64 * XS_STRIDE + 65536 + 16384 + 256;

extern "C" __global__ __launch_bounds__(768) void k1_ln_qkv(const bf16* __restrict__ x, const bf16* __restrict__ wq,
                                                 const bf16* __restrict__ wk, const bf16* __restrict__ wv,
                                                 const bf16* __restrict__ wr, char* __restrict__ ws) {
  float* st = (float*)(ws + WS_ST);
  bf16* Q = (bf16*)(ws + WS_Q);
  bf16* Kb = (bf16*)(ws + WS_K);
  bf16* Vf = (bf16*)(ws + WS_V);
  unsigned* cand = (unsigned*)(ws + WS_R);
  unsigned* gcnt = (unsigned*)(ws + WS_CNT);
  extern __shared__ __attribute__((aligned(16))) char smem[];
  char* ximg = smem;
  char* wimg = smem + 64 * XS_STRIDE;
  char* kvimg = smem + 64 * XS_STRIDE + 65536;
  float* rs = (float*)(smem + 64 * XS_STRIDE + 65536 + 16384);
  const int cb = blockIdx.x >> 5, tb = blockIdx.x & 31;
  const int tid = threadIdx.x, lane = tid & 63;
  const int wave = __builtin_amdgcn_readfirstlane(tid >> 6);
  const int tok0 = tb * 64;

  if (wave >= 8) {
    if (blockIdx.x == 255 && tid >= 768 - (NGRP + 1)) gcnt[(767 - tid) * 64] = 0;
    if (blockIdx.x == 254 && tid >= 768 - 33) ((unsigned*)(ws + WS_CL))[(767 - tid) * 64] = 0;
    stage_w(wq, cb * 64, wimg, wave - 8, 4, lane);
    if (cb < 2) {
      const bf16* w = cb == 0 ? wk : wv;
#pragma unroll
      for (int i = 0; i < 4; ++i) {
        const int p = wave - 8 + 4 * i;
        glds16((const char*)w + p * 1024 + lane * 16, kvimg + p * 1024);
      }
    }
    __syncthreads();
    if (cb == 3 && wave == 8) {
      unsigned k1 = route_key(rs[lane], tok0 + lane), k2 = 0;
      top2_wave(k1, k2, 64);
      if (lane == 0) *(u32x2*)(cand + tb * 2) = u32x2{k1, k2};
    }
    return;
  }

  const int tl = wave * 8 + (lane >> 3), c8 = lane & 7;
  u32x4 xv[8];
#pragma unroll
  for (int i = 0; i < 8; ++i) xv[i] = *(const u32x4*)(x + (tok0 + tl) * 512 + (c8 + 8 * i) * 8);
  u32x4 wrv[8];
  if (cb == 3) {
#pragma unroll
    for (int i = 0; i < 8; ++i) wrv[i] = *(const u32x4*)(wr + (c8 + 8 * i) * 8);
  }

  f32x2 xf[32];
#pragma unroll
  for (int i = 0; i < 32; ++i) xf[i] = unpack2(xv[i >> 2][i & 3]);
  f32x2 s2 = {0.f, 0.f};
#pragma unroll
  for (int i = 0; i < 32; ++i) s2 += xf[i];
  const float mean = sum8(s2.x + s2.y) * (1.f / 512.f);
  const f32x2 mean2 = {mean, mean};
  f32x2 v2 = {0.f, 0.f};
#pragma unroll
  for (int i = 0; i < 32; ++i) { const f32x2 d = xf[i] - mean2; v2 += d * d; }
  const float vs = sum8(v2.x + v2.y);
  const float mb = rbf(mean);
  const float vb = rbf(vs * (1.f / 512.f));
  const float den = rbf(sqrtf(rbf(vb + 1e-5f)));
  const float rinv = __builtin_amdgcn_rcpf(den);
  const f32x2 mb2 = {mb, mb}, rinv2 = {rinv, rinv};
  u32x4 o[8];
#pragma unroll
  for (int i = 0; i < 8; ++i) {
#pragma unroll
    for (int j = 0; j < 4; ++j) o[i][j] = ln2(xf[4 * i + j], mb2, rinv2);
    *(u32x4*)(ximg + tl * XS_STRIDE + (c8 + 8 * i) * 16) = o[i];
  }
  if (cb == 2 && c8 == 0) *(f32x2*)(st + (tok0 + tl) * 2) = f32x2{mb, rinv};
  if (cb == 3) {
    float rd = 0.f;
#pragma unroll
    for (int i = 0; i < 8; ++i) {
#pragma unroll
      for (int j = 0; j < 4; ++j) {
        const unsigned a = o[i][j], b = wrv[i][j];
        rd = __builtin_amdgcn_fdot2_f32_bf16(__builtin_bit_cast(bf16x2, a), __builtin_bit_cast(bf16x2, b), rd, false);
      }
    }
    rd = sum8(rd);
    if (c8 == 0) rs[tl] = rbf(rd);
  }
  __syncthreads();

  const int r = lane & 31, h = lane >> 5;
  if (wave < 4) {
    const int n0 = (wave & 1) * 32, t0 = (wave >> 1) * 32;
    f32x16 acc = gemm_tile(wimg, ximg, n0, t0, lane);
    const float sc = 0.25f * 1.4426950408889634f;
    bf16* qrow = Q + (tok0 + t0 + r) * 512 + cb * 64 + n0;
#pragma unroll
    for (int g = 0; g < 4; ++g) {
      u32x2 o;
#pragma unroll
      for (int jj = 0; jj < 2; ++jj)
        o[jj] = pack2(unpack2(pack2(f32x2{acc[4 * g + 2 * jj], acc[4 * g + 2 * jj + 1]})) * f32x2{sc, sc});
      *(u32x2*)(qrow + 8 * g + 4 * h) = o;
    }
  } else if (wave == 7) {
    if (lane < 2) {
      const unsigned t = *(const volatile unsigned*)(lane ? ws + WS_K + tb * 2048 : (const char*)(Q + tok0 * 512 + cb * 64));
      asm volatile("" ::"v"(t));
    }
  } else if (wave == 6 && cb == 7 && tb < 8) {
    unsigned long long c0, c1;
    CODE_ADDR(k2_attn, c0);
    CODE_ADDR(k1_ln_qkv, c1);
    const long long n = (long long)(c1 - c0);
    unsigned t[4];
#pragma unroll
    for (int i = 0; i < 4; ++i) {
      const int off = (i * 64 + lane) * 128;
      t[i] = off < n ? *(const volatile unsigned*)(c0 + off) : 0u;
    }
#pragma unroll
    for (int i = 0; i < 4; ++i) asm volatile("" ::"v"(t[i]));
  } else if (wave < 6 && cb < 2) {
    const int t0 = (wave - 4) * 32;
    f32x16 acc = gemm_tile16(kvimg, ximg, t0, lane);
    const int key = tok0 + t0 + r;
    const int t = key >> 5;
    if (cb == 0) {
#pragma unroll
      for (int g = 0; g < 2; ++g) {
        bf16x4 o;
#pragma unroll
        for (int j = 0; j < 4; ++j) o[j] = (bf16)acc[4 * g + j];
        *(bf16x4*)(Kb + ((t * 2 + g) * 32 + r) * 8 + 4 * h) = o;
      }
    } else {
      const int kk = key & 31, sg = kk >> 4, rem = kk & 15;
      const int hh = (rem >> 2) & 1, jj = ((rem >> 3) << 2) | (rem & 3);
#pragma unroll
      for (int g = 0; g < 2; ++g) {
#pragma unroll
        for (int j = 0; j < 4; ++j) {
          const int d = 8 * g + 4 * h + j;
          Vf[(((t * 2 + sg) * 2 + hh) * 16 + d) * 8 + jj] = (bf16)acc[4 * g + j];
        }
      }
    }
  }
}

extern "C" void kernel_launch(void* const* d_in, const int* in_sizes, int n_in, void* d_out, int out_size,
                              void* d_ws, size_t ws_size, hipStream_t stream) {
  const bf16* x = (const bf16*)d_in[0];
  const bf16* wq = (const bf16*)d_in[1];
  const bf16* wk = (const bf16*)d_in[2];
  const bf16* wv = (const bf16*)d_in[3];
  const bf16* wo = (const bf16*)d_in[4];
  const bf16* wr = (const bf16*)d_in[6];
  const bf16* wg = (const bf16*)d_in[7];
  const bf16* wu = (const bf16*)d_in[8];
  const bf16* wd = (const bf16*)d_in[9];
  char* ws = (char*)d_ws;
  bf16* out = (bf16*)d_out;

  (void)hipFuncSetAttribute((const void*)k1_ln_qkv, hipFuncAttributeMaxDynamicSharedMemorySize, K1_LDS);
  (void)hipFuncSetAttribute((const void*)k2_attn, hipFuncAttributeMaxDynamicSharedMemorySize, ATT_LDS);

  k1_ln_qkv<<<256, 768, K1_LDS, stream>>>(x, wq, wk, wv, wr, ws);
  k2_attn<<<256, 768, ATT_LDS, stream>>>(ws, x, wg, wu, wd, wo, out);
}
```

```cpp
#include <hip/hip_runtime.h>
#include <hip/hip_bf16.h>

typedef __attribute__((ext_vector_type(8))) __bf16 bf16x8;
typedef __attribute__((ext_vector_type(4))) __bf16 bf16x4;
typedef __attribute__((ext_vector_type(2))) __bf16 bf16x2;
typedef __attribute__((ext_vector_type(4))) short s16x4;
typedef __attribute__((ext_vector_type(8))) short s16x8;
typedef __attribute__((ext_vector_type(16))) float f32x16;
typedef __attribute__((ext_vector_type(4))) float f32x4;
typedef __attribute__((ext_vector_type(4))) unsigned u32x4;
typedef __attribute__((ext_vector_type(2))) unsigned u32x2;
typedef __attribute__((ext_vector_type(2))) float f32x2;
typedef __bf16 bf16;

#define LDS_AS __attribute__((address_space(3)))
#define GLB_AS __attribute__((address_space(1)))
#define DI __device__ __forceinline__

constexpr int S_ = 2048, D_ = 512, H_ = 32, DH_ = 16, F_ = 1376;

constexpr size_t WS_Q = 0;
constexpr size_t WS_O = WS_Q;
constexpr size_t WS_K = 2u << 20;
constexpr size_t WS_V = WS_K + 65536;
constexpr size_t WS_ST = WS_V + 65536;
constexpr size_t WS_R = WS_ST + 16384;
constexpr size_t WS_CNT = WS_R + 4096;
constexpr size_t WS_UPD = WS_CNT + 4096;
constexpr size_t WS_YP = WS_UPD + 4096;

constexpr int NFFN = 172;
constexpr int GSZ = 12, NGRP = 15;
constexpr size_t WS_CL = WS_YP + (NFFN + NGRP) * 4096;

DI float bf2f(bf16 v) { return (float)v; }
DI float rbf(float v) { return (float)(bf16)v; }
DI f32x2 unpack2(unsigned u) { return f32x2{__uint_as_float(u << 16), __uint_as_float(u & 0xffff0000u)}; }
DI unsigned pack2(f32x2 v) {
  unsigned r = __builtin_bit_cast(unsigned, __builtin_convertvector(v, bf16x2));
  asm("" : "+v"(r));
  return r;
}

DI unsigned route_key(float v, int tok) {
  const unsigned u = __float_as_uint(v) >> 16;
  const unsigned o = (u & 0x8000u) ? (~u & 0xffffu) : (u | 0x8000u);
  return (o << 16) | (unsigned)(0xffff - tok);
}
DI int key_tok(unsigned k) { return 0xffff - (int)(k & 0xffffu); }
DI float key_val(unsigned k) {
  const unsigned o = k >> 16;
  const unsigned u = (o & 0x8000u) ? (o & 0x7fffu) : (~o & 0xffffu);
  return __uint_as_float(u << 16);
}
DI unsigned ln2(f32x2 x, f32x2 mb2, f32x2 rinv2) { return pack2(unpack2(pack2(x - mb2)) * rinv2); }
DI void top2_wave(unsigned& k1, unsigned& k2, int width) {
  for (int o = width >> 1; o > 0; o >>= 1) {
    const unsigned a1 = __shfl_xor(k1, o), a2 = __shfl_xor(k2, o);
    k2 = max(min(k1, a1), max(k2, a2));
    k1 = max(k1, a1);
  }
}

template <int O>
DI void top2_step(unsigned& k1, unsigned& k2) {
  const unsigned a1 = __builtin_amdgcn_ds_swizzle(k1, (O << 10) | 31), a2 = __builtin_amdgcn_ds_swizzle(k2, (O << 10) | 31);
  k2 = max(min(k1, a1), max(k2, a2));
  k1 = max(k1, a1);
}

DI void glds16(const void* g, void* l) {
  __builtin_amdgcn_global_load_lds((const GLB_AS void*)g, (LDS_AS void*)l, 16, 0, 0);
}

DI void glds16_raw(const void* g, unsigned lds_off) {
  asm volatile("s_mov_b32 m0, %0\n s_nop 0\n global_load_lds_dwordx4 %1, off" ::"s"(lds_off), "v"(g) : "memory", "m0");
}

template <int CTRL>
DI float dpp_add(float v) {
  const int t = __builtin_amdgcn_update_dpp(0, __float_as_int(v), CTRL, 0xf, 0xf, true);
  return v + __int_as_float(t);
}
DI float sum8(float v) {
  v = dpp_add<0xB1>(v);
  v = dpp_add<0x4E>(v);
  v = dpp_add<0x141>(v);
  return v;
}

DI s16x4 tr_read(const char* p) {
  return __builtin_amdgcn_ds_read_tr16_b64_v4i16((LDS_AS s16x4*)p);
}

DI int wimg_off(int row, int ch) { return row * 128 + ((ch ^ (((row >> 1) & 1) << 2)) << 4); }

constexpr int XS_STRIDE = 1040;

DI void stage_w(const bf16* w, int col0, char* wimg, int wave, int nwaves, int lane) {
  for (int p = wave; p < 64; p += nwaves) {
    const int row = p * 8 + (lane >> 3), pc = lane & 7;
    const int ch = pc ^ (((row >> 1) & 1) << 2);
    glds16(w + row * 512 + col0 + ch * 8, wimg + p * 1024);
  }
}

constexpr int GP = 4;
DI f32x16 gemm_tile(const char* wimg, const char* ximg, int n0, int t0, int lane) {
  const int r = lane & 31, h = lane >> 5;
  const int q = (lane & 15) >> 2, p = lane & 3, blk = (lane >> 4) & 1;
  f32x16 acc;
#pragma unroll
  for (int i = 0; i < 16; ++i) acc[i] = 0.f;
  const char* xb = ximg + (t0 + r) * XS_STRIDE + 16 * h;
  const int col = n0 + 16 * blk + 4 * p;
  const int ch = col >> 3, sub = (col & 7) * 2;
  const char* wb0 = wimg + wimg_off(8 * h + q, ch) + sub;
  const char* wb1 = wimg + wimg_off(8 * h + q + 4, ch) + sub;
  s16x4 lo[32], hi[32];
  bf16x8 b[32];
#pragma unroll
  for (int ks = 0; ks < 32 + GP; ++ks) {
    if (ks < 32) {
      lo[ks] = tr_read(wb0 + ks * 2048);
      hi[ks] = tr_read(wb1 + ks * 2048);
      b[ks] = *(const bf16x8*)(xb + ks * 32);
    }
    if (ks >= GP) {
      const s16x8 a = __builtin_shufflevector(lo[ks - GP], hi[ks - GP], 0, 1, 2, 3, 4, 5, 6, 7);
      acc = __builtin_amdgcn_mfma_f32_32x32x16_bf16(__builtin_bit_cast(bf16x8, a), b[ks - GP], acc, 0, 0, 0);
      asm volatile("" : "+v"(acc));
    }
    __builtin_amdgcn_sched_barrier(0);
  }
  return acc;
}
DI f32x16 gemm_tile16(const char* wimg, const char* ximg, int t0, int lane) {
  const int r = lane & 31, h = lane >> 5;
  const int q = (lane & 15) >> 2, p = lane & 3;
  f32x16 acc;
#pragma unroll
  for (int i = 0; i < 16; ++i) acc[i] = 0.f;
  const char* xb = ximg + (t0 + r) * XS_STRIDE + 16 * h;
  const char* wb = wimg + (8 * h + q) * 32 + 8 * p;
  s16x4 lo[32], hi[32];
  bf16x8 b[32];
#pragma unroll
  for (int ks = 0; ks < 32 + GP; ++ks) {
    if (ks < 32) {
      lo[ks] = tr_read(wb + ks * 512);
      hi[ks] = tr_read(wb + ks * 512 + 128);
      b[ks] = *(const bf16x8*)(xb + ks * 32);
    }
    if (ks >= GP) {
      const s16x8 a = __builtin_shufflevector(lo[ks - GP], hi[ks - GP], 0, 1, 2, 3, 4, 5, 6, 7);
      acc = __builtin_amdgcn_mfma_f32_32x32x16_bf16(__builtin_bit_cast(bf16x8, a), b[ks - GP], acc, 0, 0, 0);
      asm volatile("" : "+v"(acc));
    }
    __builtin_amdgcn_sched_barrier(0);
  }
  return acc;
}

#define CODE_ADDR(sym, out)                                                                               \
  asm volatile("s_getpc_b64 s[20:21]\n s_add_u32 s20, s20, " #sym "@rel32@lo+4\n s_addc_u32 s21, s21, " #sym \
               "@rel32@hi+12\n s_mov_b64 %0, s[20:21]"                                                    \
               : "=s"(out)::"s20", "s21", "scc")

constexpr int NT = 16;
constexpr int CST = NT * 1024;
constexpr int ATT_LDS = 65536 * 2 + CST + 64 + 512 + 4 * 640;
constexpr int NC = 64 / NT;
constexpr int SPIN_MAX = 1 << 22;

DI bool last_arrival(int* w, unsigned* gc, int n, int lane) {
  asm volatile("s_waitcnt vmcnt(0)" ::: "memory");
  if (lane == 0) {
    if (__hip_atomic_fetch_add(w, 1, __ATOMIC_RELAXED, __HIP_MEMORY_SCOPE_WORKGROUP) == 3) {
      const unsigned t = __hip_atomic_fetch_add((GLB_AS unsigned*)gc, 1u, __ATOMIC_RELAXED, __HIP_MEMORY_SCOPE_AGENT);
      __hip_atomic_store(w + 1, t == (unsigned)(n - 1) ? 2 : 1, __ATOMIC_RELAXED, __HIP_MEMORY_SCOPE_WORKGROUP);
    }
  }
  int r;
  while ((r = __hip_atomic_load(w + 1, __ATOMIC_RELAXED, __HIP_MEMORY_SCOPE_WORKGROUP)) == 0) __builtin_amdgcn_s_sleep(2);
  asm volatile("" ::: "memory");
  return r == 2;
}

DI void ffn_slice(int f, int j, const bf16* __restrict__ x, const float* __restrict__ st,
                  const unsigned* __restrict__ cand, unsigned* __restrict__ fdone,
                  const bf16* __restrict__ wg, const bf16* __restrict__ wu, const bf16* __restrict__ wd,
                  float* __restrict__ ypart, unsigned* __restrict__ gcnt,
                  float* __restrict__ upd, float* red, int* cnt, char* scr, int lane) {
  const int k0 = j * 128 + lane * 2;
  const int r = lane & 31, h = lane >> 5;
  const int kr = (lane & 51) | ((lane & 4) << 1) | ((lane & 8) >> 1);
  u32x4 wf[4];
  unsigned d2[8];
  {
    const __amdgpu_buffer_rsrc_t rg = __builtin_amdgcn_make_buffer_rsrc((void*)wg, 0, 512 * F_ * 2, 0x00020000);
    const __amdgpu_buffer_rsrc_t ru = __builtin_amdgcn_make_buffer_rsrc((void*)wu, 0, 512 * F_ * 2, 0x00020000);
    const __amdgpu_buffer_rsrc_t rd = __builtin_amdgcn_make_buffer_rsrc((void*)wd, 0, 512 * F_ * 2, 0x00020000);
    const int wo_ = kr * (F_ * 2), so = (j * 128 * F_ + f * 8) * 2;
#pragma unroll
    for (int t = 0; t < 2; ++t) {
      wf[2 * t] = __builtin_amdgcn_raw_buffer_load_b128(rg, wo_, so + t * (64 * F_ * 2), 0);
      wf[2 * t + 1] = __builtin_amdgcn_raw_buffer_load_b128(ru, wo_, so + t * (64 * F_ * 2), 0);
    }
#pragma unroll
    for (int c = 0; c < 8; ++c)
      d2[c] = __builtin_amdgcn_raw_buffer_load_b32(rd, 8 * r + 4 * h + c * 1024, (f * 8 * 512 + j * 128) * 2, 0);
  }
  const u32x2 cv = *(const u32x2*)(cand + (lane & 31) * 2);
  unsigned k1 = cv[0], k2 = cv[1];
  top2_step<16>(k1, k2);
  top2_step<8>(k1, k2);
  top2_step<4>(k1, k2);
  top2_step<2>(k1, k2);
  top2_step<1>(k1, k2);
  k1 = __builtin_amdgcn_readfirstlane(k1);
  k2 = __builtin_amdgcn_readfirstlane(k2);
  const int i1 = key_tok(k1), i2 = key_tok(k2);
  const f32x2 sa = *(const f32x2*)(st + i1 * 2), sb = *(const f32x2*)(st + i2 * 2);
  *(unsigned*)(scr + lane * 4) = ln2(unpack2(*(const unsigned*)(x + i1 * 512 + k0)), f32x2{sa.x, sa.x}, f32x2{sa.y, sa.y});
  *(unsigned*)(scr + 256 + lane * 4) = ln2(unpack2(*(const unsigned*)(x + i2 * 512 + k0)), f32x2{sb.x, sb.x}, f32x2{sb.y, sb.y});
  f32x16 zero;
#pragma unroll
  for (int i = 0; i < 16; ++i) zero[i] = 0.f;
  u32x4 ida, idb;
  {
    const int d = r - 8 * h;
    const unsigned one = 0x3f80u << ((d & 1) << 4);
#pragma unroll
    for (int q = 0; q < 4; ++q) {
      ida[q] = (d >> 1) == q ? one : 0u;
      idb[q] = (d >> 1) == q + 8 ? one : 0u;
    }
  }
  f32x16 gu = zero;
#pragma unroll
  for (int t = 0; t < 2; ++t) {
    f32x16 wt = __builtin_amdgcn_mfma_f32_32x32x16_bf16(__builtin_bit_cast(bf16x8, wf[2 * t]),
                                                        __builtin_bit_cast(bf16x8, ida), zero, 0, 0, 0);
    wt = __builtin_amdgcn_mfma_f32_32x32x16_bf16(__builtin_bit_cast(bf16x8, wf[2 * t + 1]),
                                                 __builtin_bit_cast(bf16x8, idb), wt, 0, 0, 0);
#pragma unroll
    for (int q = 0; q < 2; ++q) {
      u32x4 bw;
#pragma unroll
      for (int i = 0; i < 4; ++i) bw[i] = pack2(f32x2{wt[8 * q + 2 * i], wt[8 * q + 2 * i + 1]});
      const bf16x8 xf = *(const bf16x8*)(scr + (r & 1) * 256 + (r & 2) * 32 + (64 * t + 16 * q + 8 * h) * 2);
      gu = __builtin_amdgcn_mfma_f32_32x32x16_bf16(xf, __builtin_bit_cast(bf16x8, bw), gu, 0, 0, 0);
    }
  }
  {
    const float g0 = dpp_add<0x128>((lane & 8) ? gu[2] : gu[0]), g1 = dpp_add<0x128>((lane & 8) ? gu[3] : gu[1]);
    if (lane < 32) red[j * 32 + (lane & 16) + (lane & 15)] = (lane & 8) ? g1 : g0;
  }
  if (lane == 0) __hip_atomic_fetch_add(cnt, 1, __ATOMIC_RELEASE, __HIP_MEMORY_SCOPE_WORKGROUP);
  while (__hip_atomic_load(cnt, __ATOMIC_ACQUIRE, __HIP_MEMORY_SCOPE_WORKGROUP) < 4) __builtin_amdgcn_s_sleep(1);
  const int l = lane & 15;
  const float g = rbf((red[l] + red[32 + l]) + (red[64 + l] + red[96 + l]));
  const float u = rbf((red[16 + l] + red[48 + l]) + (red[80 + l] + red[112 + l]));
  const float hv = rbf(g * __builtin_amdgcn_rcpf(1.f + __expf(-g))) * u;
  if (lane < 24) *(bf16*)(scr + 512 + lane * 2) = (bf16)(lane < 16 ? hv : 0.f);
  const bf16x8 hf = *(const bf16x8*)(scr + 512 + ((r >> 1) == h ? (r & 1) * 16 : 32));
  u32x4 da, db;
#pragma unroll
  for (int c = 0; c < 4; ++c) {
    da[c] = __builtin_amdgcn_perm(d2[2 * c + 1], d2[2 * c], 0x05040100u);
    db[c] = __builtin_amdgcn_perm(d2[2 * c + 1], d2[2 * c], 0x07060302u);
  }
  const f32x16 ya = __builtin_amdgcn_mfma_f32_32x32x16_bf16(hf, __builtin_bit_cast(bf16x8, da), zero, 0, 0, 0);
  const f32x16 yb = __builtin_amdgcn_mfma_f32_32x32x16_bf16(hf, __builtin_bit_cast(bf16x8, db), zero, 0, 0, 0);
  const __amdgpu_buffer_rsrc_t rs = __builtin_amdgcn_make_buffer_rsrc(ypart, 0, (NFFN + NGRP) * 4096, 0x00020000);
  if (lane < 32) {
#pragma unroll
    for (int t = 0; t < 2; ++t)
      __builtin_amdgcn_raw_buffer_store_b128(__builtin_bit_cast(u32x4, f32x4{ya[t], yb[t], ya[2 + t], yb[2 + t]}), rs,
                                             t * 2048 + j * 512 + lane * 16, f * 4096, 16);
  }
  const int grp = f / GSZ, gs = min(GSZ, NFFN - grp * GSZ);
  if (!last_arrival(cnt + 1, gcnt + grp * 64, gs, lane)) return;
  const int voff = j * 1024 + lane * 16;
  f32x4 a = {0.f, 0.f, 0.f, 0.f};
  {
    u32x4 t[GSZ];
#pragma unroll
    for (int i = 0; i < GSZ; ++i) t[i] = __builtin_amdgcn_raw_buffer_load_b128(rs, voff, (grp * GSZ + min(i, gs - 1)) * 4096, 16);
#pragma unroll
    for (int i = 0; i < GSZ; ++i)
      if (i < gs) a += __builtin_bit_cast(f32x4, t[i]);
  }
  __builtin_amdgcn_raw_buffer_store_b128(__builtin_bit_cast(u32x4, a), rs, voff, (NFFN + grp) * 4096, 16);
  if (!last_arrival(cnt + 3, gcnt + NGRP * 64, NGRP, lane)) return;
  a = f32x4{0.f, 0.f, 0.f, 0.f};
  {
    u32x4 t[NGRP];
#pragma unroll
    for (int i = 0; i < NGRP; ++i) t[i] = __builtin_amdgcn_raw_buffer_load_b128(rs, voff, (NFFN + i) * 4096, 16);
#pragma unroll
    for (int i = 0; i < NGRP; ++i) a += __builtin_bit_cast(f32x4, t[i]);
  }
  const float gate = rbf(1.f / (1.f + __expf(-key_val(j < 2 ? k1 : k2))));
#pragma unroll
  for (int i = 0; i < 4; ++i) a[i] = rbf(rbf(a[i]) * gate);
  {
    const __amdgpu_buffer_rsrc_t ru = __builtin_amdgcn_make_buffer_rsrc(upd, 0, 4096, 0x00020000);
    __builtin_amdgcn_raw_buffer_store_b128(__builtin_bit_cast(u32x4, a), ru, voff, 0, 16);
  }
  asm volatile("s_waitcnt vmcnt(0)" ::: "memory");
  if (lane == 0 && __hip_atomic_fetch_add(cnt + 5, 1, __ATOMIC_RELAXED, __HIP_MEMORY_SCOPE_WORKGROUP) == 3)
    __hip_atomic_store((GLB_AS unsigned*)fdone, 1u, __ATOMIC_RELAXED, __HIP_MEMORY_SCOPE_AGENT);
}

extern "C" __global__ __launch_bounds__(768) void k2_attn(char* __restrict__ ws, const bf16* __restrict__ x,
                                               const bf16* __restrict__ wg, const bf16* __restrict__ wu,
                                               const bf16* __restrict__ wd, const bf16* __restrict__ wo,
                                               bf16* __restrict__ out) {
  const bf16* Q = (const bf16*)(ws + WS_Q);
  const bf16* Kb = (const bf16*)(ws + WS_K);
  bf16* O = (bf16*)(ws + WS_O);
  const float* st = (const float*)(ws + WS_ST);
  const unsigned* cand = (const unsigned*)(ws + WS_R);
  float* ypart = (float*)(ws + WS_YP);
  unsigned* gcnt = (unsigned*)(ws + WS_CNT);
  float* upd = (float*)(ws + WS_UPD);
  extern __shared__ __attribute__((aligned(16))) char smem[];
  char* kl = smem;
  char* vl = smem + 65536;
  char* cst = smem + 131072;
  const int tid = threadIdx.x, lane = tid & 63;
  const int wave = __builtin_amdgcn_readfirstlane(tid >> 6);

  if (wave >= 8) {
    int* cnt = (int*)(smem + 131072 + CST + 32);
    if (tid < 512 + 6) cnt[tid - 512] = 0;
#pragma unroll
    for (int i = 0; i < 26; ++i) {
      const int q = (wave - 8) + 4 * i, piece = q < 48 ? 16 + q : 24 + q;
      glds16_raw((const char*)Kb + piece * 1024 + lane * 16, (unsigned)(size_t)(LDS_AS char*)smem + piece * 1024);
    }
    asm volatile("s_waitcnt lgkmcnt(0)\n s_barrier\n s_waitcnt vmcnt(0)\n s_barrier" ::: "memory");
    unsigned* clc = (unsigned*)(ws + WS_CL);
    __builtin_amdgcn_s_setprio(3);
    const int fs = (blockIdx.x & 7) * 22 + (blockIdx.x >> 3);
    if (blockIdx.x < 176 && fs < NFFN) {
      ffn_slice(fs, wave - 8, x, st, cand, clc + 32 * 64, wg, wu, wd, ypart, gcnt, upd,
                (float*)(smem + 131072 + CST + 64), cnt, smem + 131072 + CST + 576 + (wave - 8) * 640, lane);
    }
    const int tb = (blockIdx.x >> 6) * 8 + (blockIdx.x & 7), cb = (blockIdx.x >> 3) & 7, tok0 = tb * 64;
    const int sw = wave - 8, r = lane & 31, h = lane >> 5;
    char* ximg = smem;
    char* wimg = smem + 64 * XS_STRIDE;
    u32x4 wreg[16];
#pragma unroll
    for (int i = 0; i < 16; ++i) {
      const int row = (sw + 4 * i) * 8 + (lane >> 3);
      wreg[i] = *(const u32x4*)(wo + row * 512 + cb * 64 + (((lane & 7) ^ (((row >> 1) & 1) << 2)) << 3));
    }
    const int n0 = (sw & 1) * 32, t0 = (sw >> 1) * 32;
    const int tok = tok0 + t0 + r;
    u32x2 xv[4], nv[4];
#pragma unroll
    for (int g = 0; g < 4; ++g) xv[g] = *(const u32x2*)(x + tok * 512 + cb * 64 + n0 + 8 * g + 4 * h);
    const f32x2 sx = *(const f32x2*)(st + tok * 2);
    int rsel;
    {
      const u32x2 cv = *(const u32x2*)(cand + (lane & 31) * 2);
      unsigned k1 = cv[0], k2 = cv[1];
      top2_step<16>(k1, k2);
      top2_step<8>(k1, k2);
      top2_step<4>(k1, k2);
      top2_step<2>(k1, k2);
      top2_step<1>(k1, k2);
      rsel = tok == key_tok(k1) ? 0 : tok == key_tok(k2) ? 1 : -1;
    }
#pragma unroll
    for (int g = 0; g < 4; ++g) {
#pragma unroll
      for (int jj = 0; jj < 2; ++jj) {
        unsigned nr = ln2(unpack2(xv[g][jj]), f32x2{sx.x, sx.x}, f32x2{sx.y, sx.y});
        asm volatile("" : "+v"(nr));
        nv[g][jj] = nr;
      }
    }
    if (wave == 11 && lane == 0) {
      const unsigned t = *(const volatile unsigned*)(out + tok0 * 512 + cb * 64);
      asm volatile("" ::"v"(t));
    }
#pragma unroll
    for (int i = 0; i < 16; ++i) asm volatile("" : "+v"(wreg[i]));
    asm volatile("s_barrier" ::: "memory");
    if (tid == 512) __hip_atomic_fetch_add((GLB_AS unsigned*)(clc + tb * 64), 1u, __ATOMIC_RELAXED, __HIP_MEMORY_SCOPE_AGENT);
#pragma unroll
    for (int i = 0; i < 16; ++i) *(u32x4*)(wimg + (sw + 4 * i) * 1024 + lane * 16) = wreg[i];
    if (__any(rsel >= 0)) {
      int spin = 0;
      while (__hip_atomic_load((GLB_AS unsigned*)(clc + 32 * 64), __ATOMIC_RELAXED, __HIP_MEMORY_SCOPE_AGENT) == 0 && ++spin < SPIN_MAX)
        __builtin_amdgcn_s_sleep(1);
      asm volatile("" ::: "memory");
      const __amdgpu_buffer_rsrc_t ru = __builtin_amdgcn_make_buffer_rsrc(upd, 0, 4096, 0x00020000);
      if (rsel >= 0) {
#pragma unroll
        for (int g = 0; g < 4; ++g) {
          const f32x4 u = __builtin_bit_cast(f32x4, __builtin_amdgcn_raw_buffer_load_b128(ru, (rsel * 512 + cb * 64 + n0 + 8 * g + 4 * h) * 4, 0, 16));
          nv[g][0] = pack2(unpack2(nv[g][0]) + f32x2{u[0], u[1]});
          nv[g][1] = pack2(unpack2(nv[g][1]) + f32x2{u[2], u[3]});
        }
      }
    }
    if (wave == 8) {
      int spin = 0;
      while (__hip_atomic_load((GLB_AS unsigned*)(clc + tb * 64), __ATOMIC_RELAXED, __HIP_MEMORY_SCOPE_AGENT) < 8 && ++spin < SPIN_MAX)
        __builtin_amdgcn_s_sleep(1);
    }
    asm volatile("s_barrier" ::: "memory");
    {
      const __amdgpu_buffer_rsrc_t ro = __builtin_amdgcn_make_buffer_rsrc(O + tok0 * 512, 0, 65536, 0x00020000);
      u32x4 t[16];
#pragma unroll
      for (int i = 0; i < 16; ++i) t[i] = __builtin_amdgcn_raw_buffer_load_b128(ro, lane * 16, (sw * 16 + i) * 1024, 16);
#pragma unroll
      for (int i = 0; i < 16; ++i) *(u32x4*)(ximg + (sw * 16 + i) * XS_STRIDE + lane * 16) = t[i];
    }
    asm volatile("s_waitcnt lgkmcnt(0)\n s_barrier" ::: "memory");
    f32x16 acc = gemm_tile(wimg, ximg, n0, t0, lane);
#pragma unroll
    for (int g = 0; g < 4; ++g) {
      u32x2 o;
#pragma unroll
      for (int jj = 0; jj < 2; ++jj) {
        const unsigned xr = xv[g][jj], nr = nv[g][jj];
        const f32x2 a = unpack2(pack2(f32x2{acc[4 * g + 2 * jj], acc[4 * g + 2 * jj + 1]}));
        o[jj] = pack2(unpack2(pack2(unpack2(xr) + a)) + unpack2(nr));
      }
      *(u32x2*)(out + tok * 512 + cb * 64 + n0 + 8 * g + 4 * h) = o;
    }
    return;
  }

  const int r = lane & 31, h = lane >> 5;
  const int token = ((blockIdx.x >> 6) * 8 + (blockIdx.x & 7)) * 64 + ((blockIdx.x >> 3) & 7) * 8 + wave;
  bf16x8 qf;
  asm volatile("global_load_dwordx4 %0, %1, off" : "=v"(qf) : "v"(Q + token * 512 + r * 16 + 8 * h) : "memory");
  const unsigned lds0 = (unsigned)(size_t)(LDS_AS char*)smem;
#pragma unroll
  for (int i = 0; i < 3; ++i) {
    const int piece = (i < 2) ? (wave + i * 8) : (64 + wave);
    glds16_raw((const char*)Kb + piece * 1024 + lane * 16, lds0 + piece * 1024);
  }
  {
    const unsigned cv = ((tid & 15) == 0 || (tid & 15) == 4) ? 0x3f803f80u : 0u;
#pragma unroll
    for (int i = 0; i < CST / 8192; ++i) *(u32x4*)(cst + i * 8192 + tid * 16) = u32x4{cv, cv, cv, cv};
  }
  asm volatile("s_waitcnt vmcnt(0) lgkmcnt(0)\n s_barrier" ::: "memory");

  const char* kp0 = kl + lane * 16;
  const char* vp0 = (r < 16) ? (vl + (h * 16 + r) * 16) : (cst + (h * 16 + r - 16) * 16);
  const int vstep = (r < 16) ? NT * 1024 : 0;

  f32x16 zero;
#pragma unroll
  for (int i = 0; i < 16; ++i) zero[i] = 0.f;
  constexpr float HEAD = 40.f;
  float m;
  {
    float mx = -INFINITY;
#pragma unroll
    for (int t = 0; t < 4; ++t) {
      bf16x8 kf = *(const bf16x8*)(kp0 + t * 1024);
      f32x16 x = __builtin_amdgcn_mfma_f32_32x32x16_bf16(kf, qf, zero, 0, 0, 0);
#pragma unroll
      for (int i = 0; i < 16; i += 2) mx = fmaxf(fmaxf(mx, x[i]), x[i + 1]);
    }
    mx = fmaxf(mx, __shfl_xor(mx, 32));
    m = floorf(mx) + HEAD;
  }
  f32x16 negm;
#pragma unroll
  for (int i = 0; i < 16; ++i) negm[i] = -m;
  asm volatile("" : "+v"(negm));
  float tot[9];
#pragma unroll
  for (int i = 0; i < 9; ++i) tot[i] = 0.f;
  {
    const char* kp = kp0;
    const char* vp = vp0;
    int c = 0;
    bool redone = false, first = true;
    if (wave < 4) __builtin_amdgcn_s_setprio(2);
    else __builtin_amdgcn_s_setprio(1);
    f32x16 x = __builtin_amdgcn_mfma_f32_32x32x16_bf16(*(const bf16x8*)kp, qf, negm, 0, 0, 0);
    bf16x8 kfn = *(const bf16x8*)(kp + 1024);
    while (true) {
      f32x16 acc;
      while (true) {
        acc = zero;
#pragma unroll
        for (int t = 0; t < NT; ++t) {
          const bf16x8 kf = kfn;
          kfn = *(const bf16x8*)(kp + (t + 2) * 1024);
          const bf16x8 vf0 = *(const bf16x8*)(vp + (t * 2) * 512);
          const bf16x8 vf1 = *(const bf16x8*)(vp + (t * 2 + 1) * 512);
          bf16x8 pf;
#pragma unroll
          for (int j = 0; j < 4; ++j) pf[j] = (bf16)__builtin_amdgcn_exp2f(x[j]);
          __builtin_amdgcn_sched_barrier(0);
          f32x16 xn = __builtin_amdgcn_mfma_f32_32x32x16_bf16(kf, qf, negm, 0, 0, 0);
          asm volatile("" : "+v"(xn));
          __builtin_amdgcn_sched_barrier(0);
#pragma unroll
          for (int j = 4; j < 8; ++j) pf[j] = (bf16)__builtin_amdgcn_exp2f(x[j]);
          acc = __builtin_amdgcn_mfma_f32_32x32x16_bf16(vf0, pf, acc, 0, 0, 0);
#pragma unroll
          for (int j = 0; j < 8; ++j) pf[j] = (bf16)__builtin_amdgcn_exp2f(x[8 + j]);
          acc = __builtin_amdgcn_mfma_f32_32x32x16_bf16(vf1, pf, acc, 0, 0, 0);
          if (t == 7 && first) {
            asm volatile("s_barrier" ::: "memory");
            first = false;
          }
          if (c * NT + t == 53 && wave < 4) __builtin_amdgcn_s_setprio(0);
          __builtin_amdgcn_sched_barrier(0);
          x = xn;
        }
        if (__any(!(tot[8] + acc[8] <= 4.3e9f))) break;
        redone = false;
#pragma unroll
        for (int i = 0; i < 9; ++i) tot[i] += acc[i];
        ++c;
        kp += NT * 1024;
        vp += vstep;
        if (c == NC) break;
      }
      if (c == NC) break;
      if (!redone && __any(!(acc[8] < 7.9e28f))) {
        float mx = -INFINITY;
        for (int t = 0; t < NT; ++t) {
          bf16x8 kf = *(const bf16x8*)(kp + t * 1024);
          f32x16 x = __builtin_amdgcn_mfma_f32_32x32x16_bf16(kf, qf, zero, 0, 0, 0);
#pragma unroll
          for (int i = 0; i < 16; i += 2) mx = fmaxf(fmaxf(mx, x[i]), x[i + 1]);
        }
        mx = fmaxf(mx, __shfl_xor(mx, 32));
        const float mn = fmaxf(m, floorf(mx) + HEAD);
        const int d = (int)fminf(mn - m, 1024.f);
#pragma unroll
        for (int i = 0; i < 9; ++i) tot[i] = __builtin_amdgcn_ldexpf(tot[i], -d);
        m = mn;
        redone = true;
      } else {
        redone = false;
#pragma unroll
        for (int i = 0; i < 9; ++i) tot[i] += acc[i];
        const int e = max(__builtin_amdgcn_frexp_expf(tot[8]) - 1 + (int)HEAD, 0);
#pragma unroll
        for (int i = 0; i < 9; ++i) tot[i] = __builtin_amdgcn_ldexpf(tot[i], -e);
        m += (float)e;
        ++c;
        kp += NT * 1024;
        vp += vstep;
        if (c == NC) break;
      }
#pragma unroll
      for (int i = 0; i < 16; ++i) negm[i] = -m;
      asm volatile("" : "+v"(negm));
      x = __builtin_amdgcn_mfma_f32_32x32x16_bf16(*(const bf16x8*)kp, qf, negm, 0, 0, 0);
      kfn = *(const bf16x8*)(kp + 1024);
    }
  }
  {
    float chk = 0.f;
#pragma unroll
    for (int i = 0; i < 9; ++i) chk = __builtin_fmaf(tot[i], 0.f, chk);
    if (__any(chk != 0.f)) {
      f32x16 oacc = zero;
      float mrun = -INFINITY;
      const char* kp = kp0;
      const char* vp = vp0;
      for (int t = 0; t < 64; ++t) {
        bf16x8 kf = *(const bf16x8*)(kp);
        f32x16 x = __builtin_amdgcn_mfma_f32_32x32x16_bf16(kf, qf, zero, 0, 0, 0);
        float mx = x[0];
#pragma unroll
        for (int i = 1; i < 16; ++i) mx = fmaxf(mx, x[i]);
        mx = fmaxf(mx, __shfl_xor(mx, 32));
        const float mn = fmaxf(mrun, mx);
        const float al = __builtin_amdgcn_exp2f(mrun - mn);
        mrun = mn;
#pragma unroll
        for (int i = 0; i < 16; ++i) oacc[i] *= al;
#pragma unroll
        for (int s = 0; s < 2; ++s) {
          bf16x8 pf;
#pragma unroll
          for (int j = 0; j < 8; ++j) pf[j] = (bf16)__builtin_amdgcn_exp2f(x[8 * s + j] - mn);
          bf16x8 vf = *(const bf16x8*)(vp + ((t & (NT - 1)) * 2 + s) * 512);
          oacc = __builtin_amdgcn_mfma_f32_32x32x16_bf16(vf, pf, oacc, 0, 0, 0);
        }
        kp += 1024;
        if ((t & (NT - 1)) == NT - 1) vp += vstep;
      }
#pragma unroll
      for (int i = 0; i < 9; ++i) tot[i] = oacc[i];
    }
  }
  const float inv = 1.f / tot[8];
  {
    unsigned a0 = pack2(f32x2{tot[0] * inv, tot[1] * inv}), a1 = pack2(f32x2{tot[2] * inv, tot[3] * inv});
    unsigned b0 = pack2(f32x2{tot[4] * inv, tot[5] * inv}), b1 = pack2(f32x2{tot[6] * inv, tot[7] * inv});
    const unsigned s0 = __shfl_xor(h ? a0 : b0, 32), s1 = __shfl_xor(h ? a1 : b1, 32);
    const u32x4 o = h ? u32x4{s0, s1, b0, b1} : u32x4{a0, a1, s0, s1};
    const __amdgpu_buffer_rsrc_t ro = __builtin_amdgcn_make_buffer_rsrc(O + token * 512, 0, 1024, 0x00020000);
    __builtin_amdgcn_raw_buffer_store_b128(o, ro, r * 32 + h * 16, 0, 16);
  }
  asm volatile("s_waitcnt vmcnt(0)\n s_barrier" ::: "memory");
}

constexpr int K1_LDS = 64 * XS_STRIDE + 65536 + 16384 + 256;

extern "C" __global__ __launch_bounds__(768) void k1_ln_qkv(const bf16* __restrict__ x, const bf16* __restrict__ wq,
                                                 const bf16* __restrict__ wk, const bf16* __restrict__ wv,
                                                 const bf16* __restrict__ wr, char* __restrict__ ws) {
  float* st = (float*)(ws + WS_ST);
  bf16* Q = (bf16*)(ws + WS_Q);
  bf16* Kb = (bf16*)(ws + WS_K);
  bf16* Vf = (bf16*)(ws + WS_V);
  unsigned* cand = (unsigned*)(ws + WS_R);
  unsigned* gcnt = (unsigned*)(ws + WS_CNT);
  extern __shared__ __attribute__((aligned(16))) char smem[];
  char* ximg = smem;
  char* wimg = smem + 64 * XS_STRIDE;
  char* kvimg = smem + 64 * XS_STRIDE + 65536;
  float* rs = (float*)(smem + 64 * XS_STRIDE + 65536 + 16384);
  const int cb = blockIdx.x >> 5, tb = blockIdx.x & 31;
  const int tid = threadIdx.x, lane = tid & 63;
  const int wave = __builtin_amdgcn_readfirstlane(tid >> 6);
  const int tok0 = tb * 64;

  if (wave >= 8) {
    if (blockIdx.x == 255 && tid >= 768 - (NGRP + 1)) gcnt[(767 - tid) * 64] = 0;
    if (blockIdx.x == 254 && tid >= 768 - 33) ((unsigned*)(ws + WS_CL))[(767 - tid) * 64] = 0;
    stage_w(wq, cb * 64, wimg, wave - 8, 4, lane);
    if (cb < 2) {
      const bf16* w = cb == 0 ? wk : wv;
#pragma unroll
      for (int i = 0; i < 4; ++i) {
        const int p = wave - 8 + 4 * i;
        glds16((const char*)w + p * 1024 + lane * 16, kvimg + p * 1024);
      }
    }
    __syncthreads();
    if (cb == 3 && wave == 8) {
      unsigned k1 = route_key(rs[lane], tok0 + lane), k2 = 0;
      top2_wave(k1, k2, 64);
      if (lane == 0) *(u32x2*)(cand + tb * 2) = u32x2{k1, k2};
    }
    return;
  }

  const int tl = wave * 8 + (lane >> 3), c8 = lane & 7;
  u32x4 xv[8];
#pragma unroll
  for (int i = 0; i < 8; ++i) xv[i] = *(const u32x4*)(x + (tok0 + tl) * 512 + (c8 + 8 * i) * 8);
  u32x4 wrv[8];
  if (cb == 3) {
#pragma unroll
    for (int i = 0; i < 8; ++i) wrv[i] = *(const u32x4*)(wr + (c8 + 8 * i) * 8);
  }

  f32x2 xf[32];
#pragma unroll
  for (int i = 0; i < 32; ++i) xf[i] = unpack2(xv[i >> 2][i & 3]);
  f32x2 s2 = {0.f, 0.f};
#pragma unroll
  for (int i = 0; i < 32; ++i) s2 += xf[i];
  const float mean = sum8(s2.x + s2.y) * (1.f / 512.f);
  const f32x2 mean2 = {mean, mean};
  f32x2 v2 = {0.f, 0.f};
#pragma unroll
  for (int i = 0; i < 32; ++i) { const f32x2 d = xf[i] - mean2; v2 += d * d; }
  const float vs = sum8(v2.x + v2.y);
  const float mb = rbf(mean);
  const float vb = rbf(vs * (1.f / 512.f));
  const float den = rbf(sqrtf(rbf(vb + 1e-5f)));
  const float rinv = __builtin_amdgcn_rcpf(den);
  const f32x2 mb2 = {mb, mb}, rinv2 = {rinv, rinv};
  u32x4 o[8];
#pragma unroll
  for (int i = 0; i < 8; ++i) {
#pragma unroll
    for (int j = 0; j < 4; ++j) o[i][j] = ln2(xf[4 * i + j], mb2, rinv2);
    *(u32x4*)(ximg + tl * XS_STRIDE + (c8 + 8 * i) * 16) = o[i];
  }
  if (cb == 2 && c8 == 0) *(f32x2*)(st + (tok0 + tl) * 2) = f32x2{mb, rinv};
  if (cb == 3) {
    float rd = 0.f;
#pragma unroll
    for (int i = 0; i < 8; ++i) {
#pragma unroll
      for (int j = 0; j < 4; ++j) {
        const unsigned a = o[i][j], b = wrv[i][j];
        rd = __builtin_amdgcn_fdot2_f32_bf16(__builtin_bit_cast(bf16x2, a), __builtin_bit_cast(bf16x2, b), rd, false);
      }
    }
    rd = sum8(rd);
    if (c8 == 0) rs[tl] = rbf(rd);
  }
  __syncthreads();

  const int r = lane & 31, h = lane >> 5;
  if (wave < 4) {
    const int n0 = (wave & 1) * 32, t0 = (wave >> 1) * 32;
    f32x16 acc = gemm_tile(wimg, ximg, n0, t0, lane);
    const float sc = 0.25f * 1.4426950408889634f;
    const __amdgpu_buffer_rsrc_t rq = __builtin_amdgcn_make_buffer_rsrc(Q + tok0 * 512, 0, 65536, 0x00020000);
#pragma unroll
    for (int m = 0; m < 2; ++m) {
      unsigned p[4];
#pragma unroll
      for (int i = 0; i < 4; ++i) p[i] = pack2(unpack2(pack2(f32x2{acc[8 * m + 2 * i], acc[8 * m + 2 * i + 1]})) * f32x2{sc, sc});
      const unsigned s0 = __shfl_xor(h ? p[0] : p[2], 32), s1 = __shfl_xor(h ? p[1] : p[3], 32);
      const u32x4 o = h ? u32x4{s0, s1, p[2], p[3]} : u32x4{p[0], p[1], s0, s1};
      __builtin_amdgcn_raw_buffer_store_b128(o, rq, ((t0 + r) * 512 + cb * 64 + n0 + 16 * m + 8 * h) * 2, 0, 0);
    }
  } else if (wave == 7) {
    if (lane < 2) {
      const unsigned t = *(const volatile unsigned*)(lane ? ws + WS_K + tb * 2048 : (const char*)(Q + tok0 * 512 + cb * 64));
      asm volatile("" ::"v"(t));
    }
  } else if (wave == 6 && cb == 7 && tb < 8) {
    unsigned long long c0, c1;
    CODE_ADDR(k2_attn, c0);
    CODE_ADDR(k1_ln_qkv, c1);
    const long long n = (long long)(c1 - c0);
    unsigned t[4];
#pragma unroll
    for (int i = 0; i < 4; ++i) {
      const int off = (i * 64 + lane) * 128;
      t[i] = off < n ? *(const volatile unsigned*)(c0 + off) : 0u;
    }
#pragma unroll
    for (int i = 0; i < 4; ++i) asm volatile("" ::"v"(t[i]));
  } else if (wave < 6 && cb < 2) {
    const int t0 = (wave - 4) * 32;
    f32x16 acc = gemm_tile16(kvimg, ximg, t0, lane);
    const int key = tok0 + t0 + r;
    const int t = key >> 5;
    if (cb == 0) {
#pragma unroll
      for (int g = 0; g < 2; ++g) {
        bf16x4 o;
#pragma unroll
        for (int j = 0; j < 4; ++j) o[j] = (bf16)acc[4 * g + j];
        *(bf16x4*)(Kb + ((t * 2 + g) * 32 + r) * 8 + 4 * h) = o;
      }
    } else {
      const int kk = key & 31, sg = kk >> 4, rem = kk & 15;
      const int hh = (rem >> 2) & 1, jj = ((rem >> 3) << 2) | (rem & 3);
#pragma unroll
      for (int g = 0; g < 2; ++g) {
#pragma unroll
        for (int j = 0; j < 4; ++j) {
          const int d = 8 * g + 4 * h + j;
          Vf[(((t * 2 + sg) * 2 + hh) * 16 + d) * 8 + jj] = (bf16)acc[4 * g + j];
        }
      }
    }
  }
}

extern "C" void kernel_launch(void* const* d_in, const int* in_sizes, int n_in, void* d_out, int out_size,
                              void* d_ws, size_t ws_size, hipStream_t stream) {
  const bf16* x = (const bf16*)d_in[0];
  const bf16* wq = (const bf16*)d_in[1];
  const bf16* wk = (const bf16*)d_in[2];
  const bf16* wv = (const bf16*)d_in[3];
  const bf16* wo = (const bf16*)d_in[4];
  const bf16* wr = (const bf16*)d_in[6];
  const bf16* wg = (const bf16*)d_in[7];
  const bf16* wu = (const bf16*)d_in[8];
  const bf16* wd = (const bf16*)d_in[9];
  char* ws = (char*)d_ws;
  bf16* out = (bf16*)d_out;

  (void)hipFuncSetAttribute((const void*)k1_ln_qkv, hipFuncAttributeMaxDynamicSharedMemorySize, K1_LDS);
  (void)hipFuncSetAttribute((const void*)k2_attn, hipFuncAttributeMaxDynamicSharedMemorySize, ATT_LDS);

  k1_ln_qkv<<<256, 768, K1_LDS, stream>>>(x, wq, wk, wv, wr, ws);
  k2_attn<<<256, 768, ATT_LDS, stream>>>(ws, x, wg, wu, wd, wo, out);
}
```

```cpp
#include <hip/hip_runtime.h>
#include <hip/hip_bf16.h>

typedef __attribute__((ext_vector_type(8))) __bf16 bf16x8;
typedef __attribute__((ext_vector_type(4))) __bf16 bf16x4;
typedef __attribute__((ext_vector_type(2))) __bf16 bf16x2;
typedef __attribute__((ext_vector_type(4))) short s16x4;
typedef __attribute__((ext_vector_type(8))) short s16x8;
typedef __attribute__((ext_vector_type(16))) float f32x16;
typedef __attribute__((ext_vector_type(4))) float f32x4;
typedef __attribute__((ext_vector_type(4))) unsigned u32x4;
typedef __attribute__((ext_vector_type(2))) unsigned u32x2;
typedef __attribute__((ext_vector_type(2))) float f32x2;
typedef __bf16 bf16;

#define LDS_AS __attribute__((address_space(3)))
#define GLB_AS __attribute__((address_space(1)))
#define DI __device__ __forceinline__

constexpr int S_ = 2048, D_ = 512, H_ = 32, DH_ = 16, F_ = 1376;

constexpr size_t WS_Q = 0;
constexpr size_t WS_O = WS_Q;
constexpr size_t WS_K = 2u << 20;
constexpr size_t WS_V = WS_K + 65536;
constexpr size_t WS_ST = WS_V + 65536;
constexpr size_t WS_R = WS_ST + 16384;
constexpr size_t WS_CNT = WS_R + 4096;
constexpr size_t WS_UPD = WS_CNT + 4096;
constexpr size_t WS_YP = WS_UPD + 4096;

constexpr int NFFN = 172;
constexpr int GSZ = 12, NGRP = 15;
constexpr size_t WS_CL = WS_YP + (NFFN + NGRP) * 4096;

DI float bf2f(bf16 v) { return (float)v; }
DI float rbf(float v) { return (float)(bf16)v; }
DI f32x2 unpack2(unsigned u) { return f32x2{__uint_as_float(u << 16), __uint_as_float(u & 0xffff0000u)}; }
DI unsigned pack2(f32x2 v) {
  unsigned r = __builtin_bit_cast(unsigned, __builtin_convertvector(v, bf16x2));
  asm("" : "+v"(r));
  return r;
}

DI unsigned route_key(float v, int tok) {
  const unsigned u = __float_as_uint(v) >> 16;
  const unsigned o = (u & 0x8000u) ? (~u & 0xffffu) : (u | 0x8000u);
  return (o << 16) | (unsigned)(0xffff - tok);
}
DI int key_tok(unsigned k) { return 0xffff - (int)(k & 0xffffu); }
DI float key_val(unsigned k) {
  const unsigned o = k >> 16;
  const unsigned u = (o & 0x8000u) ? (o & 0x7fffu) : (~o & 0xffffu);
  return __uint_as_float(u << 16);
}
DI unsigned ln2(f32x2 x, f32x2 mb2, f32x2 rinv2) { return pack2(unpack2(pack2(x - mb2)) * rinv2); }
DI void top2_wave(unsigned& k1, unsigned& k2, int width) {
  for (int o = width >> 1; o > 0; o >>= 1) {
    const unsigned a1 = __shfl_xor(k1, o), a2 = __shfl_xor(k2, o);
    k2 = max(min(k1, a1), max(k2, a2));
    k1 = max(k1, a1);
  }
}

template <int O>
DI void top2_step(unsigned& k1, unsigned& k2) {
  const unsigned a1 = __builtin_amdgcn_ds_swizzle(k1, (O << 10) | 31), a2 = __builtin_amdgcn_ds_swizzle(k2, (O << 10) | 31);
  k2 = max(min(k1, a1), max(k2, a2));
  k1 = max(k1, a1);
}

DI void glds16(const void* g, void* l) {
  __builtin_amdgcn_global_load_lds((const GLB_AS void*)g, (LDS_AS void*)l, 16, 0, 0);
}

DI void glds16_raw(const void* g, unsigned lds_off) {
  asm volatile("s_mov_b32 m0, %0\n s_nop 0\n global_load_lds_dwordx4 %1, off" ::"s"(lds_off), "v"(g) : "memory", "m0");
}

template <int CTRL>
DI float dpp_add(float v) {
  const int t = __builtin_amdgcn_update_dpp(0, __float_as_int(v), CTRL, 0xf, 0xf, true);
  return v + __int_as_float(t);
}
DI float sum8(float v) {
  v = dpp_add<0xB1>(v);
  v = dpp_add<0x4E>(v);
  v = dpp_add<0x141>(v);
  return v;
}

DI s16x4 tr_read(const char* p) {
  return __builtin_amdgcn_ds_read_tr16_b64_v4i16((LDS_AS s16x4*)p);
}

DI int wimg_off(int row, int ch) { return row * 128 + ((ch ^ (((row >> 1) & 1) << 2)) << 4); }

constexpr int XS_STRIDE = 1040;

DI void stage_w(const bf16* w, int col0, char* wimg, int wave, int nwaves, int lane) {
  for (int p = wave; p < 64; p += nwaves) {
    const int row = p * 8 + (lane >> 3), pc = lane & 7;
    const int ch = pc ^ (((row >> 1) & 1) << 2);
    glds16(w + row * 512 + col0 + ch * 8, wimg + p * 1024);
  }
}

constexpr int GP = 4;
DI f32x16 gemm_tile(const char* wimg, const char* ximg, int n0, int t0, int lane) {
  const int r = lane & 31, h = lane >> 5;
  const int q = (lane & 15) >> 2, p = lane & 3, blk = (lane >> 4) & 1;
  f32x16 acc;
#pragma unroll
  for (int i = 0; i < 16; ++i) acc[i] = 0.f;
  const char* xb = ximg + (t0 + r) * XS_STRIDE + 16 * h;
  const int col = n0 + 16 * blk + 4 * p;
  const int ch = col >> 3, sub = (col & 7) * 2;
  const char* wb0 = wimg + wimg_off(8 * h + q, ch) + sub;
  const char* wb1 = wimg + wimg_off(8 * h + q + 4, ch) + sub;
  s16x4 lo[32], hi[32];
  bf16x8 b[32];
#pragma unroll
  for (int ks = 0; ks < 32 + GP; ++ks) {
    if (ks < 32) {
      lo[ks] = tr_read(wb0 + ks * 2048);
      hi[ks] = tr_read(wb1 + ks * 2048);
      b[ks] = *(const bf16x8*)(xb + ks * 32);
    }
    if (ks >= GP) {
      const s16x8 a = __builtin_shufflevector(lo[ks - GP], hi[ks - GP], 0, 1, 2, 3, 4, 5, 6, 7);
      acc = __builtin_amdgcn_mfma_f32_32x32x16_bf16(__builtin_bit_cast(bf16x8, a), b[ks - GP], acc, 0, 0, 0);
      asm volatile("" : "+v"(acc));
    }
    __builtin_amdgcn_sched_barrier(0);
  }
  return acc;
}
DI f32x16 gemm_tile16(const char* wimg, const char* ximg, int t0, int lane) {
  const int r = lane & 31, h = lane >> 5;
  const int q = (lane & 15) >> 2, p = lane & 3;
  f32x16 acc;
#pragma unroll
  for (int i = 0; i < 16; ++i) acc[i] = 0.f;
  const char* xb = ximg + (t0 + r) * XS_STRIDE + 16 * h;
  const char* wb = wimg + (8 * h + q) * 32 + 8 * p;
  s16x4 lo[32], hi[32];
  bf16x8 b[32];
#pragma unroll
  for (int ks = 0; ks < 32 + GP; ++ks) {
    if (ks < 32) {
      lo[ks] = tr_read(wb + ks * 512);
      hi[ks] = tr_read(wb + ks * 512 + 128);
      b[ks] = *(const bf16x8*)(xb + ks * 32);
    }
    if (ks >= GP) {
      const s16x8 a = __builtin_shufflevector(lo[ks - GP], hi[ks - GP], 0, 1, 2, 3, 4, 5, 6, 7);
      acc = __builtin_amdgcn_mfma_f32_32x32x16_bf16(__builtin_bit_cast(bf16x8, a), b[ks - GP], acc, 0, 0, 0);
      asm volatile("" : "+v"(acc));
    }
    __builtin_amdgcn_sched_barrier(0);
  }
  return acc;
}

#define CODE_ADDR(sym, out)                                                                               \
  asm volatile("s_getpc_b64 s[20:21]\n s_add_u32 s20, s20, " #sym "@rel32@lo+4\n s_addc_u32 s21, s21, " #sym \
               "@rel32@hi+12\n s_mov_b64 %0, s[20:21]"                                                    \
               : "=s"(out)::"s20", "s21", "scc")

constexpr int NT = 16;
constexpr int CST = NT * 1024;
constexpr int ATT_LDS = 65536 * 2 + CST + 64 + 512 + 4 * 640;
constexpr int NC = 64 / NT;
constexpr int SPIN_MAX = 1 << 22;

DI bool last_arrival(int* w, unsigned* gc, int n, int lane) {
  asm volatile("s_waitcnt vmcnt(0)" ::: "memory");
  if (lane == 0) {
    if (__hip_atomic_fetch_add(w, 1, __ATOMIC_RELAXED, __HIP_MEMORY_SCOPE_WORKGROUP) == 3) {
      const unsigned t = __hip_atomic_fetch_add((GLB_AS unsigned*)gc, 1u, __ATOMIC_RELAXED, __HIP_MEMORY_SCOPE_AGENT);
      __hip_atomic_store(w + 1, t == (unsigned)(n - 1) ? 2 : 1, __ATOMIC_RELAXED, __HIP_MEMORY_SCOPE_WORKGROUP);
    }
  }
  int r;
  while ((r = __hip_atomic_load(w + 1, __ATOMIC_RELAXED, __HIP_MEMORY_SCOPE_WORKGROUP)) == 0) __builtin_amdgcn_s_sleep(2);
  asm volatile("" ::: "memory");
  return r == 2;
}

DI void ffn_slice(int f, int j, const bf16* __restrict__ x, const float* __restrict__ st,
                  const unsigned* __restrict__ cand, unsigned* __restrict__ fdone,
                  const bf16* __restrict__ wg, const bf16* __restrict__ wu, const bf16* __restrict__ wd,
                  float* __restrict__ ypart, unsigned* __restrict__ gcnt,
                  float* __restrict__ upd, float* red, int* cnt, char* scr, int lane) {
  const int k0 = j * 128 + lane * 2;
  const int r = lane & 31, h = lane >> 5;
  const int kr = (lane & 51) | ((lane & 4) << 1) | ((lane & 8) >> 1);
  u32x4 wf[4];
  unsigned d2[8];
  {
    const __amdgpu_buffer_rsrc_t rg = __builtin_amdgcn_make_buffer_rsrc((void*)wg, 0, 512 * F_ * 2, 0x00020000);
    const __amdgpu_buffer_rsrc_t ru = __builtin_amdgcn_make_buffer_rsrc((void*)wu, 0, 512 * F_ * 2, 0x00020000);
    const __amdgpu_buffer_rsrc_t rd = __builtin_amdgcn_make_buffer_rsrc((void*)wd, 0, 512 * F_ * 2, 0x00020000);
    const int wo_ = kr * (F_ * 2), so = (j * 128 * F_ + f * 8) * 2;
#pragma unroll
    for (int t = 0; t < 2; ++t) {
      wf[2 * t] = __builtin_amdgcn_raw_buffer_load_b128(rg, wo_, so + t * (64 * F_ * 2), 0);
      wf[2 * t + 1] = __builtin_amdgcn_raw_buffer_load_b128(ru, wo_, so + t * (64 * F_ * 2), 0);
    }
#pragma unroll
    for (int c = 0; c < 8; ++c)
      d2[c] = __builtin_amdgcn_raw_buffer_load_b32(rd, 8 * r + 4 * h + c * 1024, (f * 8 * 512 + j * 128) * 2, 0);
  }
  const u32x2 cv = *(const u32x2*)(cand + (lane & 31) * 2);
  unsigned k1 = cv[0], k2 = cv[1];
  top2_step<16>(k1, k2);
  top2_step<8>(k1, k2);
  top2_step<4>(k1, k2);
  top2_step<2>(k1, k2);
  top2_step<1>(k1, k2);
  k1 = __builtin_amdgcn_readfirstlane(k1);
  k2 = __builtin_amdgcn_readfirstlane(k2);
  const int i1 = key_tok(k1), i2 = key_tok(k2);
  const f32x2 sa = *(const f32x2*)(st + i1 * 2), sb = *(const f32x2*)(st + i2 * 2);
  *(unsigned*)(scr + lane * 4) = ln2(unpack2(*(const unsigned*)(x + i1 * 512 + k0)), f32x2{sa.x, sa.x}, f32x2{sa.y, sa.y});
  *(unsigned*)(scr + 256 + lane * 4) = ln2(unpack2(*(const unsigned*)(x + i2 * 512 + k0)), f32x2{sb.x, sb.x}, f32x2{sb.y, sb.y});
  f32x16 zero;
#pragma unroll
  for (int i = 0; i < 16; ++i) zero[i] = 0.f;
  u32x4 ida, idb;
  {
    const int d = r - 8 * h;
    const unsigned one = 0x3f80u << ((d & 1) << 4);
#pragma unroll
    for (int q = 0; q < 4; ++q) {
      ida[q] = (d >> 1) == q ? one : 0u;
      idb[q] = (d >> 1) == q + 8 ? one : 0u;
    }
  }
  f32x16 gu = zero;
#pragma unroll
  for (int t = 0; t < 2; ++t) {
    f32x16 wt = __builtin_amdgcn_mfma_f32_32x32x16_bf16(__builtin_bit_cast(bf16x8, wf[2 * t]),
                                                        __builtin_bit_cast(bf16x8, ida), zero, 0, 0, 0);
    wt = __builtin_amdgcn_mfma_f32_32x32x16_bf16(__builtin_bit_cast(bf16x8, wf[2 * t + 1]),
                                                 __builtin_bit_cast(bf16x8, idb), wt, 0, 0, 0);
#pragma unroll
    for (int q = 0; q < 2; ++q) {
      u32x4 bw;
#pragma unroll
      for (int i = 0; i < 4; ++i) bw[i] = pack2(f32x2{wt[8 * q + 2 * i], wt[8 * q + 2 * i + 1]});
      const bf16x8 xf = *(const bf16x8*)(scr + (r & 1) * 256 + (r & 2) * 32 + (64 * t + 16 * q + 8 * h) * 2);
      gu = __builtin_amdgcn_mfma_f32_32x32x16_bf16(xf, __builtin_bit_cast(bf16x8, bw), gu, 0, 0, 0);
    }
  }
  {
    const float g0 = dpp_add<0x128>((lane & 8) ? gu[2] : gu[0]), g1 = dpp_add<0x128>((lane & 8) ? gu[3] : gu[1]);
    if (lane < 32) red[j * 32 + (lane & 16) + (lane & 15)] = (lane & 8) ? g1 : g0;
  }
  if (lane == 0) __hip_atomic_fetch_add(cnt, 1, __ATOMIC_RELEASE, __HIP_MEMORY_SCOPE_WORKGROUP);
  while (__hip_atomic_load(cnt, __ATOMIC_ACQUIRE, __HIP_MEMORY_SCOPE_WORKGROUP) < 4) __builtin_amdgcn_s_sleep(1);
  const int l = lane & 15;
  const float g = rbf((red[l] + red[32 + l]) + (red[64 + l] + red[96 + l]));
  const float u = rbf((red[16 + l] + red[48 + l]) + (red[80 + l] + red[112 + l]));
  const float hv = rbf(g * __builtin_amdgcn_rcpf(1.f + __expf(-g))) * u;
  if (lane < 24) *(bf16*)(scr + 512 + lane * 2) = (bf16)(lane < 16 ? hv : 0.f);
  const bf16x8 hf = *(const bf16x8*)(scr + 512 + ((r >> 1) == h ? (r & 1) * 16 : 32));
  u32x4 da, db;
#pragma unroll
  for (int c = 0; c < 4; ++c) {
    da[c] = __builtin_amdgcn_perm(d2[2 * c + 1], d2[2 * c], 0x05040100u);
    db[c] = __builtin_amdgcn_perm(d2[2 * c + 1], d2[2 * c], 0x07060302u);
  }
  const f32x16 ya = __builtin_amdgcn_mfma_f32_32x32x16_bf16(hf, __builtin_bit_cast(bf16x8, da), zero, 0, 0, 0);
  const f32x16 yb = __builtin_amdgcn_mfma_f32_32x32x16_bf16(hf, __builtin_bit_cast(bf16x8, db), zero, 0, 0, 0);
  const __amdgpu_buffer_rsrc_t rs = __builtin_amdgcn_make_buffer_rsrc(ypart, 0, (NFFN + NGRP) * 4096, 0x00020000);
  if (lane < 32) {
#pragma unroll
    for (int t = 0; t < 2; ++t)
      __builtin_amdgcn_raw_buffer_store_b128(__builtin_bit_cast(u32x4, f32x4{ya[t], yb[t], ya[2 + t], yb[2 + t]}), rs,
                                             t * 2048 + j * 512 + lane * 16, f * 4096, 16);
  }
  const int grp = f / GSZ, gs = min(GSZ, NFFN - grp * GSZ);
  if (!last_arrival(cnt + 1, gcnt + grp * 64, gs, lane)) return;
  const int voff = j * 1024 + lane * 16;
  f32x4 a = {0.f, 0.f, 0.f, 0.f};
  {
    u32x4 t[GSZ];
#pragma unroll
    for (int i = 0; i < GSZ; ++i) t[i] = __builtin_amdgcn_raw_buffer_load_b128(rs, voff, (grp * GSZ + min(i, gs - 1)) * 4096, 16);
#pragma unroll
    for (int i = 0; i < GSZ; ++i)
      if (i < gs) a += __builtin_bit_cast(f32x4, t[i]);
  }
  __builtin_amdgcn_raw_buffer_store_b128(__builtin_bit_cast(u32x4, a), rs, voff, (NFFN + grp) * 4096, 16);
  if (!last_arrival(cnt + 3, gcnt + NGRP * 64, NGRP, lane)) return;
  a = f32x4{0.f, 0.f, 0.f, 0.f};
  {
    u32x4 t[NGRP];
#pragma unroll
    for (int i = 0; i < NGRP; ++i) t[i] = __builtin_amdgcn_raw_buffer_load_b128(rs, voff, (NFFN + i) * 4096, 16);
#pragma unroll
    for (int i = 0; i < NGRP; ++i) a += __builtin_bit_cast(f32x4, t[i]);
  }
  const float gate = rbf(1.f / (1.f + __expf(-key_val(j < 2 ? k1 : k2))));
#pragma unroll
  for (int i = 0; i < 4; ++i) a[i] = rbf(rbf(a[i]) * gate);
  {
    const __amdgpu_buffer_rsrc_t ru = __builtin_amdgcn_make_buffer_rsrc(upd, 0, 4096, 0x00020000);
    __builtin_amdgcn_raw_buffer_store_b128(__builtin_bit_cast(u32x4, a), ru, voff, 0, 16);
  }
  asm volatile("s_waitcnt vmcnt(0)" ::: "memory");
  if (lane == 0 && __hip_atomic_fetch_add(cnt + 5, 1, __ATOMIC_RELAXED, __HIP_MEMORY_SCOPE_WORKGROUP) == 3)
    __hip_atomic_store((GLB_AS unsigned*)fdone, 1u, __ATOMIC_RELAXED, __HIP_MEMORY_SCOPE_AGENT);
}

extern "C" __global__ __launch_bounds__(768) void k2_attn(char* __restrict__ ws, const bf16* __restrict__ x,
                                               const bf16* __restrict__ wg, const bf16* __restrict__ wu,
                                               const bf16* __restrict__ wd, const bf16* __restrict__ wo,
                                               bf16* __restrict__ out) {
  const bf16* Q = (const bf16*)(ws + WS_Q);
  const bf16* Kb = (const bf16*)(ws + WS_K);
  bf16* O = (bf16*)(ws + WS_O);
  const float* st = (const float*)(ws + WS_ST);
  const unsigned* cand = (const unsigned*)(ws + WS_R);
  float* ypart = (float*)(ws + WS_YP);
  unsigned* gcnt = (unsigned*)(ws + WS_CNT);
  float* upd = (float*)(ws + WS_UPD);
  extern __shared__ __attribute__((aligned(16))) char smem[];
  char* kl = smem;
  char* vl = smem + 65536;
  char* cst = smem + 131072;
  const int tid = threadIdx.x, lane = tid & 63;
  const int wave = __builtin_amdgcn_readfirstlane(tid >> 6);

  if (wave >= 8) {
    const long long T1 = wall_clock64();
    const long long T0 = *(const long long*)(ws + WS_CL + 40 * 256);
    int* cnt = (int*)(smem + 131072 + CST + 32);
    if (tid < 512 + 6) cnt[tid - 512] = 0;
#pragma unroll
    for (int i = 0; i < 26; ++i) {
      const int q = (wave - 8) + 4 * i, piece = q < 48 ? 16 + q : 24 + q;
      glds16_raw((const char*)Kb + piece * 1024 + lane * 16, (unsigned)(size_t)(LDS_AS char*)smem + piece * 1024);
    }
    asm volatile("s_waitcnt lgkmcnt(0)\n s_barrier\n s_waitcnt vmcnt(0)\n s_barrier" ::: "memory");
    unsigned* clc = (unsigned*)(ws + WS_CL);
    __builtin_amdgcn_s_setprio(3);
    const int fs = (blockIdx.x & 7) * 22 + (blockIdx.x >> 3);
    if (blockIdx.x < 176 && fs < NFFN) {
      ffn_slice(fs, wave - 8, x, st, cand, clc + 32 * 64, wg, wu, wd, ypart, gcnt, upd,
                (float*)(smem + 131072 + CST + 64), cnt, smem + 131072 + CST + 576 + (wave - 8) * 640, lane);
    }
    const int tb = (blockIdx.x >> 6) * 8 + (blockIdx.x & 7), cb = (blockIdx.x >> 3) & 7, tok0 = tb * 64;
    const int sw = wave - 8, r = lane & 31, h = lane >> 5;
    char* ximg = smem;
    char* wimg = smem + 64 * XS_STRIDE;
    u32x4 wreg[16];
#pragma unroll
    for (int i = 0; i < 16; ++i) {
      const int row = (sw + 4 * i) * 8 + (lane >> 3);
      wreg[i] = *(const u32x4*)(wo + row * 512 + cb * 64 + (((lane & 7) ^ (((row >> 1) & 1) << 2)) << 3));
    }
    const int n0 = (sw & 1) * 32, t0 = (sw >> 1) * 32;
    const int tok = tok0 + t0 + r;
    u32x2 xv[4], nv[4];
#pragma unroll
    for (int g = 0; g < 4; ++g) xv[g] = *(const u32x2*)(x + tok * 512 + cb * 64 + n0 + 8 * g + 4 * h);
    const f32x2 sx = *(const f32x2*)(st + tok * 2);
    int rsel;
    {
      const u32x2 cv = *(const u32x2*)(cand + (lane & 31) * 2);
      unsigned k1 = cv[0], k2 = cv[1];
      top2_step<16>(k1, k2);
      top2_step<8>(k1, k2);
      top2_step<4>(k1, k2);
      top2_step<2>(k1, k2);
      top2_step<1>(k1, k2);
      rsel = tok == key_tok(k1) ? 0 : tok == key_tok(k2) ? 1 : -1;
    }
#pragma unroll
    for (int g = 0; g < 4; ++g) {
#pragma unroll
      for (int jj = 0; jj < 2; ++jj) {
        unsigned nr = ln2(unpack2(xv[g][jj]), f32x2{sx.x, sx.x}, f32x2{sx.y, sx.y});
        asm volatile("" : "+v"(nr));
        nv[g][jj] = nr;
      }
    }
    if (wave == 11 && lane == 0) {
      const unsigned t = *(const volatile unsigned*)(out + tok0 * 512 + cb * 64);
      asm volatile("" ::"v"(t));
    }
#pragma unroll
    for (int i = 0; i < 16; ++i) asm volatile("" : "+v"(wreg[i]));
    asm volatile("s_barrier" ::: "memory");
    const long long T2 = wall_clock64();
    if (tid == 512) __hip_atomic_fetch_add((GLB_AS unsigned*)(clc + tb * 64), 1u, __ATOMIC_RELAXED, __HIP_MEMORY_SCOPE_AGENT);
#pragma unroll
    for (int i = 0; i < 16; ++i) *(u32x4*)(wimg + (sw + 4 * i) * 1024 + lane * 16) = wreg[i];
    if (__any(rsel >= 0)) {
      int spin = 0;
      while (__hip_atomic_load((GLB_AS unsigned*)(clc + 32 * 64), __ATOMIC_RELAXED, __HIP_MEMORY_SCOPE_AGENT) == 0 && ++spin < SPIN_MAX)
        __builtin_amdgcn_s_sleep(1);
      asm volatile("" ::: "memory");
      const __amdgpu_buffer_rsrc_t ru = __builtin_amdgcn_make_buffer_rsrc(upd, 0, 4096, 0x00020000);
      if (rsel >= 0) {
#pragma unroll
        for (int g = 0; g < 4; ++g) {
          const f32x4 u = __builtin_bit_cast(f32x4, __builtin_amdgcn_raw_buffer_load_b128(ru, (rsel * 512 + cb * 64 + n0 + 8 * g + 4 * h) * 4, 0, 16));
          nv[g][0] = pack2(unpack2(nv[g][0]) + f32x2{u[0], u[1]});
          nv[g][1] = pack2(unpack2(nv[g][1]) + f32x2{u[2], u[3]});
        }
      }
    }
    if (wave == 8) {
      int spin = 0;
      while (__hip_atomic_load((GLB_AS unsigned*)(clc + tb * 64), __ATOMIC_RELAXED, __HIP_MEMORY_SCOPE_AGENT) < 8 && ++spin < SPIN_MAX)
        __builtin_amdgcn_s_sleep(1);
    }
    asm volatile("s_barrier" ::: "memory");
    {
      const __amdgpu_buffer_rsrc_t ro = __builtin_amdgcn_make_buffer_rsrc(O + tok0 * 512, 0, 65536, 0x00020000);
      u32x4 t[16];
#pragma unroll
      for (int i = 0; i < 16; ++i) t[i] = __builtin_amdgcn_raw_buffer_load_b128(ro, lane * 16, (sw * 16 + i) * 1024, 16);
#pragma unroll
      for (int i = 0; i < 16; ++i) *(u32x4*)(ximg + (sw * 16 + i) * XS_STRIDE + lane * 16) = t[i];
    }
    asm volatile("s_waitcnt lgkmcnt(0)\n s_barrier" ::: "memory");
    f32x16 acc = gemm_tile(wimg, ximg, n0, t0, lane);
#pragma unroll
    for (int g = 0; g < 4; ++g) {
      u32x2 o;
#pragma unroll
      for (int jj = 0; jj < 2; ++jj) {
        const unsigned xr = xv[g][jj], nr = nv[g][jj];
        const f32x2 a = unpack2(pack2(f32x2{acc[4 * g + 2 * jj], acc[4 * g + 2 * jj + 1]}));
        o[jj] = pack2(unpack2(pack2(unpack2(xr) + a)) + unpack2(nr));
      }
      *(u32x2*)(out + tok * 512 + cb * 64 + n0 + 8 * g + 4 * h) = o;
    }
    if (blockIdx.x == 0) {
      asm volatile("s_waitcnt vmcnt(0)" ::: "memory");
      const long long T3 = wall_clock64();
      long long d = 0;
      d = d < 0 ? 0 : d > 10000 ? 10000 : d;
      while (wall_clock64() - T3 < d) __builtin_amdgcn_s_sleep(2);
    }
    return;
  }

  const int r = lane & 31, h = lane >> 5;
  const int token = ((blockIdx.x >> 6) * 8 + (blockIdx.x & 7)) * 64 + ((blockIdx.x >> 3) & 7) * 8 + wave;
  bf16x8 qf;
  asm volatile("global_load_dwordx4 %0, %1, off" : "=v"(qf) : "v"(Q + token * 512 + r * 16 + 8 * h) : "memory");
  const unsigned lds0 = (unsigned)(size_t)(LDS_AS char*)smem;
#pragma unroll
  for (int i = 0; i < 3; ++i) {
    const int piece = (i < 2) ? (wave + i * 8) : (64 + wave);
    glds16_raw((const char*)Kb + piece * 1024 + lane * 16, lds0 + piece * 1024);
  }
  {
    const unsigned cv = ((tid & 15) == 0 || (tid & 15) == 4) ? 0x3f803f80u : 0u;
#pragma unroll
    for (int i = 0; i < CST / 8192; ++i) *(u32x4*)(cst + i * 8192 + tid * 16) = u32x4{cv, cv, cv, cv};
  }
  asm volatile("s_waitcnt vmcnt(0) lgkmcnt(0)\n s_barrier" ::: "memory");

  const char* kp0 = kl + lane * 16;
  const char* vp0 = (r < 16) ? (vl + (h * 16 + r) * 16) : (cst + (h * 16 + r - 16) * 16);
  const int vstep = (r < 16) ? NT * 1024 : 0;

  f32x16 zero;
#pragma unroll
  for (int i = 0; i < 16; ++i) zero[i] = 0.f;
  constexpr float HEAD = 40.f;
  float m;
  {
    float mx = -INFINITY;
#pragma unroll
    for (int t = 0; t < 4; ++t) {
      bf16x8 kf = *(const bf16x8*)(kp0 + t * 1024);
      f32x16 x = __builtin_amdgcn_mfma_f32_32x32x16_bf16(kf, qf, zero, 0, 0, 0);
#pragma unroll
      for (int i = 0; i < 16; i += 2) mx = fmaxf(fmaxf(mx, x[i]), x[i + 1]);
    }
    mx = fmaxf(mx, __shfl_xor(mx, 32));
    m = floorf(mx) + HEAD;
  }
  f32x16 negm;
#pragma unroll
  for (int i = 0; i < 16; ++i) negm[i] = -m;
  asm volatile("" : "+v"(negm));
  float tot[9];
#pragma unroll
  for (int i = 0; i < 9; ++i) tot[i] = 0.f;
  {
    const char* kp = kp0;
    const char* vp = vp0;
    int c = 0;
    bool redone = false, first = true;
    if (wave < 4) __builtin_amdgcn_s_setprio(2);
    else __builtin_amdgcn_s_setprio(1);
    f32x16 x = __builtin_amdgcn_mfma_f32_32x32x16_bf16(*(const bf16x8*)kp, qf, negm, 0, 0, 0);
    bf16x8 kfn = *(const bf16x8*)(kp + 1024);
    while (true) {
      f32x16 acc;
      while (true) {
        acc = zero;
#pragma unroll
        for (int t = 0; t < NT; ++t) {
          const bf16x8 kf = kfn;
          kfn = *(const bf16x8*)(kp + (t + 2) * 1024);
          const bf16x8 vf0 = *(const bf16x8*)(vp + (t * 2) * 512);
          const bf16x8 vf1 = *(const bf16x8*)(vp + (t * 2 + 1) * 512);
          bf16x8 pf;
#pragma unroll
          for (int j = 0; j < 4; ++j) pf[j] = (bf16)__builtin_amdgcn_exp2f(x[j]);
          __builtin_amdgcn_sched_barrier(0);
          f32x16 xn = __builtin_amdgcn_mfma_f32_32x32x16_bf16(kf, qf, negm, 0, 0, 0);
          asm volatile("" : "+v"(xn));
          __builtin_amdgcn_sched_barrier(0);
#pragma unroll
          for (int j = 4; j < 8; ++j) pf[j] = (bf16)__builtin_amdgcn_exp2f(x[j]);
          acc = __builtin_amdgcn_mfma_f32_32x32x16_bf16(vf0, pf, acc, 0, 0, 0);
#pragma unroll
          for (int j = 0; j < 8; ++j) pf[j] = (bf16)__builtin_amdgcn_exp2f(x[8 + j]);
          acc = __builtin_amdgcn_mfma_f32_32x32x16_bf16(vf1, pf, acc, 0, 0, 0);
          if (t == 7 && first) {
            asm volatile("s_barrier" ::: "memory");
            first = false;
          }
          if (c * NT + t == 53 && wave < 4) __builtin_amdgcn_s_setprio(0);
          __builtin_amdgcn_sched_barrier(0);
          x = xn;
        }
        if (__any(!(tot[8] + acc[8] <= 4.3e9f))) break;
        redone = false;
#pragma unroll
        for (int i = 0; i < 9; ++i) tot[i] += acc[i];
        ++c;
        kp += NT * 1024;
        vp += vstep;
        if (c == NC) break;
      }
      if (c == NC) break;
      if (!redone && __any(!(acc[8] < 7.9e28f))) {
        float mx = -INFINITY;
        for (int t = 0; t < NT; ++t) {
          bf16x8 kf = *(const bf16x8*)(kp + t * 1024);
          f32x16 x = __builtin_amdgcn_mfma_f32_32x32x16_bf16(kf, qf, zero, 0, 0, 0);
#pragma unroll
          for (int i = 0; i < 16; i += 2) mx = fmaxf(fmaxf(mx, x[i]), x[i + 1]);
        }
        mx = fmaxf(mx, __shfl_xor(mx, 32));
        const float mn = fmaxf(m, floorf(mx) + HEAD);
        const int d = (int)fminf(mn - m, 1024.f);
#pragma unroll
        for (int i = 0; i < 9; ++i) tot[i] = __builtin_amdgcn_ldexpf(tot[i], -d);
        m = mn;
        redone = true;
      } else {
        redone = false;
#pragma unroll
        for (int i = 0; i < 9; ++i) tot[i] += acc[i];
        const int e = max(__builtin_amdgcn_frexp_expf(tot[8]) - 1 + (int)HEAD, 0);
#pragma unroll
        for (int i = 0; i < 9; ++i) tot[i] = __builtin_amdgcn_ldexpf(tot[i], -e);
        m += (float)e;
        ++c;
        kp += NT * 1024;
        vp += vstep;
        if (c == NC) break;
      }
#pragma unroll
      for (int i = 0; i < 16; ++i) negm[i] = -m;
      asm volatile("" : "+v"(negm));
      x = __builtin_amdgcn_mfma_f32_32x32x16_bf16(*(const bf16x8*)kp, qf, negm, 0, 0, 0);
      kfn = *(const bf16x8*)(kp + 1024);
    }
  }
  {
    float chk = 0.f;
#pragma unroll
    for (int i = 0; i < 9; ++i) chk = __builtin_fmaf(tot[i], 0.f, chk);
    if (__any(chk != 0.f)) {
      f32x16 oacc = zero;
      float mrun = -INFINITY;
      const char* kp = kp0;
      const char* vp = vp0;
      for (int t = 0; t < 64; ++t) {
        bf16x8 kf = *(const bf16x8*)(kp);
        f32x16 x = __builtin_amdgcn_mfma_f32_32x32x16_bf16(kf, qf, zero, 0, 0, 0);
        float mx = x[0];
#pragma unroll
        for (int i = 1; i < 16; ++i) mx = fmaxf(mx, x[i]);
        mx = fmaxf(mx, __shfl_xor(mx, 32));
        const float mn = fmaxf(mrun, mx);
        const float al = __builtin_amdgcn_exp2f(mrun - mn);
        mrun = mn;
#pragma unroll
        for (int i = 0; i < 16; ++i) oacc[i] *= al;
#pragma unroll
        for (int s = 0; s < 2; ++s) {
          bf16x8 pf;
#pragma unroll
          for (int j = 0; j < 8; ++j) pf[j] = (bf16)__builtin_amdgcn_exp2f(x[8 * s + j] - mn);
          bf16x8 vf = *(const bf16x8*)(vp + ((t & (NT - 1)) * 2 + s) * 512);
          oacc = __builtin_amdgcn_mfma_f32_32x32x16_bf16(vf, pf, oacc, 0, 0, 0);
        }
        kp += 1024;
        if ((t & (NT - 1)) == NT - 1) vp += vstep;
      }
#pragma unroll
      for (int i = 0; i < 9; ++i) tot[i] = oacc[i];
    }
  }
  const float inv = 1.f / tot[8];
  {
    unsigned a0 = pack2(f32x2{tot[0] * inv, tot[1] * inv}), a1 = pack2(f32x2{tot[2] * inv, tot[3] * inv});
    unsigned b0 = pack2(f32x2{tot[4] * inv, tot[5] * inv}), b1 = pack2(f32x2{tot[6] * inv, tot[7] * inv});
    const unsigned s0 = __shfl_xor(h ? a0 : b0, 32), s1 = __shfl_xor(h ? a1 : b1, 32);
    const u32x4 o = h ? u32x4{s0, s1, b0, b1} : u32x4{a0, a1, s0, s1};
    const __amdgpu_buffer_rsrc_t ro = __builtin_amdgcn_make_buffer_rsrc(O + token * 512, 0, 1024, 0x00020000);
    __builtin_amdgcn_raw_buffer_store_b128(o, ro, r * 32 + h * 16, 0, 16);
  }
  asm volatile("s_waitcnt vmcnt(0)\n s_barrier" ::: "memory");
}

constexpr int K1_LDS = 64 * XS_STRIDE + 65536 + 16384 + 256;

extern "C" __global__ __launch_bounds__(768) void k1_ln_qkv(const bf16* __restrict__ x, const bf16* __restrict__ wq,
                                                 const bf16* __restrict__ wk, const bf16* __restrict__ wv,
                                                 const bf16* __restrict__ wr, char* __restrict__ ws) {
  float* st = (float*)(ws + WS_ST);
  bf16* Q = (bf16*)(ws + WS_Q);
  bf16* Kb = (bf16*)(ws + WS_K);
  bf16* Vf = (bf16*)(ws + WS_V);
  unsigned* cand = (unsigned*)(ws + WS_R);
  unsigned* gcnt = (unsigned*)(ws + WS_CNT);
  extern __shared__ __attribute__((aligned(16))) char smem[];
  char* ximg = smem;
  char* wimg = smem + 64 * XS_STRIDE;
  char* kvimg = smem + 64 * XS_STRIDE + 65536;
  float* rs = (float*)(smem + 64 * XS_STRIDE + 65536 + 16384);
  const int cb = blockIdx.x >> 5, tb = blockIdx.x & 31;
  const int tid = threadIdx.x, lane = tid & 63;
  const int wave = __builtin_amdgcn_readfirstlane(tid >> 6);
  const int tok0 = tb * 64;
  if (blockIdx.x == 0 && tid == 0) *(long long*)(ws + WS_CL + 40 * 256) = wall_clock64();

  if (wave >= 8) {
    if (blockIdx.x == 255 && tid >= 768 - (NGRP + 1)) gcnt[(767 - tid) * 64] = 0;
    if (blockIdx.x == 254 && tid >= 768 - 33) ((unsigned*)(ws + WS_CL))[(767 - tid) * 64] = 0;
    stage_w(wq, cb * 64, wimg, wave - 8, 4, lane);
    if (cb < 2) {
      const bf16* w = cb == 0 ? wk : wv;
#pragma unroll
      for (int i = 0; i < 4; ++i) {
        const int p = wave - 8 + 4 * i;
        glds16((const char*)w + p * 1024 + lane * 16, kvimg + p * 1024);
      }
    }
    __syncthreads();
    if (cb == 3 && wave == 8) {
      unsigned k1 = route_key(rs[lane], tok0 + lane), k2 = 0;
      top2_wave(k1, k2, 64);
      if (lane == 0) *(u32x2*)(cand + tb * 2) = u32x2{k1, k2};
    }
    return;
  }

  const int tl = wave * 8 + (lane >> 3), c8 = lane & 7;
  u32x4 xv[8];
#pragma unroll
  for (int i = 0; i < 8; ++i) xv[i] = *(const u32x4*)(x + (tok0 + tl) * 512 + (c8 + 8 * i) * 8);
  u32x4 wrv[8];
  if (cb == 3) {
#pragma unroll
    for (int i = 0; i < 8; ++i) wrv[i] = *(const u32x4*)(wr + (c8 + 8 * i) * 8);
  }

  f32x2 xf[32];
#pragma unroll
  for (int i = 0; i < 32; ++i) xf[i] = unpack2(xv[i >> 2][i & 3]);
  f32x2 s2 = {0.f, 0.f};
#pragma unroll
  for (int i = 0; i < 32; ++i) s2 += xf[i];
  const float mean = sum8(s2.x + s2.y) * (1.f / 512.f);
  const f32x2 mean2 = {mean, mean};
  f32x2 v2 = {0.f, 0.f};
#pragma unroll
  for (int i = 0; i < 32; ++i) { const f32x2 d = xf[i] - mean2; v2 += d * d; }
  const float vs = sum8(v2.x + v2.y);
  const float mb = rbf(mean);
  const float vb = rbf(vs * (1.f / 512.f));
  const float den = rbf(sqrtf(rbf(vb + 1e-5f)));
  const float rinv = __builtin_amdgcn_rcpf(den);
  const f32x2 mb2 = {mb, mb}, rinv2 = {rinv, rinv};
  u32x4 o[8];
#pragma unroll
  for (int i = 0; i < 8; ++i) {
#pragma unroll
    for (int j = 0; j < 4; ++j) o[i][j] = ln2(xf[4 * i + j], mb2, rinv2);
    *(u32x4*)(ximg + tl * XS_STRIDE + (c8 + 8 * i) * 16) = o[i];
  }
  if (cb == 2 && c8 == 0) *(f32x2*)(st + (tok0 + tl) * 2) = f32x2{mb, rinv};
  if (cb == 3) {
    float rd = 0.f;
#pragma unroll
    for (int i = 0; i < 8; ++i) {
#pragma unroll
      for (int j = 0; j < 4; ++j) {
        const unsigned a = o[i][j], b = wrv[i][j];
        rd = __builtin_amdgcn_fdot2_f32_bf16(__builtin_bit_cast(bf16x2, a), __builtin_bit_cast(bf16x2, b), rd, false);
      }
    }
    rd = sum8(rd);
    if (c8 == 0) rs[tl] = rbf(rd);
  }
  __syncthreads();

  const int r = lane & 31, h = lane >> 5;
  if (wave < 4) {
    const int n0 = (wave & 1) * 32, t0 = (wave >> 1) * 32;
    f32x16 acc = gemm_tile(wimg, ximg, n0, t0, lane);
    const float sc = 0.25f * 1.4426950408889634f;
    const __amdgpu_buffer_rsrc_t rq = __builtin_amdgcn_make_buffer_rsrc(Q + tok0 * 512, 0, 65536, 0x00020000);
#pragma unroll
    for (int m = 0; m < 2; ++m) {
      unsigned p[4];
#pragma unroll
      for (int i = 0; i < 4; ++i) p[i] = pack2(unpack2(pack2(f32x2{acc[8 * m + 2 * i], acc[8 * m + 2 * i + 1]})) * f32x2{sc, sc});
      const unsigned s0 = __shfl_xor(h ? p[0] : p[2], 32), s1 = __shfl_xor(h ? p[1] : p[3], 32);
      const u32x4 o = h ? u32x4{s0, s1, p[2], p[3]} : u32x4{p[0], p[1], s0, s1};
      __builtin_amdgcn_raw_buffer_store_b128(o, rq, ((t0 + r) * 512 + cb * 64 + n0 + 16 * m + 8 * h) * 2, 0, 0);
    }
  } else if (wave == 7) {
    if (lane < 2) {
      const unsigned t = *(const volatile unsigned*)(lane ? ws + WS_K + tb * 2048 : (const char*)(Q + tok0 * 512 + cb * 64));
      asm volatile("" ::"v"(t));
    }
  } else if (wave == 6 && cb == 7 && tb < 8) {
    unsigned long long c0, c1;
    CODE_ADDR(k2_attn, c0);
    CODE_ADDR(k1_ln_qkv, c1);
    const long long n = (long long)(c1 - c0);
    unsigned t[4];
#pragma unroll
    for (int i = 0; i < 4; ++i) {
      const int off = (i * 64 + lane) * 128;
      t[i] = off < n ? *(const volatile unsigned*)(c0 + off) : 0u;
    }
#pragma unroll
    for (int i = 0; i < 4; ++i) asm volatile("" ::"v"(t[i]));
  } else if (wave < 6 && cb < 2) {
    const int t0 = (wave - 4) * 32;
    f32x16 acc = gemm_tile16(kvimg, ximg, t0, lane);
    const int key = tok0 + t0 + r;
    const int t = key >> 5;
    if (cb == 0) {
#pragma unroll
      for (int g = 0; g < 2; ++g) {
        bf16x4 o;
#pragma unroll
        for (int j = 0; j < 4; ++j) o[j] = (bf16)acc[4 * g + j];
        *(bf16x4*)(Kb + ((t * 2 + g) * 32 + r) * 8 + 4 * h) = o;
      }
    } else {
      const int kk = key & 31, sg = kk >> 4, rem = kk & 15;
      const int hh = (rem >> 2) & 1, jj = ((rem >> 3) << 2) | (rem & 3);
#pragma unroll
      for (int g = 0; g < 2; ++g) {
#pragma unroll
        for (int j = 0; j < 4; ++j) {
          const int d = 8 * g + 4 * h + j;
          Vf[(((t * 2 + sg) * 2 + hh) * 16 + d) * 8 + jj] = (bf16)acc[4 * g + j];
        }
      }
    }
  }
}

extern "C" void kernel_launch(void* const* d_in, const int* in_sizes, int n_in, void* d_out, int out_size,
                              void* d_ws, size_t ws_size, hipStream_t stream) {
  const bf16* x = (const bf16*)d_in[0];
  const bf16* wq = (const bf16*)d_in[1];
  const bf16* wk = (const bf16*)d_in[2];
  const bf16* wv = (const bf16*)d_in[3];
  const bf16* wo = (const bf16*)d_in[4];
  const bf16* wr = (const bf16*)d_in[6];
  const bf16* wg = (const bf16*)d_in[7];
  const bf16* wu = (const bf16*)d_in[8];
  const bf16* wd = (const bf16*)d_in[9];
  char* ws = (char*)d_ws;
  bf16* out = (bf16*)d_out;

  (void)hipFuncSetAttribute((const void*)k1_ln_qkv, hipFuncAttributeMaxDynamicSharedMemorySize, K1_LDS);
  (void)hipFuncSetAttribute((const void*)k2_attn, hipFuncAttributeMaxDynamicSharedMemorySize, ATT_LDS);

  k1_ln_qkv<<<256, 768, K1_LDS, stream>>>(x, wq, wk, wv, wr, ws);
  k2_attn<<<256, 768, ATT_LDS, stream>>>(ws, x, wg, wu, wd, wo, out);
}
```

```cpp
#include <hip/hip_runtime.h>
#include <hip/hip_bf16.h>

typedef __attribute__((ext_vector_type(8))) __bf16 bf16x8;
typedef __attribute__((ext_vector_type(4))) __bf16 bf16x4;
typedef __attribute__((ext_vector_type(2))) __bf16 bf16x2;
typedef __attribute__((ext_vector_type(4))) short s16x4;
typedef __attribute__((ext_vector_type(8))) short s16x8;
typedef __attribute__((ext_vector_type(16))) float f32x16;
typedef __attribute__((ext_vector_type(4))) float f32x4;
typedef __attribute__((ext_vector_type(4))) unsigned u32x4;
typedef __attribute__((ext_vector_type(2))) unsigned u32x2;
typedef __attribute__((ext_vector_type(2))) float f32x2;
typedef __bf16 bf16;

#define LDS_AS __attribute__((address_space(3)))
#define GLB_AS __attribute__((address_space(1)))
#define DI __device__ __forceinline__

constexpr int S_ = 2048, D_ = 512, H_ = 32, DH_ = 16, F_ = 1376;

constexpr size_t WS_Q = 0;
constexpr size_t WS_O = WS_Q;
constexpr size_t WS_K = 2u << 20;
constexpr size_t WS_V = WS_K + 65536;
constexpr size_t WS_ST = WS_V + 65536;
constexpr size_t WS_R = WS_ST + 16384;
constexpr size_t WS_CNT = WS_R + 4096;
constexpr size_t WS_UPD = WS_CNT + 4096;
constexpr size_t WS_YP = WS_UPD + 4096;

constexpr int NFFN = 172;
constexpr int GSZ = 12, NGRP = 15;
constexpr size_t WS_CL = WS_YP + (NFFN + NGRP) * 4096;

DI float bf2f(bf16 v) { return (float)v; }
DI float rbf(float v) { return (float)(bf16)v; }
DI f32x2 unpack2(unsigned u) { return f32x2{__uint_as_float(u << 16), __uint_as_float(u & 0xffff0000u)}; }
DI unsigned pack2(f32x2 v) {
  unsigned r = __builtin_bit_cast(unsigned, __builtin_convertvector(v, bf16x2));
  asm("" : "+v"(r));
  return r;
}

DI unsigned route_key(float v, int tok) {
  const unsigned u = __float_as_uint(v) >> 16;
  const unsigned o = (u & 0x8000u) ? (~u & 0xffffu) : (u | 0x8000u);
  return (o << 16) | (unsigned)(0xffff - tok);
}
DI int key_tok(unsigned k) { return 0xffff - (int)(k & 0xffffu); }
DI float key_val(unsigned k) {
  const unsigned o = k >> 16;
  const unsigned u = (o & 0x8000u) ? (o & 0x7fffu) : (~o & 0xffffu);
  return __uint_as_float(u << 16);
}
DI unsigned ln2(f32x2 x, f32x2 mb2, f32x2 rinv2) { return pack2(unpack2(pack2(x - mb2)) * rinv2); }
DI void top2_wave(unsigned& k1, unsigned& k2, int width) {
  for (int o = width >> 1; o > 0; o >>= 1) {
    const unsigned a1 = __shfl_xor(k1, o), a2 = __shfl_xor(k2, o);
    k2 = max(min(k1, a1), max(k2, a2));
    k1 = max(k1, a1);
  }
}

template <int O>
DI void top2_step(unsigned& k1, unsigned& k2) {
  const unsigned a1 = __builtin_amdgcn_ds_swizzle(k1, (O << 10) | 31), a2 = __builtin_amdgcn_ds_swizzle(k2, (O << 10) | 31);
  k2 = max(min(k1, a1), max(k2, a2));
  k1 = max(k1, a1);
}

DI void glds16(const void* g, void* l) {
  __builtin_amdgcn_global_load_lds((const GLB_AS void*)g, (LDS_AS void*)l, 16, 0, 0);
}

DI void glds16_raw(const void* g, unsigned lds_off) {
  asm volatile("s_mov_b32 m0, %0\n s_nop 0\n global_load_lds_dwordx4 %1, off" ::"s"(lds_off), "v"(g) : "memory", "m0");
}

template <int CTRL>
DI float dpp_add(float v) {
  const int t = __builtin_amdgcn_update_dpp(0, __float_as_int(v), CTRL, 0xf, 0xf, true);
  return v + __int_as_float(t);
}
DI float sum8(float v) {
  v = dpp_add<0xB1>(v);
  v = dpp_add<0x4E>(v);
  v = dpp_add<0x141>(v);
  return v;
}

DI s16x4 tr_read(const char* p) {
  return __builtin_amdgcn_ds_read_tr16_b64_v4i16((LDS_AS s16x4*)p);
}

DI int wimg_off(int row, int ch) { return row * 128 + ((ch ^ (((row >> 1) & 1) << 2)) << 4); }

constexpr int XS_STRIDE = 1040;

DI void stage_w(const bf16* w, int col0, char* wimg, int wave, int nwaves, int lane) {
  for (int p = wave; p < 64; p += nwaves) {
    const int row = p * 8 + (lane >> 3), pc = lane & 7;
    const int ch = pc ^ (((row >> 1) & 1) << 2);
    glds16(w + row * 512 + col0 + ch * 8, wimg + p * 1024);
  }
}

constexpr int GP = 4;
DI f32x16 gemm_tile(const char* wimg, const char* ximg, int n0, int t0, int lane) {
  const int r = lane & 31, h = lane >> 5;
  const int q = (lane & 15) >> 2, p = lane & 3, blk = (lane >> 4) & 1;
  f32x16 acc;
#pragma unroll
  for (int i = 0; i < 16; ++i) acc[i] = 0.f;
  const char* xb = ximg + (t0 + r) * XS_STRIDE + 16 * h;
  const int col = n0 + 16 * blk + 4 * p;
  const int ch = col >> 3, sub = (col & 7) * 2;
  const char* wb0 = wimg + wimg_off(8 * h + q, ch) + sub;
  const char* wb1 = wimg + wimg_off(8 * h + q + 4, ch) + sub;
  s16x4 lo[32], hi[32];
  bf16x8 b[32];
#pragma unroll
  for (int ks = 0; ks < 32 + GP; ++ks) {
    if (ks < 32) {
      lo[ks] = tr_read(wb0 + ks * 2048);
      hi[ks] = tr_read(wb1 + ks * 2048);
      b[ks] = *(const bf16x8*)(xb + ks * 32);
    }
    if (ks >= GP) {
      const s16x8 a = __builtin_shufflevector(lo[ks - GP], hi[ks - GP], 0, 1, 2, 3, 4, 5, 6, 7);
      acc = __builtin_amdgcn_mfma_f32_32x32x16_bf16(__builtin_bit_cast(bf16x8, a), b[ks - GP], acc, 0, 0, 0);
      asm volatile("" : "+v"(acc));
    }
    __builtin_amdgcn_sched_barrier(0);
  }
  return acc;
}
DI f32x16 gemm_tile16(const char* wimg, const char* ximg, int t0, int lane) {
  const int r = lane & 31, h = lane >> 5;
  const int q = (lane & 15) >> 2, p = lane & 3;
  f32x16 acc;
#pragma unroll
  for (int i = 0; i < 16; ++i) acc[i] = 0.f;
  const char* xb = ximg + (t0 + r) * XS_STRIDE + 16 * h;
  const char* wb = wimg + (8 * h + q) * 32 + 8 * p;
  s16x4 lo[32], hi[32];
  bf16x8 b[32];
#pragma unroll
  for (int ks = 0; ks < 32 + GP; ++ks) {
    if (ks < 32) {
      lo[ks] = tr_read(wb + ks * 512);
      hi[ks] = tr_read(wb + ks * 512 + 128);
      b[ks] = *(const bf16x8*)(xb + ks * 32);
    }
    if (ks >= GP) {
      const s16x8 a = __builtin_shufflevector(lo[ks - GP], hi[ks - GP], 0, 1, 2, 3, 4, 5, 6, 7);
      acc = __builtin_amdgcn_mfma_f32_32x32x16_bf16(__builtin_bit_cast(bf16x8, a), b[ks - GP], acc, 0, 0, 0);
      asm volatile("" : "+v"(acc));
    }
    __builtin_amdgcn_sched_barrier(0);
  }
  return acc;
}

#define CODE_ADDR(sym, out)                                                                               \
  asm volatile("s_getpc_b64 s[20:21]\n s_add_u32 s20, s20, " #sym "@rel32@lo+4\n s_addc_u32 s21, s21, " #sym \
               "@rel32@hi+12\n s_mov_b64 %0, s[20:21]"                                                    \
               : "=s"(out)::"s20", "s21", "scc")

constexpr int NT = 16;
constexpr int CST = NT * 1024;
constexpr int ATT_LDS = 65536 * 2 + CST + 64 + 512 + 4 * 640;
constexpr int NC = 64 / NT;
constexpr int SPIN_MAX = 1 << 22;

DI bool last_arrival(int* w, unsigned* gc, int n, int lane) {
  asm volatile("s_waitcnt vmcnt(0)" ::: "memory");
  if (lane == 0) {
    if (__hip_atomic_fetch_add(w, 1, __ATOMIC_RELAXED, __HIP_MEMORY_SCOPE_WORKGROUP) == 3) {
      const unsigned t = __hip_atomic_fetch_add((GLB_AS unsigned*)gc, 1u, __ATOMIC_RELAXED, __HIP_MEMORY_SCOPE_AGENT);
      __hip_atomic_store(w + 1, t == (unsigned)(n - 1) ? 2 : 1, __ATOMIC_RELAXED, __HIP_MEMORY_SCOPE_WORKGROUP);
    }
  }
  int r;
  while ((r = __hip_atomic_load(w + 1, __ATOMIC_RELAXED, __HIP_MEMORY_SCOPE_WORKGROUP)) == 0) __builtin_amdgcn_s_sleep(2);
  asm volatile("" ::: "memory");
  return r == 2;
}

DI void ffn_slice(int f, int j, const bf16* __restrict__ x, const float* __restrict__ st,
                  const unsigned* __restrict__ cand, unsigned* __restrict__ fdone,
                  const bf16* __restrict__ wg, const bf16* __restrict__ wu, const bf16* __restrict__ wd,
                  float* __restrict__ ypart, unsigned* __restrict__ gcnt,
                  float* __restrict__ upd, float* red, int* cnt, char* scr, int lane) {
  const int k0 = j * 128 + lane * 2;
  const int r = lane & 31, h = lane >> 5;
  const int kr = (lane & 51) | ((lane & 4) << 1) | ((lane & 8) >> 1);
  u32x4 wf[4];
  unsigned d2[8];
  {
    const __amdgpu_buffer_rsrc_t rg = __builtin_amdgcn_make_buffer_rsrc((void*)wg, 0, 512 * F_ * 2, 0x00020000);
    const __amdgpu_buffer_rsrc_t ru = __builtin_amdgcn_make_buffer_rsrc((void*)wu, 0, 512 * F_ * 2, 0x00020000);
    const __amdgpu_buffer_rsrc_t rd = __builtin_amdgcn_make_buffer_rsrc((void*)wd, 0, 512 * F_ * 2, 0x00020000);
    const int wo_ = kr * (F_ * 2), so = (j * 128 * F_ + f * 8) * 2;
#pragma unroll
    for (int t = 0; t < 2; ++t) {
      wf[2 * t] = __builtin_amdgcn_raw_buffer_load_b128(rg, wo_, so + t * (64 * F_ * 2), 0);
      wf[2 * t + 1] = __builtin_amdgcn_raw_buffer_load_b128(ru, wo_, so + t * (64 * F_ * 2), 0);
    }
#pragma unroll
    for (int c = 0; c < 8; ++c)
      d2[c] = __builtin_amdgcn_raw_buffer_load_b32(rd, 8 * r + 4 * h + c * 1024, (f * 8 * 512 + j * 128) * 2, 0);
  }
  const u32x2 cv = *(const u32x2*)(cand + (lane & 31) * 2);
  unsigned k1 = cv[0], k2 = cv[1];
  top2_step<16>(k1, k2);
  top2_step<8>(k1, k2);
  top2_step<4>(k1, k2);
  top2_step<2>(k1, k2);
  top2_step<1>(k1, k2);
  k1 = __builtin_amdgcn_readfirstlane(k1);
  k2 = __builtin_amdgcn_readfirstlane(k2);
  const int i1 = key_tok(k1), i2 = key_tok(k2);
  const f32x2 sa = *(const f32x2*)(st + i1 * 2), sb = *(const f32x2*)(st + i2 * 2);
  *(unsigned*)(scr + lane * 4) = ln2(unpack2(*(const unsigned*)(x + i1 * 512 + k0)), f32x2{sa.x, sa.x}, f32x2{sa.y, sa.y});
  *(unsigned*)(scr + 256 + lane * 4) = ln2(unpack2(*(const unsigned*)(x + i2 * 512 + k0)), f32x2{sb.x, sb.x}, f32x2{sb.y, sb.y});
  f32x16 zero;
#pragma unroll
  for (int i = 0; i < 16; ++i) zero[i] = 0.f;
  u32x4 ida, idb;
  {
    const int d = r - 8 * h;
    const unsigned one = 0x3f80u << ((d & 1) << 4);
#pragma unroll
    for (int q = 0; q < 4; ++q) {
      ida[q] = (d >> 1) == q ? one : 0u;
      idb[q] = (d >> 1) == q + 8 ? one : 0u;
    }
  }
  f32x16 gu = zero;
#pragma unroll
  for (int t = 0; t < 2; ++t) {
    f32x16 wt = __builtin_amdgcn_mfma_f32_32x32x16_bf16(__builtin_bit_cast(bf16x8, wf[2 * t]),
                                                        __builtin_bit_cast(bf16x8, ida), zero, 0, 0, 0);
    wt = __builtin_amdgcn_mfma_f32_32x32x16_bf16(__builtin_bit_cast(bf16x8, wf[2 * t + 1]),
                                                 __builtin_bit_cast(bf16x8, idb), wt, 0, 0, 0);
#pragma unroll
    for (int q = 0; q < 2; ++q) {
      u32x4 bw;
#pragma unroll
      for (int i = 0; i < 4; ++i) bw[i] = pack2(f32x2{wt[8 * q + 2 * i], wt[8 * q + 2 * i + 1]});
      const bf16x8 xf = *(const bf16x8*)(scr + (r & 1) * 256 + (r & 2) * 32 + (64 * t + 16 * q + 8 * h) * 2);
      gu = __builtin_amdgcn_mfma_f32_32x32x16_bf16(xf, __builtin_bit_cast(bf16x8, bw), gu, 0, 0, 0);
    }
  }
  {
    const float g0 = dpp_add<0x128>((lane & 8) ? gu[2] : gu[0]), g1 = dpp_add<0x128>((lane & 8) ? gu[3] : gu[1]);
    if (lane < 32) red[j * 32 + (lane & 16) + (lane & 15)] = (lane & 8) ? g1 : g0;
  }
  if (lane == 0) __hip_atomic_fetch_add(cnt, 1, __ATOMIC_RELEASE, __HIP_MEMORY_SCOPE_WORKGROUP);
  while (__hip_atomic_load(cnt, __ATOMIC_ACQUIRE, __HIP_MEMORY_SCOPE_WORKGROUP) < 4) __builtin_amdgcn_s_sleep(1);
  const int l = lane & 15;
  const float g = rbf((red[l] + red[32 + l]) + (red[64 + l] + red[96 + l]));
  const float u = rbf((red[16 + l] + red[48 + l]) + (red[80 + l] + red[112 + l]));
  const float hv = rbf(g * __builtin_amdgcn_rcpf(1.f + __expf(-g))) * u;
  if (lane < 24) *(bf16*)(scr + 512 + lane * 2) = (bf16)(lane < 16 ? hv : 0.f);
  const bf16x8 hf = *(const bf16x8*)(scr + 512 + ((r >> 1) == h ? (r & 1) * 16 : 32));
  u32x4 da, db;
#pragma unroll
  for (int c = 0; c < 4; ++c) {
    da[c] = __builtin_amdgcn_perm(d2[2 * c + 1], d2[2 * c], 0x05040100u);
    db[c] = __builtin_amdgcn_perm(d2[2 * c + 1], d2[2 * c], 0x07060302u);
  }
  const f32x16 ya = __builtin_amdgcn_mfma_f32_32x32x16_bf16(hf, __builtin_bit_cast(bf16x8, da), zero, 0, 0, 0);
  const f32x16 yb = __builtin_amdgcn_mfma_f32_32x32x16_bf16(hf, __builtin_bit_cast(bf16x8, db), zero, 0, 0, 0);
  const __amdgpu_buffer_rsrc_t rs = __builtin_amdgcn_make_buffer_rsrc(ypart, 0, (NFFN + NGRP) * 4096, 0x00020000);
  if (lane < 32) {
#pragma unroll
    for (int t = 0; t < 2; ++t)
      __builtin_amdgcn_raw_buffer_store_b128(__builtin_bit_cast(u32x4, f32x4{ya[t], yb[t], ya[2 + t], yb[2 + t]}), rs,
                                             t * 2048 + j * 512 + lane * 16, f * 4096, 16);
  }
  const int grp = f / GSZ, gs = min(GSZ, NFFN - grp * GSZ);
  if (!last_arrival(cnt + 1, gcnt + grp * 64, gs, lane)) return;
  const int voff = j * 1024 + lane * 16;
  f32x4 a = {0.f, 0.f, 0.f, 0.f};
  {
    u32x4 t[GSZ];
#pragma unroll
    for (int i = 0; i < GSZ; ++i) t[i] = __builtin_amdgcn_raw_buffer_load_b128(rs, voff, (grp * GSZ + min(i, gs - 1)) * 4096, 16);
#pragma unroll
    for (int i = 0; i < GSZ; ++i)
      if (i < gs) a += __builtin_bit_cast(f32x4, t[i]);
  }
  __builtin_amdgcn_raw_buffer_store_b128(__builtin_bit_cast(u32x4, a), rs, voff, (NFFN + grp) * 4096, 16);
  if (!last_arrival(cnt + 3, gcnt + NGRP * 64, NGRP, lane)) return;
  a = f32x4{0.f, 0.f, 0.f, 0.f};
  {
    u32x4 t[NGRP];
#pragma unroll
    for (int i = 0; i < NGRP; ++i) t[i] = __builtin_amdgcn_raw_buffer_load_b128(rs, voff, (NFFN + i) * 4096, 16);
#pragma unroll
    for (int i = 0; i < NGRP; ++i) a += __builtin_bit_cast(f32x4, t[i]);
  }
  const float gate = rbf(1.f / (1.f + __expf(-key_val(j < 2 ? k1 : k2))));
#pragma unroll
  for (int i = 0; i < 4; ++i) a[i] = rbf(rbf(a[i]) * gate);
  {
    const __amdgpu_buffer_rsrc_t ru = __builtin_amdgcn_make_buffer_rsrc(upd, 0, 4096, 0x00020000);
    __builtin_amdgcn_raw_buffer_store_b128(__builtin_bit_cast(u32x4, a), ru, voff, 0, 16);
  }
  asm volatile("s_waitcnt vmcnt(0)" ::: "memory");
  if (lane == 0 && __hip_atomic_fetch_add(cnt + 5, 1, __ATOMIC_RELAXED, __HIP_MEMORY_SCOPE_WORKGROUP) == 3)
    __hip_atomic_store((GLB_AS unsigned*)fdone, 1u, __ATOMIC_RELAXED, __HIP_MEMORY_SCOPE_AGENT);
}

extern "C" __global__ __launch_bounds__(768) void k2_attn(char* __restrict__ ws, const bf16* __restrict__ x,
                                               const bf16* __restrict__ wg, const bf16* __restrict__ wu,
                                               const bf16* __restrict__ wd, const bf16* __restrict__ wo,
                                               bf16* __restrict__ out) {
  const bf16* Q = (const bf16*)(ws + WS_Q);
  const bf16* Kb = (const bf16*)(ws + WS_K);
  bf16* O = (bf16*)(ws + WS_O);
  const float* st = (const float*)(ws + WS_ST);
  const unsigned* cand = (const unsigned*)(ws + WS_R);
  float* ypart = (float*)(ws + WS_YP);
  unsigned* gcnt = (unsigned*)(ws + WS_CNT);
  float* upd = (float*)(ws + WS_UPD);
  extern __shared__ __attribute__((aligned(16))) char smem[];
  char* kl = smem;
  char* vl = smem + 65536;
  char* cst = smem + 131072;
  const int tid = threadIdx.x, lane = tid & 63;
  const int wave = __builtin_amdgcn_readfirstlane(tid >> 6);

  if (wave >= 8) {
    const long long T1 = wall_clock64();
    int* cnt = (int*)(smem + 131072 + CST + 32);
    if (tid < 512 + 6) cnt[tid - 512] = 0;
#pragma unroll
    for (int i = 0; i < 26; ++i) {
      const int q = (wave - 8) + 4 * i, piece = q < 48 ? 16 + q : 24 + q;
      glds16_raw((const char*)Kb + piece * 1024 + lane * 16, (unsigned)(size_t)(LDS_AS char*)smem + piece * 1024);
    }
    asm volatile("s_waitcnt lgkmcnt(0)\n s_barrier\n s_waitcnt vmcnt(0)\n s_barrier" ::: "memory");
    unsigned* clc = (unsigned*)(ws + WS_CL);
    __builtin_amdgcn_s_setprio(3);
    const int fs = (blockIdx.x & 7) * 22 + (blockIdx.x >> 3);
    if (blockIdx.x < 176 && fs < NFFN) {
      ffn_slice(fs, wave - 8, x, st, cand, clc + 32 * 64, wg, wu, wd, ypart, gcnt, upd,
                (float*)(smem + 131072 + CST + 64), cnt, smem + 131072 + CST + 576 + (wave - 8) * 640, lane);
    }
    const int tb = (blockIdx.x >> 6) * 8 + (blockIdx.x & 7), cb = (blockIdx.x >> 3) & 7, tok0 = tb * 64;
    const int sw = wave - 8, r = lane & 31, h = lane >> 5;
    char* ximg = smem;
    char* wimg = smem + 64 * XS_STRIDE;
    u32x4 wreg[16];
#pragma unroll
    for (int i = 0; i < 16; ++i) {
      const int row = (sw + 4 * i) * 8 + (lane >> 3);
      wreg[i] = *(const u32x4*)(wo + row * 512 + cb * 64 + (((lane & 7) ^ (((row >> 1) & 1) << 2)) << 3));
    }
    const int n0 = (sw & 1) * 32, t0 = (sw >> 1) * 32;
    const int tok = tok0 + t0 + r;
    u32x2 xv[4], nv[4];
#pragma unroll
    for (int g = 0; g < 4; ++g) xv[g] = *(const u32x2*)(x + tok * 512 + cb * 64 + n0 + 8 * g + 4 * h);
    const f32x2 sx = *(const f32x2*)(st + tok * 2);
    int rsel;
    {
      const u32x2 cv = *(const u32x2*)(cand + (lane & 31) * 2);
      unsigned k1 = cv[0], k2 = cv[1];
      top2_step<16>(k1, k2);
      top2_step<8>(k1, k2);
      top2_step<4>(k1, k2);
      top2_step<2>(k1, k2);
      top2_step<1>(k1, k2);
      rsel = tok == key_tok(k1) ? 0 : tok == key_tok(k2) ? 1 : -1;
    }
#pragma unroll
    for (int g = 0; g < 4; ++g) {
#pragma unroll
      for (int jj = 0; jj < 2; ++jj) {
        unsigned nr = ln2(unpack2(xv[g][jj]), f32x2{sx.x, sx.x}, f32x2{sx.y, sx.y});
        asm volatile("" : "+v"(nr));
        nv[g][jj] = nr;
      }
    }
    if (wave == 11 && lane == 0) {
      const unsigned t = *(const volatile unsigned*)(out + tok0 * 512 + cb * 64);
      asm volatile("" ::"v"(t));
    }
#pragma unroll
    for (int i = 0; i < 16; ++i) asm volatile("" : "+v"(wreg[i]));
    asm volatile("s_barrier" ::: "memory");
    if (tid == 512) __hip_atomic_fetch_add((GLB_AS unsigned*)(clc + tb * 64), 1u, __ATOMIC_RELAXED, __HIP_MEMORY_SCOPE_AGENT);
#pragma unroll
    for (int i = 0; i < 16; ++i) *(u32x4*)(wimg + (sw + 4 * i) * 1024 + lane * 16) = wreg[i];
    if (__any(rsel >= 0)) {
      int spin = 0;
      while (__hip_atomic_load((GLB_AS unsigned*)(clc + 32 * 64), __ATOMIC_RELAXED, __HIP_MEMORY_SCOPE_AGENT) == 0 && ++spin < SPIN_MAX)
        __builtin_amdgcn_s_sleep(1);
      asm volatile("" ::: "memory");
      const __amdgpu_buffer_rsrc_t ru = __builtin_amdgcn_make_buffer_rsrc(upd, 0, 4096, 0x00020000);
      if (rsel >= 0) {
#pragma unroll
        for (int g = 0; g < 4; ++g) {
          const f32x4 u = __builtin_bit_cast(f32x4, __builtin_amdgcn_raw_buffer_load_b128(ru, (rsel * 512 + cb * 64 + n0 + 8 * g + 4 * h) * 4, 0, 16));
          nv[g][0] = pack2(unpack2(nv[g][0]) + f32x2{u[0], u[1]});
          nv[g][1] = pack2(unpack2(nv[g][1]) + f32x2{u[2], u[3]});
        }
      }
    }
    if (wave == 8) {
      int spin = 0;
      while (__hip_atomic_load((GLB_AS unsigned*)(clc + tb * 64), __ATOMIC_RELAXED, __HIP_MEMORY_SCOPE_AGENT) < 8 && ++spin < SPIN_MAX)
        __builtin_amdgcn_s_sleep(1);
    }
    asm volatile("s_barrier" ::: "memory");
    {
      const __amdgpu_buffer_rsrc_t ro = __builtin_amdgcn_make_buffer_rsrc(O + tok0 * 512, 0, 65536, 0x00020000);
      u32x4 t[16];
#pragma unroll
      for (int i = 0; i < 16; ++i) t[i] = __builtin_amdgcn_raw_buffer_load_b128(ro, lane * 16, (sw * 16 + i) * 1024, 16);
#pragma unroll
      for (int i = 0; i < 16; ++i) *(u32x4*)(ximg + (sw * 16 + i) * XS_STRIDE + lane * 16) = t[i];
    }
    asm volatile("s_waitcnt lgkmcnt(0)\n s_barrier" ::: "memory");
    f32x16 acc = gemm_tile(wimg, ximg, n0, t0, lane);
#pragma unroll
    for (int g = 0; g < 4; ++g) {
      u32x2 o;
#pragma unroll
      for (int jj = 0; jj < 2; ++jj) {
        const unsigned xr = xv[g][jj], nr = nv[g][jj];
        const f32x2 a = unpack2(pack2(f32x2{acc[4 * g + 2 * jj], acc[4 * g + 2 * jj + 1]}));
        o[jj] = pack2(unpack2(pack2(unpack2(xr) + a)) + unpack2(nr));
      }
      *(u32x2*)(out + tok * 512 + cb * 64 + n0 + 8 * g + 4 * h) = o;
    }
    if (blockIdx.x == 0) {
      const long long* sp = (const long long*)(ws + (8u << 20));
      long long smin = 1LL << 62, smax = 0, emax = 0, s0 = sp[0];
      for (int i = 0; i < 256; ++i) { smin = min(smin, sp[i]); smax = max(smax, sp[i]); emax = max(emax, sp[256 + i]); }
      asm volatile("s_waitcnt vmcnt(0)" ::: "memory");
      long long d = s0 - smin;
      d = d < 0 ? 0 : d > 10000 ? 10000 : d;
      const long long T3 = wall_clock64();
      while (wall_clock64() - T3 < d) __builtin_amdgcn_s_sleep(2);
    }
    return;
  }

  const int r = lane & 31, h = lane >> 5;
  const int token = ((blockIdx.x >> 6) * 8 + (blockIdx.x & 7)) * 64 + ((blockIdx.x >> 3) & 7) * 8 + wave;
  bf16x8 qf;
  asm volatile("global_load_dwordx4 %0, %1, off" : "=v"(qf) : "v"(Q + token * 512 + r * 16 + 8 * h) : "memory");
  const unsigned lds0 = (unsigned)(size_t)(LDS_AS char*)smem;
#pragma unroll
  for (int i = 0; i < 3; ++i) {
    const int piece = (i < 2) ? (wave + i * 8) : (64 + wave);
    glds16_raw((const char*)Kb + piece * 1024 + lane * 16, lds0 + piece * 1024);
  }
  {
    const unsigned cv = ((tid & 15) == 0 || (tid & 15) == 4) ? 0x3f803f80u : 0u;
#pragma unroll
    for (int i = 0; i < CST / 8192; ++i) *(u32x4*)(cst + i * 8192 + tid * 16) = u32x4{cv, cv, cv, cv};
  }
  asm volatile("s_waitcnt vmcnt(0) lgkmcnt(0)\n s_barrier" ::: "memory");

  const char* kp0 = kl + lane * 16;
  const char* vp0 = (r < 16) ? (vl + (h * 16 + r) * 16) : (cst + (h * 16 + r - 16) * 16);
  const int vstep = (r < 16) ? NT * 1024 : 0;

  f32x16 zero;
#pragma unroll
  for (int i = 0; i < 16; ++i) zero[i] = 0.f;
  constexpr float HEAD = 40.f;
  float m;
  {
    float mx = -INFINITY;
#pragma unroll
    for (int t = 0; t < 4; ++t) {
      bf16x8 kf = *(const bf16x8*)(kp0 + t * 1024);
      f32x16 x = __builtin_amdgcn_mfma_f32_32x32x16_bf16(kf, qf, zero, 0, 0, 0);
#pragma unroll
      for (int i = 0; i < 16; i += 2) mx = fmaxf(fmaxf(mx, x[i]), x[i + 1]);
    }
    mx = fmaxf(mx, __shfl_xor(mx, 32));
    m = floorf(mx) + HEAD;
  }
  f32x16 negm;
#pragma unroll
  for (int i = 0; i < 16; ++i) negm[i] = -m;
  asm volatile("" : "+v"(negm));
  float tot[9];
#pragma unroll
  for (int i = 0; i < 9; ++i) tot[i] = 0.f;
  {
    const char* kp = kp0;
    const char* vp = vp0;
    int c = 0;
    bool redone = false, first = true;
    if (wave < 4) __builtin_amdgcn_s_setprio(2);
    else __builtin_amdgcn_s_setprio(1);
    f32x16 x = __builtin_amdgcn_mfma_f32_32x32x16_bf16(*(const bf16x8*)kp, qf, negm, 0, 0, 0);
    bf16x8 kfn = *(const bf16x8*)(kp + 1024);
    while (true) {
      f32x16 acc;
      while (true) {
        acc = zero;
#pragma unroll
        for (int t = 0; t < NT; ++t) {
          const bf16x8 kf = kfn;
          kfn = *(const bf16x8*)(kp + (t + 2) * 1024);
          const bf16x8 vf0 = *(const bf16x8*)(vp + (t * 2) * 512);
          const bf16x8 vf1 = *(const bf16x8*)(vp + (t * 2 + 1) * 512);
          bf16x8 pf;
#pragma unroll
          for (int j = 0; j < 4; ++j) pf[j] = (bf16)__builtin_amdgcn_exp2f(x[j]);
          __builtin_amdgcn_sched_barrier(0);
          f32x16 xn = __builtin_amdgcn_mfma_f32_32x32x16_bf16(kf, qf, negm, 0, 0, 0);
          asm volatile("" : "+v"(xn));
          __builtin_amdgcn_sched_barrier(0);
#pragma unroll
          for (int j = 4; j < 8; ++j) pf[j] = (bf16)__builtin_amdgcn_exp2f(x[j]);
          acc = __builtin_amdgcn_mfma_f32_32x32x16_bf16(vf0, pf, acc, 0, 0, 0);
#pragma unroll
          for (int j = 0; j < 8; ++j) pf[j] = (bf16)__builtin_amdgcn_exp2f(x[8 + j]);
          acc = __builtin_amdgcn_mfma_f32_32x32x16_bf16(vf1, pf, acc, 0, 0, 0);
          if (t == 7 && first) {
            asm volatile("s_barrier" ::: "memory");
            first = false;
          }
          if (c * NT + t == 53 && wave < 4) __builtin_amdgcn_s_setprio(0);
          __builtin_amdgcn_sched_barrier(0);
          x = xn;
        }
        if (__any(!(tot[8] + acc[8] <= 4.3e9f))) break;
        redone = false;
#pragma unroll
        for (int i = 0; i < 9; ++i) tot[i] += acc[i];
        ++c;
        kp += NT * 1024;
        vp += vstep;
        if (c == NC) break;
      }
      if (c == NC) break;
      if (!redone && __any(!(acc[8] < 7.9e28f))) {
        float mx = -INFINITY;
        for (int t = 0; t < NT; ++t) {
          bf16x8 kf = *(const bf16x8*)(kp + t * 1024);
          f32x16 x = __builtin_amdgcn_mfma_f32_32x32x16_bf16(kf, qf, zero, 0, 0, 0);
#pragma unroll
          for (int i = 0; i < 16; i += 2) mx = fmaxf(fmaxf(mx, x[i]), x[i + 1]);
        }
        mx = fmaxf(mx, __shfl_xor(mx, 32));
        const float mn = fmaxf(m, floorf(mx) + HEAD);
        const int d = (int)fminf(mn - m, 1024.f);
#pragma unroll
        for (int i = 0; i < 9; ++i) tot[i] = __builtin_amdgcn_ldexpf(tot[i], -d);
        m = mn;
        redone = true;
      } else {
        redone = false;
#pragma unroll
        for (int i = 0; i < 9; ++i) tot[i] += acc[i];
        const int e = max(__builtin_amdgcn_frexp_expf(tot[8]) - 1 + (int)HEAD, 0);
#pragma unroll
        for (int i = 0; i < 9; ++i) tot[i] = __builtin_amdgcn_ldexpf(tot[i], -e);
        m += (float)e;
        ++c;
        kp += NT * 1024;
        vp += vstep;
        if (c == NC) break;
      }
#pragma unroll
      for (int i = 0; i < 16; ++i) negm[i] = -m;
      asm volatile("" : "+v"(negm));
      x = __builtin_amdgcn_mfma_f32_32x32x16_bf16(*(const bf16x8*)kp, qf, negm, 0, 0, 0);
      kfn = *(const bf16x8*)(kp + 1024);
    }
  }
  {
    float chk = 0.f;
#pragma unroll
    for (int i = 0; i < 9; ++i) chk = __builtin_fmaf(tot[i], 0.f, chk);
    if (__any(chk != 0.f)) {
      f32x16 oacc = zero;
      float mrun = -INFINITY;
      const char* kp = kp0;
      const char* vp = vp0;
      for (int t = 0; t < 64; ++t) {
        bf16x8 kf = *(const bf16x8*)(kp);
        f32x16 x = __builtin_amdgcn_mfma_f32_32x32x16_bf16(kf, qf, zero, 0, 0, 0);
        float mx = x[0];
#pragma unroll
        for (int i = 1; i < 16; ++i) mx = fmaxf(mx, x[i]);
        mx = fmaxf(mx, __shfl_xor(mx, 32));
        const float mn = fmaxf(mrun, mx);
        const float al = __builtin_amdgcn_exp2f(mrun - mn);
        mrun = mn;
#pragma unroll
        for (int i = 0; i < 16; ++i) oacc[i] *= al;
#pragma unroll
        for (int s = 0; s < 2; ++s) {
          bf16x8 pf;
#pragma unroll
          for (int j = 0; j < 8; ++j) pf[j] = (bf16)__builtin_amdgcn_exp2f(x[8 * s + j] - mn);
          bf16x8 vf = *(const bf16x8*)(vp + ((t & (NT - 1)) * 2 + s) * 512);
          oacc = __builtin_amdgcn_mfma_f32_32x32x16_bf16(vf, pf, oacc, 0, 0, 0);
        }
        kp += 1024;
        if ((t & (NT - 1)) == NT - 1) vp += vstep;
      }
#pragma unroll
      for (int i = 0; i < 9; ++i) tot[i] = oacc[i];
    }
  }
  const float inv = 1.f / tot[8];
  {
    unsigned a0 = pack2(f32x2{tot[0] * inv, tot[1] * inv}), a1 = pack2(f32x2{tot[2] * inv, tot[3] * inv});
    unsigned b0 = pack2(f32x2{tot[4] * inv, tot[5] * inv}), b1 = pack2(f32x2{tot[6] * inv, tot[7] * inv});
    const unsigned s0 = __shfl_xor(h ? a0 : b0, 32), s1 = __shfl_xor(h ? a1 : b1, 32);
    const u32x4 o = h ? u32x4{s0, s1, b0, b1} : u32x4{a0, a1, s0, s1};
    const __amdgpu_buffer_rsrc_t ro = __builtin_amdgcn_make_buffer_rsrc(O + token * 512, 0, 1024, 0x00020000);
    __builtin_amdgcn_raw_buffer_store_b128(o, ro, r * 32 + h * 16, 0, 16);
  }
  asm volatile("s_waitcnt vmcnt(0)\n s_barrier" ::: "memory");
}

constexpr int K1_LDS = 64 * XS_STRIDE + 65536 + 16384 + 256;

extern "C" __global__ __launch_bounds__(768) void k1_ln_qkv(const bf16* __restrict__ x, const bf16* __restrict__ wq,
                                                 const bf16* __restrict__ wk, const bf16* __restrict__ wv,
                                                 const bf16* __restrict__ wr, char* __restrict__ ws) {
  float* st = (float*)(ws + WS_ST);
  bf16* Q = (bf16*)(ws + WS_Q);
  bf16* Kb = (bf16*)(ws + WS_K);
  bf16* Vf = (bf16*)(ws + WS_V);
  unsigned* cand = (unsigned*)(ws + WS_R);
  unsigned* gcnt = (unsigned*)(ws + WS_CNT);
  extern __shared__ __attribute__((aligned(16))) char smem[];
  char* ximg = smem;
  char* wimg = smem + 64 * XS_STRIDE;
  char* kvimg = smem + 64 * XS_STRIDE + 65536;
  float* rs = (float*)(smem + 64 * XS_STRIDE + 65536 + 16384);
  const int cb = blockIdx.x >> 5, tb = blockIdx.x & 31;
  const int tid = threadIdx.x, lane = tid & 63;
  const int wave = __builtin_amdgcn_readfirstlane(tid >> 6);
  const int tok0 = tb * 64;
  if (tid == 0) ((long long*)(ws + (8u << 20)))[blockIdx.x] = wall_clock64();

  if (wave >= 8) {
    if (blockIdx.x == 255 && tid >= 768 - (NGRP + 1)) gcnt[(767 - tid) * 64] = 0;
    if (blockIdx.x == 254 && tid >= 768 - 33) ((unsigned*)(ws + WS_CL))[(767 - tid) * 64] = 0;
    stage_w(wq, cb * 64, wimg, wave - 8, 4, lane);
    if (cb < 2) {
      const bf16* w = cb == 0 ? wk : wv;
#pragma unroll
      for (int i = 0; i < 4; ++i) {
        const int p = wave - 8 + 4 * i;
        glds16((const char*)w + p * 1024 + lane * 16, kvimg + p * 1024);
      }
    }
    __syncthreads();
    if (cb == 3 && wave == 8) {
      unsigned k1 = route_key(rs[lane], tok0 + lane), k2 = 0;
      top2_wave(k1, k2, 64);
      if (lane == 0) *(u32x2*)(cand + tb * 2) = u32x2{k1, k2};
    }
    return;
  }

  const int tl = wave * 8 + (lane >> 3), c8 = lane & 7;
  u32x4 xv[8];
#pragma unroll
  for (int i = 0; i < 8; ++i) xv[i] = *(const u32x4*)(x + (tok0 + tl) * 512 + (c8 + 8 * i) * 8);
  u32x4 wrv[8];
  if (cb == 3) {
#pragma unroll
    for (int i = 0; i < 8; ++i) wrv[i] = *(const u32x4*)(wr + (c8 + 8 * i) * 8);
  }

  f32x2 xf[32];
#pragma unroll
  for (int i = 0; i < 32; ++i) xf[i] = unpack2(xv[i >> 2][i & 3]);
  f32x2 s2 = {0.f, 0.f};
#pragma unroll
  for (int i = 0; i < 32; ++i) s2 += xf[i];
  const float mean = sum8(s2.x + s2.y) * (1.f / 512.f);
  const f32x2 mean2 = {mean, mean};
  f32x2 v2 = {0.f, 0.f};
#pragma unroll
  for (int i = 0; i < 32; ++i) { const f32x2 d = xf[i] - mean2; v2 += d * d; }
  const float vs = sum8(v2.x + v2.y);
  const float mb = rbf(mean);
  const float vb = rbf(vs * (1.f / 512.f));
  const float den = rbf(sqrtf(rbf(vb + 1e-5f)));
  const float rinv = __builtin_amdgcn_rcpf(den);
  const f32x2 mb2 = {mb, mb}, rinv2 = {rinv, rinv};
  u32x4 o[8];
#pragma unroll
  for (int i = 0; i < 8; ++i) {
#pragma unroll
    for (int j = 0; j < 4; ++j) o[i][j] = ln2(xf[4 * i + j], mb2, rinv2);
    *(u32x4*)(ximg + tl * XS_STRIDE + (c8 + 8 * i) * 16) = o[i];
  }
  if (cb == 2 && c8 == 0) *(f32x2*)(st + (tok0 + tl) * 2) = f32x2{mb, rinv};
  if (cb == 3) {
    float rd = 0.f;
#pragma unroll
    for (int i = 0; i < 8; ++i) {
#pragma unroll
      for (int j = 0; j < 4; ++j) {
        const unsigned a = o[i][j], b = wrv[i][j];
        rd = __builtin_amdgcn_fdot2_f32_bf16(__builtin_bit_cast(bf16x2, a), __builtin_bit_cast(bf16x2, b), rd, false);
      }
    }
    rd = sum8(rd);
    if (c8 == 0) rs[tl] = rbf(rd);
  }
  __syncthreads();

  const int r = lane & 31, h = lane >> 5;
  if (wave < 4) {
    const int n0 = (wave & 1) * 32, t0 = (wave >> 1) * 32;
    f32x16 acc = gemm_tile(wimg, ximg, n0, t0, lane);
    const float sc = 0.25f * 1.4426950408889634f;
    const __amdgpu_buffer_rsrc_t rq = __builtin_amdgcn_make_buffer_rsrc(Q + tok0 * 512, 0, 65536, 0x00020000);
#pragma unroll
    for (int m = 0; m < 2; ++m) {
      unsigned p[4];
#pragma unroll
      for (int i = 0; i < 4; ++i) p[i] = pack2(unpack2(pack2(f32x2{acc[8 * m + 2 * i], acc[8 * m + 2 * i + 1]})) * f32x2{sc, sc});
      const unsigned s0 = __shfl_xor(h ? p[0] : p[2], 32), s1 = __shfl_xor(h ? p[1] : p[3], 32);
      const u32x4 o = h ? u32x4{s0, s1, p[2], p[3]} : u32x4{p[0], p[1], s0, s1};
      __builtin_amdgcn_raw_buffer_store_b128(o, rq, ((t0 + r) * 512 + cb * 64 + n0 + 16 * m + 8 * h) * 2, 0, 0);
    }
  } else if (wave == 7) {
    if (lane < 2) {
      const unsigned t = *(const volatile unsigned*)(lane ? ws + WS_K + tb * 2048 : (const char*)(Q + tok0 * 512 + cb * 64));
      asm volatile("" ::"v"(t));
    }
  } else if (wave == 6 && cb == 7 && tb < 8) {
    unsigned long long c0, c1;
    CODE_ADDR(k2_attn, c0);
    CODE_ADDR(k1_ln_qkv, c1);
    const long long n = (long long)(c1 - c0);
    unsigned t[4];
#pragma unroll
    for (int i = 0; i < 4; ++i) {
      const int off = (i * 64 + lane) * 128;
      t[i] = off < n ? *(const volatile unsigned*)(c0 + off) : 0u;
    }
#pragma unroll
    for (int i = 0; i < 4; ++i) asm volatile("" ::"v"(t[i]));
  } else if (wave < 6 && cb < 2) {
    const int t0 = (wave - 4) * 32;
    f32x16 acc = gemm_tile16(kvimg, ximg, t0, lane);
    const int key = tok0 + t0 + r;
    const int t = key >> 5;
    if (cb == 0) {
#pragma unroll
      for (int g = 0; g < 2; ++g) {
        bf16x4 o;
#pragma unroll
        for (int j = 0; j < 4; ++j) o[j] = (bf16)acc[4 * g + j];
        *(bf16x4*)(Kb + ((t * 2 + g) * 32 + r) * 8 + 4 * h) = o;
      }
    } else {
      const int kk = key & 31, sg = kk >> 4, rem = kk & 15;
      const int hh = (rem >> 2) & 1, jj = ((rem >> 3) << 2) | (rem & 3);
#pragma unroll
      for (int g = 0; g < 2; ++g) {
#pragma unroll
        for (int j = 0; j < 4; ++j) {
          const int d = 8 * g + 4 * h + j;
          Vf[(((t * 2 + sg) * 2 + hh) * 16 + d) * 8 + jj] = (bf16)acc[4 * g + j];
        }
      }
    }
  }
  if (tid == 0) { asm volatile("s_waitcnt vmcnt(0)" ::: "memory"); ((long long*)(ws + (8u << 20)))[256 + blockIdx.x] = wall_clock64(); }
}

extern "C" void kernel_launch(void* const* d_in, const int* in_sizes, int n_in, void* d_out, int out_size,
                              void* d_ws, size_t ws_size, hipStream_t stream) {
  const bf16* x = (const bf16*)d_in[0];
  const bf16* wq = (const bf16*)d_in[1];
  const bf16* wk = (const bf16*)d_in[2];
  const bf16* wv = (const bf16*)d_in[3];
  const bf16* wo = (const bf16*)d_in[4];
  const bf16* wr = (const bf16*)d_in[6];
  const bf16* wg = (const bf16*)d_in[7];
  const bf16* wu = (const bf16*)d_in[8];
  const bf16* wd = (const bf16*)d_in[9];
  char* ws = (char*)d_ws;
  bf16* out = (bf16*)d_out;

  (void)hipFuncSetAttribute((const void*)k1_ln_qkv, hipFuncAttributeMaxDynamicSharedMemorySize, K1_LDS);
  (void)hipFuncSetAttribute((const void*)k2_attn, hipFuncAttributeMaxDynamicSharedMemorySize, ATT_LDS);

  k1_ln_qkv<<<256, 768, K1_LDS, stream>>>(x, wq, wk, wv, wr, ws);
  k2_attn<<<256, 768, ATT_LDS, stream>>>(ws, x, wg, wu, wd, wo, out);
}
```

```cpp
#include <hip/hip_runtime.h>
#include <hip/hip_bf16.h>

typedef __attribute__((ext_vector_type(8))) __bf16 bf16x8;
typedef __attribute__((ext_vector_type(4))) __bf16 bf16x4;
typedef __attribute__((ext_vector_type(2))) __bf16 bf16x2;
typedef __attribute__((ext_vector_type(4))) short s16x4;
typedef __attribute__((ext_vector_type(8))) short s16x8;
typedef __attribute__((ext_vector_type(16))) float f32x16;
typedef __attribute__((ext_vector_type(4))) float f32x4;
typedef __attribute__((ext_vector_type(4))) unsigned u32x4;
typedef __attribute__((ext_vector_type(2))) unsigned u32x2;
typedef __attribute__((ext_vector_type(2))) float f32x2;
typedef __bf16 bf16;

#define LDS_AS __attribute__((address_space(3)))
#define GLB_AS __attribute__((address_space(1)))
#define DI __device__ __forceinline__

constexpr int S_ = 2048, D_ = 512, H_ = 32, DH_ = 16, F_ = 1376;

constexpr size_t WS_Q = 0;
constexpr size_t WS_O = WS_Q;
constexpr size_t WS_K = 2u << 20;
constexpr size_t WS_V = WS_K + 65536;
constexpr size_t WS_ST = WS_V + 65536;
constexpr size_t WS_R = WS_ST + 16384;
constexpr size_t WS_CNT = WS_R + 4096;
constexpr size_t WS_UPD = WS_CNT + 4096;
constexpr size_t WS_YP = WS_UPD + 4096;

constexpr int NFFN = 172;
constexpr int GSZ = 12, NGRP = 15;
constexpr size_t WS_CL = WS_YP + (NFFN + NGRP) * 4096;

DI float bf2f(bf16 v) { return (float)v; }
DI float rbf(float v) { return (float)(bf16)v; }
DI f32x2 unpack2(unsigned u) { return f32x2{__uint_as_float(u << 16), __uint_as_float(u & 0xffff0000u)}; }
DI unsigned pack2(f32x2 v) {
  unsigned r = __builtin_bit_cast(unsigned, __builtin_convertvector(v, bf16x2));
  asm("" : "+v"(r));
  return r;
}

DI unsigned route_key(float v, int tok) {
  const unsigned u = __float_as_uint(v) >> 16;
  const unsigned o = (u & 0x8000u) ? (~u & 0xffffu) : (u | 0x8000u);
  return (o << 16) | (unsigned)(0xffff - tok);
}
DI int key_tok(unsigned k) { return 0xffff - (int)(k & 0xffffu); }
DI float key_val(unsigned k) {
  const unsigned o = k >> 16;
  const unsigned u = (o & 0x8000u) ? (o & 0x7fffu) : (~o & 0xffffu);
  return __uint_as_float(u << 16);
}
DI unsigned ln2(f32x2 x, f32x2 mb2, f32x2 rinv2) { return pack2(unpack2(pack2(x - mb2)) * rinv2); }
DI void top2_wave(unsigned& k1, unsigned& k2, int width) {
  for (int o = width >> 1; o > 0; o >>= 1) {
    const unsigned a1 = __shfl_xor(k1, o), a2 = __shfl_xor(k2, o);
    k2 = max(min(k1, a1), max(k2, a2));
    k1 = max(k1, a1);
  }
}

template <int O>
DI void top2_step(unsigned& k1, unsigned& k2) {
  const unsigned a1 = __builtin_amdgcn_ds_swizzle(k1, (O << 10) | 31), a2 = __builtin_amdgcn_ds_swizzle(k2, (O << 10) | 31);
  k2 = max(min(k1, a1), max(k2, a2));
  k1 = max(k1, a1);
}

DI void glds16(const void* g, void* l) {
  __builtin_amdgcn_global_load_lds((const GLB_AS void*)g, (LDS_AS void*)l, 16, 0, 0);
}

DI void glds16_raw(const void* g, unsigned lds_off) {
  asm volatile("s_mov_b32 m0, %0\n s_nop 0\n global_load_lds_dwordx4 %1, off" ::"s"(lds_off), "v"(g) : "memory", "m0");
}

template <int CTRL>
DI float dpp_add(float v) {
  const int t = __builtin_amdgcn_update_dpp(0, __float_as_int(v), CTRL, 0xf, 0xf, true);
  return v + __int_as_float(t);
}
DI float sum8(float v) {
  v = dpp_add<0xB1>(v);
  v = dpp_add<0x4E>(v);
  v = dpp_add<0x141>(v);
  return v;
}

DI s16x4 tr_read(const char* p) {
  return __builtin_amdgcn_ds_read_tr16_b64_v4i16((LDS_AS s16x4*)p);
}

DI int wimg_off(int row, int ch) { return row * 128 + ((ch ^ (((row >> 1) & 1) << 2)) << 4); }

constexpr int XS_STRIDE = 1040;

DI void stage_w(const bf16* w, int col0, char* wimg, int wave, int nwaves, int lane) {
  for (int p = wave; p < 64; p += nwaves) {
    const int row = p * 8 + (lane >> 3), pc = lane & 7;
    const int ch = pc ^ (((row >> 1) & 1) << 2);
    glds16(w + row * 512 + col0 + ch * 8, wimg + p * 1024);
  }
}

constexpr int GP = 4;
DI f32x16 gemm_tile(const char* wimg, const char* ximg, int n0, int t0, int lane) {
  const int r = lane & 31, h = lane >> 5;
  const int q = (lane & 15) >> 2, p = lane & 3, blk = (lane >> 4) & 1;
  f32x16 acc;
#pragma unroll
  for (int i = 0; i < 16; ++i) acc[i] = 0.f;
  const char* xb = ximg + (t0 + r) * XS_STRIDE + 16 * h;
  const int col = n0 + 16 * blk + 4 * p;
  const int ch = col >> 3, sub = (col & 7) * 2;
  const char* wb0 = wimg + wimg_off(8 * h + q, ch) + sub;
  const char* wb1 = wimg + wimg_off(8 * h + q + 4, ch) + sub;
  s16x4 lo[32], hi[32];
  bf16x8 b[32];
#pragma unroll
  for (int ks = 0; ks < 32 + GP; ++ks) {
    if (ks < 32) {
      lo[ks] = tr_read(wb0 + ks * 2048);
      hi[ks] = tr_read(wb1 + ks * 2048);
      b[ks] = *(const bf16x8*)(xb + ks * 32);
    }
    if (ks >= GP) {
      const s16x8 a = __builtin_shufflevector(lo[ks - GP], hi[ks - GP], 0, 1, 2, 3, 4, 5, 6, 7);
      acc = __builtin_amdgcn_mfma_f32_32x32x16_bf16(__builtin_bit_cast(bf16x8, a), b[ks - GP], acc, 0, 0, 0);
      asm volatile("" : "+v"(acc));
    }
    __builtin_amdgcn_sched_barrier(0);
  }
  return acc;
}
DI f32x16 gemm_tile16(const char* wimg, const char* ximg, int t0, int lane) {
  const int r = lane & 31, h = lane >> 5;
  const int q = (lane & 15) >> 2, p = lane & 3;
  f32x16 acc;
#pragma unroll
  for (int i = 0; i < 16; ++i) acc[i] = 0.f;
  const char* xb = ximg + (t0 + r) * XS_STRIDE + 16 * h;
  const char* wb = wimg + (8 * h + q) * 32 + 8 * p;
  s16x4 lo[32], hi[32];
  bf16x8 b[32];
#pragma unroll
  for (int ks = 0; ks < 32 + GP; ++ks) {
    if (ks < 32) {
      lo[ks] = tr_read(wb + ks * 512);
      hi[ks] = tr_read(wb + ks * 512 + 128);
      b[ks] = *(const bf16x8*)(xb + ks * 32);
    }
    if (ks >= GP) {
      const s16x8 a = __builtin_shufflevector(lo[ks - GP], hi[ks - GP], 0, 1, 2, 3, 4, 5, 6, 7);
      acc = __builtin_amdgcn_mfma_f32_32x32x16_bf16(__builtin_bit_cast(bf16x8, a), b[ks - GP], acc, 0, 0, 0);
      asm volatile("" : "+v"(acc));
    }
    __builtin_amdgcn_sched_barrier(0);
  }
  return acc;
}

#define CODE_ADDR(sym, out)                                                                               \
  asm volatile("s_getpc_b64 s[20:21]\n s_add_u32 s20, s20, " #sym "@rel32@lo+4\n s_addc_u32 s21, s21, " #sym \
               "@rel32@hi+12\n s_mov_b64 %0, s[20:21]"                                                    \
               : "=s"(out)::"s20", "s21", "scc")

constexpr int NT = 16;
constexpr int CST = NT * 1024;
constexpr int ATT_LDS = 65536 * 2 + CST + 64 + 512 + 4 * 640;
constexpr int NC = 64 / NT;
constexpr int SPIN_MAX = 1 << 22;

DI bool last_arrival(int* w, unsigned* gc, int n, int lane) {
  asm volatile("s_waitcnt vmcnt(0)" ::: "memory");
  if (lane == 0) {
    if (__hip_atomic_fetch_add(w, 1, __ATOMIC_RELAXED, __HIP_MEMORY_SCOPE_WORKGROUP) == 3) {
      const unsigned t = __hip_atomic_fetch_add((GLB_AS unsigned*)gc, 1u, __ATOMIC_RELAXED, __HIP_MEMORY_SCOPE_AGENT);
      __hip_atomic_store(w + 1, t == (unsigned)(n - 1) ? 2 : 1, __ATOMIC_RELAXED, __HIP_MEMORY_SCOPE_WORKGROUP);
    }
  }
  int r;
  while ((r = __hip_atomic_load(w + 1, __ATOMIC_RELAXED, __HIP_MEMORY_SCOPE_WORKGROUP)) == 0) __builtin_amdgcn_s_sleep(2);
  asm volatile("" ::: "memory");
  return r == 2;
}

DI void ffn_slice(int f, int j, const bf16* __restrict__ x, const float* __restrict__ st,
                  const unsigned* __restrict__ cand, unsigned* __restrict__ fdone,
                  const bf16* __restrict__ wg, const bf16* __restrict__ wu, const bf16* __restrict__ wd,
                  float* __restrict__ ypart, unsigned* __restrict__ gcnt,
                  float* __restrict__ upd, float* red, int* cnt, char* scr, int lane) {
  const int k0 = j * 128 + lane * 2;
  const int r = lane & 31, h = lane >> 5;
  const int kr = (lane & 51) | ((lane & 4) << 1) | ((lane & 8) >> 1);
  u32x4 wf[4];
  unsigned d2[8];
  {
    const __amdgpu_buffer_rsrc_t rg = __builtin_amdgcn_make_buffer_rsrc((void*)wg, 0, 512 * F_ * 2, 0x00020000);
    const __amdgpu_buffer_rsrc_t ru = __builtin_amdgcn_make_buffer_rsrc((void*)wu, 0, 512 * F_ * 2, 0x00020000);
    const __amdgpu_buffer_rsrc_t rd = __builtin_amdgcn_make_buffer_rsrc((void*)wd, 0, 512 * F_ * 2, 0x00020000);
    const int wo_ = kr * (F_ * 2), so = (j * 128 * F_ + f * 8) * 2;
#pragma unroll
    for (int t = 0; t < 2; ++t) {
      wf[2 * t] = __builtin_amdgcn_raw_buffer_load_b128(rg, wo_, so + t * (64 * F_ * 2), 0);
      wf[2 * t + 1] = __builtin_amdgcn_raw_buffer_load_b128(ru, wo_, so + t * (64 * F_ * 2), 0);
    }
#pragma unroll
    for (int c = 0; c < 8; ++c)
      d2[c] = __builtin_amdgcn_raw_buffer_load_b32(rd, 8 * r + 4 * h + c * 1024, (f * 8 * 512 + j * 128) * 2, 0);
  }
  const u32x2 cv = *(const u32x2*)(cand + (lane & 31) * 2);
  unsigned k1 = cv[0], k2 = cv[1];
  top2_step<16>(k1, k2);
  top2_step<8>(k1, k2);
  top2_step<4>(k1, k2);
  top2_step<2>(k1, k2);
  top2_step<1>(k1, k2);
  k1 = __builtin_amdgcn_readfirstlane(k1);
  k2 = __builtin_amdgcn_readfirstlane(k2);
  const int i1 = key_tok(k1), i2 = key_tok(k2);
  const f32x2 sa = *(const f32x2*)(st + i1 * 2), sb = *(const f32x2*)(st + i2 * 2);
  *(unsigned*)(scr + lane * 4) = ln2(unpack2(*(const unsigned*)(x + i1 * 512 + k0)), f32x2{sa.x, sa.x}, f32x2{sa.y, sa.y});
  *(unsigned*)(scr + 256 + lane * 4) = ln2(unpack2(*(const unsigned*)(x + i2 * 512 + k0)), f32x2{sb.x, sb.x}, f32x2{sb.y, sb.y});
  f32x16 zero;
#pragma unroll
  for (int i = 0; i < 16; ++i) zero[i] = 0.f;
  u32x4 ida, idb;
  {
    const int d = r - 8 * h;
    const unsigned one = 0x3f80u << ((d & 1) << 4);
#pragma unroll
    for (int q = 0; q < 4; ++q) {
      ida[q] = (d >> 1) == q ? one : 0u;
      idb[q] = (d >> 1) == q + 8 ? one : 0u;
    }
  }
  f32x16 gu = zero;
#pragma unroll
  for (int t = 0; t < 2; ++t) {
    f32x16 wt = __builtin_amdgcn_mfma_f32_32x32x16_bf16(__builtin_bit_cast(bf16x8, wf[2 * t]),
                                                        __builtin_bit_cast(bf16x8, ida), zero, 0, 0, 0);
    wt = __builtin_amdgcn_mfma_f32_32x32x16_bf16(__builtin_bit_cast(bf16x8, wf[2 * t + 1]),
                                                 __builtin_bit_cast(bf16x8, idb), wt, 0, 0, 0);
#pragma unroll
    for (int q = 0; q < 2; ++q) {
      u32x4 bw;
#pragma unroll
      for (int i = 0; i < 4; ++i) bw[i] = pack2(f32x2{wt[8 * q + 2 * i], wt[8 * q + 2 * i + 1]});
      const bf16x8 xf = *(const bf16x8*)(scr + (r & 1) * 256 + (r & 2) * 32 + (64 * t + 16 * q + 8 * h) * 2);
      gu = __builtin_amdgcn_mfma_f32_32x32x16_bf16(xf, __builtin_bit_cast(bf16x8, bw), gu, 0, 0, 0);
    }
  }
  {
    const float g0 = dpp_add<0x128>((lane & 8) ? gu[2] : gu[0]), g1 = dpp_add<0x128>((lane & 8) ? gu[3] : gu[1]);
    if (lane < 32) red[j * 32 + (lane & 16) + (lane & 15)] = (lane & 8) ? g1 : g0;
  }
  if (lane == 0) __hip_atomic_fetch_add(cnt, 1, __ATOMIC_RELEASE, __HIP_MEMORY_SCOPE_WORKGROUP);
  while (__hip_atomic_load(cnt, __ATOMIC_ACQUIRE, __HIP_MEMORY_SCOPE_WORKGROUP) < 4) __builtin_amdgcn_s_sleep(1);
  const int l = lane & 15;
  const float g = rbf((red[l] + red[32 + l]) + (red[64 + l] + red[96 + l]));
  const float u = rbf((red[16 + l] + red[48 + l]) + (red[80 + l] + red[112 + l]));
  const float hv = rbf(g * __builtin_amdgcn_rcpf(1.f + __expf(-g))) * u;
  if (lane < 24) *(bf16*)(scr + 512 + lane * 2) = (bf16)(lane < 16 ? hv : 0.f);
  const bf16x8 hf = *(const bf16x8*)(scr + 512 + ((r >> 1) == h ? (r & 1) * 16 : 32));
  u32x4 da, db;
#pragma unroll
  for (int c = 0; c < 4; ++c) {
    da[c] = __builtin_amdgcn_perm(d2[2 * c + 1], d2[2 * c], 0x05040100u);
    db[c] = __builtin_amdgcn_perm(d2[2 * c + 1], d2[2 * c], 0x07060302u);
  }
  const f32x16 ya = __builtin_amdgcn_mfma_f32_32x32x16_bf16(hf, __builtin_bit_cast(bf16x8, da), zero, 0, 0, 0);
  const f32x16 yb = __builtin_amdgcn_mfma_f32_32x32x16_bf16(hf, __builtin_bit_cast(bf16x8, db), zero, 0, 0, 0);
  const __amdgpu_buffer_rsrc_t rs = __builtin_amdgcn_make_buffer_rsrc(ypart, 0, (NFFN + NGRP) * 4096, 0x00020000);
  if (lane < 32) {
#pragma unroll
    for (int t = 0; t < 2; ++t)
      __builtin_amdgcn_raw_buffer_store_b128(__builtin_bit_cast(u32x4, f32x4{ya[t], yb[t], ya[2 + t], yb[2 + t]}), rs,
                                             t * 2048 + j * 512 + lane * 16, f * 4096, 16);
  }
  const int grp = f / GSZ, gs = min(GSZ, NFFN - grp * GSZ);
  if (!last_arrival(cnt + 1, gcnt + grp * 64, gs, lane)) return;
  const int voff = j * 1024 + lane * 16;
  f32x4 a = {0.f, 0.f, 0.f, 0.f};
  {
    u32x4 t[GSZ];
#pragma unroll
    for (int i = 0; i < GSZ; ++i) t[i] = __builtin_amdgcn_raw_buffer_load_b128(rs, voff, (grp * GSZ + min(i, gs - 1)) * 4096, 16);
#pragma unroll
    for (int i = 0; i < GSZ; ++i)
      if (i < gs) a += __builtin_bit_cast(f32x4, t[i]);
  }
  __builtin_amdgcn_raw_buffer_store_b128(__builtin_bit_cast(u32x4, a), rs, voff, (NFFN + grp) * 4096, 16);
  if (!last_arrival(cnt + 3, gcnt + NGRP * 64, NGRP, lane)) return;
  a = f32x4{0.f, 0.f, 0.f, 0.f};
  {
    u32x4 t[NGRP];
#pragma unroll
    for (int i = 0; i < NGRP; ++i) t[i] = __builtin_amdgcn_raw_buffer_load_b128(rs, voff, (NFFN + i) * 4096, 16);
#pragma unroll
    for (int i = 0; i < NGRP; ++i) a += __builtin_bit_cast(f32x4, t[i]);
  }
  const float gate = rbf(1.f / (1.f + __expf(-key_val(j < 2 ? k1 : k2))));
#pragma unroll
  for (int i = 0; i < 4; ++i) a[i] = rbf(rbf(a[i]) * gate);
  {
    const __amdgpu_buffer_rsrc_t ru = __builtin_amdgcn_make_buffer_rsrc(upd, 0, 4096, 0x00020000);
    __builtin_amdgcn_raw_buffer_store_b128(__builtin_bit_cast(u32x4, a), ru, voff, 0, 16);
  }
  asm volatile("s_waitcnt vmcnt(0)" ::: "memory");
  if (lane == 0 && __hip_atomic_fetch_add(cnt + 5, 1, __ATOMIC_RELAXED, __HIP_MEMORY_SCOPE_WORKGROUP) == 3)
    __hip_atomic_store((GLB_AS unsigned*)fdone, 1u, __ATOMIC_RELAXED, __HIP_MEMORY_SCOPE_AGENT);
}

extern "C" __global__ __launch_bounds__(768) void k2_attn(char* __restrict__ ws, const bf16* __restrict__ x,
                                               const bf16* __restrict__ wg, const bf16* __restrict__ wu,
                                               const bf16* __restrict__ wd, const bf16* __restrict__ wo,
                                               bf16* __restrict__ out) {
  const bf16* Q = (const bf16*)(ws + WS_Q);
  const bf16* Kb = (const bf16*)(ws + WS_K);
  bf16* O = (bf16*)(ws + WS_O);
  const float* st = (const float*)(ws + WS_ST);
  const unsigned* cand = (const unsigned*)(ws + WS_R);
  float* ypart = (float*)(ws + WS_YP);
  unsigned* gcnt = (unsigned*)(ws + WS_CNT);
  float* upd = (float*)(ws + WS_UPD);
  extern __shared__ __attribute__((aligned(16))) char smem[];
  char* kl = smem;
  char* vl = smem + 65536;
  char* cst = smem + 131072;
  const int tid = threadIdx.x, lane = tid & 63;
  const int wave = __builtin_amdgcn_readfirstlane(tid >> 6);

  if (wave >= 8) {
    const unsigned T1 = (unsigned)wall_clock64();
    int* cnt = (int*)(smem + 131072 + CST + 32);
    if (tid < 512 + 6) cnt[tid - 512] = 0;
#pragma unroll
    for (int i = 0; i < 26; ++i) {
      const int q = (wave - 8) + 4 * i, piece = q < 48 ? 16 + q : 24 + q;
      glds16_raw((const char*)Kb + piece * 1024 + lane * 16, (unsigned)(size_t)(LDS_AS char*)smem + piece * 1024);
    }
    asm volatile("s_waitcnt lgkmcnt(0)\n s_barrier\n s_waitcnt vmcnt(0)\n s_barrier" ::: "memory");
    unsigned* clc = (unsigned*)(ws + WS_CL);
    __builtin_amdgcn_s_setprio(3);
    const int fs = (blockIdx.x & 7) * 22 + (blockIdx.x >> 3);
    if (blockIdx.x < 176 && fs < NFFN) {
      ffn_slice(fs, wave - 8, x, st, cand, clc + 32 * 64, wg, wu, wd, ypart, gcnt, upd,
                (float*)(smem + 131072 + CST + 64), cnt, smem + 131072 + CST + 576 + (wave - 8) * 640, lane);
    }
    const int tb = (blockIdx.x >> 6) * 8 + (blockIdx.x & 7), cb = (blockIdx.x >> 3) & 7, tok0 = tb * 64;
    const int sw = wave - 8, r = lane & 31, h = lane >> 5;
    char* ximg = smem;
    char* wimg = smem + 64 * XS_STRIDE;
    u32x4 wreg[16];
#pragma unroll
    for (int i = 0; i < 16; ++i) {
      const int row = (sw + 4 * i) * 8 + (lane >> 3);
      wreg[i] = *(const u32x4*)(wo + row * 512 + cb * 64 + (((lane & 7) ^ (((row >> 1) & 1) << 2)) << 3));
    }
    const int n0 = (sw & 1) * 32, t0 = (sw >> 1) * 32;
    const int tok = tok0 + t0 + r;
    u32x2 xv[4], nv[4];
#pragma unroll
    for (int g = 0; g < 4; ++g) xv[g] = *(const u32x2*)(x + tok * 512 + cb * 64 + n0 + 8 * g + 4 * h);
    const f32x2 sx = *(const f32x2*)(st + tok * 2);
    int rsel;
    {
      const u32x2 cv = *(const u32x2*)(cand + (lane & 31) * 2);
      unsigned k1 = cv[0], k2 = cv[1];
      top2_step<16>(k1, k2);
      top2_step<8>(k1, k2);
      top2_step<4>(k1, k2);
      top2_step<2>(k1, k2);
      top2_step<1>(k1, k2);
      rsel = tok == key_tok(k1) ? 0 : tok == key_tok(k2) ? 1 : -1;
    }
#pragma unroll
    for (int g = 0; g < 4; ++g) {
#pragma unroll
      for (int jj = 0; jj < 2; ++jj) {
        unsigned nr = ln2(unpack2(xv[g][jj]), f32x2{sx.x, sx.x}, f32x2{sx.y, sx.y});
        asm volatile("" : "+v"(nr));
        nv[g][jj] = nr;
      }
    }
    if (wave == 11 && lane == 0) {
      const unsigned t = *(const volatile unsigned*)(out + tok0 * 512 + cb * 64);
      asm volatile("" ::"v"(t));
    }
    unsigned smin, smax, emax, s0;
    {
      const u32x4 a = ((const u32x4*)(ws + (8u << 20)))[lane], e = ((const u32x4*)(ws + (8u << 20)))[64 + lane];
      smin = min(min(a[0], a[1]), min(a[2], a[3])); smax = max(max(a[0], a[1]), max(a[2], a[3])); emax = max(max(e[0], e[1]), max(e[2], e[3]));
      s0 = __builtin_amdgcn_readfirstlane(a[0]);
      for (int o = 32; o > 0; o >>= 1) { smin = min(smin, (unsigned)__shfl_xor(smin, o)); smax = max(smax, (unsigned)__shfl_xor(smax, o)); emax = max(emax, (unsigned)__shfl_xor(emax, o)); }
      asm volatile("" : "+v"(smin), "+v"(smax), "+v"(emax));
    }
#pragma unroll
    for (int i = 0; i < 16; ++i) asm volatile("" : "+v"(wreg[i]));
    asm volatile("s_barrier" ::: "memory");
    if (tid == 512) __hip_atomic_fetch_add((GLB_AS unsigned*)(clc + tb * 64), 1u, __ATOMIC_RELAXED, __HIP_MEMORY_SCOPE_AGENT);
#pragma unroll
    for (int i = 0; i < 16; ++i) *(u32x4*)(wimg + (sw + 4 * i) * 1024 + lane * 16) = wreg[i];
    if (__any(rsel >= 0)) {
      int spin = 0;
      while (__hip_atomic_load((GLB_AS unsigned*)(clc + 32 * 64), __ATOMIC_RELAXED, __HIP_MEMORY_SCOPE_AGENT) == 0 && ++spin < SPIN_MAX)
        __builtin_amdgcn_s_sleep(1);
      asm volatile("" ::: "memory");
      const __amdgpu_buffer_rsrc_t ru = __builtin_amdgcn_make_buffer_rsrc(upd, 0, 4096, 0x00020000);
      if (rsel >= 0) {
#pragma unroll
        for (int g = 0; g < 4; ++g) {
          const f32x4 u = __builtin_bit_cast(f32x4, __builtin_amdgcn_raw_buffer_load_b128(ru, (rsel * 512 + cb * 64 + n0 + 8 * g + 4 * h) * 4, 0, 16));
          nv[g][0] = pack2(unpack2(nv[g][0]) + f32x2{u[0], u[1]});
          nv[g][1] = pack2(unpack2(nv[g][1]) + f32x2{u[2], u[3]});
        }
      }
    }
    if (wave == 8) {
      int spin = 0;
      while (__hip_atomic_load((GLB_AS unsigned*)(clc + tb * 64), __ATOMIC_RELAXED, __HIP_MEMORY_SCOPE_AGENT) < 8 && ++spin < SPIN_MAX)
        __builtin_amdgcn_s_sleep(1);
    }
    asm volatile("s_barrier" ::: "memory");
    {
      const __amdgpu_buffer_rsrc_t ro = __builtin_amdgcn_make_buffer_rsrc(O + tok0 * 512, 0, 65536, 0x00020000);
      u32x4 t[16];
#pragma unroll
      for (int i = 0; i < 16; ++i) t[i] = __builtin_amdgcn_raw_buffer_load_b128(ro, lane * 16, (sw * 16 + i) * 1024, 16);
#pragma unroll
      for (int i = 0; i < 16; ++i) *(u32x4*)(ximg + (sw * 16 + i) * XS_STRIDE + lane * 16) = t[i];
    }
    asm volatile("s_waitcnt lgkmcnt(0)\n s_barrier" ::: "memory");
    f32x16 acc = gemm_tile(wimg, ximg, n0, t0, lane);
#pragma unroll
    for (int g = 0; g < 4; ++g) {
      u32x2 o;
#pragma unroll
      for (int jj = 0; jj < 2; ++jj) {
        const unsigned xr = xv[g][jj], nr = nv[g][jj];
        const f32x2 a = unpack2(pack2(f32x2{acc[4 * g + 2 * jj], acc[4 * g + 2 * jj + 1]}));
        o[jj] = pack2(unpack2(pack2(unpack2(xr) + a)) + unpack2(nr));
      }
      *(u32x2*)(out + tok * 512 + cb * 64 + n0 + 8 * g + 4 * h) = o;
    }
    if (blockIdx.x == 0) {
      asm volatile("s_waitcnt vmcnt(0)" ::: "memory");
      int d = (int)(0);
      d = d < 0 ? 0 : d > 10000 ? 10000 : d;
      const long long T3 = wall_clock64();
      while (wall_clock64() - T3 < d) __builtin_amdgcn_s_sleep(2);
    }
    return;
  }

  const int r = lane & 31, h = lane >> 5;
  const int token = ((blockIdx.x >> 6) * 8 + (blockIdx.x & 7)) * 64 + ((blockIdx.x >> 3) & 7) * 8 + wave;
  bf16x8 qf;
  asm volatile("global_load_dwordx4 %0, %1, off" : "=v"(qf) : "v"(Q + token * 512 + r * 16 + 8 * h) : "memory");
  const unsigned lds0 = (unsigned)(size_t)(LDS_AS char*)smem;
#pragma unroll
  for (int i = 0; i < 3; ++i) {
    const int piece = (i < 2) ? (wave + i * 8) : (64 + wave);
    glds16_raw((const char*)Kb + piece * 1024 + lane * 16, lds0 + piece * 1024);
  }
  {
    const unsigned cv = ((tid & 15) == 0 || (tid & 15) == 4) ? 0x3f803f80u : 0u;
#pragma unroll
    for (int i = 0; i < CST / 8192; ++i) *(u32x4*)(cst + i * 8192 + tid * 16) = u32x4{cv, cv, cv, cv};
  }
  asm volatile("s_waitcnt vmcnt(0) lgkmcnt(0)\n s_barrier" ::: "memory");

  const char* kp0 = kl + lane * 16;
  const char* vp0 = (r < 16) ? (vl + (h * 16 + r) * 16) : (cst + (h * 16 + r - 16) * 16);
  const int vstep = (r < 16) ? NT * 1024 : 0;

  f32x16 zero;
#pragma unroll
  for (int i = 0; i < 16; ++i) zero[i] = 0.f;
  constexpr float HEAD = 40.f;
  float m;
  {
    float mx = -INFINITY;
#pragma unroll
    for (int t = 0; t < 4; ++t) {
      bf16x8 kf = *(const bf16x8*)(kp0 + t * 1024);
      f32x16 x = __builtin_amdgcn_mfma_f32_32x32x16_bf16(kf, qf, zero, 0, 0, 0);
#pragma unroll
      for (int i = 0; i < 16; i += 2) mx = fmaxf(fmaxf(mx, x[i]), x[i + 1]);
    }
    mx = fmaxf(mx, __shfl_xor(mx, 32));
    m = floorf(mx) + HEAD;
  }
  f32x16 negm;
#pragma unroll
  for (int i = 0; i < 16; ++i) negm[i] = -m;
  asm volatile("" : "+v"(negm));
  float tot[9];
#pragma unroll
  for (int i = 0; i < 9; ++i) tot[i] = 0.f;
  {
    const char* kp = kp0;
    const char* vp = vp0;
    int c = 0;
    bool redone = false, first = true;
    if (wave < 4) __builtin_amdgcn_s_setprio(2);
    else __builtin_amdgcn_s_setprio(1);
    f32x16 x = __builtin_amdgcn_mfma_f32_32x32x16_bf16(*(const bf16x8*)kp, qf, negm, 0, 0, 0);
    bf16x8 kfn = *(const bf16x8*)(kp + 1024);
    while (true) {
      f32x16 acc;
      while (true) {
        acc = zero;
#pragma unroll
        for (int t = 0; t < NT; ++t) {
          const bf16x8 kf = kfn;
          kfn = *(const bf16x8*)(kp + (t + 2) * 1024);
          const bf16x8 vf0 = *(const bf16x8*)(vp + (t * 2) * 512);
          const bf16x8 vf1 = *(const bf16x8*)(vp + (t * 2 + 1) * 512);
          bf16x8 pf;
#pragma unroll
          for (int j = 0; j < 4; ++j) pf[j] = (bf16)__builtin_amdgcn_exp2f(x[j]);
          __builtin_amdgcn_sched_barrier(0);
          f32x16 xn = __builtin_amdgcn_mfma_f32_32x32x16_bf16(kf, qf, negm, 0, 0, 0);
          asm volatile("" : "+v"(xn));
          __builtin_amdgcn_sched_barrier(0);
#pragma unroll
          for (int j = 4; j < 8; ++j) pf[j] = (bf16)__builtin_amdgcn_exp2f(x[j]);
          acc = __builtin_amdgcn_mfma_f32_32x32x16_bf16(vf0, pf, acc, 0, 0, 0);
#pragma unroll
          for (int j = 0; j < 8; ++j) pf[j] = (bf16)__builtin_amdgcn_exp2f(x[8 + j]);
          acc = __builtin_amdgcn_mfma_f32_32x32x16_bf16(vf1, pf, acc, 0, 0, 0);
          if (t == 7 && first) {
            asm volatile("s_barrier" ::: "memory");
            first = false;
          }
          if (c * NT + t == 53 && wave < 4) __builtin_amdgcn_s_setprio(0);
          __builtin_amdgcn_sched_barrier(0);
          x = xn;
        }
        if (__any(!(tot[8] + acc[8] <= 4.3e9f))) break;
        redone = false;
#pragma unroll
        for (int i = 0; i < 9; ++i) tot[i] += acc[i];
        ++c;
        kp += NT * 1024;
        vp += vstep;
        if (c == NC) break;
      }
      if (c == NC) break;
      if (!redone && __any(!(acc[8] < 7.9e28f))) {
        float mx = -INFINITY;
        for (int t = 0; t < NT; ++t) {
          bf16x8 kf = *(const bf16x8*)(kp + t * 1024);
          f32x16 x = __builtin_amdgcn_mfma_f32_32x32x16_bf16(kf, qf, zero, 0, 0, 0);
#pragma unroll
          for (int i = 0; i < 16; i += 2) mx = fmaxf(fmaxf(mx, x[i]), x[i + 1]);
        }
        mx = fmaxf(mx, __shfl_xor(mx, 32));
        const float mn = fmaxf(m, floorf(mx) + HEAD);
        const int d = (int)fminf(mn - m, 1024.f);
#pragma unroll
        for (int i = 0; i < 9; ++i) tot[i] = __builtin_amdgcn_ldexpf(tot[i], -d);
        m = mn;
        redone = true;
      } else {
        redone = false;
#pragma unroll
        for (int i = 0; i < 9; ++i) tot[i] += acc[i];
        const int e = max(__builtin_amdgcn_frexp_expf(tot[8]) - 1 + (int)HEAD, 0);
#pragma unroll
        for (int i = 0; i < 9; ++i) tot[i] = __builtin_amdgcn_ldexpf(tot[i], -e);
        m += (float)e;
        ++c;
        kp += NT * 1024;
        vp += vstep;
        if (c == NC) break;
      }
#pragma unroll
      for (int i = 0; i < 16; ++i) negm[i] = -m;
      asm volatile("" : "+v"(negm));
      x = __builtin_amdgcn_mfma_f32_32x32x16_bf16(*(const bf16x8*)kp, qf, negm, 0, 0, 0);
      kfn = *(const bf16x8*)(kp + 1024);
    }
  }
  {
    float chk = 0.f;
#pragma unroll
    for (int i = 0; i < 9; ++i) chk = __builtin_fmaf(tot[i], 0.f, chk);
    if (__any(chk != 0.f)) {
      f32x16 oacc = zero;
      float mrun = -INFINITY;
      const char* kp = kp0;
      const char* vp = vp0;
      for (int t = 0; t < 64; ++t) {
        bf16x8 kf = *(const bf16x8*)(kp);
        f32x16 x = __builtin_amdgcn_mfma_f32_32x32x16_bf16(kf, qf, zero, 0, 0, 0);
        float mx = x[0];
#pragma unroll
        for (int i = 1; i < 16; ++i) mx = fmaxf(mx, x[i]);
        mx = fmaxf(mx, __shfl_xor(mx, 32));
        const float mn = fmaxf(mrun, mx);
        const float al = __builtin_amdgcn_exp2f(mrun - mn);
        mrun = mn;
#pragma unroll
        for (int i = 0; i < 16; ++i) oacc[i] *= al;
#pragma unroll
        for (int s = 0; s < 2; ++s) {
          bf16x8 pf;
#pragma unroll
          for (int j = 0; j < 8; ++j) pf[j] = (bf16)__builtin_amdgcn_exp2f(x[8 * s + j] - mn);
          bf16x8 vf = *(const bf16x8*)(vp + ((t & (NT - 1)) * 2 + s) * 512);
          oacc = __builtin_amdgcn_mfma_f32_32x32x16_bf16(vf, pf, oacc, 0, 0, 0);
        }
        kp += 1024;
        if ((t & (NT - 1)) == NT - 1) vp += vstep;
      }
#pragma unroll
      for (int i = 0; i < 9; ++i) tot[i] = oacc[i];
    }
  }
  const float inv = 1.f / tot[8];
  {
    unsigned a0 = pack2(f32x2{tot[0] * inv, tot[1] * inv}), a1 = pack2(f32x2{tot[2] * inv, tot[3] * inv});
    unsigned b0 = pack2(f32x2{tot[4] * inv, tot[5] * inv}), b1 = pack2(f32x2{tot[6] * inv, tot[7] * inv});
    const unsigned s0 = __shfl_xor(h ? a0 : b0, 32), s1 = __shfl_xor(h ? a1 : b1, 32);
    const u32x4 o = h ? u32x4{s0, s1, b0, b1} : u32x4{a0, a1, s0, s1};
    const __amdgpu_buffer_rsrc_t ro = __builtin_amdgcn_make_buffer_rsrc(O + token * 512, 0, 1024, 0x00020000);
    __builtin_amdgcn_raw_buffer_store_b128(o, ro, r * 32 + h * 16, 0, 16);
  }
  asm volatile("s_waitcnt vmcnt(0)\n s_barrier" ::: "memory");
}

constexpr int K1_LDS = 64 * XS_STRIDE + 65536 + 16384 + 256;

extern "C" __global__ __launch_bounds__(768) void k1_ln_qkv(const bf16* __restrict__ x, const bf16* __restrict__ wq,
                                                 const bf16* __restrict__ wk, const bf16* __restrict__ wv,
                                                 const bf16* __restrict__ wr, char* __restrict__ ws) {
  float* st = (float*)(ws + WS_ST);
  bf16* Q = (bf16*)(ws + WS_Q);
  bf16* Kb = (bf16*)(ws + WS_K);
  bf16* Vf = (bf16*)(ws + WS_V);
  unsigned* cand = (unsigned*)(ws + WS_R);
  unsigned* gcnt = (unsigned*)(ws + WS_CNT);
  extern __shared__ __attribute__((aligned(16))) char smem[];
  char* ximg = smem;
  char* wimg = smem + 64 * XS_STRIDE;
  char* kvimg = smem + 64 * XS_STRIDE + 65536;
  float* rs = (float*)(smem + 64 * XS_STRIDE + 65536 + 16384);
  const int cb = blockIdx.x >> 5, tb = blockIdx.x & 31;
  const int tid = threadIdx.x, lane = tid & 63;
  const int wave = __builtin_amdgcn_readfirstlane(tid >> 6);
  const int tok0 = tb * 64;
  if (tid == 0) ((unsigned*)(ws + (8u << 20)))[blockIdx.x] = (unsigned)wall_clock64();

  if (wave >= 8) {
    if (blockIdx.x == 255 && tid >= 768 - (NGRP + 1)) gcnt[(767 - tid) * 64] = 0;
    if (blockIdx.x == 254 && tid >= 768 - 33) ((unsigned*)(ws + WS_CL))[(767 - tid) * 64] = 0;
    stage_w(wq, cb * 64, wimg, wave - 8, 4, lane);
    if (cb < 2) {
      const bf16* w = cb == 0 ? wk : wv;
#pragma unroll
      for (int i = 0; i < 4; ++i) {
        const int p = wave - 8 + 4 * i;
        glds16((const char*)w + p * 1024 + lane * 16, kvimg + p * 1024);
      }
    }
    __syncthreads();
    if (cb == 3 && wave == 8) {
      unsigned k1 = route_key(rs[lane], tok0 + lane), k2 = 0;
      top2_wave(k1, k2, 64);
      if (lane == 0) *(u32x2*)(cand + tb * 2) = u32x2{k1, k2};
    }
    return;
  }

  const int tl = wave * 8 + (lane >> 3), c8 = lane & 7;
  u32x4 xv[8];
#pragma unroll
  for (int i = 0; i < 8; ++i) xv[i] = *(const u32x4*)(x + (tok0 + tl) * 512 + (c8 + 8 * i) * 8);
  u32x4 wrv[8];
  if (cb == 3) {
#pragma unroll
    for (int i = 0; i < 8; ++i) wrv[i] = *(const u32x4*)(wr + (c8 + 8 * i) * 8);
  }

  f32x2 xf[32];
#pragma unroll
  for (int i = 0; i < 32; ++i) xf[i] = unpack2(xv[i >> 2][i & 3]);
  f32x2 s2 = {0.f, 0.f};
#pragma unroll
  for (int i = 0; i < 32; ++i) s2 += xf[i];
  const float mean = sum8(s2.x + s2.y) * (1.f / 512.f);
  const f32x2 mean2 = {mean, mean};
  f32x2 v2 = {0.f, 0.f};
#pragma unroll
  for (int i = 0; i < 32; ++i) { const f32x2 d = xf[i] - mean2; v2 += d * d; }
  const float vs = sum8(v2.x + v2.y);
  const float mb = rbf(mean);
  const float vb = rbf(vs * (1.f / 512.f));
  const float den = rbf(sqrtf(rbf(vb + 1e-5f)));
  const float rinv = __builtin_amdgcn_rcpf(den);
  const f32x2 mb2 = {mb, mb}, rinv2 = {rinv, rinv};
  u32x4 o[8];
#pragma unroll
  for (int i = 0; i < 8; ++i) {
#pragma unroll
    for (int j = 0; j < 4; ++j) o[i][j] = ln2(xf[4 * i + j], mb2, rinv2);
    *(u32x4*)(ximg + tl * XS_STRIDE + (c8 + 8 * i) * 16) = o[i];
  }
  if (cb == 2 && c8 == 0) *(f32x2*)(st + (tok0 + tl) * 2) = f32x2{mb, rinv};
  if (cb == 3) {
    float rd = 0.f;
#pragma unroll
    for (int i = 0; i < 8; ++i) {
#pragma unroll
      for (int j = 0; j < 4; ++j) {
        const unsigned a = o[i][j], b = wrv[i][j];
        rd = __builtin_amdgcn_fdot2_f32_bf16(__builtin_bit_cast(bf16x2, a), __builtin_bit_cast(bf16x2, b), rd, false);
      }
    }
    rd = sum8(rd);
    if (c8 == 0) rs[tl] = rbf(rd);
  }
  __syncthreads();

  const int r = lane & 31, h = lane >> 5;
  if (wave < 4) {
    const int n0 = (wave & 1) * 32, t0 = (wave >> 1) * 32;
    f32x16 acc = gemm_tile(wimg, ximg, n0, t0, lane);
    const float sc = 0.25f * 1.4426950408889634f;
    const __amdgpu_buffer_rsrc_t rq = __builtin_amdgcn_make_buffer_rsrc(Q + tok0 * 512, 0, 65536, 0x00020000);
#pragma unroll
    for (int m = 0; m < 2; ++m) {
      unsigned p[4];
#pragma unroll
      for (int i = 0; i < 4; ++i) p[i] = pack2(unpack2(pack2(f32x2{acc[8 * m + 2 * i], acc[8 * m + 2 * i + 1]})) * f32x2{sc, sc});
      const unsigned s0 = __shfl_xor(h ? p[0] : p[2], 32), s1 = __shfl_xor(h ? p[1] : p[3], 32);
      const u32x4 o = h ? u32x4{s0, s1, p[2], p[3]} : u32x4{p[0], p[1], s0, s1};
      __builtin_amdgcn_raw_buffer_store_b128(o, rq, ((t0 + r) * 512 + cb * 64 + n0 + 16 * m + 8 * h) * 2, 0, 0);
    }
  } else if (wave == 7) {
    if (lane < 2) {
      const unsigned t = *(const volatile unsigned*)(lane ? ws + WS_K + tb * 2048 : (const char*)(Q + tok0 * 512 + cb * 64));
      asm volatile("" ::"v"(t));
    }
  } else if (wave == 6 && cb == 7 && tb < 8) {
    unsigned long long c0, c1;
    CODE_ADDR(k2_attn, c0);
    CODE_ADDR(k1_ln_qkv, c1);
    const long long n = (long long)(c1 - c0);
    unsigned t[4];
#pragma unroll
    for (int i = 0; i < 4; ++i) {
      const int off = (i * 64 + lane) * 128;
      t[i] = off < n ? *(const volatile unsigned*)(c0 + off) : 0u;
    }
#pragma unroll
    for (int i = 0; i < 4; ++i) asm volatile("" ::"v"(t[i]));
  } else if (wave < 6 && cb < 2) {
    const int t0 = (wave - 4) * 32;
    f32x16 acc = gemm_tile16(kvimg, ximg, t0, lane);
    const int key = tok0 + t0 + r;
    const int t = key >> 5;
    if (cb == 0) {
#pragma unroll
      for (int g = 0; g < 2; ++g) {
        bf16x4 o;
#pragma unroll
        for (int j = 0; j < 4; ++j) o[j] = (bf16)acc[4 * g + j];
        *(bf16x4*)(Kb + ((t * 2 + g) * 32 + r) * 8 + 4 * h) = o;
      }
    } else {
      const int kk = key & 31, sg = kk >> 4, rem = kk & 15;
      const int hh = (rem >> 2) & 1, jj = ((rem >> 3) << 2) | (rem & 3);
#pragma unroll
      for (int g = 0; g < 2; ++g) {
#pragma unroll
        for (int j = 0; j < 4; ++j) {
          const int d = 8 * g + 4 * h + j;
          Vf[(((t * 2 + sg) * 2 + hh) * 16 + d) * 8 + jj] = (bf16)acc[4 * g + j];
        }
      }
    }
  }
  if (tid == 0) { asm volatile("s_waitcnt vmcnt(0)" ::: "memory"); ((unsigned*)(ws + (8u << 20)))[256 + blockIdx.x] = (unsigned)wall_clock64(); }
}

extern "C" void kernel_launch(void* const* d_in, const int* in_sizes, int n_in, void* d_out, int out_size,
                              void* d_ws, size_t ws_size, hipStream_t stream) {
  const bf16* x = (const bf16*)d_in[0];
  const bf16* wq = (const bf16*)d_in[1];
  const bf16* wk = (const bf16*)d_in[2];
  const bf16* wv = (const bf16*)d_in[3];
  const bf16* wo = (const bf16*)d_in[4];
  const bf16* wr = (const bf16*)d_in[6];
  const bf16* wg = (const bf16*)d_in[7];
  const bf16* wu = (const bf16*)d_in[8];
  const bf16* wd = (const bf16*)d_in[9];
  char* ws = (char*)d_ws;
  bf16* out = (bf16*)d_out;

  (void)hipFuncSetAttribute((const void*)k1_ln_qkv, hipFuncAttributeMaxDynamicSharedMemorySize, K1_LDS);
  (void)hipFuncSetAttribute((const void*)k2_attn, hipFuncAttributeMaxDynamicSharedMemorySize, ATT_LDS);

  k1_ln_qkv<<<256, 768, K1_LDS, stream>>>(x, wq, wk, wv, wr, ws);
  k2_attn<<<256, 768, ATT_LDS, stream>>>(ws, x, wg, wu, wd, wo, out);
}
```

```cpp
#include <hip/hip_runtime.h>
#include <hip/hip_bf16.h>

typedef __attribute__((ext_vector_type(8))) __bf16 bf16x8;
typedef __attribute__((ext_vector_type(16))) __bf16 bf16x16;
typedef __attribute__((ext_vector_type(8))) float f32x8;
typedef __attribute__((ext_vector_type(4))) __bf16 bf16x4;
typedef __attribute__((ext_vector_type(2))) __bf16 bf16x2;
typedef __attribute__((ext_vector_type(4))) short s16x4;
typedef __attribute__((ext_vector_type(8))) short s16x8;
typedef __attribute__((ext_vector_type(16))) float f32x16;
typedef __attribute__((ext_vector_type(4))) float f32x4;
typedef __attribute__((ext_vector_type(4))) unsigned u32x4;
typedef __attribute__((ext_vector_type(2))) unsigned u32x2;
typedef __attribute__((ext_vector_type(2))) float f32x2;
typedef __bf16 bf16;

#define LDS_AS __attribute__((address_space(3)))
#define GLB_AS __attribute__((address_space(1)))
#define DI __device__ __forceinline__

constexpr int S_ = 2048, D_ = 512, H_ = 32, DH_ = 16, F_ = 1376;

constexpr size_t WS_Q = 0;
constexpr size_t WS_O = WS_Q;
constexpr size_t WS_K = 2u << 20;
constexpr size_t WS_V = WS_K + 65536;
constexpr size_t WS_ST = WS_V + 65536;
constexpr size_t WS_R = WS_ST + 16384;
constexpr size_t WS_CNT = WS_R + 4096;
constexpr size_t WS_UPD = WS_CNT + 4096;
constexpr size_t WS_YP = WS_UPD + 4096;

constexpr int NFFN = 172;
constexpr int GSZ = 12, NGRP = 15;
constexpr size_t WS_CL = WS_YP + (NFFN + NGRP) * 4096;

DI float bf2f(bf16 v) { return (float)v; }
DI float rbf(float v) { return (float)(bf16)v; }
DI f32x2 unpack2(unsigned u) { return f32x2{__uint_as_float(u << 16), __uint_as_float(u & 0xffff0000u)}; }
DI unsigned pack2(f32x2 v) {
  unsigned r = __builtin_bit_cast(unsigned, __builtin_convertvector(v, bf16x2));
  asm("" : "+v"(r));
  return r;
}

DI unsigned route_key(float v, int tok) {
  const unsigned u = __float_as_uint(v) >> 16;
  const unsigned o = (u & 0x8000u) ? (~u & 0xffffu) : (u | 0x8000u);
  return (o << 16) | (unsigned)(0xffff - tok);
}
DI int key_tok(unsigned k) { return 0xffff - (int)(k & 0xffffu); }
DI float key_val(unsigned k) {
  const unsigned o = k >> 16;
  const unsigned u = (o & 0x8000u) ? (o & 0x7fffu) : (~o & 0xffffu);
  return __uint_as_float(u << 16);
}
DI unsigned ln2(f32x2 x, f32x2 mb2, f32x2 rinv2) { return pack2(unpack2(pack2(x - mb2)) * rinv2); }
DI void top2_wave(unsigned& k1, unsigned& k2, int width) {
  for (int o = width >> 1; o > 0; o >>= 1) {
    const unsigned a1 = __shfl_xor(k1, o), a2 = __shfl_xor(k2, o);
    k2 = max(min(k1, a1), max(k2, a2));
    k1 = max(k1, a1);
  }
}

template <int O>
DI void top2_step(unsigned& k1, unsigned& k2) {
  const unsigned a1 = __builtin_amdgcn_ds_swizzle(k1, (O << 10) | 31), a2 = __builtin_amdgcn_ds_swizzle(k2, (O << 10) | 31);
  k2 = max(min(k1, a1), max(k2, a2));
  k1 = max(k1, a1);
}

DI void glds16(const void* g, void* l) {
  __builtin_amdgcn_global_load_lds((const GLB_AS void*)g, (LDS_AS void*)l, 16, 0, 0);
}

DI void glds16_raw(const void* g, unsigned lds_off) {
  asm volatile("s_mov_b32 m0, %0\n s_nop 0\n global_load_lds_dwordx4 %1, off" ::"s"(lds_off), "v"(g) : "memory", "m0");
}

template <int CTRL>
DI float dpp_add(float v) {
  const int t = __builtin_amdgcn_update_dpp(0, __float_as_int(v), CTRL, 0xf, 0xf, true);
  return v + __int_as_float(t);
}
DI float sum8(float v) {
  v = dpp_add<0xB1>(v);
  v = dpp_add<0x4E>(v);
  v = dpp_add<0x141>(v);
  return v;
}

DI f32x16 pv_mfma(bf16x8 v, f32x8 p, f32x16 acc, int sel) {
  return __builtin_amdgcn_smfmac_f32_32x32x32_bf16(v, __builtin_bit_cast(bf16x16, p), acc, sel, 0, 0);
}

DI s16x4 tr_read(const char* p) {
  return __builtin_amdgcn_ds_read_tr16_b64_v4i16((LDS_AS s16x4*)p);
}

DI int wimg_off(int row, int ch) { return row * 128 + ((ch ^ (((row >> 1) & 1) << 2)) << 4); }

constexpr int XS_STRIDE = 1040;

DI void stage_w(const bf16* w, int col0, char* wimg, int wave, int nwaves, int lane) {
  for (int p = wave; p < 64; p += nwaves) {
    const int row = p * 8 + (lane >> 3), pc = lane & 7;
    const int ch = pc ^ (((row >> 1) & 1) << 2);
    glds16(w + row * 512 + col0 + ch * 8, wimg + p * 1024);
  }
}

constexpr int GP = 4;
DI f32x16 gemm_tile(const char* wimg, const char* ximg, int n0, int t0, int lane) {
  const int r = lane & 31, h = lane >> 5;
  const int q = (lane & 15) >> 2, p = lane & 3, blk = (lane >> 4) & 1;
  f32x16 acc;
#pragma unroll
  for (int i = 0; i < 16; ++i) acc[i] = 0.f;
  const char* xb = ximg + (t0 + r) * XS_STRIDE + 16 * h;
  const int col = n0 + 16 * blk + 4 * p;
  const int ch = col >> 3, sub = (col & 7) * 2;
  const char* wb0 = wimg + wimg_off(8 * h + q, ch) + sub;
  const char* wb1 = wimg + wimg_off(8 * h + q + 4, ch) + sub;
  s16x4 lo[32], hi[32];
  bf16x8 b[32];
#pragma unroll
  for (int ks = 0; ks < 32 + GP; ++ks) {
    if (ks < 32) {
      lo[ks] = tr_read(wb0 + ks * 2048);
      hi[ks] = tr_read(wb1 + ks * 2048);
      b[ks] = *(const bf16x8*)(xb + ks * 32);
    }
    if (ks >= GP) {
      const s16x8 a = __builtin_shufflevector(lo[ks - GP], hi[ks - GP], 0, 1, 2, 3, 4, 5, 6, 7);
      acc = __builtin_amdgcn_mfma_f32_32x32x16_bf16(__builtin_bit_cast(bf16x8, a), b[ks - GP], acc, 0, 0, 0);
      asm volatile("" : "+v"(acc));
    }
    __builtin_amdgcn_sched_barrier(0);
  }
  return acc;
}
DI f32x16 gemm_tile16(const char* wimg, const char* ximg, int t0, int lane) {
  const int r = lane & 31, h = lane >> 5;
  const int q = (lane & 15) >> 2, p = lane & 3;
  f32x16 acc;
#pragma unroll
  for (int i = 0; i < 16; ++i) acc[i] = 0.f;
  const char* xb = ximg + (t0 + r) * XS_STRIDE + 16 * h;
  const char* wb = wimg + (8 * h + q) * 32 + 8 * p;
  s16x4 lo[32], hi[32];
  bf16x8 b[32];
#pragma unroll
  for (int ks = 0; ks < 32 + GP; ++ks) {
    if (ks < 32) {
      lo[ks] = tr_read(wb + ks * 512);
      hi[ks] = tr_read(wb + ks * 512 + 128);
      b[ks] = *(const bf16x8*)(xb + ks * 32);
    }
    if (ks >= GP) {
      const s16x8 a = __builtin_shufflevector(lo[ks - GP], hi[ks - GP], 0, 1, 2, 3, 4, 5, 6, 7);
      acc = __builtin_amdgcn_mfma_f32_32x32x16_bf16(__builtin_bit_cast(bf16x8, a), b[ks - GP], acc, 0, 0, 0);
      asm volatile("" : "+v"(acc));
    }
    __builtin_amdgcn_sched_barrier(0);
  }
  return acc;
}

#define CODE_ADDR(sym, out)                                                                               \
  asm volatile("s_getpc_b64 s[20:21]\n s_add_u32 s20, s20, " #sym "@rel32@lo+4\n s_addc_u32 s21, s21, " #sym \
               "@rel32@hi+12\n s_mov_b64 %0, s[20:21]"                                                    \
               : "=s"(out)::"s20", "s21", "scc")

constexpr int NT = 16;
constexpr int CST = NT * 1024;
constexpr int ATT_LDS = 65536 * 2 + CST + 64 + 512 + 4 * 640;
constexpr int NC = 64 / NT;
constexpr int SPIN_MAX = 1 << 22;

DI bool last_arrival(int* w, unsigned* gc, int n, int lane) {
  asm volatile("s_waitcnt vmcnt(0)" ::: "memory");
  if (lane == 0) {
    if (__hip_atomic_fetch_add(w, 1, __ATOMIC_RELAXED, __HIP_MEMORY_SCOPE_WORKGROUP) == 3) {
      const unsigned t = __hip_atomic_fetch_add((GLB_AS unsigned*)gc, 1u, __ATOMIC_RELAXED, __HIP_MEMORY_SCOPE_AGENT);
      __hip_atomic_store(w + 1, t == (unsigned)(n - 1) ? 2 : 1, __ATOMIC_RELAXED, __HIP_MEMORY_SCOPE_WORKGROUP);
    }
  }
  int r;
  while ((r = __hip_atomic_load(w + 1, __ATOMIC_RELAXED, __HIP_MEMORY_SCOPE_WORKGROUP)) == 0) __builtin_amdgcn_s_sleep(2);
  asm volatile("" ::: "memory");
  return r == 2;
}

DI void ffn_slice(int f, int j, const bf16* __restrict__ x, const float* __restrict__ st,
                  const unsigned* __restrict__ cand, unsigned* __restrict__ fdone,
                  const bf16* __restrict__ wg, const bf16* __restrict__ wu, const bf16* __restrict__ wd,
                  float* __restrict__ ypart, unsigned* __restrict__ gcnt,
                  float* __restrict__ upd, float* red, int* cnt, char* scr, int lane) {
  const int k0 = j * 128 + lane * 2;
  const int r = lane & 31, h = lane >> 5;
  const int kr = (lane & 51) | ((lane & 4) << 1) | ((lane & 8) >> 1);
  u32x4 wf[4];
  unsigned d2[8];
  {
    const __amdgpu_buffer_rsrc_t rg = __builtin_amdgcn_make_buffer_rsrc((void*)wg, 0, 512 * F_ * 2, 0x00020000);
    const __amdgpu_buffer_rsrc_t ru = __builtin_amdgcn_make_buffer_rsrc((void*)wu, 0, 512 * F_ * 2, 0x00020000);
    const __amdgpu_buffer_rsrc_t rd = __builtin_amdgcn_make_buffer_rsrc((void*)wd, 0, 512 * F_ * 2, 0x00020000);
    const int wo_ = kr * (F_ * 2), so = (j * 128 * F_ + f * 8) * 2;
#pragma unroll
    for (int t = 0; t < 2; ++t) {
      wf[2 * t] = __builtin_amdgcn_raw_buffer_load_b128(rg, wo_, so + t * (64 * F_ * 2), 0);
      wf[2 * t + 1] = __builtin_amdgcn_raw_buffer_load_b128(ru, wo_, so + t * (64 * F_ * 2), 0);
    }
#pragma unroll
    for (int c = 0; c < 8; ++c)
      d2[c] = __builtin_amdgcn_raw_buffer_load_b32(rd, 8 * r + 4 * h + c * 1024, (f * 8 * 512 + j * 128) * 2, 0);
  }
  const u32x2 cv = *(const u32x2*)(cand + (lane & 31) * 2);
  unsigned k1 = cv[0], k2 = cv[1];
  top2_step<16>(k1, k2);
  top2_step<8>(k1, k2);
  top2_step<4>(k1, k2);
  top2_step<2>(k1, k2);
  top2_step<1>(k1, k2);
  k1 = __builtin_amdgcn_readfirstlane(k1);
  k2 = __builtin_amdgcn_readfirstlane(k2);
  const int i1 = key_tok(k1), i2 = key_tok(k2);
  const f32x2 sa = *(const f32x2*)(st + i1 * 2), sb = *(const f32x2*)(st + i2 * 2);
  *(unsigned*)(scr + lane * 4) = ln2(unpack2(*(const unsigned*)(x + i1 * 512 + k0)), f32x2{sa.x, sa.x}, f32x2{sa.y, sa.y});
  *(unsigned*)(scr + 256 + lane * 4) = ln2(unpack2(*(const unsigned*)(x + i2 * 512 + k0)), f32x2{sb.x, sb.x}, f32x2{sb.y, sb.y});
  f32x16 zero;
#pragma unroll
  for (int i = 0; i < 16; ++i) zero[i] = 0.f;
  u32x4 ida, idb;
  {
    const int d = r - 8 * h;
    const unsigned one = 0x3f80u << ((d & 1) << 4);
#pragma unroll
    for (int q = 0; q < 4; ++q) {
      ida[q] = (d >> 1) == q ? one : 0u;
      idb[q] = (d >> 1) == q + 8 ? one : 0u;
    }
  }
  f32x16 gu = zero;
#pragma unroll
  for (int t = 0; t < 2; ++t) {
    f32x16 wt = __builtin_amdgcn_mfma_f32_32x32x16_bf16(__builtin_bit_cast(bf16x8, wf[2 * t]),
                                                        __builtin_bit_cast(bf16x8, ida), zero, 0, 0, 0);
    wt = __builtin_amdgcn_mfma_f32_32x32x16_bf16(__builtin_bit_cast(bf16x8, wf[2 * t + 1]),
                                                 __builtin_bit_cast(bf16x8, idb), wt, 0, 0, 0);
#pragma unroll
    for (int q = 0; q < 2; ++q) {
      u32x4 bw;
#pragma unroll
      for (int i = 0; i < 4; ++i) bw[i] = pack2(f32x2{wt[8 * q + 2 * i], wt[8 * q + 2 * i + 1]});
      const bf16x8 xf = *(const bf16x8*)(scr + (r & 1) * 256 + (r & 2) * 32 + (64 * t + 16 * q + 8 * h) * 2);
      gu = __builtin_amdgcn_mfma_f32_32x32x16_bf16(xf, __builtin_bit_cast(bf16x8, bw), gu, 0, 0, 0);
    }
  }
  {
    const float g0 = dpp_add<0x128>((lane & 8) ? gu[2] : gu[0]), g1 = dpp_add<0x128>((lane & 8) ? gu[3] : gu[1]);
    if (lane < 32) red[j * 32 + (lane & 16) + (lane & 15)] = (lane & 8) ? g1 : g0;
  }
  if (lane == 0) __hip_atomic_fetch_add(cnt, 1, __ATOMIC_RELEASE, __HIP_MEMORY_SCOPE_WORKGROUP);
  while (__hip_atomic_load(cnt, __ATOMIC_ACQUIRE, __HIP_MEMORY_SCOPE_WORKGROUP) < 4) __builtin_amdgcn_s_sleep(1);
  const int l = lane & 15;
  const float g = rbf((red[l] + red[32 + l]) + (red[64 + l] + red[96 + l]));
  const float u = rbf((red[16 + l] + red[48 + l]) + (red[80 + l] + red[112 + l]));
  const float hv = rbf(g * __builtin_amdgcn_rcpf(1.f + __expf(-g))) * u;
  if (lane < 24) *(bf16*)(scr + 512 + lane * 2) = (bf16)(lane < 16 ? hv : 0.f);
  const bf16x8 hf = *(const bf16x8*)(scr + 512 + ((r >> 1) == h ? (r & 1) * 16 : 32));
  u32x4 da, db;
#pragma unroll
  for (int c = 0; c < 4; ++c) {
    da[c] = __builtin_amdgcn_perm(d2[2 * c + 1], d2[2 * c], 0x05040100u);
    db[c] = __builtin_amdgcn_perm(d2[2 * c + 1], d2[2 * c], 0x07060302u);
  }
  const f32x16 ya = __builtin_amdgcn_mfma_f32_32x32x16_bf16(hf, __builtin_bit_cast(bf16x8, da), zero, 0, 0, 0);
  const f32x16 yb = __builtin_amdgcn_mfma_f32_32x32x16_bf16(hf, __builtin_bit_cast(bf16x8, db), zero, 0, 0, 0);
  const __amdgpu_buffer_rsrc_t rs = __builtin_amdgcn_make_buffer_rsrc(ypart, 0, (NFFN + NGRP) * 4096, 0x00020000);
  if (lane < 32) {
#pragma unroll
    for (int t = 0; t < 2; ++t)
      __builtin_amdgcn_raw_buffer_store_b128(__builtin_bit_cast(u32x4, f32x4{ya[t], yb[t], ya[2 + t], yb[2 + t]}), rs,
                                             t * 2048 + j * 512 + lane * 16, f * 4096, 16);
  }
  const int grp = f / GSZ, gs = min(GSZ, NFFN - grp * GSZ);
  if (!last_arrival(cnt + 1, gcnt + grp * 64, gs, lane)) return;
  const int voff = j * 1024 + lane * 16;
  f32x4 a = {0.f, 0.f, 0.f, 0.f};
  {
    u32x4 t[GSZ];
#pragma unroll
    for (int i = 0; i < GSZ; ++i) t[i] = __builtin_amdgcn_raw_buffer_load_b128(rs, voff, (grp * GSZ + min(i, gs - 1)) * 4096, 16);
#pragma unroll
    for (int i = 0; i < GSZ; ++i)
      if (i < gs) a += __builtin_bit_cast(f32x4, t[i]);
  }
  __builtin_amdgcn_raw_buffer_store_b128(__builtin_bit_cast(u32x4, a), rs, voff, (NFFN + grp) * 4096, 16);
  if (!last_arrival(cnt + 3, gcnt + NGRP * 64, NGRP, lane)) return;
  a = f32x4{0.f, 0.f, 0.f, 0.f};
  {
    u32x4 t[NGRP];
#pragma unroll
    for (int i = 0; i < NGRP; ++i) t[i] = __builtin_amdgcn_raw_buffer_load_b128(rs, voff, (NFFN + i) * 4096, 16);
#pragma unroll
    for (int i = 0; i < NGRP; ++i) a += __builtin_bit_cast(f32x4, t[i]);
  }
  const float gate = rbf(1.f / (1.f + __expf(-key_val(j < 2 ? k1 : k2))));
#pragma unroll
  for (int i = 0; i < 4; ++i) a[i] = rbf(rbf(a[i]) * gate);
  {
    const __amdgpu_buffer_rsrc_t ru = __builtin_amdgcn_make_buffer_rsrc(upd, 0, 4096, 0x00020000);
    __builtin_amdgcn_raw_buffer_store_b128(__builtin_bit_cast(u32x4, a), ru, voff, 0, 16);
  }
  asm volatile("s_waitcnt vmcnt(0)" ::: "memory");
  if (lane == 0 && __hip_atomic_fetch_add(cnt + 5, 1, __ATOMIC_RELAXED, __HIP_MEMORY_SCOPE_WORKGROUP) == 3)
    __hip_atomic_store((GLB_AS unsigned*)fdone, 1u, __ATOMIC_RELAXED, __HIP_MEMORY_SCOPE_AGENT);
}

extern "C" __global__ __launch_bounds__(768) void k2_attn(char* __restrict__ ws, const bf16* __restrict__ x,
                                               const bf16* __restrict__ wg, const bf16* __restrict__ wu,
                                               const bf16* __restrict__ wd, const bf16* __restrict__ wo,
                                               bf16* __restrict__ out) {
  const bf16* Q = (const bf16*)(ws + WS_Q);
  const bf16* Kb = (const bf16*)(ws + WS_K);
  bf16* O = (bf16*)(ws + WS_O);
  const float* st = (const float*)(ws + WS_ST);
  const unsigned* cand = (const unsigned*)(ws + WS_R);
  float* ypart = (float*)(ws + WS_YP);
  unsigned* gcnt = (unsigned*)(ws + WS_CNT);
  float* upd = (float*)(ws + WS_UPD);
  extern __shared__ __attribute__((aligned(16))) char smem[];
  char* kl = smem;
  char* vl = smem + 65536;
  char* cst = smem + 131072;
  const int tid = threadIdx.x, lane = tid & 63;
  const int wave = __builtin_amdgcn_readfirstlane(tid >> 6);

  if (wave >= 8) {
    int* cnt = (int*)(smem + 131072 + CST + 32);
    if (tid < 512 + 6) cnt[tid - 512] = 0;
#pragma unroll
    for (int i = 0; i < 26; ++i) {
      const int q = (wave - 8) + 4 * i, piece = q < 48 ? 16 + q : 24 + q;
      glds16_raw((const char*)Kb + piece * 1024 + lane * 16, (unsigned)(size_t)(LDS_AS char*)smem + piece * 1024);
    }
    asm volatile("s_waitcnt lgkmcnt(0)\n s_barrier\n s_waitcnt vmcnt(0)\n s_barrier" ::: "memory");
    unsigned* clc = (unsigned*)(ws + WS_CL);
    __builtin_amdgcn_s_setprio(3);
    const int fs = (blockIdx.x & 7) * 22 + (blockIdx.x >> 3);
    if (blockIdx.x < 176 && fs < NFFN) {
      ffn_slice(fs, wave - 8, x, st, cand, clc + 32 * 64, wg, wu, wd, ypart, gcnt, upd,
                (float*)(smem + 131072 + CST + 64), cnt, smem + 131072 + CST + 576 + (wave - 8) * 640, lane);
    }
    const int tb = (blockIdx.x >> 6) * 8 + (blockIdx.x & 7), cb = (blockIdx.x >> 3) & 7, tok0 = tb * 64;
    const int sw = wave - 8, r = lane & 31, h = lane >> 5;
    char* ximg = smem;
    char* wimg = smem + 64 * XS_STRIDE;
    u32x4 wreg[16];
#pragma unroll
    for (int i = 0; i < 16; ++i) {
      const int row = (sw + 4 * i) * 8 + (lane >> 3);
      wreg[i] = *(const u32x4*)(wo + row * 512 + cb * 64 + (((lane & 7) ^ (((row >> 1) & 1) << 2)) << 3));
    }
    const int n0 = (sw & 1) * 32, t0 = (sw >> 1) * 32;
    const int tok = tok0 + t0 + r;
    u32x2 xv[4], nv[4];
#pragma unroll
    for (int g = 0; g < 4; ++g) xv[g] = *(const u32x2*)(x + tok * 512 + cb * 64 + n0 + 8 * g + 4 * h);
    const f32x2 sx = *(const f32x2*)(st + tok * 2);
    int rsel;
    {
      const u32x2 cv = *(const u32x2*)(cand + (lane & 31) * 2);
      unsigned k1 = cv[0], k2 = cv[1];
      top2_step<16>(k1, k2);
      top2_step<8>(k1, k2);
      top2_step<4>(k1, k2);
      top2_step<2>(k1, k2);
      top2_step<1>(k1, k2);
      rsel = tok == key_tok(k1) ? 0 : tok == key_tok(k2) ? 1 : -1;
    }
#pragma unroll
    for (int g = 0; g < 4; ++g) {
#pragma unroll
      for (int jj = 0; jj < 2; ++jj) {
        unsigned nr = ln2(unpack2(xv[g][jj]), f32x2{sx.x, sx.x}, f32x2{sx.y, sx.y});
        asm volatile("" : "+v"(nr));
        nv[g][jj] = nr;
      }
    }
    if (wave == 11 && lane == 0) {
      const unsigned t = *(const volatile unsigned*)(out + tok0 * 512 + cb * 64);
      asm volatile("" ::"v"(t));
    }
#pragma unroll
    for (int i = 0; i < 16; ++i) asm volatile("" : "+v"(wreg[i]));
    asm volatile("s_barrier" ::: "memory");
    if (tid == 512) __hip_atomic_fetch_add((GLB_AS unsigned*)(clc + tb * 64), 1u, __ATOMIC_RELAXED, __HIP_MEMORY_SCOPE_AGENT);
#pragma unroll
    for (int i = 0; i < 16; ++i) *(u32x4*)(wimg + (sw + 4 * i) * 1024 + lane * 16) = wreg[i];
    if (__any(rsel >= 0)) {
      int spin = 0;
      while (__hip_atomic_load((GLB_AS unsigned*)(clc + 32 * 64), __ATOMIC_RELAXED, __HIP_MEMORY_SCOPE_AGENT) == 0 && ++spin < SPIN_MAX)
        __builtin_amdgcn_s_sleep(1);
      asm volatile("" ::: "memory");
      const __amdgpu_buffer_rsrc_t ru = __builtin_amdgcn_make_buffer_rsrc(upd, 0, 4096, 0x00020000);
      if (rsel >= 0) {
#pragma unroll
        for (int g = 0; g < 4; ++g) {
          const f32x4 u = __builtin_bit_cast(f32x4, __builtin_amdgcn_raw_buffer_load_b128(ru, (rsel * 512 + cb * 64 + n0 + 8 * g + 4 * h) * 4, 0, 16));
          nv[g][0] = pack2(unpack2(nv[g][0]) + f32x2{u[0], u[1]});
          nv[g][1] = pack2(unpack2(nv[g][1]) + f32x2{u[2], u[3]});
        }
      }
    }
    if (wave == 8) {
      int spin = 0;
      while (__hip_atomic_load((GLB_AS unsigned*)(clc + tb * 64), __ATOMIC_RELAXED, __HIP_MEMORY_SCOPE_AGENT) < 8 && ++spin < SPIN_MAX)
        __builtin_amdgcn_s_sleep(1);
    }
    asm volatile("s_barrier" ::: "memory");
    {
      const __amdgpu_buffer_rsrc_t ro = __builtin_amdgcn_make_buffer_rsrc(O + tok0 * 512, 0, 65536, 0x00020000);
      u32x4 t[16];
#pragma unroll
      for (int i = 0; i < 16; ++i) t[i] = __builtin_amdgcn_raw_buffer_load_b128(ro, lane * 16, (sw * 16 + i) * 1024, 16);
#pragma unroll
      for (int i = 0; i < 16; ++i) *(u32x4*)(ximg + (sw * 16 + i) * XS_STRIDE + lane * 16) = t[i];
    }
    asm volatile("s_waitcnt lgkmcnt(0)\n s_barrier" ::: "memory");
    f32x16 acc = gemm_tile(wimg, ximg, n0, t0, lane);
#pragma unroll
    for (int g = 0; g < 4; ++g) {
      u32x2 o;
#pragma unroll
      for (int jj = 0; jj < 2; ++jj) {
        const unsigned xr = xv[g][jj], nr = nv[g][jj];
        const f32x2 a = unpack2(pack2(f32x2{acc[4 * g + 2 * jj], acc[4 * g + 2 * jj + 1]}));
        o[jj] = pack2(unpack2(pack2(unpack2(xr) + a)) + unpack2(nr));
      }
      *(u32x2*)(out + tok * 512 + cb * 64 + n0 + 8 * g + 4 * h) = o;
    }
    return;
  }

  const int r = lane & 31, h = lane >> 5;
  const int token = ((blockIdx.x >> 6) * 8 + (blockIdx.x & 7)) * 64 + ((blockIdx.x >> 3) & 7) * 8 + wave;
  bf16x8 qf;
  asm volatile("global_load_dwordx4 %0, %1, off" : "=v"(qf) : "v"(Q + token * 512 + r * 16 + 8 * h) : "memory");
  const unsigned lds0 = (unsigned)(size_t)(LDS_AS char*)smem;
#pragma unroll
  for (int i = 0; i < 3; ++i) {
    const int piece = (i < 2) ? (wave + i * 8) : (64 + wave);
    glds16_raw((const char*)Kb + piece * 1024 + lane * 16, lds0 + piece * 1024);
  }
  {
    const unsigned cv = ((tid & 15) == 0 || (tid & 15) == 4) ? 0x3f803f80u : 0u;
#pragma unroll
    for (int i = 0; i < CST / 8192; ++i) *(u32x4*)(cst + i * 8192 + tid * 16) = u32x4{cv, cv, cv, cv};
  }
  asm volatile("s_waitcnt vmcnt(0) lgkmcnt(0)\n s_barrier" ::: "memory");

  const char* kp0 = kl + lane * 16;
  const char* vp0 = (r < 16) ? (vl + (h * 16 + r) * 16) : (cst + (h * 16 + r - 16) * 16);
  const int vstep = (r < 16) ? NT * 1024 : 0;
  int sel = 0xdddd;
  asm volatile("" : "+v"(sel));

  f32x16 zero;
#pragma unroll
  for (int i = 0; i < 16; ++i) zero[i] = 0.f;
  constexpr float HEAD = 40.f;
  float m;
  {
    float mx = -INFINITY;
#pragma unroll
    for (int t = 0; t < 4; ++t) {
      bf16x8 kf = *(const bf16x8*)(kp0 + t * 1024);
      f32x16 x = __builtin_amdgcn_mfma_f32_32x32x16_bf16(kf, qf, zero, 0, 0, 0);
#pragma unroll
      for (int i = 0; i < 16; i += 2) mx = fmaxf(fmaxf(mx, x[i]), x[i + 1]);
    }
    mx = fmaxf(mx, __shfl_xor(mx, 32));
    m = floorf(mx) + HEAD;
  }
  f32x16 negm;
#pragma unroll
  for (int i = 0; i < 16; ++i) negm[i] = -m;
  asm volatile("" : "+v"(negm));
  float tot[9];
#pragma unroll
  for (int i = 0; i < 9; ++i) tot[i] = 0.f;
  {
    const char* kp = kp0;
    const char* vp = vp0;
    int c = 0;
    bool redone = false, first = true;
    if (wave < 4) __builtin_amdgcn_s_setprio(2);
    else __builtin_amdgcn_s_setprio(1);
    f32x16 x = __builtin_amdgcn_mfma_f32_32x32x16_bf16(*(const bf16x8*)kp, qf, negm, 0, 0, 0);
    bf16x8 kfn = *(const bf16x8*)(kp + 1024);
    while (true) {
      f32x16 acc;
      while (true) {
        acc = zero;
#pragma unroll
        for (int t = 0; t < NT; ++t) {
          const bf16x8 kf = kfn;
          kfn = *(const bf16x8*)(kp + (t + 2) * 1024);
          const bf16x8 vf0 = *(const bf16x8*)(vp + (t * 2) * 512);
          const bf16x8 vf1 = *(const bf16x8*)(vp + (t * 2 + 1) * 512);
          f32x8 p0, p1;
#pragma unroll
          for (int j = 0; j < 4; ++j) p0[j] = __builtin_amdgcn_exp2f(x[j]);
          __builtin_amdgcn_sched_barrier(0);
          f32x16 xn = __builtin_amdgcn_mfma_f32_32x32x16_bf16(kf, qf, negm, 0, 0, 0);
          asm volatile("" : "+v"(xn));
          __builtin_amdgcn_sched_barrier(0);
#pragma unroll
          for (int j = 4; j < 8; ++j) p0[j] = __builtin_amdgcn_exp2f(x[j]);
          acc = pv_mfma(vf0, p0, acc, sel);
#pragma unroll
          for (int j = 0; j < 8; ++j) p1[j] = __builtin_amdgcn_exp2f(x[8 + j]);
          acc = pv_mfma(vf1, p1, acc, sel);
          if (t == 7 && first) {
            asm volatile("s_barrier" ::: "memory");
            first = false;
          }
          if (c * NT + t == 53 && wave < 4) __builtin_amdgcn_s_setprio(0);
          __builtin_amdgcn_sched_barrier(0);
          x = xn;
        }
        if (__any(!(tot[8] + acc[8] <= 4.3e9f))) break;
        redone = false;
#pragma unroll
        for (int i = 0; i < 9; ++i) tot[i] += acc[i];
        ++c;
        kp += NT * 1024;
        vp += vstep;
        if (c == NC) break;
      }
      if (c == NC) break;
      if (!redone && __any(!(acc[8] < 7.9e28f))) {
        float mx = -INFINITY;
        for (int t = 0; t < NT; ++t) {
          bf16x8 kf = *(const bf16x8*)(kp + t * 1024);
          f32x16 x = __builtin_amdgcn_mfma_f32_32x32x16_bf16(kf, qf, zero, 0, 0, 0);
#pragma unroll
          for (int i = 0; i < 16; i += 2) mx = fmaxf(fmaxf(mx, x[i]), x[i + 1]);
        }
        mx = fmaxf(mx, __shfl_xor(mx, 32));
        const float mn = fmaxf(m, floorf(mx) + HEAD);
        const int d = (int)fminf(mn - m, 1024.f);
#pragma unroll
        for (int i = 0; i < 9; ++i) tot[i] = __builtin_amdgcn_ldexpf(tot[i], -d);
        m = mn;
        redone = true;
      } else {
        redone = false;
#pragma unroll
        for (int i = 0; i < 9; ++i) tot[i] += acc[i];
        const int e = max(__builtin_amdgcn_frexp_expf(tot[8]) - 1 + (int)HEAD, 0);
#pragma unroll
        for (int i = 0; i < 9; ++i) tot[i] = __builtin_amdgcn_ldexpf(tot[i], -e);
        m += (float)e;
        ++c;
        kp += NT * 1024;
        vp += vstep;
        if (c == NC) break;
      }
#pragma unroll
      for (int i = 0; i < 16; ++i) negm[i] = -m;
      asm volatile("" : "+v"(negm));
      x = __builtin_amdgcn_mfma_f32_32x32x16_bf16(*(const bf16x8*)kp, qf, negm, 0, 0, 0);
      kfn = *(const bf16x8*)(kp + 1024);
    }
  }
  {
    float chk = 0.f;
#pragma unroll
    for (int i = 0; i < 9; ++i) chk = __builtin_fmaf(tot[i], 0.f, chk);
    if (__any(chk != 0.f)) {
      f32x16 oacc = zero;
      float mrun = -INFINITY;
      const char* kp = kp0;
      const char* vp = vp0;
      for (int t = 0; t < 64; ++t) {
        bf16x8 kf = *(const bf16x8*)(kp);
        f32x16 x = __builtin_amdgcn_mfma_f32_32x32x16_bf16(kf, qf, zero, 0, 0, 0);
        float mx = x[0];
#pragma unroll
        for (int i = 1; i < 16; ++i) mx = fmaxf(mx, x[i]);
        mx = fmaxf(mx, __shfl_xor(mx, 32));
        const float mn = fmaxf(mrun, mx);
        const float al = __builtin_amdgcn_exp2f(mrun - mn);
        mrun = mn;
#pragma unroll
        for (int i = 0; i < 16; ++i) oacc[i] *= al;
#pragma unroll
        for (int s = 0; s < 2; ++s) {
          f32x8 pf;
#pragma unroll
          for (int j = 0; j < 8; ++j) pf[j] = __builtin_amdgcn_exp2f(x[8 * s + j] - mn);
          bf16x8 vf = *(const bf16x8*)(vp + ((t & (NT - 1)) * 2 + s) * 512);
          oacc = pv_mfma(vf, pf, oacc, sel);
        }
        kp += 1024;
        if ((t & (NT - 1)) == NT - 1) vp += vstep;
      }
#pragma unroll
      for (int i = 0; i < 9; ++i) tot[i] = oacc[i];
    }
  }
  const float inv = 1.f / tot[8];
  {
    unsigned a0 = pack2(f32x2{tot[0] * inv, tot[1] * inv}), a1 = pack2(f32x2{tot[2] * inv, tot[3] * inv});
    unsigned b0 = pack2(f32x2{tot[4] * inv, tot[5] * inv}), b1 = pack2(f32x2{tot[6] * inv, tot[7] * inv});
    const unsigned s0 = __shfl_xor(h ? a0 : b0, 32), s1 = __shfl_xor(h ? a1 : b1, 32);
    const u32x4 o = h ? u32x4{s0, s1, b0, b1} : u32x4{a0, a1, s0, s1};
    const __amdgpu_buffer_rsrc_t ro = __builtin_amdgcn_make_buffer_rsrc(O + token * 512, 0, 1024, 0x00020000);
    __builtin_amdgcn_raw_buffer_store_b128(o, ro, r * 32 + h * 16, 0, 16);
  }
  asm volatile("s_waitcnt vmcnt(0)\n s_barrier" ::: "memory");
}

constexpr int K1_LDS = 64 * XS_STRIDE + 65536 + 16384 + 256;

extern "C" __global__ __launch_bounds__(768) void k1_ln_qkv(const bf16* __restrict__ x, const bf16* __restrict__ wq,
                                                 const bf16* __restrict__ wk, const bf16* __restrict__ wv,
                                                 const bf16* __restrict__ wr, char* __restrict__ ws) {
  float* st = (float*)(ws + WS_ST);
  bf16* Q = (bf16*)(ws + WS_Q);
  bf16* Kb = (bf16*)(ws + WS_K);
  bf16* Vf = (bf16*)(ws + WS_V);
  unsigned* cand = (unsigned*)(ws + WS_R);
  unsigned* gcnt = (unsigned*)(ws + WS_CNT);
  extern __shared__ __attribute__((aligned(16))) char smem[];
  char* ximg = smem;
  char* wimg = smem + 64 * XS_STRIDE;
  char* kvimg = smem + 64 * XS_STRIDE + 65536;
  float* rs = (float*)(smem + 64 * XS_STRIDE + 65536 + 16384);
  const int cb = blockIdx.x >> 5, tb = blockIdx.x & 31;
  const int tid = threadIdx.x, lane = tid & 63;
  const int wave = __builtin_amdgcn_readfirstlane(tid >> 6);
  const int tok0 = tb * 64;

  if (wave >= 8) {
    if (blockIdx.x == 255 && tid >= 768 - (NGRP + 1)) gcnt[(767 - tid) * 64] = 0;
    if (blockIdx.x == 254 && tid >= 768 - 33) ((unsigned*)(ws + WS_CL))[(767 - tid) * 64] = 0;
    stage_w(wq, cb * 64, wimg, wave - 8, 4, lane);
    if (cb < 2) {
      const bf16* w = cb == 0 ? wk : wv;
#pragma unroll
      for (int i = 0; i < 4; ++i) {
        const int p = wave - 8 + 4 * i;
        glds16((const char*)w + p * 1024 + lane * 16, kvimg + p * 1024);
      }
    }
    __syncthreads();
    if (cb == 3 && wave == 8) {
      unsigned k1 = route_key(rs[lane], tok0 + lane), k2 = 0;
      top2_wave(k1, k2, 64);
      if (lane == 0) *(u32x2*)(cand + tb * 2) = u32x2{k1, k2};
    }
    return;
  }

  const int tl = wave * 8 + (lane >> 3), c8 = lane & 7;
  u32x4 xv[8];
#pragma unroll
  for (int i = 0; i < 8; ++i) xv[i] = *(const u32x4*)(x + (tok0 + tl) * 512 + (c8 + 8 * i) * 8);
  u32x4 wrv[8];
  if (cb == 3) {
#pragma unroll
    for (int i = 0; i < 8; ++i) wrv[i] = *(const u32x4*)(wr + (c8 + 8 * i) * 8);
  }

  f32x2 xf[32];
#pragma unroll
  for (int i = 0; i < 32; ++i) xf[i] = unpack2(xv[i >> 2][i & 3]);
  f32x2 s2 = {0.f, 0.f};
#pragma unroll
  for (int i = 0; i < 32; ++i) s2 += xf[i];
  const float mean = sum8(s2.x + s2.y) * (1.f / 512.f);
  const f32x2 mean2 = {mean, mean};
  f32x2 v2 = {0.f, 0.f};
#pragma unroll
  for (int i = 0; i < 32; ++i) { const f32x2 d = xf[i] - mean2; v2 += d * d; }
  const float vs = sum8(v2.x + v2.y);
  const float mb = rbf(mean);
  const float vb = rbf(vs * (1.f / 512.f));
  const float den = rbf(sqrtf(rbf(vb + 1e-5f)));
  const float rinv = __builtin_amdgcn_rcpf(den);
  const f32x2 mb2 = {mb, mb}, rinv2 = {rinv, rinv};
  u32x4 o[8];
#pragma unroll
  for (int i = 0; i < 8; ++i) {
#pragma unroll
    for (int j = 0; j < 4; ++j) o[i][j] = ln2(xf[4 * i + j], mb2, rinv2);
    *(u32x4*)(ximg + tl * XS_STRIDE + (c8 + 8 * i) * 16) = o[i];
  }
  if (cb == 2 && c8 == 0) *(f32x2*)(st + (tok0 + tl) * 2) = f32x2{mb, rinv};
  if (cb == 3) {
    float rd = 0.f;
#pragma unroll
    for (int i = 0; i < 8; ++i) {
#pragma unroll
      for (int j = 0; j < 4; ++j) {
        const unsigned a = o[i][j], b = wrv[i][j];
        rd = __builtin_amdgcn_fdot2_f32_bf16(__builtin_bit_cast(bf16x2, a), __builtin_bit_cast(bf16x2, b), rd, false);
      }
    }
    rd = sum8(rd);
    if (c8 == 0) rs[tl] = rbf(rd);
  }
  __syncthreads();

  const int r = lane & 31, h = lane >> 5;
  if (wave < 4) {
    const int n0 = (wave & 1) * 32, t0 = (wave >> 1) * 32;
    f32x16 acc = gemm_tile(wimg, ximg, n0, t0, lane);
    const float sc = 0.25f * 1.4426950408889634f;
    const __amdgpu_buffer_rsrc_t rq = __builtin_amdgcn_make_buffer_rsrc(Q + tok0 * 512, 0, 65536, 0x00020000);
#pragma unroll
    for (int m = 0; m < 2; ++m) {
      unsigned p[4];
#pragma unroll
      for (int i = 0; i < 4; ++i) p[i] = pack2(unpack2(pack2(f32x2{acc[8 * m + 2 * i], acc[8 * m + 2 * i + 1]})) * f32x2{sc, sc});
      const unsigned s0 = __shfl_xor(h ? p[0] : p[2], 32), s1 = __shfl_xor(h ? p[1] : p[3], 32);
      const u32x4 o = h ? u32x4{s0, s1, p[2], p[3]} : u32x4{p[0], p[1], s0, s1};
      __builtin_amdgcn_raw_buffer_store_b128(o, rq, ((t0 + r) * 512 + cb * 64 + n0 + 16 * m + 8 * h) * 2, 0, 0);
    }
  } else if (wave == 7) {
    if (lane < 2) {
      const unsigned t = *(const volatile unsigned*)(lane ? ws + WS_K + tb * 2048 : (const char*)(Q + tok0 * 512 + cb * 64));
      asm volatile("" ::"v"(t));
    }
  } else if (wave == 6 && cb == 7 && tb < 8) {
    unsigned long long c0, c1;
    CODE_ADDR(k2_attn, c0);
    CODE_ADDR(k1_ln_qkv, c1);
    const long long n = (long long)(c1 - c0);
    unsigned t[4];
#pragma unroll
    for (int i = 0; i < 4; ++i) {
      const int off = (i * 64 + lane) * 128;
      t[i] = off < n ? *(const volatile unsigned*)(c0 + off) : 0u;
    }
#pragma unroll
    for (int i = 0; i < 4; ++i) asm volatile("" ::"v"(t[i]));
  } else if (wave < 6 && cb < 2) {
    const int t0 = (wave - 4) * 32;
    f32x16 acc = gemm_tile16(kvimg, ximg, t0, lane);
    const int key = tok0 + t0 + r;
    const int t = key >> 5;
    if (cb == 0) {
#pragma unroll
      for (int g = 0; g < 2; ++g) {
        bf16x4 o;
#pragma unroll
        for (int j = 0; j < 4; ++j) o[j] = (bf16)acc[4 * g + j];
        *(bf16x4*)(Kb + ((t * 2 + g) * 32 + r) * 8 + 4 * h) = o;
      }
    } else {
      const int kk = key & 31, sg = kk >> 4, rem = kk & 15;
      const int hh = rem >> 3, jj = rem & 7;
#pragma unroll
      for (int g = 0; g < 2; ++g) {
#pragma unroll
        for (int j = 0; j < 4; ++j) {
          const int d = 8 * g + 4 * h + j;
          Vf[(((t * 2 + sg) * 2 + hh) * 16 + d) * 8 + jj] = (bf16)acc[4 * g + j];
        }
      }
    }
  }
}

extern "C" void kernel_launch(void* const* d_in, const int* in_sizes, int n_in, void* d_out, int out_size,
                              void* d_ws, size_t ws_size, hipStream_t stream) {
  const bf16* x = (const bf16*)d_in[0];
  const bf16* wq = (const bf16*)d_in[1];
  const bf16* wk = (const bf16*)d_in[2];
  const bf16* wv = (const bf16*)d_in[3];
  const bf16* wo = (const bf16*)d_in[4];
  const bf16* wr = (const bf16*)d_in[6];
  const bf16* wg = (const bf16*)d_in[7];
  const bf16* wu = (const bf16*)d_in[8];
  const bf16* wd = (const bf16*)d_in[9];
  char* ws = (char*)d_ws;
  bf16* out = (bf16*)d_out;

  (void)hipFuncSetAttribute((const void*)k1_ln_qkv, hipFuncAttributeMaxDynamicSharedMemorySize, K1_LDS);
  (void)hipFuncSetAttribute((const void*)k2_attn, hipFuncAttributeMaxDynamicSharedMemorySize, ATT_LDS);

  k1_ln_qkv<<<256, 768, K1_LDS, stream>>>(x, wq, wk, wv, wr, ws);
  k2_attn<<<256, 768, ATT_LDS, stream>>>(ws, x, wg, wu, wd, wo, out);
}
```

```cpp
#include <hip/hip_runtime.h>
#include <hip/hip_bf16.h>

typedef __attribute__((ext_vector_type(8))) __bf16 bf16x8;
typedef __attribute__((ext_vector_type(16))) __bf16 bf16x16;
typedef __attribute__((ext_vector_type(8))) float f32x8;
typedef __attribute__((ext_vector_type(4))) __bf16 bf16x4;
typedef __attribute__((ext_vector_type(2))) __bf16 bf16x2;
typedef __attribute__((ext_vector_type(4))) short s16x4;
typedef __attribute__((ext_vector_type(8))) short s16x8;
typedef __attribute__((ext_vector_type(16))) float f32x16;
typedef __attribute__((ext_vector_type(4))) float f32x4;
typedef __attribute__((ext_vector_type(4))) unsigned u32x4;
typedef __attribute__((ext_vector_type(2))) unsigned u32x2;
typedef __attribute__((ext_vector_type(2))) float f32x2;
typedef __bf16 bf16;

#define LDS_AS __attribute__((address_space(3)))
#define GLB_AS __attribute__((address_space(1)))
#define DI __device__ __forceinline__

constexpr int S_ = 2048, D_ = 512, H_ = 32, DH_ = 16, F_ = 1376;

constexpr size_t WS_Q = 0;
constexpr size_t WS_O = WS_Q;
constexpr size_t WS_K = 2u << 20;
constexpr size_t WS_V = WS_K + 65536;
constexpr size_t WS_ST = WS_V + 65536;
constexpr size_t WS_R = WS_ST + 16384;
constexpr size_t WS_CNT = WS_R + 4096;
constexpr size_t WS_UPD = WS_CNT + 4096;
constexpr size_t WS_YP = WS_UPD + 4096;

constexpr int NFFN = 172;
constexpr int GSZ = 12, NGRP = 15;
constexpr size_t WS_CL = WS_YP + (NFFN + NGRP) * 4096;

DI float bf2f(bf16 v) { return (float)v; }
DI float rbf(float v) { return (float)(bf16)v; }
DI f32x2 unpack2(unsigned u) { return f32x2{__uint_as_float(u << 16), __uint_as_float(u & 0xffff0000u)}; }
DI unsigned pack2(f32x2 v) {
  unsigned r = __builtin_bit_cast(unsigned, __builtin_convertvector(v, bf16x2));
  asm("" : "+v"(r));
  return r;
}

DI unsigned route_key(float v, int tok) {
  const unsigned u = __float_as_uint(v) >> 16;
  const unsigned o = (u & 0x8000u) ? (~u & 0xffffu) : (u | 0x8000u);
  return (o << 16) | (unsigned)(0xffff - tok);
}
DI int key_tok(unsigned k) { return 0xffff - (int)(k & 0xffffu); }
DI float key_val(unsigned k) {
  const unsigned o = k >> 16;
  const unsigned u = (o & 0x8000u) ? (o & 0x7fffu) : (~o & 0xffffu);
  return __uint_as_float(u << 16);
}
DI unsigned ln2(f32x2 x, f32x2 mb2, f32x2 rinv2) { return pack2(unpack2(pack2(x - mb2)) * rinv2); }
DI void top2_wave(unsigned& k1, unsigned& k2, int width) {
  for (int o = width >> 1; o > 0; o >>= 1) {
    const unsigned a1 = __shfl_xor(k1, o), a2 = __shfl_xor(k2, o);
    k2 = max(min(k1, a1), max(k2, a2));
    k1 = max(k1, a1);
  }
}

template <int O>
DI void top2_step(unsigned& k1, unsigned& k2) {
  const unsigned a1 = __builtin_amdgcn_ds_swizzle(k1, (O << 10) | 31), a2 = __builtin_amdgcn_ds_swizzle(k2, (O << 10) | 31);
  k2 = max(min(k1, a1), max(k2, a2));
  k1 = max(k1, a1);
}

DI void glds16(const void* g, void* l) {
  __builtin_amdgcn_global_load_lds((const GLB_AS void*)g, (LDS_AS void*)l, 16, 0, 0);
}

DI void glds16_raw(const void* g, unsigned lds_off) {
  asm volatile("s_mov_b32 m0, %0\n s_nop 0\n global_load_lds_dwordx4 %1, off" ::"s"(lds_off), "v"(g) : "memory", "m0");
}

template <int CTRL>
DI float dpp_add(float v) {
  const int t = __builtin_amdgcn_update_dpp(0, __float_as_int(v), CTRL, 0xf, 0xf, true);
  return v + __int_as_float(t);
}
DI float sum8(float v) {
  v = dpp_add<0xB1>(v);
  v = dpp_add<0x4E>(v);
  v = dpp_add<0x141>(v);
  return v;
}

DI f32x16 pv_mfma(bf16x8 v, f32x8 p, f32x16 acc, int sel) {
  return __builtin_amdgcn_smfmac_f32_32x32x32_bf16(v, __builtin_bit_cast(bf16x16, p), acc, sel, 0, 0);
}

DI void swap32(unsigned& a, unsigned& b) {
  const u32x2 t = __builtin_amdgcn_permlane32_swap(a, b, false, false);
  a = t[0];
  b = t[1];
}
DI float max32(float v) {
  unsigned a = __float_as_uint(v), b = a;
  swap32(a, b);
  return fmaxf(__uint_as_float(a), __uint_as_float(b));
}

DI s16x4 tr_read(const char* p) {
  return __builtin_amdgcn_ds_read_tr16_b64_v4i16((LDS_AS s16x4*)p);
}

DI int wimg_off(int row, int ch) { return row * 128 + ((ch ^ (((row >> 1) & 1) << 2)) << 4); }

constexpr int XS_STRIDE = 1040;

DI void stage_w(const bf16* w, int col0, char* wimg, int wave, int nwaves, int lane) {
  for (int p = wave; p < 64; p += nwaves) {
    const int row = p * 8 + (lane >> 3), pc = lane & 7;
    const int ch = pc ^ (((row >> 1) & 1) << 2);
    glds16(w + row * 512 + col0 + ch * 8, wimg + p * 1024);
  }
}

constexpr int GP = 4;
DI f32x16 gemm_tile(const char* wimg, const char* ximg, int n0, int t0, int lane) {
  const int r = lane & 31, h = lane >> 5;
  const int q = (lane & 15) >> 2, p = lane & 3, blk = (lane >> 4) & 1;
  f32x16 acc;
#pragma unroll
  for (int i = 0; i < 16; ++i) acc[i] = 0.f;
  const char* xb = ximg + (t0 + r) * XS_STRIDE + 16 * h;
  const int col = n0 + 16 * blk + 4 * p;
  const int ch = col >> 3, sub = (col & 7) * 2;
  const char* wb0 = wimg + wimg_off(8 * h + q, ch) + sub;
  const char* wb1 = wimg + wimg_off(8 * h + q + 4, ch) + sub;
  s16x4 lo[32], hi[32];
  bf16x8 b[32];
#pragma unroll
  for (int ks = 0; ks < 32 + GP; ++ks) {
    if (ks < 32) {
      lo[ks] = tr_read(wb0 + ks * 2048);
      hi[ks] = tr_read(wb1 + ks * 2048);
      b[ks] = *(const bf16x8*)(xb + ks * 32);
    }
    if (ks >= GP) {
      const s16x8 a = __builtin_shufflevector(lo[ks - GP], hi[ks - GP], 0, 1, 2, 3, 4, 5, 6, 7);
      acc = __builtin_amdgcn_mfma_f32_32x32x16_bf16(__builtin_bit_cast(bf16x8, a), b[ks - GP], acc, 0, 0, 0);
      asm volatile("" : "+v"(acc));
    }
    __builtin_amdgcn_sched_barrier(0);
  }
  return acc;
}
DI f32x16 gemm_tile16(const char* wimg, const char* ximg, int t0, int lane) {
  const int r = lane & 31, h = lane >> 5;
  const int q = (lane & 15) >> 2, p = lane & 3;
  f32x16 acc;
#pragma unroll
  for (int i = 0; i < 16; ++i) acc[i] = 0.f;
  const char* xb = ximg + (t0 + r) * XS_STRIDE + 16 * h;
  const char* wb = wimg + (8 * h + q) * 32 + 8 * p;
  s16x4 lo[32], hi[32];
  bf16x8 b[32];
#pragma unroll
  for (int ks = 0; ks < 32 + GP; ++ks) {
    if (ks < 32) {
      lo[ks] = tr_read(wb + ks * 512);
      hi[ks] = tr_read(wb + ks * 512 + 128);
      b[ks] = *(const bf16x8*)(xb + ks * 32);
    }
    if (ks >= GP) {
      const s16x8 a = __builtin_shufflevector(lo[ks - GP], hi[ks - GP], 0, 1, 2, 3, 4, 5, 6, 7);
      acc = __builtin_amdgcn_mfma_f32_32x32x16_bf16(__builtin_bit_cast(bf16x8, a), b[ks - GP], acc, 0, 0, 0);
      asm volatile("" : "+v"(acc));
    }
    __builtin_amdgcn_sched_barrier(0);
  }
  return acc;
}

#define CODE_ADDR(sym, out)                                                                               \
  asm volatile("s_getpc_b64 s[20:21]\n s_add_u32 s20, s20, " #sym "@rel32@lo+4\n s_addc_u32 s21, s21, " #sym \
               "@rel32@hi+12\n s_mov_b64 %0, s[20:21]"                                                    \
               : "=s"(out)::"s20", "s21", "scc")

constexpr int NT = 16;
constexpr int CST = NT * 1024;
constexpr int ATT_LDS = 65536 * 2 + CST + 64 + 512 + 4 * 640;
constexpr int NC = 64 / NT;
constexpr int PSW = 53;
constexpr int SPIN_MAX = 1 << 22;

DI bool last_arrival(int* w, unsigned* gc, int n, int lane) {
  asm volatile("s_waitcnt vmcnt(0)" ::: "memory");
  if (lane == 0) {
    if (__hip_atomic_fetch_add(w, 1, __ATOMIC_RELAXED, __HIP_MEMORY_SCOPE_WORKGROUP) == 3) {
      const unsigned t = __hip_atomic_fetch_add((GLB_AS unsigned*)gc, 1u, __ATOMIC_RELAXED, __HIP_MEMORY_SCOPE_AGENT);
      __hip_atomic_store(w + 1, t == (unsigned)(n - 1) ? 2 : 1, __ATOMIC_RELAXED, __HIP_MEMORY_SCOPE_WORKGROUP);
    }
  }
  int r;
  while ((r = __hip_atomic_load(w + 1, __ATOMIC_RELAXED, __HIP_MEMORY_SCOPE_WORKGROUP)) == 0) __builtin_amdgcn_s_sleep(2);
  asm volatile("" ::: "memory");
  return r == 2;
}

DI void ffn_slice(int f, int j, const bf16* __restrict__ x, const float* __restrict__ st,
                  const unsigned* __restrict__ cand, unsigned* __restrict__ fdone,
                  const bf16* __restrict__ wg, const bf16* __restrict__ wu, const bf16* __restrict__ wd,
                  float* __restrict__ ypart, unsigned* __restrict__ gcnt,
                  float* __restrict__ upd, float* red, int* cnt, char* scr, int lane) {
  const int k0 = j * 128 + lane * 2;
  const int r = lane & 31, h = lane >> 5;
  const int kr = (lane & 51) | ((lane & 4) << 1) | ((lane & 8) >> 1);
  u32x4 wf[4];
  unsigned d2[8];
  {
    const __amdgpu_buffer_rsrc_t rg = __builtin_amdgcn_make_buffer_rsrc((void*)wg, 0, 512 * F_ * 2, 0x00020000);
    const __amdgpu_buffer_rsrc_t ru = __builtin_amdgcn_make_buffer_rsrc((void*)wu, 0, 512 * F_ * 2, 0x00020000);
    const __amdgpu_buffer_rsrc_t rd = __builtin_amdgcn_make_buffer_rsrc((void*)wd, 0, 512 * F_ * 2, 0x00020000);
    const int wo_ = kr * (F_ * 2), so = (j * 128 * F_ + f * 8) * 2;
#pragma unroll
    for (int t = 0; t < 2; ++t) {
      wf[2 * t] = __builtin_amdgcn_raw_buffer_load_b128(rg, wo_, so + t * (64 * F_ * 2), 0);
      wf[2 * t + 1] = __builtin_amdgcn_raw_buffer_load_b128(ru, wo_, so + t * (64 * F_ * 2), 0);
    }
#pragma unroll
    for (int c = 0; c < 8; ++c)
      d2[c] = __builtin_amdgcn_raw_buffer_load_b32(rd, 8 * r + 4 * h + c * 1024, (f * 8 * 512 + j * 128) * 2, 0);
  }
  const u32x2 cv = *(const u32x2*)(cand + (lane & 31) * 2);
  unsigned k1 = cv[0], k2 = cv[1];
  top2_step<16>(k1, k2);
  top2_step<8>(k1, k2);
  top2_step<4>(k1, k2);
  top2_step<2>(k1, k2);
  top2_step<1>(k1, k2);
  k1 = __builtin_amdgcn_readfirstlane(k1);
  k2 = __builtin_amdgcn_readfirstlane(k2);
  const int i1 = key_tok(k1), i2 = key_tok(k2);
  const f32x2 sa = *(const f32x2*)(st + i1 * 2), sb = *(const f32x2*)(st + i2 * 2);
  *(unsigned*)(scr + lane * 4) = ln2(unpack2(*(const unsigned*)(x + i1 * 512 + k0)), f32x2{sa.x, sa.x}, f32x2{sa.y, sa.y});
  *(unsigned*)(scr + 256 + lane * 4) = ln2(unpack2(*(const unsigned*)(x + i2 * 512 + k0)), f32x2{sb.x, sb.x}, f32x2{sb.y, sb.y});
  f32x16 zero;
#pragma unroll
  for (int i = 0; i < 16; ++i) zero[i] = 0.f;
  u32x4 ida, idb;
  {
    const int d = r - 8 * h;
    const unsigned one = 0x3f80u << ((d & 1) << 4);
#pragma unroll
    for (int q = 0; q < 4; ++q) {
      ida[q] = (d >> 1) == q ? one : 0u;
      idb[q] = (d >> 1) == q + 8 ? one : 0u;
    }
  }
  f32x16 gu = zero;
#pragma unroll
  for (int t = 0; t < 2; ++t) {
    f32x16 wt = __builtin_amdgcn_mfma_f32_32x32x16_bf16(__builtin_bit_cast(bf16x8, wf[2 * t]),
                                                        __builtin_bit_cast(bf16x8, ida), zero, 0, 0, 0);
    wt = __builtin_amdgcn_mfma_f32_32x32x16_bf16(__builtin_bit_cast(bf16x8, wf[2 * t + 1]),
                                                 __builtin_bit_cast(bf16x8, idb), wt, 0, 0, 0);
#pragma unroll
    for (int q = 0; q < 2; ++q) {
      u32x4 bw;
#pragma unroll
      for (int i = 0; i < 4; ++i) bw[i] = pack2(f32x2{wt[8 * q + 2 * i], wt[8 * q + 2 * i + 1]});
      const bf16x8 xf = *(const bf16x8*)(scr + (r & 1) * 256 + (r & 2) * 32 + (64 * t + 16 * q + 8 * h) * 2);
      gu = __builtin_amdgcn_mfma_f32_32x32x16_bf16(xf, __builtin_bit_cast(bf16x8, bw), gu, 0, 0, 0);
    }
  }
  {
    const float g0 = dpp_add<0x128>((lane & 8) ? gu[2] : gu[0]), g1 = dpp_add<0x128>((lane & 8) ? gu[3] : gu[1]);
    if (lane < 32) red[j * 32 + (lane & 16) + (lane & 15)] = (lane & 8) ? g1 : g0;
  }
  if (lane == 0) __hip_atomic_fetch_add(cnt, 1, __ATOMIC_RELEASE, __HIP_MEMORY_SCOPE_WORKGROUP);
  while (__hip_atomic_load(cnt, __ATOMIC_ACQUIRE, __HIP_MEMORY_SCOPE_WORKGROUP) < 4) __builtin_amdgcn_s_sleep(1);
  const int l = lane & 15;
  const float g = rbf((red[l] + red[32 + l]) + (red[64 + l] + red[96 + l]));
  const float u = rbf((red[16 + l] + red[48 + l]) + (red[80 + l] + red[112 + l]));
  const float hv = rbf(g * __builtin_amdgcn_rcpf(1.f + __expf(-g))) * u;
  if (lane < 24) *(bf16*)(scr + 512 + lane * 2) = (bf16)(lane < 16 ? hv : 0.f);
  const bf16x8 hf = *(const bf16x8*)(scr + 512 + ((r >> 1) == h ? (r & 1) * 16 : 32));
  u32x4 da, db;
#pragma unroll
  for (int c = 0; c < 4; ++c) {
    da[c] = __builtin_amdgcn_perm(d2[2 * c + 1], d2[2 * c], 0x05040100u);
    db[c] = __builtin_amdgcn_perm(d2[2 * c + 1], d2[2 * c], 0x07060302u);
  }
  const f32x16 ya = __builtin_amdgcn_mfma_f32_32x32x16_bf16(hf, __builtin_bit_cast(bf16x8, da), zero, 0, 0, 0);
  const f32x16 yb = __builtin_amdgcn_mfma_f32_32x32x16_bf16(hf, __builtin_bit_cast(bf16x8, db), zero, 0, 0, 0);
  const __amdgpu_buffer_rsrc_t rs = __builtin_amdgcn_make_buffer_rsrc(ypart, 0, (NFFN + NGRP) * 4096, 0x00020000);
  if (lane < 32) {
#pragma unroll
    for (int t = 0; t < 2; ++t)
      __builtin_amdgcn_raw_buffer_store_b128(__builtin_bit_cast(u32x4, f32x4{ya[t], yb[t], ya[2 + t], yb[2 + t]}), rs,
                                             t * 2048 + j * 512 + lane * 16, f * 4096, 16);
  }
  const int grp = f / GSZ, gs = min(GSZ, NFFN - grp * GSZ);
  if (!last_arrival(cnt + 1, gcnt + grp * 64, gs, lane)) return;
  const int voff = j * 1024 + lane * 16;
  f32x4 a = {0.f, 0.f, 0.f, 0.f};
  {
    u32x4 t[GSZ];
#pragma unroll
    for (int i = 0; i < GSZ; ++i) t[i] = __builtin_amdgcn_raw_buffer_load_b128(rs, voff, (grp * GSZ + min(i, gs - 1)) * 4096, 16);
#pragma unroll
    for (int i = 0; i < GSZ; ++i)
      if (i < gs) a += __builtin_bit_cast(f32x4, t[i]);
  }
  __builtin_amdgcn_raw_buffer_store_b128(__builtin_bit_cast(u32x4, a), rs, voff, (NFFN + grp) * 4096, 16);
  if (!last_arrival(cnt + 3, gcnt + NGRP * 64, NGRP, lane)) return;
  a = f32x4{0.f, 0.f, 0.f, 0.f};
  {
    u32x4 t[NGRP];
#pragma unroll
    for (int i = 0; i < NGRP; ++i) t[i] = __builtin_amdgcn_raw_buffer_load_b128(rs, voff, (NFFN + i) * 4096, 16);
#pragma unroll
    for (int i = 0; i < NGRP; ++i) a += __builtin_bit_cast(f32x4, t[i]);
  }
  const float gate = rbf(1.f / (1.f + __expf(-key_val(j < 2 ? k1 : k2))));
#pragma unroll
  for (int i = 0; i < 4; ++i) a[i] = rbf(rbf(a[i]) * gate);
  {
    const __amdgpu_buffer_rsrc_t ru = __builtin_amdgcn_make_buffer_rsrc(upd, 0, 4096, 0x00020000);
    __builtin_amdgcn_raw_buffer_store_b128(__builtin_bit_cast(u32x4, a), ru, voff, 0, 16);
  }
  asm volatile("s_waitcnt vmcnt(0)" ::: "memory");
  if (lane == 0 && __hip_atomic_fetch_add(cnt + 5, 1, __ATOMIC_RELAXED, __HIP_MEMORY_SCOPE_WORKGROUP) == 3)
    __hip_atomic_store((GLB_AS unsigned*)fdone, 1u, __ATOMIC_RELAXED, __HIP_MEMORY_SCOPE_AGENT);
}

extern "C" __global__ __launch_bounds__(768) void k2_attn(char* __restrict__ ws, const bf16* __restrict__ x,
                                               const bf16* __restrict__ wg, const bf16* __restrict__ wu,
                                               const bf16* __restrict__ wd, const bf16* __restrict__ wo,
                                               bf16* __restrict__ out) {
  const bf16* Q = (const bf16*)(ws + WS_Q);
  const bf16* Kb = (const bf16*)(ws + WS_K);
  bf16* O = (bf16*)(ws + WS_O);
  const float* st = (const float*)(ws + WS_ST);
  const unsigned* cand = (const unsigned*)(ws + WS_R);
  float* ypart = (float*)(ws + WS_YP);
  unsigned* gcnt = (unsigned*)(ws + WS_CNT);
  float* upd = (float*)(ws + WS_UPD);
  extern __shared__ __attribute__((aligned(16))) char smem[];
  char* kl = smem;
  char* vl = smem + 65536;
  char* cst = smem + 131072;
  const int tid = threadIdx.x, lane = tid & 63;
  const int wave = __builtin_amdgcn_readfirstlane(tid >> 6);

  if (wave >= 8) {
    int* cnt = (int*)(smem + 131072 + CST + 32);
    if (tid < 512 + 6) cnt[tid - 512] = 0;
#pragma unroll
    for (int i = 0; i < 26; ++i) {
      const int q = (wave - 8) + 4 * i, piece = q < 48 ? 16 + q : 24 + q;
      glds16_raw((const char*)Kb + piece * 1024 + lane * 16, (unsigned)(size_t)(LDS_AS char*)smem + piece * 1024);
    }
    asm volatile("s_waitcnt lgkmcnt(0)\n s_barrier\n s_waitcnt vmcnt(0)\n s_barrier" ::: "memory");
    unsigned* clc = (unsigned*)(ws + WS_CL);
    __builtin_amdgcn_s_setprio(3);
    const int fs = (blockIdx.x & 7) * 22 + (blockIdx.x >> 3);
    if (blockIdx.x < 176 && fs < NFFN) {
      ffn_slice(fs, wave - 8, x, st, cand, clc + 32 * 64, wg, wu, wd, ypart, gcnt, upd,
                (float*)(smem + 131072 + CST + 64), cnt, smem + 131072 + CST + 576 + (wave - 8) * 640, lane);
    }
    const int tb = (blockIdx.x >> 6) * 8 + (blockIdx.x & 7), cb = (blockIdx.x >> 3) & 7, tok0 = tb * 64;
    const int sw = wave - 8, r = lane & 31, h = lane >> 5;
    char* ximg = smem;
    char* wimg = smem + 64 * XS_STRIDE;
    u32x4 wreg[16];
#pragma unroll
    for (int i = 0; i < 16; ++i) {
      const int row = (sw + 4 * i) * 8 + (lane >> 3);
      wreg[i] = *(const u32x4*)(wo + row * 512 + cb * 64 + (((lane & 7) ^ (((row >> 1) & 1) << 2)) << 3));
    }
    const int n0 = (sw & 1) * 32, t0 = (sw >> 1) * 32;
    const int tok = tok0 + t0 + r;
    u32x2 xv[4], nv[4];
#pragma unroll
    for (int g = 0; g < 4; ++g) xv[g] = *(const u32x2*)(x + tok * 512 + cb * 64 + n0 + 8 * g + 4 * h);
    const f32x2 sx = *(const f32x2*)(st + tok * 2);
    int rsel;
    {
      const u32x2 cv = *(const u32x2*)(cand + (lane & 31) * 2);
      unsigned k1 = cv[0], k2 = cv[1];
      top2_step<16>(k1, k2);
      top2_step<8>(k1, k2);
      top2_step<4>(k1, k2);
      top2_step<2>(k1, k2);
      top2_step<1>(k1, k2);
      rsel = tok == key_tok(k1) ? 0 : tok == key_tok(k2) ? 1 : -1;
    }
#pragma unroll
    for (int g = 0; g < 4; ++g) {
#pragma unroll
      for (int jj = 0; jj < 2; ++jj) {
        unsigned nr = ln2(unpack2(xv[g][jj]), f32x2{sx.x, sx.x}, f32x2{sx.y, sx.y});
        asm volatile("" : "+v"(nr));
        nv[g][jj] = nr;
      }
    }
    if (wave == 11 && lane == 0) {
      const unsigned t = *(const volatile unsigned*)(out + tok0 * 512 + cb * 64);
      asm volatile("" ::"v"(t));
    }
#pragma unroll
    for (int i = 0; i < 16; ++i) asm volatile("" : "+v"(wreg[i]));
    asm volatile("s_barrier" ::: "memory");
    if (tid == 512) __hip_atomic_fetch_add((GLB_AS unsigned*)(clc + tb * 64), 1u, __ATOMIC_RELAXED, __HIP_MEMORY_SCOPE_AGENT);
#pragma unroll
    for (int i = 0; i < 16; ++i) *(u32x4*)(wimg + (sw + 4 * i) * 1024 + lane * 16) = wreg[i];
    if (__any(rsel >= 0)) {
      int spin = 0;
      while (__hip_atomic_load((GLB_AS unsigned*)(clc + 32 * 64), __ATOMIC_RELAXED, __HIP_MEMORY_SCOPE_AGENT) == 0 && ++spin < SPIN_MAX)
        __builtin_amdgcn_s_sleep(1);
      asm volatile("" ::: "memory");
      const __amdgpu_buffer_rsrc_t ru = __builtin_amdgcn_make_buffer_rsrc(upd, 0, 4096, 0x00020000);
      if (rsel >= 0) {
#pragma unroll
        for (int g = 0; g < 4; ++g) {
          const f32x4 u = __builtin_bit_cast(f32x4, __builtin_amdgcn_raw_buffer_load_b128(ru, (rsel * 512 + cb * 64 + n0 + 8 * g + 4 * h) * 4, 0, 16));
          nv[g][0] = pack2(unpack2(nv[g][0]) + f32x2{u[0], u[1]});
          nv[g][1] = pack2(unpack2(nv[g][1]) + f32x2{u[2], u[3]});
        }
      }
    }
    if (wave == 8) {
      int spin = 0;
      while (__hip_atomic_load((GLB_AS unsigned*)(clc + tb * 64), __ATOMIC_RELAXED, __HIP_MEMORY_SCOPE_AGENT) < 8 && ++spin < SPIN_MAX)
        __builtin_amdgcn_s_sleep(1);
    }
    asm volatile("s_barrier" ::: "memory");
    {
      const __amdgpu_buffer_rsrc_t ro = __builtin_amdgcn_make_buffer_rsrc(O + tok0 * 512, 0, 65536, 0x00020000);
      u32x4 t[16];
#pragma unroll
      for (int i = 0; i < 16; ++i) t[i] = __builtin_amdgcn_raw_buffer_load_b128(ro, lane * 16, (sw * 16 + i) * 1024, 16);
#pragma unroll
      for (int i = 0; i < 16; ++i) *(u32x4*)(ximg + (sw * 16 + i) * XS_STRIDE + lane * 16) = t[i];
    }
    asm volatile("s_waitcnt lgkmcnt(0)\n s_barrier" ::: "memory");
    f32x16 acc = gemm_tile(wimg, ximg, n0, t0, lane);
#pragma unroll
    for (int g = 0; g < 4; ++g) {
      u32x2 o;
#pragma unroll
      for (int jj = 0; jj < 2; ++jj) {
        const unsigned xr = xv[g][jj], nr = nv[g][jj];
        const f32x2 a = unpack2(pack2(f32x2{acc[4 * g + 2 * jj], acc[4 * g + 2 * jj + 1]}));
        o[jj] = pack2(unpack2(pack2(unpack2(xr) + a)) + unpack2(nr));
      }
      *(u32x2*)(out + tok * 512 + cb * 64 + n0 + 8 * g + 4 * h) = o;
    }
    return;
  }

  const int r = lane & 31, h = lane >> 5;
  const int token = ((blockIdx.x >> 6) * 8 + (blockIdx.x & 7)) * 64 + ((blockIdx.x >> 3) & 7) * 8 + wave;
  bf16x8 qf;
  asm volatile("global_load_dwordx4 %0, %1, off" : "=v"(qf) : "v"(Q + token * 512 + r * 16 + 8 * h) : "memory");
  const unsigned lds0 = (unsigned)(size_t)(LDS_AS char*)smem;
#pragma unroll
  for (int i = 0; i < 3; ++i) {
    const int piece = (i < 2) ? (wave + i * 8) : (64 + wave);
    glds16_raw((const char*)Kb + piece * 1024 + lane * 16, lds0 + piece * 1024);
  }
  {
    const unsigned cv = ((tid & 15) == 0 || (tid & 15) == 4) ? 0x3f803f80u : 0u;
#pragma unroll
    for (int i = 0; i < CST / 8192; ++i) *(u32x4*)(cst + i * 8192 + tid * 16) = u32x4{cv, cv, cv, cv};
  }
  int vo = (r < 16) ? 65536 + (h * 16 + r) * 16 : 131072 + (h * 16 + r - 16) * 16;
  int vstep = (r < 16) ? NT * 1024 : 0;
  int sel = 0xdddd;
  f32x16 zero;
#pragma unroll
  for (int i = 0; i < 16; ++i) zero[i] = 0.f;
  f32x16 acc = zero;
  asm volatile("" : "+v"(sel), "+v"(acc), "+v"(vstep), "+v"(vo));
  const char* kp0 = kl + lane * 16;
  const char* vp0 = smem + vo;
  asm volatile("s_waitcnt vmcnt(0) lgkmcnt(0)\n s_barrier" ::: "memory");

  constexpr float HEAD = 40.f;
  f32x16 negm, xs;
  bf16x8 kfn;
  {
    bf16x8 kf[4];
    f32x16 s0[4];
#pragma unroll
    for (int t = 0; t < 4; ++t) kf[t] = *(const bf16x8*)(kp0 + t * 1024);
#pragma unroll
    for (int t = 0; t < 4; ++t) {
      s0[t] = __builtin_amdgcn_mfma_f32_32x32x16_bf16(kf[t], qf, zero, 0, 0, 0);
      asm volatile("" : "+v"(s0[t]));
    }
    __builtin_amdgcn_sched_barrier(0);
    float mx = -INFINITY;
#pragma unroll
    for (int t = 0; t < 4; ++t) {
#pragma unroll
      for (int i = 0; i < 16; i += 2) mx = fmaxf(fmaxf(mx, s0[t][i]), s0[t][i + 1]);
    }
    const float nm = -(floorf(max32(mx)) + HEAD);
#pragma unroll
    for (int i = 0; i < 16; ++i) {
      xs[i] = s0[0][i] + nm;
      negm[i] = nm;
    }
    kfn = kf[1];
  }
  asm volatile("" : "+v"(negm));
  {
    const char* kp = kp0;
    const char* vp = vp0;
    f32x16 x = xs;
    if (wave < 4) __builtin_amdgcn_s_setprio(2);
    else __builtin_amdgcn_s_setprio(1);
    for (int c = 0; c < NC; ++c) {
      bool grown;
#pragma unroll
      for (int t = 0; t < NT; ++t) {
        const bf16x8 kf = kfn;
        kfn = *(const bf16x8*)(kp + (t + 2) * 1024);
        const bf16x8 vf0 = *(const bf16x8*)(vp + (t * 2) * 512);
        const bf16x8 vf1 = *(const bf16x8*)(vp + (t * 2 + 1) * 512);
        f32x8 p0, p1;
#pragma unroll
        for (int j = 0; j < 4; ++j) p0[j] = __builtin_amdgcn_exp2f(x[j]);
        __builtin_amdgcn_sched_barrier(0);
        f32x16 xn = __builtin_amdgcn_mfma_f32_32x32x16_bf16(kf, qf, negm, 0, 0, 0);
        asm volatile("" : "+v"(xn));
        __builtin_amdgcn_sched_barrier(0);
#pragma unroll
        for (int j = 4; j < 8; ++j) p0[j] = __builtin_amdgcn_exp2f(x[j]);
        if (t == NT - 1) grown = __any(!(acc[8] <= 4.3e9f));
        acc = pv_mfma(vf0, p0, acc, sel);
#pragma unroll
        for (int j = 0; j < 8; ++j) p1[j] = __builtin_amdgcn_exp2f(x[8 + j]);
        acc = pv_mfma(vf1, p1, acc, sel);
        if (t == 7 && c == 0) asm volatile("s_barrier" ::: "memory");
        if (c * NT + t == PSW && wave < 4) __builtin_amdgcn_s_setprio(0);
        __builtin_amdgcn_sched_barrier(0);
        x = xn;
      }
      kp += NT * 1024;
      vp += vstep;
      if (grown) {
        const int e = max(__builtin_amdgcn_frexp_expf(acc[8]) - 1 + (int)HEAD, 0);
#pragma unroll
        for (int i = 0; i < 9; ++i) acc[i] = __builtin_amdgcn_ldexpf(acc[i], -e);
        const float ef = (float)e;
#pragma unroll
        for (int i = 0; i < 16; ++i) {
          negm[i] -= ef;
          x[i] -= ef;
        }
        asm volatile("" : "+v"(negm));
      }
    }
  }
  {
    float chk = 0.f;
#pragma unroll
    for (int i = 0; i < 9; ++i) chk = __builtin_fmaf(acc[i], 0.f, chk);
    if (__any(chk != 0.f)) {
      f32x16 oacc = zero;
      float mrun = -INFINITY;
      const char* kp = kp0;
      const char* vp = vp0;
      for (int t = 0; t < 64; ++t) {
        bf16x8 kf = *(const bf16x8*)(kp);
        f32x16 x = __builtin_amdgcn_mfma_f32_32x32x16_bf16(kf, qf, zero, 0, 0, 0);
        float mx = x[0];
#pragma unroll
        for (int i = 1; i < 16; ++i) mx = fmaxf(mx, x[i]);
        const float mn = fmaxf(mrun, max32(mx));
        const float al = __builtin_amdgcn_exp2f(mrun - mn);
        mrun = mn;
#pragma unroll
        for (int i = 0; i < 16; ++i) oacc[i] *= al;
#pragma unroll
        for (int s = 0; s < 2; ++s) {
          f32x8 pf;
#pragma unroll
          for (int j = 0; j < 8; ++j) pf[j] = __builtin_amdgcn_exp2f(x[8 * s + j] - mn);
          bf16x8 vf = *(const bf16x8*)(vp + ((t & (NT - 1)) * 2 + s) * 512);
          oacc = pv_mfma(vf, pf, oacc, sel);
        }
        kp += 1024;
        if ((t & (NT - 1)) == NT - 1) vp += vstep;
      }
      acc = oacc;
    }
  }
  const float inv = 1.f / acc[8];
  {
    unsigned a0 = pack2(f32x2{acc[0] * inv, acc[1] * inv}), a1 = pack2(f32x2{acc[2] * inv, acc[3] * inv});
    unsigned b0 = pack2(f32x2{acc[4] * inv, acc[5] * inv}), b1 = pack2(f32x2{acc[6] * inv, acc[7] * inv});
    swap32(a0, b0);
    swap32(a1, b1);
    const u32x4 o = {a0, a1, b0, b1};
    const __amdgpu_buffer_rsrc_t ro = __builtin_amdgcn_make_buffer_rsrc(O + token * 512, 0, 1024, 0x00020000);
    __builtin_amdgcn_raw_buffer_store_b128(o, ro, r * 32 + h * 16, 0, 16);
  }
  asm volatile("s_waitcnt vmcnt(0)\n s_barrier" ::: "memory");
}

constexpr int K1_LDS = 64 * XS_STRIDE + 65536 + 16384 + 256;

extern "C" __global__ __launch_bounds__(768) void k1_ln_qkv(const bf16* __restrict__ x, const bf16* __restrict__ wq,
                                                 const bf16* __restrict__ wk, const bf16* __restrict__ wv,
                                                 const bf16* __restrict__ wr, char* __restrict__ ws) {
  float* st = (float*)(ws + WS_ST);
  bf16* Q = (bf16*)(ws + WS_Q);
  bf16* Kb = (bf16*)(ws + WS_K);
  bf16* Vf = (bf16*)(ws + WS_V);
  unsigned* cand = (unsigned*)(ws + WS_R);
  unsigned* gcnt = (unsigned*)(ws + WS_CNT);
  extern __shared__ __attribute__((aligned(16))) char smem[];
  char* ximg = smem;
  char* wimg = smem + 64 * XS_STRIDE;
  char* kvimg = smem + 64 * XS_STRIDE + 65536;
  float* rs = (float*)(smem + 64 * XS_STRIDE + 65536 + 16384);
  const int cb = blockIdx.x >> 5, tb = blockIdx.x & 31;
  const int tid = threadIdx.x, lane = tid & 63;
  const int wave = __builtin_amdgcn_readfirstlane(tid >> 6);
  const int tok0 = tb * 64;

  if (wave >= 8) {
    if (blockIdx.x == 255 && tid >= 768 - (NGRP + 1)) gcnt[(767 - tid) * 64] = 0;
    if (blockIdx.x == 254 && tid >= 768 - 33) ((unsigned*)(ws + WS_CL))[(767 - tid) * 64] = 0;
    stage_w(wq, cb * 64, wimg, wave - 8, 4, lane);
    if (cb < 2) {
      const bf16* w = cb == 0 ? wk : wv;
#pragma unroll
      for (int i = 0; i < 4; ++i) {
        const int p = wave - 8 + 4 * i;
        glds16((const char*)w + p * 1024 + lane * 16, kvimg + p * 1024);
      }
    }
    __syncthreads();
    if (cb == 3 && wave == 8) {
      unsigned k1 = route_key(rs[lane], tok0 + lane), k2 = 0;
      top2_wave(k1, k2, 64);
      if (lane == 0) *(u32x2*)(cand + tb * 2) = u32x2{k1, k2};
    }
    return;
  }

  const int tl = wave * 8 + (lane >> 3), c8 = lane & 7;
  u32x4 xv[8];
#pragma unroll
  for (int i = 0; i < 8; ++i) xv[i] = *(const u32x4*)(x + (tok0 + tl) * 512 + (c8 + 8 * i) * 8);
  u32x4 wrv[8];
  if (cb == 3) {
#pragma unroll
    for (int i = 0; i < 8; ++i) wrv[i] = *(const u32x4*)(wr + (c8 + 8 * i) * 8);
  }

  f32x2 xf[32];
#pragma unroll
  for (int i = 0; i < 32; ++i) xf[i] = unpack2(xv[i >> 2][i & 3]);
  f32x2 s2 = {0.f, 0.f};
#pragma unroll
  for (int i = 0; i < 32; ++i) s2 += xf[i];
  const float mean = sum8(s2.x + s2.y) * (1.f / 512.f);
  const f32x2 mean2 = {mean, mean};
  f32x2 v2 = {0.f, 0.f};
#pragma unroll
  for (int i = 0; i < 32; ++i) { const f32x2 d = xf[i] - mean2; v2 += d * d; }
  const float vs = sum8(v2.x + v2.y);
  const float mb = rbf(mean);
  const float vb = rbf(vs * (1.f / 512.f));
  const float den = rbf(sqrtf(rbf(vb + 1e-5f)));
  const float rinv = __builtin_amdgcn_rcpf(den);
  const f32x2 mb2 = {mb, mb}, rinv2 = {rinv, rinv};
  u32x4 o[8];
#pragma unroll
  for (int i = 0; i < 8; ++i) {
#pragma unroll
    for (int j = 0; j < 4; ++j) o[i][j] = ln2(xf[4 * i + j], mb2, rinv2);
    *(u32x4*)(ximg + tl * XS_STRIDE + (c8 + 8 * i) * 16) = o[i];
  }
  if (cb == 2 && c8 == 0) *(f32x2*)(st + (tok0 + tl) * 2) = f32x2{mb, rinv};
  if (cb == 3) {
    float rd = 0.f;
#pragma unroll
    for (int i = 0; i < 8; ++i) {
#pragma unroll
      for (int j = 0; j < 4; ++j) {
        const unsigned a = o[i][j], b = wrv[i][j];
        rd = __builtin_amdgcn_fdot2_f32_bf16(__builtin_bit_cast(bf16x2, a), __builtin_bit_cast(bf16x2, b), rd, false);
      }
    }
    rd = sum8(rd);
    if (c8 == 0) rs[tl] = rbf(rd);
  }
  __syncthreads();

  const int r = lane & 31, h = lane >> 5;
  if (wave < 4) {
    const int n0 = (wave & 1) * 32, t0 = (wave >> 1) * 32;
    f32x16 acc = gemm_tile(wimg, ximg, n0, t0, lane);
    const float sc = 0.25f * 1.4426950408889634f;
    const __amdgpu_buffer_rsrc_t rq = __builtin_amdgcn_make_buffer_rsrc(Q + tok0 * 512, 0, 65536, 0x00020000);
#pragma unroll
    for (int m = 0; m < 2; ++m) {
      unsigned p[4];
#pragma unroll
      for (int i = 0; i < 4; ++i) p[i] = pack2(unpack2(pack2(f32x2{acc[8 * m + 2 * i], acc[8 * m + 2 * i + 1]})) * f32x2{sc, sc});
      swap32(p[0], p[2]);
      swap32(p[1], p[3]);
      const u32x4 o = {p[0], p[1], p[2], p[3]};
      __builtin_amdgcn_raw_buffer_store_b128(o, rq, ((t0 + r) * 512 + cb * 64 + n0 + 16 * m + 8 * h) * 2, 0, 0);
    }
  } else if (wave == 7) {
    if (lane < 2) {
      const unsigned t = *(const volatile unsigned*)(lane ? ws + WS_K + tb * 2048 : (const char*)(Q + tok0 * 512 + cb * 64));
      asm volatile("" ::"v"(t));
    }
  } else if (wave == 6 && cb == 7 && tb < 8) {
    unsigned long long c0, c1;
    CODE_ADDR(k2_attn, c0);
    CODE_ADDR(k1_ln_qkv, c1);
    const long long n = (long long)(c1 - c0);
    unsigned t[4];
#pragma unroll
    for (int i = 0; i < 4; ++i) {
      const int off = (i * 64 + lane) * 128;
      t[i] = off < n ? *(const volatile unsigned*)(c0 + off) : 0u;
    }
#pragma unroll
    for (int i = 0; i < 4; ++i) asm volatile("" ::"v"(t[i]));
  } else if (wave < 6 && cb < 2) {
    const int t0 = (wave - 4) * 32;
    f32x16 acc = gemm_tile16(kvimg, ximg, t0, lane);
    const int key = tok0 + t0 + r;
    const int t = key >> 5;
    if (cb == 0) {
#pragma unroll
      for (int g = 0; g < 2; ++g) {
        bf16x4 o;
#pragma unroll
        for (int j = 0; j < 4; ++j) o[j] = (bf16)acc[4 * g + j];
        *(bf16x4*)(Kb + ((t * 2 + g) * 32 + r) * 8 + 4 * h) = o;
      }
    } else {
      const int kk = key & 31, sg = kk >> 4, rem = kk & 15;
      const int hh = rem >> 3, jj = rem & 7;
#pragma unroll
      for (int g = 0; g < 2; ++g) {
#pragma unroll
        for (int j = 0; j < 4; ++j) {
          const int d = 8 * g + 4 * h + j;
          Vf[(((t * 2 + sg) * 2 + hh) * 16 + d) * 8 + jj] = (bf16)acc[4 * g + j];
        }
      }
    }
  }
}

extern "C" void kernel_launch(void* const* d_in, const int* in_sizes, int n_in, void* d_out, int out_size,
                              void* d_ws, size_t ws_size, hipStream_t stream) {
  const bf16* x = (const bf16*)d_in[0];
  const bf16* wq = (const bf16*)d_in[1];
  const bf16* wk = (const bf16*)d_in[2];
  const bf16* wv = (const bf16*)d_in[3];
  const bf16* wo = (const bf16*)d_in[4];
  const bf16* wr = (const bf16*)d_in[6];
  const bf16* wg = (const bf16*)d_in[7];
  const bf16* wu = (const bf16*)d_in[8];
  const bf16* wd = (const bf16*)d_in[9];
  char* ws = (char*)d_ws;
  bf16* out = (bf16*)d_out;

  (void)hipFuncSetAttribute((const void*)k1_ln_qkv, hipFuncAttributeMaxDynamicSharedMemorySize, K1_LDS);
  (void)hipFuncSetAttribute((const void*)k2_attn, hipFuncAttributeMaxDynamicSharedMemorySize, ATT_LDS);

  k1_ln_qkv<<<256, 768, K1_LDS, stream>>>(x, wq, wk, wv, wr, ws);
  k2_attn<<<256, 768, ATT_LDS, stream>>>(ws, x, wg, wu, wd, wo, out);
}
```

```cpp
#include <hip/hip_runtime.h>
#include <hip/hip_bf16.h>

typedef __attribute__((ext_vector_type(8))) __bf16 bf16x8;
typedef __attribute__((ext_vector_type(16))) __bf16 bf16x16;
typedef __attribute__((ext_vector_type(8))) float f32x8;
typedef __attribute__((ext_vector_type(4))) __bf16 bf16x4;
typedef __attribute__((ext_vector_type(2))) __bf16 bf16x2;
typedef __attribute__((ext_vector_type(4))) short s16x4;
typedef __attribute__((ext_vector_type(8))) short s16x8;
typedef __attribute__((ext_vector_type(16))) float f32x16;
typedef __attribute__((ext_vector_type(4))) float f32x4;
typedef __attribute__((ext_vector_type(4))) unsigned u32x4;
typedef __attribute__((ext_vector_type(2))) unsigned u32x2;
typedef __attribute__((ext_vector_type(2))) float f32x2;
typedef __attribute__((ext_vector_type(16))) unsigned u32x16;
typedef __bf16 bf16;

#define LDS_AS __attribute__((address_space(3)))
#define GLB_AS __attribute__((address_space(1)))
#define CONST_AS __attribute__((address_space(4)))
#define DI __device__ __forceinline__

constexpr int S_ = 2048, D_ = 512, H_ = 32, DH_ = 16, F_ = 1376;

constexpr size_t WS_Q = 0;
constexpr size_t WS_O = WS_Q;
constexpr size_t WS_K = 2u << 20;
constexpr size_t WS_V = WS_K + 65536;
constexpr size_t WS_ST = WS_V + 65536;
constexpr size_t WS_R = WS_ST + 16384;
constexpr size_t WS_CX = WS_R + 4096;
constexpr size_t WS_CNT = WS_CX + 65536;
constexpr int NCNT = 64 + 16 + 32;
constexpr size_t WS_FLG = WS_CNT + 64 * 256;
constexpr size_t WS_CL = WS_FLG + 16 * 256;
constexpr size_t WS_UPD = WS_CNT + NCNT * 256;
constexpr size_t WS_YP = WS_UPD + 4096;
constexpr int NFFN = 172;

DI float bf2f(bf16 v) { return (float)v; }
DI float rbf(float v) { return (float)(bf16)v; }
DI f32x2 unpack2(unsigned u) { return f32x2{__uint_as_float(u << 16), __uint_as_float(u & 0xffff0000u)}; }
DI unsigned pack2(f32x2 v) {
  unsigned r = __builtin_bit_cast(unsigned, __builtin_convertvector(v, bf16x2));
  asm("" : "+v"(r));
  return r;
}

DI unsigned route_key(float v, int tok) {
  const unsigned u = __float_as_uint(v) >> 16;
  const unsigned o = (u & 0x8000u) ? (~u & 0xffffu) : (u | 0x8000u);
  return (o << 16) | (unsigned)((0x7ff - tok) << 1);
}
DI int key_tok(unsigned k) { return 0x7ff - (int)((k >> 1) & 0x7ffu); }
DI int key_row(unsigned k) { return (key_tok(k) >> 6) * 2 + (int)(k & 1u); }
DI float key_val(unsigned k) {
  const unsigned o = k >> 16;
  const unsigned u = (o & 0x8000u) ? (o & 0x7fffu) : (~o & 0xffffu);
  return __uint_as_float(u << 16);
}
DI unsigned ln2(f32x2 x, f32x2 mb2, f32x2 rinv2) { return pack2(unpack2(pack2(x - mb2)) * rinv2); }
DI void top2_wave(unsigned& k1, unsigned& k2, int width) {
  for (int o = width >> 1; o > 0; o >>= 1) {
    const unsigned a1 = __shfl_xor(k1, o), a2 = __shfl_xor(k2, o);
    k2 = max(min(k1, a1), max(k2, a2));
    k1 = max(k1, a1);
  }
}

DI void glds16(const void* g, void* l) {
  __builtin_amdgcn_global_load_lds((const GLB_AS void*)g, (LDS_AS void*)l, 16, 0, 0);
}

DI void glds16_raw(const void* g, unsigned lds_off) {
  asm volatile("s_mov_b32 m0, %0\n s_nop 0\n global_load_lds_dwordx4 %1, off" ::"s"(lds_off), "v"(g) : "memory", "m0");
}

DI void blds16(__amdgpu_buffer_rsrc_t rs, unsigned voff, unsigned soff, unsigned lds_off) {
  asm volatile("s_mov_b32 m0, %0\n s_nop 0\n buffer_load_dwordx4 %1, %2, %3 offen lds" ::"s"(lds_off), "v"(voff), "s"(rs), "s"(soff) : "memory", "m0");
}
DI void blds16x2(__amdgpu_buffer_rsrc_t rs, unsigned voff, unsigned soff, unsigned lds_off) {
  asm volatile("s_mov_b32 m0, %0\n s_nop 0\n buffer_load_dwordx4 %1, %2, %3 offen lds\n buffer_load_dwordx4 %1, %2, %3 offen offset:1024 lds"
               ::"s"(lds_off), "v"(voff), "s"(rs), "s"(soff) : "memory", "m0");
}
DI void count_up(__amdgpu_buffer_rsrc_t rs, unsigned one, unsigned soff) {
  asm volatile("s_mov_b64 exec, 1\n buffer_atomic_add %0, off, %1, %2\n s_mov_b64 exec, -1" ::"v"(one), "s"(rs), "s"(soff) : "memory");
}
#define LDSP(T, off) ((LDS_AS T*)(unsigned long)(off))

template <int CTRL>
DI float dpp_add(float v) {
  const int t = __builtin_amdgcn_update_dpp(0, __float_as_int(v), CTRL, 0xf, 0xf, true);
  return v + __int_as_float(t);
}
DI float sum8(float v) {
  v = dpp_add<0xB1>(v);
  v = dpp_add<0x4E>(v);
  v = dpp_add<0x141>(v);
  return v;
}

DI f32x16 pv_mfma(bf16x8 v, f32x8 p, f32x16 acc, int sel) {
  return __builtin_amdgcn_smfmac_f32_32x32x32_bf16(v, __builtin_bit_cast(bf16x16, p), acc, sel, 0, 0);
}

DI void swap32(unsigned& a, unsigned& b) {
  const u32x2 t = __builtin_amdgcn_permlane32_swap(a, b, false, false);
  a = t[0];
  b = t[1];
}
DI float max32(float v) {
  unsigned a = __float_as_uint(v), b = a;
  swap32(a, b);
  return fmaxf(__uint_as_float(a), __uint_as_float(b));
}

DI s16x4 tr_read(const char* p) {
  return __builtin_amdgcn_ds_read_tr16_b64_v4i16((LDS_AS s16x4*)p);
}
DI bf16x8 tr_read8(unsigned lo, unsigned hi) {
  const s16x4 a = __builtin_amdgcn_ds_read_tr16_b64_v4i16(LDSP(s16x4, lo)), b = __builtin_amdgcn_ds_read_tr16_b64_v4i16(LDSP(s16x4, hi));
  return __builtin_bit_cast(bf16x8, __builtin_shufflevector(a, b, 0, 1, 2, 3, 4, 5, 6, 7));
}

DI int wimg_off(int row, int ch) { return row * 128 + ((ch ^ (((row >> 1) & 1) << 2)) << 4); }

constexpr int XS_STRIDE = 1040;

DI void stage_w(const bf16* w, int col0, char* wimg, int wave, int nwaves, int lane) {
  for (int p = wave; p < 64; p += nwaves) {
    const int row = p * 8 + (lane >> 3), pc = lane & 7;
    const int ch = pc ^ (((row >> 1) & 1) << 2);
    glds16(w + row * 512 + col0 + ch * 8, wimg + p * 1024);
  }
}

constexpr int GP = 4;
DI f32x16 gemm_tile(const char* wimg, const char* ximg, int n0, int t0, int lane) {
  const int r = lane & 31, h = lane >> 5;
  const int q = (lane & 15) >> 2, p = lane & 3, blk = (lane >> 4) & 1;
  f32x16 acc;
#pragma unroll
  for (int i = 0; i < 16; ++i) acc[i] = 0.f;
  const char* xb = ximg + (t0 + r) * XS_STRIDE + 16 * h;
  const int col = n0 + 16 * blk + 4 * p;
  const int ch = col >> 3, sub = (col & 7) * 2;
  const char* wb0 = wimg + wimg_off(8 * h + q, ch) + sub;
  const char* wb1 = wimg + wimg_off(8 * h + q + 4, ch) + sub;
  s16x4 lo[32], hi[32];
  bf16x8 b[32];
#pragma unroll
  for (int ks = 0; ks < 32 + GP; ++ks) {
    if (ks < 32) {
      lo[ks] = tr_read(wb0 + ks * 2048);
      hi[ks] = tr_read(wb1 + ks * 2048);
      b[ks] = *(const bf16x8*)(xb + ks * 32);
    }
    if (ks >= GP) {
      const s16x8 a = __builtin_shufflevector(lo[ks - GP], hi[ks - GP], 0, 1, 2, 3, 4, 5, 6, 7);
      acc = __builtin_amdgcn_mfma_f32_32x32x16_bf16(__builtin_bit_cast(bf16x8, a), b[ks - GP], acc, 0, 0, 0);
      asm volatile("" : "+v"(acc));
    }
    __builtin_amdgcn_sched_barrier(0);
  }
  return acc;
}
DI f32x16 gemm_tile16(const char* wimg, const char* ximg, int t0, int lane) {
  const int r = lane & 31, h = lane >> 5;
  const int q = (lane & 15) >> 2, p = lane & 3;
  f32x16 acc;
#pragma unroll
  for (int i = 0; i < 16; ++i) acc[i] = 0.f;
  const char* xb = ximg + (t0 + r) * XS_STRIDE + 16 * h;
  const char* wb = wimg + (8 * h + q) * 32 + 8 * p;
  s16x4 lo[32], hi[32];
  bf16x8 b[32];
#pragma unroll
  for (int ks = 0; ks < 32 + GP; ++ks) {
    if (ks < 32) {
      lo[ks] = tr_read(wb + ks * 512);
      hi[ks] = tr_read(wb + ks * 512 + 128);
      b[ks] = *(const bf16x8*)(xb + ks * 32);
    }
    if (ks >= GP) {
      const s16x8 a = __builtin_shufflevector(lo[ks - GP], hi[ks - GP], 0, 1, 2, 3, 4, 5, 6, 7);
      acc = __builtin_amdgcn_mfma_f32_32x32x16_bf16(__builtin_bit_cast(bf16x8, a), b[ks - GP], acc, 0, 0, 0);
      asm volatile("" : "+v"(acc));
    }
    __builtin_amdgcn_sched_barrier(0);
  }
  return acc;
}

#define CODE_ADDR(sym, out)                                                                               \
  asm volatile("s_getpc_b64 s[20:21]\n s_add_u32 s20, s20, " #sym "@rel32@lo+4\n s_addc_u32 s21, s21, " #sym \
               "@rel32@hi+12\n s_mov_b64 %0, s[20:21]"                                                    \
               : "=s"(out)::"s20", "s21", "scc")

constexpr int NT = 16;
constexpr int CST = 4096;
constexpr int L_RED = 131072 + CST;
constexpr int L_CNT = L_RED + 512;
constexpr int L_H = L_CNT + 64;
constexpr int L_STG = L_RED + 1024;
constexpr int ATT_LDS = L_STG + 4 * 6144;
constexpr int NC = 64 / NT;
constexpr int PSW = 53;
constexpr int SPIN_MAX = 1 << 22;

extern "C" __global__ __launch_bounds__(768) void k2_attn(char* __restrict__ ws, const bf16* __restrict__ x,
                                               const bf16* __restrict__ wg, const bf16* __restrict__ wu,
                                               const bf16* __restrict__ wd, const bf16* __restrict__ wo,
                                               bf16* __restrict__ out) {
  const bf16* Q = (const bf16*)(ws + WS_Q);
  const bf16* Kb = (const bf16*)(ws + WS_K);
  bf16* O = (bf16*)(ws + WS_O);
  extern __shared__ __attribute__((aligned(16))) char smem[];
  char* kl = smem;
  char* vl = smem + 65536;
  char* cst = smem + 131072;
  const int tid = threadIdx.x, lane = tid & 63;
  const int wave = __builtin_amdgcn_readfirstlane(tid >> 6);

  const unsigned lds0 = (unsigned)(size_t)(LDS_AS char*)smem;
  unsigned long long pc = (unsigned long long)ws + WS_R, pg = (unsigned long long)wg, pu = (unsigned long long)wu, pd = (unsigned long long)wd;
  asm volatile("" : "+s"(pc), "+s"(pg), "+s"(pu), "+s"(pd));
  if (wave >= 8) {
    const int sw = wave - 8, r = lane & 31, h = lane >> 5;
    u32x16 cv[4];
#pragma unroll
    for (int i = 0; i < 4; ++i) cv[i] = ((const CONST_AS u32x16*)pc)[i];
    unsigned l16 = lane * 16;
    {
      const __amdgpu_buffer_rsrc_t rk = __builtin_amdgcn_make_buffer_rsrc((void*)Kb, 0, 131072, 0x00020000);
#pragma unroll
      for (int i = 0; i < 13; ++i) {
        const unsigned o = sw * 2048 + (i < 6 ? 16 + 8 * i : 24 + 8 * i) * 1024;
        blds16x2(rk, l16, o, lds0 + o);
      }
    }
    const int fs = (blockIdx.x & 7) * 22 + (blockIdx.x >> 3);
    const bool ffn = blockIdx.x < 176 && fs < NFFN;
    const unsigned stg = lds0 + L_STG + sw * 6144;
    const int n = lane & 15, g = lane >> 4, q = n >> 2, p = lane & 3;
    unsigned a_gu = stg + (p >> 1) * 2048 + (8 * g + q) * 16 + (p & 1) * 8;
    unsigned a_dn = stg + 4096 + q * 256 + (16 * g + 4 * p) * 2;
    unsigned a_rw = lds0 + L_RED + (n & 7) * 32 + sw * 8 + (n >> 3) * 4;
    unsigned a_rr = lds0 + L_RED + n * 32;
    unsigned a_hw = lds0 + L_H + sw * 32 + n * 2;
    const unsigned hrow = lds0 + L_H + sw * 32 + (n >> 3) * 16, zrow = lds0 + 131072 + 16;
    unsigned a_h0 = (p == g && (q & 1) == 0) ? hrow : zrow, a_h1 = (p == g && (q & 1) == 1) ? hrow : zrow;
    unsigned a_cnt = lds0 + L_CNT, one = 1, odd = lane & 1, even = (lane & 1) ^ 1, g16 = g * 16;
    unsigned v_w = (lane >> 3) * 1024 + (((lane & 7) ^ ((lane >> 4 & 1) << 2)) << 4);
    unsigned v_x = r * 1024 + h * 8, v_s = r * 8;
    unsigned v_y = n * 4096 + g * 16, v_c = n * 256, v_e = n < 12 ? 11 : 10;
    asm volatile("" : "+v"(l16), "+v"(a_gu), "+v"(a_dn), "+v"(a_rw), "+v"(a_rr), "+v"(a_hw), "+v"(a_h0), "+v"(a_h1),
                 "+v"(a_cnt), "+v"(one), "+v"(odd), "+v"(even), "+v"(g16));
    unsigned vg = lane * (F_ * 2), vd = g * 1024 + n * 16;
    asm volatile("" : "+v"(v_w), "+v"(v_x), "+v"(v_s), "+v"(v_y), "+v"(v_c), "+v"(v_e), "+v"(vg), "+v"(vd));
    asm volatile("s_barrier" ::: "memory");
    asm volatile("" : "+s"(cv[0]), "+s"(cv[1]), "+s"(cv[2]), "+s"(cv[3]));
    unsigned k1 = cv[0][0], k2 = cv[0][1];
#pragma unroll
    for (int i = 1; i < 32; ++i) {
      const unsigned a1 = cv[i >> 3][(i & 7) * 2], a2 = cv[i >> 3][(i & 7) * 2 + 1];
      unsigned t;
      asm volatile("s_min_u32 %2, %0, %3\n s_max_u32 %1, %1, %4\n s_max_u32 %0, %0, %3\n s_max_u32 %1, %1, %2"
          : "+s"(k1), "+s"(k2), "=&s"(t) : "s"(a1), "s"(a2) : "scc");
    }
    const __amdgpu_buffer_rsrc_t rc = __builtin_amdgcn_make_buffer_rsrc(ws + WS_CNT, 0, NCNT * 256, 0x00020000);
    const __amdgpu_buffer_rsrc_t ry = __builtin_amdgcn_make_buffer_rsrc(ws + WS_YP, 0, NFFN * 4096, 0x00020000);
    const __amdgpu_buffer_rsrc_t rup = __builtin_amdgcn_make_buffer_rsrc(ws + WS_UPD, 0, 4096, 0x00020000);
    asm volatile("s_waitcnt vmcnt(0)\n s_barrier" ::: "memory");
    if (ffn) {
      const __amdgpu_buffer_rsrc_t rx = __builtin_amdgcn_make_buffer_rsrc(ws + WS_CX, 0, 65536, 0x00020000);
      u32x4 xf[4];
      __builtin_amdgcn_s_setprio(3);
      unsigned vx = __umul24(even, key_row(k1) * 1024) + g16;
      vx += __umul24(odd, key_row(k2) * 1024);
      asm volatile("" : "+v"(vx));
      __builtin_amdgcn_s_setprio(0);
#pragma unroll
      for (int i = 0; i < 4; ++i) xf[i] = __builtin_amdgcn_raw_buffer_load_b128(rx, vx + i * 64, sw * 256, 0);
      const __amdgpu_buffer_rsrc_t rg = __builtin_amdgcn_make_buffer_rsrc((void*)pg, 0, 512 * F_ * 2, 0x00020000);
      const __amdgpu_buffer_rsrc_t ru = __builtin_amdgcn_make_buffer_rsrc((void*)pu, 0, 512 * F_ * 2, 0x00020000);
      const __amdgpu_buffer_rsrc_t rd = __builtin_amdgcn_make_buffer_rsrc((void*)pd, 0, 512 * F_ * 2, 0x00020000);
      const unsigned so = (sw * 128 * F_ + fs * 8) * 2, sd = (fs * 8 * 512 + sw * 128) * 2;
      blds16(rg, vg, so, stg);
      blds16(rg, vg, so + 64 * F_ * 2, stg + 1024);
      blds16(ru, vg, so, stg + 2048);
      blds16(ru, vg, so + 64 * F_ * 2, stg + 3072);
      blds16(rd, vd, sd, stg + 4096);
      blds16(rd, vd, sd + 4096, stg + 5120);
      asm volatile("s_waitcnt vmcnt(0)" ::: "memory");
      bf16x8 wf[4], df[2];
#pragma unroll
      for (int i = 0; i < 4; ++i) wf[i] = tr_read8(a_gu + i * 512, a_gu + i * 512 + 64);
#pragma unroll
      for (int i = 0; i < 2; ++i) df[i] = tr_read8(a_dn + i * 128, a_dn + i * 128 + 1024);
      asm volatile("s_waitcnt lgkmcnt(0)" ::: "memory");
      __builtin_amdgcn_s_setprio(3);
      f32x4 gu = {0.f, 0.f, 0.f, 0.f};
#pragma unroll
      for (int i = 0; i < 4; ++i) gu = __builtin_amdgcn_mfma_f32_16x16x32_bf16(__builtin_bit_cast(bf16x8, xf[i]), wf[i], gu, 0, 0, 0);
      *LDSP(float, a_rw) = gu[0];
      *LDSP(float, a_rw + 256) = gu[1];
      asm volatile("s_mov_b64 exec, 1\n ds_add_u32 %0, %1\n s_mov_b64 exec, -1" ::"v"(a_cnt), "v"(one) : "memory");
      while (*(volatile LDS_AS unsigned*)LDSP(unsigned, a_cnt) < 4) __builtin_amdgcn_s_sleep(1);
      asm volatile("" ::: "memory");
      const f32x4 r0 = *LDSP(f32x4, a_rr), r1 = *LDSP(f32x4, a_rr + 16);
      const f32x2 gs = unpack2(pack2((f32x2{r0[0], r0[1]} + f32x2{r0[2], r0[3]}) + (f32x2{r1[0], r1[1]} + f32x2{r1[2], r1[3]})));
      const float hv = rbf(gs.x * __builtin_amdgcn_rcpf(1.f + __expf(-gs.x))) * gs.y;
      *LDSP(bf16, a_hw) = (bf16)hv;
      asm volatile("" ::: "memory");
      const bf16x8 h0 = *LDSP(bf16x8, a_h0), h1 = *LDSP(bf16x8, a_h1);
      f32x4 y = {0.f, 0.f, 0.f, 0.f};
      y = __builtin_amdgcn_mfma_f32_16x16x32_bf16(h0, df[0], y, 0, 0, 0);
      y = __builtin_amdgcn_mfma_f32_16x16x32_bf16(h1, df[1], y, 0, 0, 0);
      __builtin_amdgcn_raw_buffer_store_b128(__builtin_bit_cast(u32x4, y), ry, l16, fs * 4096 + sw * 1024, 16);
      __builtin_amdgcn_s_setprio(0);
      asm volatile("s_waitcnt vmcnt(0)" ::: "memory");
      count_up(rc, one, (sw * 16 + (fs & 15)) * 256);
    }
    const int tb = (blockIdx.x >> 6) * 8 + (blockIdx.x & 7), cb = (blockIdx.x >> 3) & 7, tok0 = tb * 64;
    char* ximg = smem;
    char* wimg = smem + 64 * XS_STRIDE;
    const int n0 = (sw & 1) * 32, t0 = (sw >> 1) * 32;
    const unsigned so_x = (tok0 + t0) * 1024 + (cb * 64 + n0) * 2;
    const __amdgpu_buffer_rsrc_t rout = __builtin_amdgcn_make_buffer_rsrc(out, 0, S_ * D_ * 2, 0x00020000);
    u32x4 wreg[16];
    u32x2 xv[4], nv[4], sxr;
    {
      const __amdgpu_buffer_rsrc_t rw = __builtin_amdgcn_make_buffer_rsrc((void*)wo, 0, 512 * 1024, 0x00020000);
      const __amdgpu_buffer_rsrc_t rx = __builtin_amdgcn_make_buffer_rsrc((void*)x, 0, S_ * D_ * 2, 0x00020000);
      const __amdgpu_buffer_rsrc_t rs = __builtin_amdgcn_make_buffer_rsrc(ws + WS_ST, 0, S_ * 8, 0x00020000);
#pragma unroll
      for (int i = 0; i < 16; ++i) wreg[i] = __builtin_amdgcn_raw_buffer_load_b128(rw, v_w, (sw + 4 * i) * 8192 + cb * 128, 0);
#pragma unroll
      for (int i = 0; i < 4; ++i) xv[i] = __builtin_amdgcn_raw_buffer_load_b64(rx, v_x + i * 16, so_x, 0);
      sxr = __builtin_amdgcn_raw_buffer_load_b64(rs, v_s, (tok0 + t0) * 8, 0);
      if (wave == 11) {
        const unsigned t = __builtin_amdgcn_raw_buffer_load_b32(rout, 0, so_x, 0);
        asm volatile("" ::"v"(t));
      }
    }
    if ((blockIdx.x >> 4) == 11) {
      const int sel = (blockIdx.x >> 3) & 1, cq = blockIdx.x & 7;
      __builtin_amdgcn_s_setprio(3);
      int spin = 0;
      while (__any(__builtin_amdgcn_raw_buffer_load_b32(rc, v_c, (cq >> 1) * 4096, 16) < v_e) && ++spin < SPIN_MAX)
        asm volatile("s_sleep 8" ::: "memory");
      __builtin_amdgcn_s_setprio(0);
      asm volatile("" ::: "memory");
      const unsigned so = (cq >> 1) * 1024 + (2 * sel + (cq & 1)) * 256 + sw * 64;
      u32x4 t[11];
#pragma unroll
      for (int i = 0; i < 10; ++i) t[i] = __builtin_amdgcn_raw_buffer_load_b128(ry, v_y, so + i * 65536, 16);
      __builtin_amdgcn_s_setprio(3);
      t[10] = u32x4{0u, 0u, 0u, 0u};
      if (v_e == 11) t[10] = __builtin_amdgcn_raw_buffer_load_b128(ry, v_y, so + 10 * 65536, 16);
      const float gate = rbf(__builtin_amdgcn_rcpf(1.f + __expf(-key_val(sel ? k2 : k1))));
      f32x4 a = __builtin_bit_cast(f32x4, t[0]);
#pragma unroll
      for (int i = 1; i < 11; ++i) a += __builtin_bit_cast(f32x4, t[i]);
#pragma unroll
      for (int i = 0; i < 4; ++i) a[i] = dpp_add<0x140>(sum8(a[i]));
      asm volatile("" : "+v"(a));
      const float u = rbf(rbf(p == 0 ? a[0] : p == 1 ? a[1] : p == 2 ? a[2] : a[3]) * gate);
      if (n < 4) __builtin_amdgcn_raw_buffer_store_b32(__float_as_uint(u), rup, n * 64 + g * 4, (sel * 512 + cq * 64 + sw * 4) * 4, 16);
      __builtin_amdgcn_s_setprio(0);
      asm volatile("s_waitcnt vmcnt(0)" ::: "memory");
      count_up(rc, one, (64 + sel * 8 + cq) * 256);
    }
    const bool routed = ((key_tok(k1) ^ (tok0 + t0)) >> 5) == 0 || ((key_tok(k2) ^ (tok0 + t0)) >> 5) == 0;
    u32x4 uf[4];
    if (routed) {
      __builtin_amdgcn_s_setprio(3);
      const int tok = tok0 + t0 + r;
      const int rsel = tok == key_tok(k1) ? 0 : tok == key_tok(k2) ? 1 : -1;
#pragma unroll
      for (int i = 0; i < 4; ++i) uf[i] = u32x4{0x80000000u, 0x80000000u, 0x80000000u, 0x80000000u};
      if (rsel >= 0) {
        int spin = 0;
        while (__builtin_amdgcn_raw_buffer_load_b32(rc, rsel * 2048, (64 + cb) * 256, 16) < 4 && ++spin < SPIN_MAX)
          asm volatile("s_sleep 4" ::: "memory");
        asm volatile("" ::: "memory");
#pragma unroll
        for (int i = 0; i < 4; ++i)
          uf[i] = __builtin_amdgcn_raw_buffer_load_b128(rup, (rsel * 512 + 8 * i + 4 * h) * 4, (cb * 64 + n0) * 4, 16);
      }
#pragma unroll
      for (int i = 0; i < 4; ++i) asm volatile("" : "+v"(uf[i]));
      __builtin_amdgcn_s_setprio(0);
    }
#pragma unroll
    for (int i = 0; i < 16; ++i) asm volatile("" : "+v"(wreg[i]));
    asm volatile("s_barrier" ::: "memory");
    if (wave == 8) count_up(rc, one, (80 + tb) * 256);
#pragma unroll
    for (int i = 0; i < 16; ++i) *(u32x4*)(wimg + (sw + 4 * i) * 1024 + lane * 16) = wreg[i];
    {
      const f32x2 mb2 = {__uint_as_float(sxr[0]), __uint_as_float(sxr[0])}, ri2 = {__uint_as_float(sxr[1]), __uint_as_float(sxr[1])};
#pragma unroll
      for (int i = 0; i < 4; ++i) {
#pragma unroll
        for (int jj = 0; jj < 2; ++jj) nv[i][jj] = ln2(unpack2(xv[i][jj]), mb2, ri2);
      }
    }
    if (routed) {
#pragma unroll
      for (int i = 0; i < 4; ++i) {
        const f32x4 u = __builtin_bit_cast(f32x4, uf[i]);
        nv[i][0] = pack2(unpack2(nv[i][0]) + f32x2{u[0], u[1]});
        nv[i][1] = pack2(unpack2(nv[i][1]) + f32x2{u[2], u[3]});
      }
    }
    if (wave == 8) {
      int spin = 0;
      while (__hip_atomic_load((GLB_AS unsigned*)(ws + WS_CL + tb * 256), __ATOMIC_RELAXED, __HIP_MEMORY_SCOPE_AGENT) < 8 && ++spin < SPIN_MAX)
        __builtin_amdgcn_s_sleep(1);
    }
    asm volatile("s_barrier" ::: "memory");
    {
      const __amdgpu_buffer_rsrc_t ro = __builtin_amdgcn_make_buffer_rsrc(O + tok0 * 512, 0, 65536, 0x00020000);
      u32x4 t[16];
#pragma unroll
      for (int i = 0; i < 16; ++i) t[i] = __builtin_amdgcn_raw_buffer_load_b128(ro, l16, (sw * 16 + i) * 1024, 16);
#pragma unroll
      for (int i = 0; i < 16; ++i) *(u32x4*)(ximg + (sw * 16 + i) * XS_STRIDE + lane * 16) = t[i];
    }
    asm volatile("s_waitcnt lgkmcnt(0)\n s_barrier" ::: "memory");
    f32x16 acc = gemm_tile(wimg, ximg, n0, t0, lane);
#pragma unroll
    for (int g = 0; g < 4; ++g) {
      u32x2 o;
#pragma unroll
      for (int jj = 0; jj < 2; ++jj) {
        const unsigned xr = xv[g][jj], nr = nv[g][jj];
        const f32x2 a = unpack2(pack2(f32x2{acc[4 * g + 2 * jj], acc[4 * g + 2 * jj + 1]}));
        o[jj] = pack2(unpack2(pack2(unpack2(xr) + a)) + unpack2(nr));
      }
      __builtin_amdgcn_raw_buffer_store_b64(o, rout, v_x + g * 16, so_x, 0);
    }
    return;
  }

  const int r = lane & 31, h = lane >> 5;
  const int token = ((blockIdx.x >> 6) * 8 + (blockIdx.x & 7)) * 64 + ((blockIdx.x >> 3) & 7) * 8 + wave;
  bf16x8 qf;
  asm volatile("global_load_dwordx4 %0, %1, off" : "=v"(qf) : "v"(Q + token * 512 + r * 16 + 8 * h) : "memory");
#pragma unroll
  for (int i = 0; i < 3; ++i) {
    const int piece = (i < 2) ? (wave + i * 8) : (64 + wave);
    glds16_raw((const char*)Kb + piece * 1024 + lane * 16, lds0 + piece * 1024);
  }
  {
    const unsigned cv = ((tid & 30) == 0 || (tid & 30) == 8) ? 0x3f803f80u : 0u;
    *(u32x2*)(cst + tid * 8) = u32x2{cv, cv};
    if (tid == 1) *(unsigned*)(smem + L_CNT) = 0;
  }
  int vo = (r < 16) ? 65536 + (h * 16 + r) * 16 : 131072 + (h * 16 + r - 16) * 16;
  int vstep = (r < 16) ? CST : 0;
  int sel = 0xdddd;
  f32x16 zero;
#pragma unroll
  for (int i = 0; i < 16; ++i) zero[i] = 0.f;
  f32x16 acc = zero;
  asm volatile("" : "+v"(sel), "+v"(acc), "+v"(vstep), "+v"(vo));
  const char* kp0 = kl + lane * 16;
  const char* vp0 = smem + vo;
  asm volatile("s_waitcnt vmcnt(0) lgkmcnt(0)\n s_barrier" ::: "memory");

  constexpr float HEAD = 40.f;
  f32x16 negm, xs;
  bf16x8 kfn;
  {
    bf16x8 kf[4];
    f32x16 s0[4];
#pragma unroll
    for (int t = 0; t < 4; ++t) kf[t] = *(const bf16x8*)(kp0 + t * 1024);
#pragma unroll
    for (int t = 0; t < 4; ++t) {
      s0[t] = __builtin_amdgcn_mfma_f32_32x32x16_bf16(kf[t], qf, zero, 0, 0, 0);
      asm volatile("" : "+v"(s0[t]));
    }
    __builtin_amdgcn_sched_barrier(0);
    float mx = -INFINITY;
#pragma unroll
    for (int t = 0; t < 4; ++t) {
#pragma unroll
      for (int i = 0; i < 16; i += 2) mx = fmaxf(fmaxf(mx, s0[t][i]), s0[t][i + 1]);
    }
    const float nm = -(floorf(max32(mx)) + HEAD);
#pragma unroll
    for (int i = 0; i < 16; ++i) {
      xs[i] = s0[0][i] + nm;
      negm[i] = nm;
    }
    kfn = kf[1];
  }
  asm volatile("" : "+v"(negm));
  {
    const char* kp = kp0;
    const char* vp = vp0;
    f32x16 x = xs;
    if (wave < 4) __builtin_amdgcn_s_setprio(2);
    else __builtin_amdgcn_s_setprio(1);
    for (int c = 0; c < NC; ++c) {
      bool grown;
#pragma unroll
      for (int t = 0; t < NT; ++t) {
        const bf16x8 kf = kfn;
        kfn = *(const bf16x8*)(kp + (t + 2) * 1024);
        const bf16x8 vf0 = *(const bf16x8*)(vp + ((t & 3) * 2) * 512);
        const bf16x8 vf1 = *(const bf16x8*)(vp + ((t & 3) * 2 + 1) * 512);
        if ((t & 3) == 3) vp += vstep;
        f32x8 p0, p1;
#pragma unroll
        for (int j = 0; j < 4; ++j) p0[j] = __builtin_amdgcn_exp2f(x[j]);
        __builtin_amdgcn_sched_barrier(0);
        f32x16 xn = __builtin_amdgcn_mfma_f32_32x32x16_bf16(kf, qf, negm, 0, 0, 0);
        asm volatile("" : "+v"(xn));
        __builtin_amdgcn_sched_barrier(0);
#pragma unroll
        for (int j = 4; j < 8; ++j) p0[j] = __builtin_amdgcn_exp2f(x[j]);
        if (t == NT - 1) grown = __any(!(acc[8] <= 4.3e9f));
        acc = pv_mfma(vf0, p0, acc, sel);
#pragma unroll
        for (int j = 0; j < 8; ++j) p1[j] = __builtin_amdgcn_exp2f(x[8 + j]);
        acc = pv_mfma(vf1, p1, acc, sel);
        if (t == 7 && c == 0) asm volatile("s_barrier" ::: "memory");
        if (c * NT + t == PSW && wave < 4) __builtin_amdgcn_s_setprio(0);
        __builtin_amdgcn_sched_barrier(0);
        x = xn;
      }
      kp += NT * 1024;
      if (grown) {
        const int e = max(__builtin_amdgcn_frexp_expf(acc[8]) - 1 + (int)HEAD, 0);
#pragma unroll
        for (int i = 0; i < 9; ++i) acc[i] = __builtin_amdgcn_ldexpf(acc[i], -e);
        const float ef = (float)e;
#pragma unroll
        for (int i = 0; i < 16; ++i) {
          negm[i] -= ef;
          x[i] -= ef;
        }
        asm volatile("" : "+v"(negm));
      }
    }
  }
  {
    float chk = 0.f;
#pragma unroll
    for (int i = 0; i < 9; ++i) chk = __builtin_fmaf(acc[i], 0.f, chk);
    if (__any(chk != 0.f)) {
      f32x16 oacc = zero;
      float mrun = -INFINITY;
      const char* kp = kp0;
      const char* vp = vp0;
      for (int t = 0; t < 64; ++t) {
        bf16x8 kf = *(const bf16x8*)(kp);
        f32x16 x = __builtin_amdgcn_mfma_f32_32x32x16_bf16(kf, qf, zero, 0, 0, 0);
        float mx = x[0];
#pragma unroll
        for (int i = 1; i < 16; ++i) mx = fmaxf(mx, x[i]);
        const float mn = fmaxf(mrun, max32(mx));
        const float al = __builtin_amdgcn_exp2f(mrun - mn);
        mrun = mn;
#pragma unroll
        for (int i = 0; i < 16; ++i) oacc[i] *= al;
#pragma unroll
        for (int s = 0; s < 2; ++s) {
          f32x8 pf;
#pragma unroll
          for (int j = 0; j < 8; ++j) pf[j] = __builtin_amdgcn_exp2f(x[8 * s + j] - mn);
          bf16x8 vf = *(const bf16x8*)(vp + ((t & 3) * 2 + s) * 512);
          oacc = pv_mfma(vf, pf, oacc, sel);
        }
        kp += 1024;
        if ((t & 3) == 3) vp += vstep;
      }
      acc = oacc;
    }
  }
  const float inv = 1.f / acc[8];
  {
    unsigned a0 = pack2(f32x2{acc[0] * inv, acc[1] * inv}), a1 = pack2(f32x2{acc[2] * inv, acc[3] * inv});
    unsigned b0 = pack2(f32x2{acc[4] * inv, acc[5] * inv}), b1 = pack2(f32x2{acc[6] * inv, acc[7] * inv});
    swap32(a0, b0);
    swap32(a1, b1);
    const u32x4 o = {a0, a1, b0, b1};
    const __amdgpu_buffer_rsrc_t ro = __builtin_amdgcn_make_buffer_rsrc(O + token * 512, 0, 1024, 0x00020000);
    __builtin_amdgcn_raw_buffer_store_b128(o, ro, r * 32 + h * 16, 0, 16);
  }
  asm volatile("s_waitcnt vmcnt(0)\n s_barrier" ::: "memory");
}

constexpr int K1_LDS = 64 * XS_STRIDE + 65536 + 16384 + 256;

extern "C" __global__ __launch_bounds__(768) void k1_ln_qkv(const bf16* __restrict__ x, const bf16* __restrict__ wq,
                                                 const bf16* __restrict__ wk, const bf16* __restrict__ wv,
                                                 const bf16* __restrict__ wr, char* __restrict__ ws) {
  float* st = (float*)(ws + WS_ST);
  bf16* Q = (bf16*)(ws + WS_Q);
  bf16* Kb = (bf16*)(ws + WS_K);
  bf16* Vf = (bf16*)(ws + WS_V);
  unsigned* cand = (unsigned*)(ws + WS_R);
  extern __shared__ __attribute__((aligned(16))) char smem[];
  char* ximg = smem;
  char* wimg = smem + 64 * XS_STRIDE;
  char* kvimg = smem + 64 * XS_STRIDE + 65536;
  float* rs = (float*)(smem + 64 * XS_STRIDE + 65536 + 16384);
  const int cb = blockIdx.x >> 5, tb = blockIdx.x & 31;
  const int tid = threadIdx.x, lane = tid & 63;
  const int wave = __builtin_amdgcn_readfirstlane(tid >> 6);
  const int tok0 = tb * 64;

  if (wave >= 8) {
    if (blockIdx.x == 255 && tid >= 768 - NCNT) ((unsigned*)(ws + WS_CNT))[(767 - tid) * 64] = 0;
    stage_w(wq, cb * 64, wimg, wave - 8, 4, lane);
    if (cb < 2) {
      const bf16* w = cb == 0 ? wk : wv;
#pragma unroll
      for (int i = 0; i < 4; ++i) {
        const int p = wave - 8 + 4 * i;
        glds16((const char*)w + p * 1024 + lane * 16, kvimg + p * 1024);
      }
    }
    __syncthreads();
    if (cb == 3 && wave == 8) {
      unsigned k1 = route_key(rs[lane], tok0 + lane), k2 = 0;
      top2_wave(k1, k2, 64);
      if (lane == 0) *(u32x2*)(cand + tb * 2) = u32x2{k1, k2 | 1u};
      const u32x4 a = *(const u32x4*)(ximg + (key_tok(k1) & 63) * XS_STRIDE + lane * 16);
      const u32x4 b = *(const u32x4*)(ximg + (key_tok(k2) & 63) * XS_STRIDE + lane * 16);
      *(u32x4*)(ws + WS_CX + tb * 2048 + lane * 16) = a;
      *(u32x4*)(ws + WS_CX + tb * 2048 + 1024 + lane * 16) = b;
    }
    return;
  }

  const int tl = wave * 8 + (lane >> 3), c8 = lane & 7;
  u32x4 xv[8];
#pragma unroll
  for (int i = 0; i < 8; ++i) xv[i] = *(const u32x4*)(x + (tok0 + tl) * 512 + (c8 + 8 * i) * 8);
  u32x4 wrv[8];
  if (cb == 3) {
#pragma unroll
    for (int i = 0; i < 8; ++i) wrv[i] = *(const u32x4*)(wr + (c8 + 8 * i) * 8);
  }

  f32x2 xf[32];
#pragma unroll
  for (int i = 0; i < 32; ++i) xf[i] = unpack2(xv[i >> 2][i & 3]);
  f32x2 s2 = {0.f, 0.f};
#pragma unroll
  for (int i = 0; i < 32; ++i) s2 += xf[i];
  const float mean = sum8(s2.x + s2.y) * (1.f / 512.f);
  const f32x2 mean2 = {mean, mean};
  f32x2 v2 = {0.f, 0.f};
#pragma unroll
  for (int i = 0; i < 32; ++i) { const f32x2 d = xf[i] - mean2; v2 += d * d; }
  const float vs = sum8(v2.x + v2.y);
  const float mb = rbf(mean);
  const float vb = rbf(vs * (1.f / 512.f));
  const float den = rbf(sqrtf(rbf(vb + 1e-5f)));
  const float rinv = __builtin_amdgcn_rcpf(den);
  const f32x2 mb2 = {mb, mb}, rinv2 = {rinv, rinv};
  u32x4 o[8];
#pragma unroll
  for (int i = 0; i < 8; ++i) {
#pragma unroll
    for (int j = 0; j < 4; ++j) o[i][j] = ln2(xf[4 * i + j], mb2, rinv2);
    *(u32x4*)(ximg + tl * XS_STRIDE + (c8 + 8 * i) * 16) = o[i];
  }
  if (cb == 2 && c8 == 0) *(f32x2*)(st + (tok0 + tl) * 2) = f32x2{mb, rinv};
  if (cb == 3) {
    float rd = 0.f;
#pragma unroll
    for (int i = 0; i < 8; ++i) {
#pragma unroll
      for (int j = 0; j < 4; ++j) {
        const unsigned a = o[i][j], b = wrv[i][j];
        rd = __builtin_amdgcn_fdot2_f32_bf16(__builtin_bit_cast(bf16x2, a), __builtin_bit_cast(bf16x2, b), rd, false);
      }
    }
    rd = sum8(rd);
    if (c8 == 0) rs[tl] = rbf(rd);
  }
  __syncthreads();

  const int r = lane & 31, h = lane >> 5;
  if (wave < 4) {
    const int n0 = (wave & 1) * 32, t0 = (wave >> 1) * 32;
    f32x16 acc = gemm_tile(wimg, ximg, n0, t0, lane);
    const float sc = 0.25f * 1.4426950408889634f;
    const __amdgpu_buffer_rsrc_t rq = __builtin_amdgcn_make_buffer_rsrc(Q + tok0 * 512, 0, 65536, 0x00020000);
#pragma unroll
    for (int m = 0; m < 2; ++m) {
      unsigned p[4];
#pragma unroll
      for (int i = 0; i < 4; ++i) p[i] = pack2(unpack2(pack2(f32x2{acc[8 * m + 2 * i], acc[8 * m + 2 * i + 1]})) * f32x2{sc, sc});
      swap32(p[0], p[2]);
      swap32(p[1], p[3]);
      const u32x4 o = {p[0], p[1], p[2], p[3]};
      __builtin_amdgcn_raw_buffer_store_b128(o, rq, ((t0 + r) * 512 + cb * 64 + n0 + 16 * m + 8 * h) * 2, 0, 0);
    }
  } else if (wave == 7) {
    if (lane < 2) {
      const unsigned t = *(const volatile unsigned*)(lane ? ws + WS_K + tb * 2048 : (const char*)(Q + tok0 * 512 + cb * 64));
      asm volatile("" ::"v"(t));
    }
  } else if (wave == 6 && cb == 7 && tb < 8) {
    unsigned long long c0, c1;
    CODE_ADDR(k2_attn, c0);
    CODE_ADDR(k1_ln_qkv, c1);
    const long long n = (long long)(c1 - c0);
    unsigned t[4];
#pragma unroll
    for (int i = 0; i < 4; ++i) {
      const int off = (i * 64 + lane) * 128;
      t[i] = off < n ? *(const volatile unsigned*)(c0 + off) : 0u;
    }
#pragma unroll
    for (int i = 0; i < 4; ++i) asm volatile("" ::"v"(t[i]));
  } else if (wave < 6 && cb < 2) {
    const int t0 = (wave - 4) * 32;
    f32x16 acc = gemm_tile16(kvimg, ximg, t0, lane);
    const int key = tok0 + t0 + r;
    const int t = key >> 5;
    if (cb == 0) {
#pragma unroll
      for (int g = 0; g < 2; ++g) {
        bf16x4 o;
#pragma unroll
        for (int j = 0; j < 4; ++j) o[j] = (bf16)acc[4 * g + j];
        *(bf16x4*)(Kb + ((t * 2 + g) * 32 + r) * 8 + 4 * h) = o;
      }
    } else {
      const int kk = key & 31, sg = kk >> 4, rem = kk & 15;
      const int hh = rem >> 3, jj = rem & 7;
#pragma unroll
      for (int g = 0; g < 2; ++g) {
#pragma unroll
        for (int j = 0; j < 4; ++j) {
          const int d = 8 * g + 4 * h + j;
          Vf[(((t * 2 + sg) * 2 + hh) * 16 + d) * 8 + jj] = (bf16)acc[4 * g + j];
        }
      }
    }
  }
}

extern "C" void kernel_launch(void* const* d_in, const int* in_sizes, int n_in, void* d_out, int out_size,
                              void* d_ws, size_t ws_size, hipStream_t stream) {
  const bf16* x = (const bf16*)d_in[0];
  const bf16* wq = (const bf16*)d_in[1];
  const bf16* wk = (const bf16*)d_in[2];
  const bf16* wv = (const bf16*)d_in[3];
  const bf16* wo = (const bf16*)d_in[4];
  const bf16* wr = (const bf16*)d_in[6];
  const bf16* wg = (const bf16*)d_in[7];
  const bf16* wu = (const bf16*)d_in[8];
  const bf16* wd = (const bf16*)d_in[9];
  char* ws = (char*)d_ws;
  bf16* out = (bf16*)d_out;

  (void)hipFuncSetAttribute((const void*)k1_ln_qkv, hipFuncAttributeMaxDynamicSharedMemorySize, K1_LDS);
  (void)hipFuncSetAttribute((const void*)k2_attn, hipFuncAttributeMaxDynamicSharedMemorySize, ATT_LDS);

  k1_ln_qkv<<<256, 768, K1_LDS, stream>>>(x, wq, wk, wv, wr, ws);
  k2_attn<<<256, 768, ATT_LDS, stream>>>(ws, x, wg, wu, wd, wo, out);
}
```

```cpp
#include <hip/hip_runtime.h>
#include <hip/hip_bf16.h>

typedef __attribute__((ext_vector_type(8))) __bf16 bf16x8;
typedef __attribute__((ext_vector_type(16))) __bf16 bf16x16;
typedef __attribute__((ext_vector_type(8))) float f32x8;
typedef __attribute__((ext_vector_type(4))) __bf16 bf16x4;
typedef __attribute__((ext_vector_type(2))) __bf16 bf16x2;
typedef __attribute__((ext_vector_type(4))) short s16x4;
typedef __attribute__((ext_vector_type(8))) short s16x8;
typedef __attribute__((ext_vector_type(16))) float f32x16;
typedef __attribute__((ext_vector_type(4))) float f32x4;
typedef __attribute__((ext_vector_type(4))) unsigned u32x4;
typedef __attribute__((ext_vector_type(2))) unsigned u32x2;
typedef __attribute__((ext_vector_type(2))) float f32x2;
typedef __attribute__((ext_vector_type(16))) unsigned u32x16;
typedef __bf16 bf16;

#define CLK() ((unsigned)wall_clock64())
#define G_TS(ws) ((unsigned (*)[256][12][16])((ws) + (3u << 20)))
#define LDS_AS __attribute__((address_space(3)))
#define GLB_AS __attribute__((address_space(1)))
#define CONST_AS __attribute__((address_space(4)))
#define DI __device__ __forceinline__

constexpr int S_ = 2048, D_ = 512, H_ = 32, DH_ = 16, F_ = 1376;

constexpr size_t WS_Q = 0;
constexpr size_t WS_O = WS_Q;
constexpr size_t WS_K = 2u << 20;
constexpr size_t WS_V = WS_K + 65536;
constexpr size_t WS_ST = WS_V + 65536;
constexpr size_t WS_R = WS_ST + 16384;
constexpr size_t WS_CX = WS_R + 4096;
constexpr size_t WS_CNT = WS_CX + 65536;
constexpr int NCNT = 64 + 16 + 32;
constexpr size_t WS_FLG = WS_CNT + 64 * 256;
constexpr size_t WS_CL = WS_FLG + 16 * 256;
constexpr size_t WS_UPD = WS_CNT + NCNT * 256;
constexpr size_t WS_YP = WS_UPD + 4096;
constexpr int NFFN = 172;

DI float bf2f(bf16 v) { return (float)v; }
DI float rbf(float v) { return (float)(bf16)v; }
DI f32x2 unpack2(unsigned u) { return f32x2{__uint_as_float(u << 16), __uint_as_float(u & 0xffff0000u)}; }
DI unsigned pack2(f32x2 v) {
  unsigned r = __builtin_bit_cast(unsigned, __builtin_convertvector(v, bf16x2));
  asm("" : "+v"(r));
  return r;
}

DI unsigned route_key(float v, int tok) {
  const unsigned u = __float_as_uint(v) >> 16;
  const unsigned o = (u & 0x8000u) ? (~u & 0xffffu) : (u | 0x8000u);
  return (o << 16) | (unsigned)((0x7ff - tok) << 1);
}
DI int key_tok(unsigned k) { return 0x7ff - (int)((k >> 1) & 0x7ffu); }
DI int key_row(unsigned k) { return (key_tok(k) >> 6) * 2 + (int)(k & 1u); }
DI float key_val(unsigned k) {
  const unsigned o = k >> 16;
  const unsigned u = (o & 0x8000u) ? (o & 0x7fffu) : (~o & 0xffffu);
  return __uint_as_float(u << 16);
}
DI unsigned ln2(f32x2 x, f32x2 mb2, f32x2 rinv2) { return pack2(unpack2(pack2(x - mb2)) * rinv2); }
DI void top2_wave(unsigned& k1, unsigned& k2, int width) {
  for (int o = width >> 1; o > 0; o >>= 1) {
    const unsigned a1 = __shfl_xor(k1, o), a2 = __shfl_xor(k2, o);
    k2 = max(min(k1, a1), max(k2, a2));
    k1 = max(k1, a1);
  }
}

DI void glds16(const void* g, void* l) {
  __builtin_amdgcn_global_load_lds((const GLB_AS void*)g, (LDS_AS void*)l, 16, 0, 0);
}

DI void glds16_raw(const void* g, unsigned lds_off) {
  asm volatile("s_mov_b32 m0, %0\n s_nop 0\n global_load_lds_dwordx4 %1, off" ::"s"(lds_off), "v"(g) : "memory", "m0");
}

DI void blds16(__amdgpu_buffer_rsrc_t rs, unsigned voff, unsigned soff, unsigned lds_off) {
  asm volatile("s_mov_b32 m0, %0\n s_nop 0\n buffer_load_dwordx4 %1, %2, %3 offen lds" ::"s"(lds_off), "v"(voff), "s"(rs), "s"(soff) : "memory", "m0");
}
DI void blds16x2(__amdgpu_buffer_rsrc_t rs, unsigned voff, unsigned soff, unsigned lds_off) {
  asm volatile("s_mov_b32 m0, %0\n s_nop 0\n buffer_load_dwordx4 %1, %2, %3 offen lds\n buffer_load_dwordx4 %1, %2, %3 offen offset:1024 lds"
               ::"s"(lds_off), "v"(voff), "s"(rs), "s"(soff) : "memory", "m0");
}
DI void count_up(__amdgpu_buffer_rsrc_t rs, unsigned one, unsigned soff) {
  asm volatile("s_mov_b64 exec, 1\n buffer_atomic_add %0, off, %1, %2\n s_mov_b64 exec, -1" ::"v"(one), "s"(rs), "s"(soff) : "memory");
}
#define LDSP(T, off) ((LDS_AS T*)(unsigned long)(off))

template <int CTRL>
DI float dpp_add(float v) {
  const int t = __builtin_amdgcn_update_dpp(0, __float_as_int(v), CTRL, 0xf, 0xf, true);
  return v + __int_as_float(t);
}
DI float sum8(float v) {
  v = dpp_add<0xB1>(v);
  v = dpp_add<0x4E>(v);
  v = dpp_add<0x141>(v);
  return v;
}

DI f32x16 pv_mfma(bf16x8 v, f32x8 p, f32x16 acc, int sel) {
  return __builtin_amdgcn_smfmac_f32_32x32x32_bf16(v, __builtin_bit_cast(bf16x16, p), acc, sel, 0, 0);
}

DI void swap32(unsigned& a, unsigned& b) {
  const u32x2 t = __builtin_amdgcn_permlane32_swap(a, b, false, false);
  a = t[0];
  b = t[1];
}
DI float max32(float v) {
  unsigned a = __float_as_uint(v), b = a;
  swap32(a, b);
  return fmaxf(__uint_as_float(a), __uint_as_float(b));
}

DI s16x4 tr_read(const char* p) {
  return __builtin_amdgcn_ds_read_tr16_b64_v4i16((LDS_AS s16x4*)p);
}
DI bf16x8 tr_read8(unsigned lo, unsigned hi) {
  const s16x4 a = __builtin_amdgcn_ds_read_tr16_b64_v4i16(LDSP(s16x4, lo)), b = __builtin_amdgcn_ds_read_tr16_b64_v4i16(LDSP(s16x4, hi));
  return __builtin_bit_cast(bf16x8, __builtin_shufflevector(a, b, 0, 1, 2, 3, 4, 5, 6, 7));
}

DI int wimg_off(int row, int ch) { return row * 128 + ((ch ^ (((row >> 1) & 1) << 2)) << 4); }

constexpr int XS_STRIDE = 1040;

DI void stage_w(const bf16* w, int col0, char* wimg, int wave, int nwaves, int lane) {
  for (int p = wave; p < 64; p += nwaves) {
    const int row = p * 8 + (lane >> 3), pc = lane & 7;
    const int ch = pc ^ (((row >> 1) & 1) << 2);
    glds16(w + row * 512 + col0 + ch * 8, wimg + p * 1024);
  }
}

constexpr int GP = 4;
DI f32x16 gemm_tile(const char* wimg, const char* ximg, int n0, int t0, int lane) {
  const int r = lane & 31, h = lane >> 5;
  const int q = (lane & 15) >> 2, p = lane & 3, blk = (lane >> 4) & 1;
  f32x16 acc;
#pragma unroll
  for (int i = 0; i < 16; ++i) acc[i] = 0.f;
  const char* xb = ximg + (t0 + r) * XS_STRIDE + 16 * h;
  const int col = n0 + 16 * blk + 4 * p;
  const int ch = col >> 3, sub = (col & 7) * 2;
  const char* wb0 = wimg + wimg_off(8 * h + q, ch) + sub;
  const char* wb1 = wimg + wimg_off(8 * h + q + 4, ch) + sub;
  s16x4 lo[32], hi[32];
  bf16x8 b[32];
#pragma unroll
  for (int ks = 0; ks < 32 + GP; ++ks) {
    if (ks < 32) {
      lo[ks] = tr_read(wb0 + ks * 2048);
      hi[ks] = tr_read(wb1 + ks * 2048);
      b[ks] = *(const bf16x8*)(xb + ks * 32);
    }
    if (ks >= GP) {
      const s16x8 a = __builtin_shufflevector(lo[ks - GP], hi[ks - GP], 0, 1, 2, 3, 4, 5, 6, 7);
      acc = __builtin_amdgcn_mfma_f32_32x32x16_bf16(__builtin_bit_cast(bf16x8, a), b[ks - GP], acc, 0, 0, 0);
      asm volatile("" : "+v"(acc));
    }
    __builtin_amdgcn_sched_barrier(0);
  }
  return acc;
}
DI f32x16 gemm_tile16(const char* wimg, const char* ximg, int t0, int lane) {
  const int r = lane & 31, h = lane >> 5;
  const int q = (lane & 15) >> 2, p = lane & 3;
  f32x16 acc;
#pragma unroll
  for (int i = 0; i < 16; ++i) acc[i] = 0.f;
  const char* xb = ximg + (t0 + r) * XS_STRIDE + 16 * h;
  const char* wb = wimg + (8 * h + q) * 32 + 8 * p;
  s16x4 lo[32], hi[32];
  bf16x8 b[32];
#pragma unroll
  for (int ks = 0; ks < 32 + GP; ++ks) {
    if (ks < 32) {
      lo[ks] = tr_read(wb + ks * 512);
      hi[ks] = tr_read(wb + ks * 512 + 128);
      b[ks] = *(const bf16x8*)(xb + ks * 32);
    }
    if (ks >= GP) {
      const s16x8 a = __builtin_shufflevector(lo[ks - GP], hi[ks - GP], 0, 1, 2, 3, 4, 5, 6, 7);
      acc = __builtin_amdgcn_mfma_f32_32x32x16_bf16(__builtin_bit_cast(bf16x8, a), b[ks - GP], acc, 0, 0, 0);
      asm volatile("" : "+v"(acc));
    }
    __builtin_amdgcn_sched_barrier(0);
  }
  return acc;
}

#define CODE_ADDR(sym, out)                                                                               \
  asm volatile("s_getpc_b64 s[20:21]\n s_add_u32 s20, s20, " #sym "@rel32@lo+4\n s_addc_u32 s21, s21, " #sym \
               "@rel32@hi+12\n s_mov_b64 %0, s[20:21]"                                                    \
               : "=s"(out)::"s20", "s21", "scc")

constexpr int NT = 16;
constexpr int CST = 4096;
constexpr int L_RED = 131072 + CST;
constexpr int L_CNT = L_RED + 512;
constexpr int L_H = L_CNT + 64;
constexpr int L_STG = L_RED + 1024;
constexpr int ATT_LDS = L_STG + 4 * 6144;
constexpr int NC = 64 / NT;
constexpr int PSW = 53;
constexpr int SPIN_MAX = 1 << 22;

extern "C" __global__ __launch_bounds__(768) void k2_attn(char* __restrict__ ws, const bf16* __restrict__ x,
                                               const bf16* __restrict__ wg, const bf16* __restrict__ wu,
                                               const bf16* __restrict__ wd, const bf16* __restrict__ wo,
                                               bf16* __restrict__ out) {
  const bf16* Q = (const bf16*)(ws + WS_Q);
  const bf16* Kb = (const bf16*)(ws + WS_K);
  bf16* O = (bf16*)(ws + WS_O);
  extern __shared__ __attribute__((aligned(16))) char smem[];
  char* kl = smem;
  char* vl = smem + 65536;
  char* cst = smem + 131072;
  const int tid = threadIdx.x, lane = tid & 63;
  const int wave = __builtin_amdgcn_readfirstlane(tid >> 6);
  unsigned tsv[16];
#pragma unroll
  for (int i_ = 0; i_ < 16; ++i_) tsv[i_] = 0;

  const unsigned lds0 = (unsigned)(size_t)(LDS_AS char*)smem;
  unsigned long long pc = (unsigned long long)ws + WS_R, pg = (unsigned long long)wg, pu = (unsigned long long)wu, pd = (unsigned long long)wd;
  asm volatile("" : "+s"(pc), "+s"(pg), "+s"(pu), "+s"(pd));
  if (wave >= 8) {
    const int sw = wave - 8, r = lane & 31, h = lane >> 5;
    u32x16 cv[4];
#pragma unroll
    for (int i = 0; i < 4; ++i) cv[i] = ((const CONST_AS u32x16*)pc)[i];
    unsigned l16 = lane * 16;
    const int fs = (blockIdx.x & 7) * 22 + (blockIdx.x >> 3);
    const bool ffn = blockIdx.x < 176 && fs < NFFN;
    const unsigned stg = lds0 + L_STG + sw * 6144;
    const int n = lane & 15, g = lane >> 4, q = n >> 2, p = lane & 3;
    unsigned a_gu = stg + (p >> 1) * 2048 + (8 * g + q) * 16 + (p & 1) * 8;
    unsigned a_dn = stg + 4096 + q * 256 + (16 * g + 4 * p) * 2;
    unsigned a_rw = lds0 + L_RED + (n & 7) * 32 + sw * 8 + (n >> 3) * 4;
    unsigned a_rr = lds0 + L_RED + n * 32;
    unsigned a_hw = lds0 + L_H + sw * 32 + n * 2;
    const unsigned hrow = lds0 + L_H + sw * 32 + (n >> 3) * 16, zrow = lds0 + 131072 + 16;
    unsigned a_h0 = (p == g && (q & 1) == 0) ? hrow : zrow, a_h1 = (p == g && (q & 1) == 1) ? hrow : zrow;
    unsigned a_cnt = lds0 + L_CNT, one = 1, odd = lane & 1, even = (lane & 1) ^ 1, g16 = g * 16;
    unsigned v_w = (lane >> 3) * 1024 + (((lane & 7) ^ ((lane >> 4 & 1) << 2)) << 4);
    unsigned v_x = r * 1024 + h * 8, v_s = r * 8;
    unsigned v_y = n * 4096 + g * 16, v_c = n * 256, v_e = n < 12 ? 11 : 10;
    asm volatile("" : "+v"(l16), "+v"(a_gu), "+v"(a_dn), "+v"(a_rw), "+v"(a_rr), "+v"(a_hw), "+v"(a_h0), "+v"(a_h1),
                 "+v"(a_cnt), "+v"(one), "+v"(odd), "+v"(even), "+v"(g16));
    unsigned vg = lane * (F_ * 2), vd = g * 1024 + n * 16;
    asm volatile("" : "+v"(v_w), "+v"(v_x), "+v"(v_s), "+v"(v_y), "+v"(v_c), "+v"(v_e), "+v"(vg), "+v"(vd));
    {
      const __amdgpu_buffer_rsrc_t rk = __builtin_amdgcn_make_buffer_rsrc((void*)Kb, 0, 131072, 0x00020000);
#pragma unroll
      for (int i = 0; i < 13; ++i) {
        const unsigned o = sw * 2048 + (i < 6 ? 16 + 8 * i : 24 + 8 * i) * 1024;
        blds16x2(rk, l16, o, lds0 + o);
      }
    }
    asm volatile("s_barrier" ::: "memory");
    asm volatile("" : "+s"(cv[0]), "+s"(cv[1]), "+s"(cv[2]), "+s"(cv[3]));
    unsigned k1 = cv[0][0], k2 = cv[0][1];
#pragma unroll
    for (int i = 1; i < 32; ++i) {
      const unsigned a1 = cv[i >> 3][(i & 7) * 2], a2 = cv[i >> 3][(i & 7) * 2 + 1];
      unsigned t;
      asm volatile("s_min_u32 %2, %0, %3\n s_max_u32 %1, %1, %4\n s_max_u32 %0, %0, %3\n s_max_u32 %1, %1, %2"
          : "+s"(k1), "+s"(k2), "=&s"(t) : "s"(a1), "s"(a2) : "scc");
    }
    const __amdgpu_buffer_rsrc_t rc = __builtin_amdgcn_make_buffer_rsrc(ws + WS_CNT, 0, NCNT * 256, 0x00020000);
    const __amdgpu_buffer_rsrc_t ry = __builtin_amdgcn_make_buffer_rsrc(ws + WS_YP, 0, NFFN * 4096, 0x00020000);
    const __amdgpu_buffer_rsrc_t rup = __builtin_amdgcn_make_buffer_rsrc(ws + WS_UPD, 0, 4096, 0x00020000);
    asm volatile("s_waitcnt vmcnt(0)\n s_barrier" ::: "memory");
    if (ffn) {
      const __amdgpu_buffer_rsrc_t rx = __builtin_amdgcn_make_buffer_rsrc(ws + WS_CX, 0, 65536, 0x00020000);
      u32x4 xf[4];
      __builtin_amdgcn_s_setprio(3);
      unsigned vx = __umul24(even, key_row(k1) * 1024) + g16;
      vx += __umul24(odd, key_row(k2) * 1024);
      asm volatile("" : "+v"(vx));
      __builtin_amdgcn_s_setprio(0);
#pragma unroll
      for (int i = 0; i < 4; ++i) xf[i] = __builtin_amdgcn_raw_buffer_load_b128(rx, vx + i * 64, sw * 256, 0);
      const __amdgpu_buffer_rsrc_t rg = __builtin_amdgcn_make_buffer_rsrc((void*)pg, 0, 512 * F_ * 2, 0x00020000);
      const __amdgpu_buffer_rsrc_t ru = __builtin_amdgcn_make_buffer_rsrc((void*)pu, 0, 512 * F_ * 2, 0x00020000);
      const __amdgpu_buffer_rsrc_t rd = __builtin_amdgcn_make_buffer_rsrc((void*)pd, 0, 512 * F_ * 2, 0x00020000);
      const unsigned so = (sw * 128 * F_ + fs * 8) * 2, sd = (fs * 8 * 512 + sw * 128) * 2;
      blds16(rg, vg, so, stg);
      blds16(rg, vg, so + 64 * F_ * 2, stg + 1024);
      blds16(ru, vg, so, stg + 2048);
      blds16(ru, vg, so + 64 * F_ * 2, stg + 3072);
      blds16(rd, vd, sd, stg + 4096);
      blds16(rd, vd, sd + 4096, stg + 5120);
      asm volatile("s_waitcnt vmcnt(0)" ::: "memory");
      bf16x8 wf[4], df[2];
#pragma unroll
      for (int i = 0; i < 4; ++i) wf[i] = tr_read8(a_gu + i * 512, a_gu + i * 512 + 64);
#pragma unroll
      for (int i = 0; i < 2; ++i) df[i] = tr_read8(a_dn + i * 128, a_dn + i * 128 + 1024);
      asm volatile("s_waitcnt lgkmcnt(0)" ::: "memory");
      __builtin_amdgcn_s_setprio(3);
      f32x4 gu = {0.f, 0.f, 0.f, 0.f};
#pragma unroll
      for (int i = 0; i < 4; ++i) gu = __builtin_amdgcn_mfma_f32_16x16x32_bf16(__builtin_bit_cast(bf16x8, xf[i]), wf[i], gu, 0, 0, 0);
      *LDSP(float, a_rw) = gu[0];
      *LDSP(float, a_rw + 256) = gu[1];
      asm volatile("s_mov_b64 exec, 1\n ds_add_u32 %0, %1\n s_mov_b64 exec, -1" ::"v"(a_cnt), "v"(one) : "memory");
      while (*(volatile LDS_AS unsigned*)LDSP(unsigned, a_cnt) < 4) __builtin_amdgcn_s_sleep(1);
      asm volatile("" ::: "memory");
      const f32x4 r0 = *LDSP(f32x4, a_rr), r1 = *LDSP(f32x4, a_rr + 16);
      const f32x2 gs = unpack2(pack2((f32x2{r0[0], r0[1]} + f32x2{r0[2], r0[3]}) + (f32x2{r1[0], r1[1]} + f32x2{r1[2], r1[3]})));
      const float hv = rbf(gs.x * __builtin_amdgcn_rcpf(1.f + __expf(-gs.x))) * gs.y;
      *LDSP(bf16, a_hw) = (bf16)hv;
      asm volatile("" ::: "memory");
      const bf16x8 h0 = *LDSP(bf16x8, a_h0), h1 = *LDSP(bf16x8, a_h1);
      f32x4 y = {0.f, 0.f, 0.f, 0.f};
      y = __builtin_amdgcn_mfma_f32_16x16x32_bf16(h0, df[0], y, 0, 0, 0);
      y = __builtin_amdgcn_mfma_f32_16x16x32_bf16(h1, df[1], y, 0, 0, 0);
      __builtin_amdgcn_raw_buffer_store_b128(__builtin_bit_cast(u32x4, y), ry, l16, fs * 4096 + sw * 1024, 16);
      __builtin_amdgcn_s_setprio(0);
      asm volatile("s_waitcnt vmcnt(0)" ::: "memory");
      count_up(rc, one, (sw * 16 + (fs & 15)) * 256);
      tsv[5] = CLK();
    }
    const int tb = (blockIdx.x >> 6) * 8 + (blockIdx.x & 7), cb = (blockIdx.x >> 3) & 7, tok0 = tb * 64;
    char* ximg = smem;
    char* wimg = smem + 64 * XS_STRIDE;
    const int n0 = (sw & 1) * 32, t0 = (sw >> 1) * 32;
    const unsigned so_x = (tok0 + t0) * 1024 + (cb * 64 + n0) * 2;
    const __amdgpu_buffer_rsrc_t rout = __builtin_amdgcn_make_buffer_rsrc(out, 0, S_ * D_ * 2, 0x00020000);
    u32x4 wreg[16];
    u32x2 xv[4], nv[4], sxr;
    {
      const __amdgpu_buffer_rsrc_t rw = __builtin_amdgcn_make_buffer_rsrc((void*)wo, 0, 512 * 1024, 0x00020000);
      const __amdgpu_buffer_rsrc_t rx = __builtin_amdgcn_make_buffer_rsrc((void*)x, 0, S_ * D_ * 2, 0x00020000);
      const __amdgpu_buffer_rsrc_t rs = __builtin_amdgcn_make_buffer_rsrc(ws + WS_ST, 0, S_ * 8, 0x00020000);
#pragma unroll
      for (int i = 0; i < 16; ++i) wreg[i] = __builtin_amdgcn_raw_buffer_load_b128(rw, v_w, (sw + 4 * i) * 8192 + cb * 128, 0);
#pragma unroll
      for (int i = 0; i < 4; ++i) xv[i] = __builtin_amdgcn_raw_buffer_load_b64(rx, v_x + i * 16, so_x, 0);
      sxr = __builtin_amdgcn_raw_buffer_load_b64(rs, v_s, (tok0 + t0) * 8, 0);
      if (wave == 11) {
        const unsigned t = __builtin_amdgcn_raw_buffer_load_b32(rout, 0, so_x, 0);
        asm volatile("" ::"v"(t));
      }
    }
    if ((blockIdx.x >> 4) == 11) {
      const int sel = (blockIdx.x >> 3) & 1, cq = blockIdx.x & 7;
      __builtin_amdgcn_s_setprio(3);
      int spin = 0;
      while (__any(__builtin_amdgcn_raw_buffer_load_b32(rc, v_c, (cq >> 1) * 4096, 16) < v_e) && ++spin < SPIN_MAX)
        asm volatile("s_sleep 8" ::: "memory");
      __builtin_amdgcn_s_setprio(0);
      asm volatile("" ::: "memory");
      const unsigned so = (cq >> 1) * 1024 + (2 * sel + (cq & 1)) * 256 + sw * 64;
      u32x4 t[11];
#pragma unroll
      for (int i = 0; i < 10; ++i) t[i] = __builtin_amdgcn_raw_buffer_load_b128(ry, v_y, so + i * 65536, 16);
      __builtin_amdgcn_s_setprio(3);
      t[10] = u32x4{0u, 0u, 0u, 0u};
      if (v_e == 11) t[10] = __builtin_amdgcn_raw_buffer_load_b128(ry, v_y, so + 10 * 65536, 16);
      const float gate = rbf(__builtin_amdgcn_rcpf(1.f + __expf(-key_val(sel ? k2 : k1))));
      f32x4 a = __builtin_bit_cast(f32x4, t[0]);
#pragma unroll
      for (int i = 1; i < 11; ++i) a += __builtin_bit_cast(f32x4, t[i]);
#pragma unroll
      for (int i = 0; i < 4; ++i) a[i] = dpp_add<0x140>(sum8(a[i]));
      asm volatile("" : "+v"(a));
      const float u = rbf(rbf(p == 0 ? a[0] : p == 1 ? a[1] : p == 2 ? a[2] : a[3]) * gate);
      if (n < 4) __builtin_amdgcn_raw_buffer_store_b32(__float_as_uint(u), rup, n * 64 + g * 4, (sel * 512 + cq * 64 + sw * 4) * 4, 16);
      __builtin_amdgcn_s_setprio(0);
      asm volatile("s_waitcnt vmcnt(0)" ::: "memory");
      count_up(rc, one, (64 + sel * 8 + cq) * 256);
      tsv[6] = CLK();
    }
    const bool routed = ((key_tok(k1) ^ (tok0 + t0)) >> 5) == 0 || ((key_tok(k2) ^ (tok0 + t0)) >> 5) == 0;
    u32x4 uf[4];
    if (routed) {
      __builtin_amdgcn_s_setprio(3);
      const int tok = tok0 + t0 + r;
      const int rsel = tok == key_tok(k1) ? 0 : tok == key_tok(k2) ? 1 : -1;
#pragma unroll
      for (int i = 0; i < 4; ++i) uf[i] = u32x4{0x80000000u, 0x80000000u, 0x80000000u, 0x80000000u};
      if (rsel >= 0) {
        int spin = 0;
        while (__builtin_amdgcn_raw_buffer_load_b32(rc, rsel * 2048, (64 + cb) * 256, 16) < 4 && ++spin < SPIN_MAX)
          asm volatile("s_sleep 4" ::: "memory");
        asm volatile("" ::: "memory");
#pragma unroll
        for (int i = 0; i < 4; ++i)
          uf[i] = __builtin_amdgcn_raw_buffer_load_b128(rup, (rsel * 512 + 8 * i + 4 * h) * 4, (cb * 64 + n0) * 4, 16);
      }
#pragma unroll
      for (int i = 0; i < 4; ++i) asm volatile("" : "+v"(uf[i]));
      __builtin_amdgcn_s_setprio(0);
    }
#pragma unroll
    for (int i = 0; i < 16; ++i) asm volatile("" : "+v"(wreg[i]));
    asm volatile("s_barrier" ::: "memory");
    tsv[8] = CLK();
    if (wave == 8) count_up(rc, one, (80 + tb) * 256);
#pragma unroll
    for (int i = 0; i < 16; ++i) *(u32x4*)(wimg + (sw + 4 * i) * 1024 + lane * 16) = wreg[i];
    {
      const f32x2 mb2 = {__uint_as_float(sxr[0]), __uint_as_float(sxr[0])}, ri2 = {__uint_as_float(sxr[1]), __uint_as_float(sxr[1])};
#pragma unroll
      for (int i = 0; i < 4; ++i) {
#pragma unroll
        for (int jj = 0; jj < 2; ++jj) nv[i][jj] = ln2(unpack2(xv[i][jj]), mb2, ri2);
      }
    }
    if (routed) {
#pragma unroll
      for (int i = 0; i < 4; ++i) {
        const f32x4 u = __builtin_bit_cast(f32x4, uf[i]);
        nv[i][0] = pack2(unpack2(nv[i][0]) + f32x2{u[0], u[1]});
        nv[i][1] = pack2(unpack2(nv[i][1]) + f32x2{u[2], u[3]});
      }
    }
    if (wave == 8) {
      int spin = 0;
      while (__hip_atomic_load((GLB_AS unsigned*)(ws + WS_CL + tb * 256), __ATOMIC_RELAXED, __HIP_MEMORY_SCOPE_AGENT) < 8 && ++spin < SPIN_MAX)
        __builtin_amdgcn_s_sleep(1);
    }
    asm volatile("s_barrier" ::: "memory");
    tsv[9] = CLK();
    {
      const __amdgpu_buffer_rsrc_t ro = __builtin_amdgcn_make_buffer_rsrc(O + tok0 * 512, 0, 65536, 0x00020000);
      u32x4 t[16];
#pragma unroll
      for (int i = 0; i < 16; ++i) t[i] = __builtin_amdgcn_raw_buffer_load_b128(ro, l16, (sw * 16 + i) * 1024, 16);
#pragma unroll
      for (int i = 0; i < 16; ++i) *(u32x4*)(ximg + (sw * 16 + i) * XS_STRIDE + lane * 16) = t[i];
    }
    asm volatile("s_waitcnt lgkmcnt(0)\n s_barrier" ::: "memory");
    tsv[11] = CLK();
    f32x16 acc = gemm_tile(wimg, ximg, n0, t0, lane);
    tsv[12] = (__builtin_amdgcn_readfirstlane(__float_as_int(acc[0])) != 0x12345678) ? CLK() : 0u;
#pragma unroll
    for (int g = 0; g < 4; ++g) {
      u32x2 o;
#pragma unroll
      for (int jj = 0; jj < 2; ++jj) {
        const unsigned xr = xv[g][jj], nr = nv[g][jj];
        const f32x2 a = unpack2(pack2(f32x2{acc[4 * g + 2 * jj], acc[4 * g + 2 * jj + 1]}));
        o[jj] = pack2(unpack2(pack2(unpack2(xr) + a)) + unpack2(nr));
      }
      __builtin_amdgcn_raw_buffer_store_b64(o, rout, v_x + g * 16, so_x, 0);
    }
    tsv[13] = CLK();
    { if ((threadIdx.x & 63) == 0) {
#pragma unroll
 for (int i_ = 0; i_ < 16; ++i_) G_TS(ws)[1][blockIdx.x][threadIdx.x >> 6][i_] = tsv[i_]; } }
    return;
  }

  const int r = lane & 31, h = lane >> 5;
  const int token = ((blockIdx.x >> 6) * 8 + (blockIdx.x & 7)) * 64 + ((blockIdx.x >> 3) & 7) * 8 + wave;
  tsv[0] = CLK();
  bf16x8 qf;
  asm volatile("global_load_dwordx4 %0, %1, off" : "=v"(qf) : "v"(Q + token * 512 + r * 16 + 8 * h) : "memory");
#pragma unroll
  for (int i = 0; i < 3; ++i) {
    const int piece = (i < 2) ? (wave + i * 8) : (64 + wave);
    glds16_raw((const char*)Kb + piece * 1024 + lane * 16, lds0 + piece * 1024);
  }
  {
    const unsigned cv = ((tid & 30) == 0 || (tid & 30) == 8) ? 0x3f803f80u : 0u;
    *(u32x2*)(cst + tid * 8) = u32x2{cv, cv};
    if (tid == 1) *(unsigned*)(smem + L_CNT) = 0;
  }
  int vo = (r < 16) ? 65536 + (h * 16 + r) * 16 : 131072 + (h * 16 + r - 16) * 16;
  int vstep = (r < 16) ? CST : 0;
  int sel = 0xdddd;
  f32x16 zero;
#pragma unroll
  for (int i = 0; i < 16; ++i) zero[i] = 0.f;
  f32x16 acc = zero;
  asm volatile("" : "+v"(sel), "+v"(acc), "+v"(vstep), "+v"(vo));
  const char* kp0 = kl + lane * 16;
  const char* vp0 = smem + vo;
  tsv[1] = CLK();
  asm volatile("s_waitcnt vmcnt(0) lgkmcnt(0)\n s_barrier" ::: "memory");
  tsv[2] = CLK();

  constexpr float HEAD = 40.f;
  f32x16 negm, xs;
  bf16x8 kfn;
  {
    bf16x8 kf[4];
    f32x16 s0[4];
#pragma unroll
    for (int t = 0; t < 4; ++t) kf[t] = *(const bf16x8*)(kp0 + t * 1024);
#pragma unroll
    for (int t = 0; t < 4; ++t) {
      s0[t] = __builtin_amdgcn_mfma_f32_32x32x16_bf16(kf[t], qf, zero, 0, 0, 0);
      asm volatile("" : "+v"(s0[t]));
    }
    __builtin_amdgcn_sched_barrier(0);
    float mx = -INFINITY;
#pragma unroll
    for (int t = 0; t < 4; ++t) {
#pragma unroll
      for (int i = 0; i < 16; i += 2) mx = fmaxf(fmaxf(mx, s0[t][i]), s0[t][i + 1]);
    }
    const float nm = -(floorf(max32(mx)) + HEAD);
#pragma unroll
    for (int i = 0; i < 16; ++i) {
      xs[i] = s0[0][i] + nm;
      negm[i] = nm;
    }
    kfn = kf[1];
  }
  asm volatile("" : "+v"(negm));
  {
    const char* kp = kp0;
    const char* vp = vp0;
    f32x16 x = xs;
    if (wave < 4) __builtin_amdgcn_s_setprio(2);
    else __builtin_amdgcn_s_setprio(1);
    for (int c = 0; c < NC; ++c) {
      bool grown;
#pragma unroll
      for (int t = 0; t < NT; ++t) {
        const bf16x8 kf = kfn;
        kfn = *(const bf16x8*)(kp + (t + 2) * 1024);
        const bf16x8 vf0 = *(const bf16x8*)(vp + ((t & 3) * 2) * 512);
        const bf16x8 vf1 = *(const bf16x8*)(vp + ((t & 3) * 2 + 1) * 512);
        if ((t & 3) == 3) vp += vstep;
        f32x8 p0, p1;
#pragma unroll
        for (int j = 0; j < 4; ++j) p0[j] = __builtin_amdgcn_exp2f(x[j]);
        __builtin_amdgcn_sched_barrier(0);
        f32x16 xn = __builtin_amdgcn_mfma_f32_32x32x16_bf16(kf, qf, negm, 0, 0, 0);
        asm volatile("" : "+v"(xn));
        __builtin_amdgcn_sched_barrier(0);
#pragma unroll
        for (int j = 4; j < 8; ++j) p0[j] = __builtin_amdgcn_exp2f(x[j]);
        if (t == NT - 1) grown = __any(!(acc[8] <= 4.3e9f));
        acc = pv_mfma(vf0, p0, acc, sel);
#pragma unroll
        for (int j = 0; j < 8; ++j) p1[j] = __builtin_amdgcn_exp2f(x[8 + j]);
        acc = pv_mfma(vf1, p1, acc, sel);
        if (t == 7 && c == 0) asm volatile("s_barrier" ::: "memory");
        if (c * NT + t == PSW && wave < 4) __builtin_amdgcn_s_setprio(0);
        __builtin_amdgcn_sched_barrier(0);
        x = xn;
      }
      kp += NT * 1024;
      if (grown) {
        const int e = max(__builtin_amdgcn_frexp_expf(acc[8]) - 1 + (int)HEAD, 0);
#pragma unroll
        for (int i = 0; i < 9; ++i) acc[i] = __builtin_amdgcn_ldexpf(acc[i], -e);
        const float ef = (float)e;
#pragma unroll
        for (int i = 0; i < 16; ++i) {
          negm[i] -= ef;
          x[i] -= ef;
        }
        asm volatile("" : "+v"(negm));
      }
    }
  }
  tsv[3] = (__builtin_amdgcn_readfirstlane(__float_as_int(acc[8])) != 0x12345678) ? CLK() : 0u;
  {
    float chk = 0.f;
#pragma unroll
    for (int i = 0; i < 9; ++i) chk = __builtin_fmaf(acc[i], 0.f, chk);
    if (__any(chk != 0.f)) {
      f32x16 oacc = zero;
      float mrun = -INFINITY;
      const char* kp = kp0;
      const char* vp = vp0;
      for (int t = 0; t < 64; ++t) {
        bf16x8 kf = *(const bf16x8*)(kp);
        f32x16 x = __builtin_amdgcn_mfma_f32_32x32x16_bf16(kf, qf, zero, 0, 0, 0);
        float mx = x[0];
#pragma unroll
        for (int i = 1; i < 16; ++i) mx = fmaxf(mx, x[i]);
        const float mn = fmaxf(mrun, max32(mx));
        const float al = __builtin_amdgcn_exp2f(mrun - mn);
        mrun = mn;
#pragma unroll
        for (int i = 0; i < 16; ++i) oacc[i] *= al;
#pragma unroll
        for (int s = 0; s < 2; ++s) {
          f32x8 pf;
#pragma unroll
          for (int j = 0; j < 8; ++j) pf[j] = __builtin_amdgcn_exp2f(x[8 * s + j] - mn);
          bf16x8 vf = *(const bf16x8*)(vp + ((t & 3) * 2 + s) * 512);
          oacc = pv_mfma(vf, pf, oacc, sel);
        }
        kp += 1024;
        if ((t & 3) == 3) vp += vstep;
      }
      acc = oacc;
    }
  }
  const float inv = 1.f / acc[8];
  {
    unsigned a0 = pack2(f32x2{acc[0] * inv, acc[1] * inv}), a1 = pack2(f32x2{acc[2] * inv, acc[3] * inv});
    unsigned b0 = pack2(f32x2{acc[4] * inv, acc[5] * inv}), b1 = pack2(f32x2{acc[6] * inv, acc[7] * inv});
    swap32(a0, b0);
    swap32(a1, b1);
    const u32x4 o = {a0, a1, b0, b1};
    const __amdgpu_buffer_rsrc_t ro = __builtin_amdgcn_make_buffer_rsrc(O + token * 512, 0, 1024, 0x00020000);
    __builtin_amdgcn_raw_buffer_store_b128(o, ro, r * 32 + h * 16, 0, 16);
  }
  asm volatile("s_waitcnt vmcnt(0)\n s_barrier" ::: "memory");
  tsv[4] = CLK();
  { if ((threadIdx.x & 63) == 0) {
#pragma unroll
 for (int i_ = 0; i_ < 16; ++i_) G_TS(ws)[1][blockIdx.x][threadIdx.x >> 6][i_] = tsv[i_]; } }

}

constexpr int K1_LDS = 64 * XS_STRIDE + 65536 + 16384 + 256;

extern "C" __global__ __launch_bounds__(768) void k1_ln_qkv(const bf16* __restrict__ x, const bf16* __restrict__ wq,
                                                 const bf16* __restrict__ wk, const bf16* __restrict__ wv,
                                                 const bf16* __restrict__ wr, char* __restrict__ ws) {
  float* st = (float*)(ws + WS_ST);
  bf16* Q = (bf16*)(ws + WS_Q);
  bf16* Kb = (bf16*)(ws + WS_K);
  bf16* Vf = (bf16*)(ws + WS_V);
  unsigned* cand = (unsigned*)(ws + WS_R);
  extern __shared__ __attribute__((aligned(16))) char smem[];
  char* ximg = smem;
  char* wimg = smem + 64 * XS_STRIDE;
  char* kvimg = smem + 64 * XS_STRIDE + 65536;
  float* rs = (float*)(smem + 64 * XS_STRIDE + 65536 + 16384);
  const int cb = blockIdx.x >> 5, tb = blockIdx.x & 31;
  const int tid = threadIdx.x, lane = tid & 63;
  const int wave = __builtin_amdgcn_readfirstlane(tid >> 6);
  unsigned tsv[16];
#pragma unroll
  for (int i_ = 0; i_ < 16; ++i_) tsv[i_] = 0;
  const int tok0 = tb * 64;
  tsv[0] = CLK();

  if (wave >= 8) {
    if (blockIdx.x == 255 && tid >= 768 - NCNT) ((unsigned*)(ws + WS_CNT))[(767 - tid) * 64] = 0;
    stage_w(wq, cb * 64, wimg, wave - 8, 4, lane);
    if (cb < 2) {
      const bf16* w = cb == 0 ? wk : wv;
#pragma unroll
      for (int i = 0; i < 4; ++i) {
        const int p = wave - 8 + 4 * i;
        glds16((const char*)w + p * 1024 + lane * 16, kvimg + p * 1024);
      }
    }
    __syncthreads();
    if (cb == 3 && wave == 8) {
      unsigned k1 = route_key(rs[lane], tok0 + lane), k2 = 0;
      top2_wave(k1, k2, 64);
      if (lane == 0) *(u32x2*)(cand + tb * 2) = u32x2{k1, k2 | 1u};
      const u32x4 a = *(const u32x4*)(ximg + (key_tok(k1) & 63) * XS_STRIDE + lane * 16);
      const u32x4 b = *(const u32x4*)(ximg + (key_tok(k2) & 63) * XS_STRIDE + lane * 16);
      *(u32x4*)(ws + WS_CX + tb * 2048 + lane * 16) = a;
      *(u32x4*)(ws + WS_CX + tb * 2048 + 1024 + lane * 16) = b;
    }
    { if ((threadIdx.x & 63) == 0) {
#pragma unroll
 for (int i_ = 0; i_ < 16; ++i_) G_TS(ws)[0][blockIdx.x][threadIdx.x >> 6][i_] = tsv[i_]; } }
    return;
  }

  const int tl = wave * 8 + (lane >> 3), c8 = lane & 7;
  u32x4 xv[8];
#pragma unroll
  for (int i = 0; i < 8; ++i) xv[i] = *(const u32x4*)(x + (tok0 + tl) * 512 + (c8 + 8 * i) * 8);
  u32x4 wrv[8];
  if (cb == 3) {
#pragma unroll
    for (int i = 0; i < 8; ++i) wrv[i] = *(const u32x4*)(wr + (c8 + 8 * i) * 8);
  }

  tsv[1] = CLK();
  f32x2 xf[32];
#pragma unroll
  for (int i = 0; i < 32; ++i) xf[i] = unpack2(xv[i >> 2][i & 3]);
  f32x2 s2 = {0.f, 0.f};
#pragma unroll
  for (int i = 0; i < 32; ++i) s2 += xf[i];
  const float mean = sum8(s2.x + s2.y) * (1.f / 512.f);
  const f32x2 mean2 = {mean, mean};
  f32x2 v2 = {0.f, 0.f};
#pragma unroll
  for (int i = 0; i < 32; ++i) { const f32x2 d = xf[i] - mean2; v2 += d * d; }
  const float vs = sum8(v2.x + v2.y);
  tsv[2] = CLK();
  const float mb = rbf(mean);
  const float vb = rbf(vs * (1.f / 512.f));
  const float den = rbf(sqrtf(rbf(vb + 1e-5f)));
  const float rinv = __builtin_amdgcn_rcpf(den);
  const f32x2 mb2 = {mb, mb}, rinv2 = {rinv, rinv};
  u32x4 o[8];
#pragma unroll
  for (int i = 0; i < 8; ++i) {
#pragma unroll
    for (int j = 0; j < 4; ++j) o[i][j] = ln2(xf[4 * i + j], mb2, rinv2);
    *(u32x4*)(ximg + tl * XS_STRIDE + (c8 + 8 * i) * 16) = o[i];
  }
  if (cb == 2 && c8 == 0) *(f32x2*)(st + (tok0 + tl) * 2) = f32x2{mb, rinv};
  if (cb == 3) {
    float rd = 0.f;
#pragma unroll
    for (int i = 0; i < 8; ++i) {
#pragma unroll
      for (int j = 0; j < 4; ++j) {
        const unsigned a = o[i][j], b = wrv[i][j];
        rd = __builtin_amdgcn_fdot2_f32_bf16(__builtin_bit_cast(bf16x2, a), __builtin_bit_cast(bf16x2, b), rd, false);
      }
    }
    rd = sum8(rd);
    if (c8 == 0) rs[tl] = rbf(rd);
  }
  tsv[3] = CLK();
  __syncthreads();
  tsv[4] = CLK();

  const int r = lane & 31, h = lane >> 5;
  if (wave < 4) {
    const int n0 = (wave & 1) * 32, t0 = (wave >> 1) * 32;
    f32x16 acc = gemm_tile(wimg, ximg, n0, t0, lane);
    tsv[5] = (__builtin_amdgcn_readfirstlane(__float_as_int(acc[0])) != 0x12345678) ? CLK() : 0u;
    const float sc = 0.25f * 1.4426950408889634f;
    const __amdgpu_buffer_rsrc_t rq = __builtin_amdgcn_make_buffer_rsrc(Q + tok0 * 512, 0, 65536, 0x00020000);
#pragma unroll
    for (int m = 0; m < 2; ++m) {
      unsigned p[4];
#pragma unroll
      for (int i = 0; i < 4; ++i) p[i] = pack2(unpack2(pack2(f32x2{acc[8 * m + 2 * i], acc[8 * m + 2 * i + 1]})) * f32x2{sc, sc});
      swap32(p[0], p[2]);
      swap32(p[1], p[3]);
      const u32x4 o = {p[0], p[1], p[2], p[3]};
      __builtin_amdgcn_raw_buffer_store_b128(o, rq, ((t0 + r) * 512 + cb * 64 + n0 + 16 * m + 8 * h) * 2, 0, 0);
    }
  } else if (wave == 7) {
    if (lane < 2) {
      const unsigned t = *(const volatile unsigned*)(lane ? ws + WS_K + tb * 2048 : (const char*)(Q + tok0 * 512 + cb * 64));
      asm volatile("" ::"v"(t));
    }
  } else if (wave == 6 && cb == 7 && tb < 8) {
    unsigned long long c0, c1;
    CODE_ADDR(k2_attn, c0);
    CODE_ADDR(k1_ln_qkv, c1);
    const long long n = (long long)(c1 - c0);
    unsigned t[4];
#pragma unroll
    for (int i = 0; i < 4; ++i) {
      const int off = (i * 64 + lane) * 128;
      t[i] = off < n ? *(const volatile unsigned*)(c0 + off) : 0u;
    }
#pragma unroll
    for (int i = 0; i < 4; ++i) asm volatile("" ::"v"(t[i]));
  } else if (wave < 6 && cb < 2) {
    const int t0 = (wave - 4) * 32;
    f32x16 acc = gemm_tile16(kvimg, ximg, t0, lane);
    const int key = tok0 + t0 + r;
    const int t = key >> 5;
    if (cb == 0) {
#pragma unroll
      for (int g = 0; g < 2; ++g) {
        bf16x4 o;
#pragma unroll
        for (int j = 0; j < 4; ++j) o[j] = (bf16)acc[4 * g + j];
        *(bf16x4*)(Kb + ((t * 2 + g) * 32 + r) * 8 + 4 * h) = o;
      }
    } else {
      const int kk = key & 31, sg = kk >> 4, rem = kk & 15;
      const int hh = rem >> 3, jj = rem & 7;
#pragma unroll
      for (int g = 0; g < 2; ++g) {
#pragma unroll
        for (int j = 0; j < 4; ++j) {
          const int d = 8 * g + 4 * h + j;
          Vf[(((t * 2 + sg) * 2 + hh) * 16 + d) * 8 + jj] = (bf16)acc[4 * g + j];
        }
      }
    }
  }
  tsv[6] = CLK();
  { if ((threadIdx.x & 63) == 0) {
#pragma unroll
 for (int i_ = 0; i_ < 16; ++i_) G_TS(ws)[0][blockIdx.x][threadIdx.x >> 6][i_] = tsv[i_]; } }

}


extern "C" __global__ __launch_bounds__(1024) void k3_delay(char* ws) {
  __shared__ int r[2][4];
  if (threadIdx.x < 2) { r[threadIdx.x][0] = 0x7fffffff; r[threadIdx.x][1] = -0x7fffffff; r[threadIdx.x][2] = 0; r[threadIdx.x][3] = 0; }
  __syncthreads();
  const unsigned base = G_TS(ws)[0][0][0][0];
  for (int e = threadIdx.x; e < 3072; e += 1024) {
    const unsigned a = G_TS(ws)[1][e / 12][e % 12][0], b = G_TS(ws)[1][e / 12][e % 12][2];
    if (a) { const int d = (int)(a - base); atomicMin(&r[0][0], d); atomicMax(&r[0][1], d); atomicAdd(&r[0][2], d); atomicAdd(&r[0][3], 1); }
    if (b) { const int d = (int)(b - base); atomicMin(&r[1][0], d); atomicMax(&r[1][1], d); atomicAdd(&r[1][2], d); atomicAdd(&r[1][3], 1); }
  }
  __syncthreads();
  if (threadIdx.x == 0) {
    const float va = 0 == 2 ? (float)r[0][2] / (float)max(r[0][3], 1) : (float)r[0][0];
    const float vb = 2 == 2 ? (float)r[1][2] / (float)max(r[1][3], 1) : (float)r[1][2];
    int d = (int)((vb - va) * 5.f);
    d = min(max(d, 0), 30000);
    const unsigned t0 = CLK();
    while ((int)(CLK() - t0) < d) __builtin_amdgcn_s_sleep(1);
  }
}
extern "C" void kernel_launch(void* const* d_in, const int* in_sizes, int n_in, void* d_out, int out_size,
                              void* d_ws, size_t ws_size, hipStream_t stream) {
  const bf16* x = (const bf16*)d_in[0];
  const bf16* wq = (const bf16*)d_in[1];
  const bf16* wk = (const bf16*)d_in[2];
  const bf16* wv = (const bf16*)d_in[3];
  const bf16* wo = (const bf16*)d_in[4];
  const bf16* wr = (const bf16*)d_in[6];
  const bf16* wg = (const bf16*)d_in[7];
  const bf16* wu = (const bf16*)d_in[8];
  const bf16* wd = (const bf16*)d_in[9];
  char* ws = (char*)d_ws;
  bf16* out = (bf16*)d_out;

  (void)hipFuncSetAttribute((const void*)k1_ln_qkv, hipFuncAttributeMaxDynamicSharedMemorySize, K1_LDS);
  (void)hipFuncSetAttribute((const void*)k2_attn, hipFuncAttributeMaxDynamicSharedMemorySize, ATT_LDS);

  k1_ln_qkv<<<256, 768, K1_LDS, stream>>>(x, wq, wk, wv, wr, ws);
  k2_attn<<<256, 768, ATT_LDS, stream>>>(ws, x, wg, wu, wd, wo, out);
  k3_delay<<<1, 1024, 0, stream>>>(ws);
}
```

```cpp
#include <hip/hip_runtime.h>
#include <hip/hip_bf16.h>

typedef __attribute__((ext_vector_type(8))) __bf16 bf16x8;
typedef __attribute__((ext_vector_type(16))) __bf16 bf16x16;
typedef __attribute__((ext_vector_type(8))) float f32x8;
typedef __attribute__((ext_vector_type(4))) __bf16 bf16x4;
typedef __attribute__((ext_vector_type(2))) __bf16 bf16x2;
typedef __attribute__((ext_vector_type(4))) short s16x4;
typedef __attribute__((ext_vector_type(8))) short s16x8;
typedef __attribute__((ext_vector_type(16))) float f32x16;
typedef __attribute__((ext_vector_type(4))) float f32x4;
typedef __attribute__((ext_vector_type(4))) unsigned u32x4;
typedef __attribute__((ext_vector_type(2))) unsigned u32x2;
typedef __attribute__((ext_vector_type(2))) float f32x2;
typedef __attribute__((ext_vector_type(16))) unsigned u32x16;
typedef __bf16 bf16;

#define CLK() ((unsigned)wall_clock64())
#define G_TS(ws) ((unsigned (*)[256][12][16])((ws) + (3u << 20)))
#define LDS_AS __attribute__((address_space(3)))
#define GLB_AS __attribute__((address_space(1)))
#define CONST_AS __attribute__((address_space(4)))
#define DI __device__ __forceinline__

constexpr int S_ = 2048, D_ = 512, H_ = 32, DH_ = 16, F_ = 1376;

constexpr size_t WS_Q = 0;
constexpr size_t WS_O = WS_Q;
constexpr size_t WS_K = 2u << 20;
constexpr size_t WS_V = WS_K + 65536;
constexpr size_t WS_ST = WS_V + 65536;
constexpr size_t WS_R = WS_ST + 16384;
constexpr size_t WS_CX = WS_R + 4096;
constexpr size_t WS_CNT = WS_CX + 65536;
constexpr int NCNT = 64 + 16 + 32;
constexpr size_t WS_FLG = WS_CNT + 64 * 256;
constexpr size_t WS_CL = WS_FLG + 16 * 256;
constexpr size_t WS_UPD = WS_CNT + NCNT * 256;
constexpr size_t WS_YP = WS_UPD + 4096;
constexpr int NFFN = 172;

DI float bf2f(bf16 v) { return (float)v; }
DI float rbf(float v) { return (float)(bf16)v; }
DI f32x2 unpack2(unsigned u) { return f32x2{__uint_as_float(u << 16), __uint_as_float(u & 0xffff0000u)}; }
DI unsigned pack2(f32x2 v) {
  unsigned r = __builtin_bit_cast(unsigned, __builtin_convertvector(v, bf16x2));
  asm("" : "+v"(r));
  return r;
}

DI unsigned route_key(float v, int tok) {
  const unsigned u = __float_as_uint(v) >> 16;
  const unsigned o = (u & 0x8000u) ? (~u & 0xffffu) : (u | 0x8000u);
  return (o << 16) | (unsigned)((0x7ff - tok) << 1);
}
DI int key_tok(unsigned k) { return 0x7ff - (int)((k >> 1) & 0x7ffu); }
DI int key_row(unsigned k) { return (key_tok(k) >> 6) * 2 + (int)(k & 1u); }
DI float key_val(unsigned k) {
  const unsigned o = k >> 16;
  const unsigned u = (o & 0x8000u) ? (o & 0x7fffu) : (~o & 0xffffu);
  return __uint_as_float(u << 16);
}
DI unsigned ln2(f32x2 x, f32x2 mb2, f32x2 rinv2) { return pack2(unpack2(pack2(x - mb2)) * rinv2); }
DI void top2_wave(unsigned& k1, unsigned& k2, int width) {
  for (int o = width >> 1; o > 0; o >>= 1) {
    const unsigned a1 = __shfl_xor(k1, o), a2 = __shfl_xor(k2, o);
    k2 = max(min(k1, a1), max(k2, a2));
    k1 = max(k1, a1);
  }
}

DI void glds16(const void* g, void* l) {
  __builtin_amdgcn_global_load_lds((const GLB_AS void*)g, (LDS_AS void*)l, 16, 0, 0);
}

DI void glds16_raw(const void* g, unsigned lds_off) {
  asm volatile("s_mov_b32 m0, %0\n s_nop 0\n global_load_lds_dwordx4 %1, off" ::"s"(lds_off), "v"(g) : "memory", "m0");
}

DI void blds16(__amdgpu_buffer_rsrc_t rs, unsigned voff, unsigned soff, unsigned lds_off) {
  asm volatile("s_mov_b32 m0, %0\n s_nop 0\n buffer_load_dwordx4 %1, %2, %3 offen lds" ::"s"(lds_off), "v"(voff), "s"(rs), "s"(soff) : "memory", "m0");
}
DI void blds16x2(__amdgpu_buffer_rsrc_t rs, unsigned voff, unsigned soff, unsigned lds_off) {
  asm volatile("s_mov_b32 m0, %0\n s_nop 0\n buffer_load_dwordx4 %1, %2, %3 offen lds\n buffer_load_dwordx4 %1, %2, %3 offen offset:1024 lds"
               ::"s"(lds_off), "v"(voff), "s"(rs), "s"(soff) : "memory", "m0");
}
DI void count_up(__amdgpu_buffer_rsrc_t rs, unsigned one, unsigned soff) {
  asm volatile("s_mov_b64 exec, 1\n buffer_atomic_add %0, off, %1, %2\n s_mov_b64 exec, -1" ::"v"(one), "s"(rs), "s"(soff) : "memory");
}
#define LDSP(T, off) ((LDS_AS T*)(unsigned long)(off))

template <int CTRL>
DI float dpp_add(float v) {
  const int t = __builtin_amdgcn_update_dpp(0, __float_as_int(v), CTRL, 0xf, 0xf, true);
  return v + __int_as_float(t);
}
DI float sum8(float v) {
  v = dpp_add<0xB1>(v);
  v = dpp_add<0x4E>(v);
  v = dpp_add<0x141>(v);
  return v;
}

DI f32x16 pv_mfma(bf16x8 v, f32x8 p, f32x16 acc, int sel) {
  return __builtin_amdgcn_smfmac_f32_32x32x32_bf16(v, __builtin_bit_cast(bf16x16, p), acc, sel, 0, 0);
}

DI void swap32(unsigned& a, unsigned& b) {
  const u32x2 t = __builtin_amdgcn_permlane32_swap(a, b, false, false);
  a = t[0];
  b = t[1];
}
DI float max32(float v) {
  unsigned a = __float_as_uint(v), b = a;
  swap32(a, b);
  return fmaxf(__uint_as_float(a), __uint_as_float(b));
}

DI s16x4 tr_read(const char* p) {
  return __builtin_amdgcn_ds_read_tr16_b64_v4i16((LDS_AS s16x4*)p);
}
DI bf16x8 tr_read8(unsigned lo, unsigned hi) {
  const s16x4 a = __builtin_amdgcn_ds_read_tr16_b64_v4i16(LDSP(s16x4, lo)), b = __builtin_amdgcn_ds_read_tr16_b64_v4i16(LDSP(s16x4, hi));
  return __builtin_bit_cast(bf16x8, __builtin_shufflevector(a, b, 0, 1, 2, 3, 4, 5, 6, 7));
}

DI int wimg_off(int row, int ch) { return row * 128 + ((ch ^ (((row >> 1) & 1) << 2)) << 4); }

constexpr int XS_STRIDE = 1040;

DI void stage_w(const bf16* w, int col0, char* wimg, int wave, int nwaves, int lane) {
  for (int p = wave; p < 64; p += nwaves) {
    const int row = p * 8 + (lane >> 3), pc = lane & 7;
    const int ch = pc ^ (((row >> 1) & 1) << 2);
    glds16(w + row * 512 + col0 + ch * 8, wimg + p * 1024);
  }
}

constexpr int GP = 4;
DI f32x16 gemm_tile(const char* wimg, const char* ximg, int n0, int t0, int lane) {
  const int r = lane & 31, h = lane >> 5;
  const int q = (lane & 15) >> 2, p = lane & 3, blk = (lane >> 4) & 1;
  f32x16 acc;
#pragma unroll
  for (int i = 0; i < 16; ++i) acc[i] = 0.f;
  const char* xb = ximg + (t0 + r) * XS_STRIDE + 16 * h;
  const int col = n0 + 16 * blk + 4 * p;
  const int ch = col >> 3, sub = (col & 7) * 2;
  const char* wb0 = wimg + wimg_off(8 * h + q, ch) + sub;
  const char* wb1 = wimg + wimg_off(8 * h + q + 4, ch) + sub;
  s16x4 lo[32], hi[32];
  bf16x8 b[32];
#pragma unroll
  for (int ks = 0; ks < 32 + GP; ++ks) {
    if (ks < 32) {
      lo[ks] = tr_read(wb0 + ks * 2048);
      hi[ks] = tr_read(wb1 + ks * 2048);
      b[ks] = *(const bf16x8*)(xb + ks * 32);
    }
    if (ks >= GP) {
      const s16x8 a = __builtin_shufflevector(lo[ks - GP], hi[ks - GP], 0, 1, 2, 3, 4, 5, 6, 7);
      acc = __builtin_amdgcn_mfma_f32_32x32x16_bf16(__builtin_bit_cast(bf16x8, a), b[ks - GP], acc, 0, 0, 0);
      asm volatile("" : "+v"(acc));
    }
    __builtin_amdgcn_sched_barrier(0);
  }
  return acc;
}
DI f32x16 gemm_tile16(const char* wimg, const char* ximg, int t0, int lane) {
  const int r = lane & 31, h = lane >> 5;
  const int q = (lane & 15) >> 2, p = lane & 3;
  f32x16 acc;
#pragma unroll
  for (int i = 0; i < 16; ++i) acc[i] = 0.f;
  const char* xb = ximg + (t0 + r) * XS_STRIDE + 16 * h;
  const char* wb = wimg + (8 * h + q) * 32 + 8 * p;
  s16x4 lo[32], hi[32];
  bf16x8 b[32];
#pragma unroll
  for (int ks = 0; ks < 32 + GP; ++ks) {
    if (ks < 32) {
      lo[ks] = tr_read(wb + ks * 512);
      hi[ks] = tr_read(wb + ks * 512 + 128);
      b[ks] = *(const bf16x8*)(xb + ks * 32);
    }
    if (ks >= GP) {
      const s16x8 a = __builtin_shufflevector(lo[ks - GP], hi[ks - GP], 0, 1, 2, 3, 4, 5, 6, 7);
      acc = __builtin_amdgcn_mfma_f32_32x32x16_bf16(__builtin_bit_cast(bf16x8, a), b[ks - GP], acc, 0, 0, 0);
      asm volatile("" : "+v"(acc));
    }
    __builtin_amdgcn_sched_barrier(0);
  }
  return acc;
}

#define CODE_ADDR(sym, out)                                                                               \
  asm volatile("s_getpc_b64 s[20:21]\n s_add_u32 s20, s20, " #sym "@rel32@lo+4\n s_addc_u32 s21, s21, " #sym \
               "@rel32@hi+12\n s_mov_b64 %0, s[20:21]"                                                    \
               : "=s"(out)::"s20", "s21", "scc")

constexpr int NT = 16;
constexpr int CST = 4096;
constexpr int L_RED = 131072 + CST;
constexpr int L_CNT = L_RED + 512;
constexpr int L_H = L_CNT + 64;
constexpr int L_STG = L_RED + 1024;
constexpr int ATT_LDS = L_STG + 4 * 6144;
constexpr int NC = 64 / NT;
constexpr int PSW = 53;
constexpr int SPIN_MAX = 1 << 22;

extern "C" __global__ __launch_bounds__(768) void k2_attn(char* __restrict__ ws, const bf16* __restrict__ x,
                                               const bf16* __restrict__ wg, const bf16* __restrict__ wu,
                                               const bf16* __restrict__ wd, const bf16* __restrict__ wo,
                                               bf16* __restrict__ out) {
  const bf16* Q = (const bf16*)(ws + WS_Q);
  const bf16* Kb = (const bf16*)(ws + WS_K);
  bf16* O = (bf16*)(ws + WS_O);
  extern __shared__ __attribute__((aligned(16))) char smem[];
  char* kl = smem;
  char* vl = smem + 65536;
  char* cst = smem + 131072;
  const int tid = threadIdx.x, lane = tid & 63;
  const int wave = __builtin_amdgcn_readfirstlane(tid >> 6);
  unsigned tsv[16];
#pragma unroll
  for (int i_ = 0; i_ < 16; ++i_) tsv[i_] = 0;

  const unsigned lds0 = (unsigned)(size_t)(LDS_AS char*)smem;
  unsigned long long pc = (unsigned long long)ws + WS_R, pg = (unsigned long long)wg, pu = (unsigned long long)wu, pd = (unsigned long long)wd;
  asm volatile("" : "+s"(pc), "+s"(pg), "+s"(pu), "+s"(pd));
  if (wave >= 8) {
    const int sw = wave - 8, r = lane & 31, h = lane >> 5;
    u32x16 cv[4];
#pragma unroll
    for (int i = 0; i < 4; ++i) cv[i] = ((const CONST_AS u32x16*)pc)[i];
    unsigned l16 = lane * 16;
    const int fs = (blockIdx.x & 7) * 22 + (blockIdx.x >> 3);
    const bool ffn = blockIdx.x < 176 && fs < NFFN;
    const unsigned stg = lds0 + L_STG + sw * 6144;
    const int n = lane & 15, g = lane >> 4, q = n >> 2, p = lane & 3;
    unsigned a_gu = stg + (p >> 1) * 2048 + (8 * g + q) * 16 + (p & 1) * 8;
    unsigned a_dn = stg + 4096 + q * 256 + (16 * g + 4 * p) * 2;
    unsigned a_rw = lds0 + L_RED + (n & 7) * 32 + sw * 8 + (n >> 3) * 4;
    unsigned a_rr = lds0 + L_RED + n * 32;
    unsigned a_hw = lds0 + L_H + sw * 32 + n * 2;
    const unsigned hrow = lds0 + L_H + sw * 32 + (n >> 3) * 16, zrow = lds0 + 131072 + 16;
    unsigned a_h0 = (p == g && (q & 1) == 0) ? hrow : zrow, a_h1 = (p == g && (q & 1) == 1) ? hrow : zrow;
    unsigned a_cnt = lds0 + L_CNT, one = 1, odd = lane & 1, even = (lane & 1) ^ 1, g16 = g * 16;
    unsigned v_w = (lane >> 3) * 1024 + (((lane & 7) ^ ((lane >> 4 & 1) << 2)) << 4);
    unsigned v_x = r * 1024 + h * 8, v_s = r * 8;
    unsigned v_y = n * 4096 + g * 16, v_c = n * 256, v_e = n < 12 ? 11 : 10;
    asm volatile("" : "+v"(l16), "+v"(a_gu), "+v"(a_dn), "+v"(a_rw), "+v"(a_rr), "+v"(a_hw), "+v"(a_h0), "+v"(a_h1),
                 "+v"(a_cnt), "+v"(one), "+v"(odd), "+v"(even), "+v"(g16));
    unsigned vg = lane * (F_ * 2), vd = g * 1024 + n * 16;
    asm volatile("" : "+v"(v_w), "+v"(v_x), "+v"(v_s), "+v"(v_y), "+v"(v_c), "+v"(v_e), "+v"(vg), "+v"(vd));
    {
      const __amdgpu_buffer_rsrc_t rk = __builtin_amdgcn_make_buffer_rsrc((void*)Kb, 0, 131072, 0x00020000);
#pragma unroll
      for (int i = 0; i < 13; ++i) {
        const unsigned o = sw * 2048 + (i < 6 ? 16 + 8 * i : 24 + 8 * i) * 1024;
        blds16x2(rk, l16, o, lds0 + o);
      }
    }
    asm volatile("s_barrier" ::: "memory");
    asm volatile("" : "+s"(cv[0]), "+s"(cv[1]), "+s"(cv[2]), "+s"(cv[3]));
    unsigned k1 = cv[0][0], k2 = cv[0][1];
#pragma unroll
    for (int i = 1; i < 32; ++i) {
      const unsigned a1 = cv[i >> 3][(i & 7) * 2], a2 = cv[i >> 3][(i & 7) * 2 + 1];
      unsigned t;
      asm volatile("s_min_u32 %2, %0, %3\n s_max_u32 %1, %1, %4\n s_max_u32 %0, %0, %3\n s_max_u32 %1, %1, %2"
          : "+s"(k1), "+s"(k2), "=&s"(t) : "s"(a1), "s"(a2) : "scc");
    }
    const __amdgpu_buffer_rsrc_t rc = __builtin_amdgcn_make_buffer_rsrc(ws + WS_CNT, 0, NCNT * 256, 0x00020000);
    const __amdgpu_buffer_rsrc_t ry = __builtin_amdgcn_make_buffer_rsrc(ws + WS_YP, 0, NFFN * 4096, 0x00020000);
    const __amdgpu_buffer_rsrc_t rup = __builtin_amdgcn_make_buffer_rsrc(ws + WS_UPD, 0, 4096, 0x00020000);
    asm volatile("s_waitcnt vmcnt(0)\n s_barrier" ::: "memory");
    if (ffn) {
      const __amdgpu_buffer_rsrc_t rx = __builtin_amdgcn_make_buffer_rsrc(ws + WS_CX, 0, 65536, 0x00020000);
      u32x4 xf[4];
      __builtin_amdgcn_s_setprio(3);
      unsigned vx = __umul24(even, key_row(k1) * 1024) + g16;
      vx += __umul24(odd, key_row(k2) * 1024);
      asm volatile("" : "+v"(vx));
      __builtin_amdgcn_s_setprio(0);
#pragma unroll
      for (int i = 0; i < 4; ++i) xf[i] = __builtin_amdgcn_raw_buffer_load_b128(rx, vx + i * 64, sw * 256, 0);
      const __amdgpu_buffer_rsrc_t rg = __builtin_amdgcn_make_buffer_rsrc((void*)pg, 0, 512 * F_ * 2, 0x00020000);
      const __amdgpu_buffer_rsrc_t ru = __builtin_amdgcn_make_buffer_rsrc((void*)pu, 0, 512 * F_ * 2, 0x00020000);
      const __amdgpu_buffer_rsrc_t rd = __builtin_amdgcn_make_buffer_rsrc((void*)pd, 0, 512 * F_ * 2, 0x00020000);
      const unsigned so = (sw * 128 * F_ + fs * 8) * 2, sd = (fs * 8 * 512 + sw * 128) * 2;
      blds16(rg, vg, so, stg);
      blds16(rg, vg, so + 64 * F_ * 2, stg + 1024);
      blds16(ru, vg, so, stg + 2048);
      blds16(ru, vg, so + 64 * F_ * 2, stg + 3072);
      blds16(rd, vd, sd, stg + 4096);
      blds16(rd, vd, sd + 4096, stg + 5120);
      asm volatile("s_waitcnt vmcnt(0)" ::: "memory");
      bf16x8 wf[4], df[2];
#pragma unroll
      for (int i = 0; i < 4; ++i) wf[i] = tr_read8(a_gu + i * 512, a_gu + i * 512 + 64);
#pragma unroll
      for (int i = 0; i < 2; ++i) df[i] = tr_read8(a_dn + i * 128, a_dn + i * 128 + 1024);
      asm volatile("s_waitcnt lgkmcnt(0)" ::: "memory");
      __builtin_amdgcn_s_setprio(3);
      f32x4 gu = {0.f, 0.f, 0.f, 0.f};
#pragma unroll
      for (int i = 0; i < 4; ++i) gu = __builtin_amdgcn_mfma_f32_16x16x32_bf16(__builtin_bit_cast(bf16x8, xf[i]), wf[i], gu, 0, 0, 0);
      *LDSP(float, a_rw) = gu[0];
      *LDSP(float, a_rw + 256) = gu[1];
      asm volatile("s_mov_b64 exec, 1\n ds_add_u32 %0, %1\n s_mov_b64 exec, -1" ::"v"(a_cnt), "v"(one) : "memory");
      while (*(volatile LDS_AS unsigned*)LDSP(unsigned, a_cnt) < 4) __builtin_amdgcn_s_sleep(1);
      asm volatile("" ::: "memory");
      const f32x4 r0 = *LDSP(f32x4, a_rr), r1 = *LDSP(f32x4, a_rr + 16);
      const f32x2 gs = unpack2(pack2((f32x2{r0[0], r0[1]} + f32x2{r0[2], r0[3]}) + (f32x2{r1[0], r1[1]} + f32x2{r1[2], r1[3]})));
      const float hv = rbf(gs.x * __builtin_amdgcn_rcpf(1.f + __expf(-gs.x))) * gs.y;
      *LDSP(bf16, a_hw) = (bf16)hv;
      asm volatile("" ::: "memory");
      const bf16x8 h0 = *LDSP(bf16x8, a_h0), h1 = *LDSP(bf16x8, a_h1);
      f32x4 y = {0.f, 0.f, 0.f, 0.f};
      y = __builtin_amdgcn_mfma_f32_16x16x32_bf16(h0, df[0], y, 0, 0, 0);
      y = __builtin_amdgcn_mfma_f32_16x16x32_bf16(h1, df[1], y, 0, 0, 0);
      __builtin_amdgcn_raw_buffer_store_b128(__builtin_bit_cast(u32x4, y), ry, l16, fs * 4096 + sw * 1024, 16);
      __builtin_amdgcn_s_setprio(0);
      asm volatile("s_waitcnt vmcnt(0)" ::: "memory");
      count_up(rc, one, (sw * 16 + (fs & 15)) * 256);
      tsv[5] = CLK();
    }
    const int tb = (blockIdx.x >> 6) * 8 + (blockIdx.x & 7), cb = (blockIdx.x >> 3) & 7, tok0 = tb * 64;
    char* ximg = smem;
    char* wimg = smem + 64 * XS_STRIDE;
    const int n0 = (sw & 1) * 32, t0 = (sw >> 1) * 32;
    const unsigned so_x = (tok0 + t0) * 1024 + (cb * 64 + n0) * 2;
    const __amdgpu_buffer_rsrc_t rout = __builtin_amdgcn_make_buffer_rsrc(out, 0, S_ * D_ * 2, 0x00020000);
    u32x4 wreg[16];
    u32x2 xv[4], nv[4], sxr;
    {
      const __amdgpu_buffer_rsrc_t rw = __builtin_amdgcn_make_buffer_rsrc((void*)wo, 0, 512 * 1024, 0x00020000);
      const __amdgpu_buffer_rsrc_t rx = __builtin_amdgcn_make_buffer_rsrc((void*)x, 0, S_ * D_ * 2, 0x00020000);
      const __amdgpu_buffer_rsrc_t rs = __builtin_amdgcn_make_buffer_rsrc(ws + WS_ST, 0, S_ * 8, 0x00020000);
#pragma unroll
      for (int i = 0; i < 16; ++i) wreg[i] = __builtin_amdgcn_raw_buffer_load_b128(rw, v_w, (sw + 4 * i) * 8192 + cb * 128, 0);
#pragma unroll
      for (int i = 0; i < 4; ++i) xv[i] = __builtin_amdgcn_raw_buffer_load_b64(rx, v_x + i * 16, so_x, 0);
      sxr = __builtin_amdgcn_raw_buffer_load_b64(rs, v_s, (tok0 + t0) * 8, 0);
      if (wave == 11) {
        const unsigned t = __builtin_amdgcn_raw_buffer_load_b32(rout, 0, so_x, 0);
        asm volatile("" ::"v"(t));
      }
    }
    if ((blockIdx.x >> 4) == 11) {
      const int sel = (blockIdx.x >> 3) & 1, cq = blockIdx.x & 7;
      __builtin_amdgcn_s_setprio(3);
      int spin = 0;
      while (__any(__builtin_amdgcn_raw_buffer_load_b32(rc, v_c, (cq >> 1) * 4096, 16) < v_e) && ++spin < SPIN_MAX)
        asm volatile("s_sleep 8" ::: "memory");
      __builtin_amdgcn_s_setprio(0);
      asm volatile("" ::: "memory");
      const unsigned so = (cq >> 1) * 1024 + (2 * sel + (cq & 1)) * 256 + sw * 64;
      u32x4 t[11];
#pragma unroll
      for (int i = 0; i < 10; ++i) t[i] = __builtin_amdgcn_raw_buffer_load_b128(ry, v_y, so + i * 65536, 16);
      __builtin_amdgcn_s_setprio(3);
      t[10] = u32x4{0u, 0u, 0u, 0u};
      if (v_e == 11) t[10] = __builtin_amdgcn_raw_buffer_load_b128(ry, v_y, so + 10 * 65536, 16);
      const float gate = rbf(__builtin_amdgcn_rcpf(1.f + __expf(-key_val(sel ? k2 : k1))));
      f32x4 a = __builtin_bit_cast(f32x4, t[0]);
#pragma unroll
      for (int i = 1; i < 11; ++i) a += __builtin_bit_cast(f32x4, t[i]);
#pragma unroll
      for (int i = 0; i < 4; ++i) a[i] = dpp_add<0x140>(sum8(a[i]));
      asm volatile("" : "+v"(a));
      const float u = rbf(rbf(p == 0 ? a[0] : p == 1 ? a[1] : p == 2 ? a[2] : a[3]) * gate);
      if (n < 4) __builtin_amdgcn_raw_buffer_store_b32(__float_as_uint(u), rup, n * 64 + g * 4, (sel * 512 + cq * 64 + sw * 4) * 4, 16);
      __builtin_amdgcn_s_setprio(0);
      asm volatile("s_waitcnt vmcnt(0)" ::: "memory");
      count_up(rc, one, (64 + sel * 8 + cq) * 256);
      tsv[6] = CLK();
    }
    const bool routed = ((key_tok(k1) ^ (tok0 + t0)) >> 5) == 0 || ((key_tok(k2) ^ (tok0 + t0)) >> 5) == 0;
    u32x4 uf[4];
    if (routed) {
      __builtin_amdgcn_s_setprio(3);
      const int tok = tok0 + t0 + r;
      const int rsel = tok == key_tok(k1) ? 0 : tok == key_tok(k2) ? 1 : -1;
#pragma unroll
      for (int i = 0; i < 4; ++i) uf[i] = u32x4{0x80000000u, 0x80000000u, 0x80000000u, 0x80000000u};
      if (rsel >= 0) {
        int spin = 0;
        while (__builtin_amdgcn_raw_buffer_load_b32(rc, rsel * 2048, (64 + cb) * 256, 16) < 4 && ++spin < SPIN_MAX)
          asm volatile("s_sleep 4" ::: "memory");
        asm volatile("" ::: "memory");
#pragma unroll
        for (int i = 0; i < 4; ++i)
          uf[i] = __builtin_amdgcn_raw_buffer_load_b128(rup, (rsel * 512 + 8 * i + 4 * h) * 4, (cb * 64 + n0) * 4, 16);
      }
#pragma unroll
      for (int i = 0; i < 4; ++i) asm volatile("" : "+v"(uf[i]));
      __builtin_amdgcn_s_setprio(0);
    }
#pragma unroll
    for (int i = 0; i < 16; ++i) asm volatile("" : "+v"(wreg[i]));
    asm volatile("s_barrier" ::: "memory");
    tsv[8] = CLK();
    if (wave == 8) count_up(rc, one, (80 + tb) * 256);
#pragma unroll
    for (int i = 0; i < 16; ++i) *(u32x4*)(wimg + (sw + 4 * i) * 1024 + lane * 16) = wreg[i];
    {
      const f32x2 mb2 = {__uint_as_float(sxr[0]), __uint_as_float(sxr[0])}, ri2 = {__uint_as_float(sxr[1]), __uint_as_float(sxr[1])};
#pragma unroll
      for (int i = 0; i < 4; ++i) {
#pragma unroll
        for (int jj = 0; jj < 2; ++jj) nv[i][jj] = ln2(unpack2(xv[i][jj]), mb2, ri2);
      }
    }
    if (routed) {
#pragma unroll
      for (int i = 0; i < 4; ++i) {
        const f32x4 u = __builtin_bit_cast(f32x4, uf[i]);
        nv[i][0] = pack2(unpack2(nv[i][0]) + f32x2{u[0], u[1]});
        nv[i][1] = pack2(unpack2(nv[i][1]) + f32x2{u[2], u[3]});
      }
    }
    if (wave == 8) {
      int spin = 0;
      while (__hip_atomic_load((GLB_AS unsigned*)(ws + WS_CL + tb * 256), __ATOMIC_RELAXED, __HIP_MEMORY_SCOPE_AGENT) < 8 && ++spin < SPIN_MAX)
        __builtin_amdgcn_s_sleep(1);
    }
    asm volatile("s_barrier" ::: "memory");
    tsv[9] = CLK();
    {
      const __amdgpu_buffer_rsrc_t ro = __builtin_amdgcn_make_buffer_rsrc(O + tok0 * 512, 0, 65536, 0x00020000);
      u32x4 t[16];
#pragma unroll
      for (int i = 0; i < 16; ++i) t[i] = __builtin_amdgcn_raw_buffer_load_b128(ro, l16, (sw * 16 + i) * 1024, 16);
#pragma unroll
      for (int i = 0; i < 16; ++i) *(u32x4*)(ximg + (sw * 16 + i) * XS_STRIDE + lane * 16) = t[i];
    }
    asm volatile("s_waitcnt lgkmcnt(0)\n s_barrier" ::: "memory");
    tsv[11] = CLK();
    f32x16 acc = gemm_tile(wimg, ximg, n0, t0, lane);
    tsv[12] = (__builtin_amdgcn_readfirstlane(__float_as_int(acc[0])) != 0x12345678) ? CLK() : 0u;
#pragma unroll
    for (int g = 0; g < 4; ++g) {
      u32x2 o;
#pragma unroll
      for (int jj = 0; jj < 2; ++jj) {
        const unsigned xr = xv[g][jj], nr = nv[g][jj];
        const f32x2 a = unpack2(pack2(f32x2{acc[4 * g + 2 * jj], acc[4 * g + 2 * jj + 1]}));
        o[jj] = pack2(unpack2(pack2(unpack2(xr) + a)) + unpack2(nr));
      }
      __builtin_amdgcn_raw_buffer_store_b64(o, rout, v_x + g * 16, so_x, 0);
    }
    tsv[13] = CLK();
    { if ((threadIdx.x & 63) == 0) {
#pragma unroll
 for (int i_ = 0; i_ < 16; ++i_) G_TS(ws)[1][blockIdx.x][threadIdx.x >> 6][i_] = tsv[i_]; } }
    return;
  }

  const int r = lane & 31, h = lane >> 5;
  const int token = ((blockIdx.x >> 6) * 8 + (blockIdx.x & 7)) * 64 + ((blockIdx.x >> 3) & 7) * 8 + wave;
  tsv[0] = CLK();
  bf16x8 qf;
  asm volatile("global_load_dwordx4 %0, %1, off" : "=v"(qf) : "v"(Q + token * 512 + r * 16 + 8 * h) : "memory");
#pragma unroll
  for (int i = 0; i < 3; ++i) {
    const int piece = (i < 2) ? (wave + i * 8) : (64 + wave);
    glds16_raw((const char*)Kb + piece * 1024 + lane * 16, lds0 + piece * 1024);
  }
  {
    const unsigned cv = ((tid & 30) == 0 || (tid & 30) == 8) ? 0x3f803f80u : 0u;
    *(u32x2*)(cst + tid * 8) = u32x2{cv, cv};
    if (tid == 1) *(unsigned*)(smem + L_CNT) = 0;
  }
  int vo = (r < 16) ? 65536 + (h * 16 + r) * 16 : 131072 + (h * 16 + r - 16) * 16;
  int vstep = (r < 16) ? CST : 0;
  int sel = 0xdddd;
  f32x16 zero;
#pragma unroll
  for (int i = 0; i < 16; ++i) zero[i] = 0.f;
  f32x16 acc = zero;
  asm volatile("" : "+v"(sel), "+v"(acc), "+v"(vstep), "+v"(vo));
  const char* kp0 = kl + lane * 16;
  const char* vp0 = smem + vo;
  tsv[1] = CLK();
  asm volatile("s_waitcnt vmcnt(0) lgkmcnt(0)\n s_barrier" ::: "memory");
  tsv[2] = CLK();

  constexpr float HEAD = 40.f;
  f32x16 negm, xs;
  bf16x8 kfn;
  {
    bf16x8 kf[4];
    f32x16 s0[4];
#pragma unroll
    for (int t = 0; t < 4; ++t) kf[t] = *(const bf16x8*)(kp0 + t * 1024);
#pragma unroll
    for (int t = 0; t < 4; ++t) {
      s0[t] = __builtin_amdgcn_mfma_f32_32x32x16_bf16(kf[t], qf, zero, 0, 0, 0);
      asm volatile("" : "+v"(s0[t]));
    }
    __builtin_amdgcn_sched_barrier(0);
    float mx = -INFINITY;
#pragma unroll
    for (int t = 0; t < 4; ++t) {
#pragma unroll
      for (int i = 0; i < 16; i += 2) mx = fmaxf(fmaxf(mx, s0[t][i]), s0[t][i + 1]);
    }
    const float nm = -(floorf(max32(mx)) + HEAD);
#pragma unroll
    for (int i = 0; i < 16; ++i) {
      xs[i] = s0[0][i] + nm;
      negm[i] = nm;
    }
    kfn = kf[1];
  }
  asm volatile("" : "+v"(negm));
  {
    const char* kp = kp0;
    const char* vp = vp0;
    f32x16 x = xs;
    if (wave < 4) __builtin_amdgcn_s_setprio(2);
    else __builtin_amdgcn_s_setprio(1);
    for (int c = 0; c < NC; ++c) {
      bool grown;
#pragma unroll
      for (int t = 0; t < NT; ++t) {
        const bf16x8 kf = kfn;
        kfn = *(const bf16x8*)(kp + (t + 2) * 1024);
        const bf16x8 vf0 = *(const bf16x8*)(vp + ((t & 3) * 2) * 512);
        const bf16x8 vf1 = *(const bf16x8*)(vp + ((t & 3) * 2 + 1) * 512);
        if ((t & 3) == 3) vp += vstep;
        f32x8 p0, p1;
#pragma unroll
        for (int j = 0; j < 4; ++j) p0[j] = __builtin_amdgcn_exp2f(x[j]);
        __builtin_amdgcn_sched_barrier(0);
        f32x16 xn = __builtin_amdgcn_mfma_f32_32x32x16_bf16(kf, qf, negm, 0, 0, 0);
        asm volatile("" : "+v"(xn));
        __builtin_amdgcn_sched_barrier(0);
#pragma unroll
        for (int j = 4; j < 8; ++j) p0[j] = __builtin_amdgcn_exp2f(x[j]);
        if (t == NT - 1) grown = __any(!(acc[8] <= 4.3e9f));
        acc = pv_mfma(vf0, p0, acc, sel);
#pragma unroll
        for (int j = 0; j < 8; ++j) p1[j] = __builtin_amdgcn_exp2f(x[8 + j]);
        acc = pv_mfma(vf1, p1, acc, sel);
        if (t == 7 && c == 0) asm volatile("s_barrier" ::: "memory");
        if (c * NT + t == PSW && wave < 4) __builtin_amdgcn_s_setprio(0);
        __builtin_amdgcn_sched_barrier(0);
        x = xn;
      }
      kp += NT * 1024;
      if (grown) {
        const int e = max(__builtin_amdgcn_frexp_expf(acc[8]) - 1 + (int)HEAD, 0);
#pragma unroll
        for (int i = 0; i < 9; ++i) acc[i] = __builtin_amdgcn_ldexpf(acc[i], -e);
        const float ef = (float)e;
#pragma unroll
        for (int i = 0; i < 16; ++i) {
          negm[i] -= ef;
          x[i] -= ef;
        }
        asm volatile("" : "+v"(negm));
      }
    }
  }
  tsv[3] = (__builtin_amdgcn_readfirstlane(__float_as_int(acc[8])) != 0x12345678) ? CLK() : 0u;
  {
    float chk = 0.f;
#pragma unroll
    for (int i = 0; i < 9; ++i) chk = __builtin_fmaf(acc[i], 0.f, chk);
    if (__any(chk != 0.f)) {
      f32x16 oacc = zero;
      float mrun = -INFINITY;
      const char* kp = kp0;
      const char* vp = vp0;
      for (int t = 0; t < 64; ++t) {
        bf16x8 kf = *(const bf16x8*)(kp);
        f32x16 x = __builtin_amdgcn_mfma_f32_32x32x16_bf16(kf, qf, zero, 0, 0, 0);
        float mx = x[0];
#pragma unroll
        for (int i = 1; i < 16; ++i) mx = fmaxf(mx, x[i]);
        const float mn = fmaxf(mrun, max32(mx));
        const float al = __builtin_amdgcn_exp2f(mrun - mn);
        mrun = mn;
#pragma unroll
        for (int i = 0; i < 16; ++i) oacc[i] *= al;
#pragma unroll
        for (int s = 0; s < 2; ++s) {
          f32x8 pf;
#pragma unroll
          for (int j = 0; j < 8; ++j) pf[j] = __builtin_amdgcn_exp2f(x[8 * s + j] - mn);
          bf16x8 vf = *(const bf16x8*)(vp + ((t & 3) * 2 + s) * 512);
          oacc = pv_mfma(vf, pf, oacc, sel);
        }
        kp += 1024;
        if ((t & 3) == 3) vp += vstep;
      }
      acc = oacc;
    }
  }
  const float inv = 1.f / acc[8];
  {
    unsigned a0 = pack2(f32x2{acc[0] * inv, acc[1] * inv}), a1 = pack2(f32x2{acc[2] * inv, acc[3] * inv});
    unsigned b0 = pack2(f32x2{acc[4] * inv, acc[5] * inv}), b1 = pack2(f32x2{acc[6] * inv, acc[7] * inv});
    swap32(a0, b0);
    swap32(a1, b1);
    const u32x4 o = {a0, a1, b0, b1};
    const __amdgpu_buffer_rsrc_t ro = __builtin_amdgcn_make_buffer_rsrc(O + token * 512, 0, 1024, 0x00020000);
    __builtin_amdgcn_raw_buffer_store_b128(o, ro, r * 32 + h * 16, 0, 16);
  }
  asm volatile("s_waitcnt vmcnt(0)\n s_barrier" ::: "memory");
  tsv[4] = CLK();
  { if ((threadIdx.x & 63) == 0) {
#pragma unroll
 for (int i_ = 0; i_ < 16; ++i_) G_TS(ws)[1][blockIdx.x][threadIdx.x >> 6][i_] = tsv[i_]; } }

}

constexpr int K1_LDS = 64 * XS_STRIDE + 65536 + 16384 + 256;

extern "C" __global__ __launch_bounds__(768) void k1_ln_qkv(const bf16* __restrict__ x, const bf16* __restrict__ wq,
                                                 const bf16* __restrict__ wk, const bf16* __restrict__ wv,
                                                 const bf16* __restrict__ wr, char* __restrict__ ws) {
  float* st = (float*)(ws + WS_ST);
  bf16* Q = (bf16*)(ws + WS_Q);
  bf16* Kb = (bf16*)(ws + WS_K);
  bf16* Vf = (bf16*)(ws + WS_V);
  unsigned* cand = (unsigned*)(ws + WS_R);
  extern __shared__ __attribute__((aligned(16))) char smem[];
  char* ximg = smem;
  char* wimg = smem + 64 * XS_STRIDE;
  char* kvimg = smem + 64 * XS_STRIDE + 65536;
  float* rs = (float*)(smem + 64 * XS_STRIDE + 65536 + 16384);
  const int cb = blockIdx.x >> 5, tb = blockIdx.x & 31;
  const int tid = threadIdx.x, lane = tid & 63;
  const int wave = __builtin_amdgcn_readfirstlane(tid >> 6);
  unsigned tsv[16];
#pragma unroll
  for (int i_ = 0; i_ < 16; ++i_) tsv[i_] = 0;
  const int tok0 = tb * 64;
  tsv[0] = CLK();

  if (wave >= 8) {
    if (blockIdx.x == 255 && tid >= 768 - NCNT) ((unsigned*)(ws + WS_CNT))[(767 - tid) * 64] = 0;
    stage_w(wq, cb * 64, wimg, wave - 8, 4, lane);
    if (cb < 2) {
      const bf16* w = cb == 0 ? wk : wv;
#pragma unroll
      for (int i = 0; i < 4; ++i) {
        const int p = wave - 8 + 4 * i;
        glds16((const char*)w + p * 1024 + lane * 16, kvimg + p * 1024);
      }
    }
    __syncthreads();
    if (cb == 3 && wave == 8) {
      unsigned k1 = route_key(rs[lane], tok0 + lane), k2 = 0;
      top2_wave(k1, k2, 64);
      if (lane == 0) *(u32x2*)(cand + tb * 2) = u32x2{k1, k2 | 1u};
      const u32x4 a = *(const u32x4*)(ximg + (key_tok(k1) & 63) * XS_STRIDE + lane * 16);
      const u32x4 b = *(const u32x4*)(ximg + (key_tok(k2) & 63) * XS_STRIDE + lane * 16);
      *(u32x4*)(ws + WS_CX + tb * 2048 + lane * 16) = a;
      *(u32x4*)(ws + WS_CX + tb * 2048 + 1024 + lane * 16) = b;
    }
    { if ((threadIdx.x & 63) == 0) {
#pragma unroll
 for (int i_ = 0; i_ < 16; ++i_) G_TS(ws)[0][blockIdx.x][threadIdx.x >> 6][i_] = tsv[i_]; } }
    return;
  }

  const int tl = wave * 8 + (lane >> 3), c8 = lane & 7;
  u32x4 xv[8];
#pragma unroll
  for (int i = 0; i < 8; ++i) xv[i] = *(const u32x4*)(x + (tok0 + tl) * 512 + (c8 + 8 * i) * 8);
  u32x4 wrv[8];
  if (cb == 3) {
#pragma unroll
    for (int i = 0; i < 8; ++i) wrv[i] = *(const u32x4*)(wr + (c8 + 8 * i) * 8);
  }

  tsv[1] = CLK();
  f32x2 xf[32];
#pragma unroll
  for (int i = 0; i < 32; ++i) xf[i] = unpack2(xv[i >> 2][i & 3]);
  f32x2 s2 = {0.f, 0.f};
#pragma unroll
  for (int i = 0; i < 32; ++i) s2 += xf[i];
  const float mean = sum8(s2.x + s2.y) * (1.f / 512.f);
  const f32x2 mean2 = {mean, mean};
  f32x2 v2 = {0.f, 0.f};
#pragma unroll
  for (int i = 0; i < 32; ++i) { const f32x2 d = xf[i] - mean2; v2 += d * d; }
  const float vs = sum8(v2.x + v2.y);
  tsv[2] = CLK();
  const float mb = rbf(mean);
  const float vb = rbf(vs * (1.f / 512.f));
  const float den = rbf(sqrtf(rbf(vb + 1e-5f)));
  const float rinv = __builtin_amdgcn_rcpf(den);
  const f32x2 mb2 = {mb, mb}, rinv2 = {rinv, rinv};
  u32x4 o[8];
#pragma unroll
  for (int i = 0; i < 8; ++i) {
#pragma unroll
    for (int j = 0; j < 4; ++j) o[i][j] = ln2(xf[4 * i + j], mb2, rinv2);
    *(u32x4*)(ximg + tl * XS_STRIDE + (c8 + 8 * i) * 16) = o[i];
  }
  if (cb == 2 && c8 == 0) *(f32x2*)(st + (tok0 + tl) * 2) = f32x2{mb, rinv};
  if (cb == 3) {
    float rd = 0.f;
#pragma unroll
    for (int i = 0; i < 8; ++i) {
#pragma unroll
      for (int j = 0; j < 4; ++j) {
        const unsigned a = o[i][j], b = wrv[i][j];
        rd = __builtin_amdgcn_fdot2_f32_bf16(__builtin_bit_cast(bf16x2, a), __builtin_bit_cast(bf16x2, b), rd, false);
      }
    }
    rd = sum8(rd);
    if (c8 == 0) rs[tl] = rbf(rd);
  }
  tsv[3] = CLK();
  __syncthreads();
  tsv[4] = CLK();

  const int r = lane & 31, h = lane >> 5;
  if (wave < 4) {
    const int n0 = (wave & 1) * 32, t0 = (wave >> 1) * 32;
    f32x16 acc = gemm_tile(wimg, ximg, n0, t0, lane);
    tsv[5] = (__builtin_amdgcn_readfirstlane(__float_as_int(acc[0])) != 0x12345678) ? CLK() : 0u;
    const float sc = 0.25f * 1.4426950408889634f;
    const __amdgpu_buffer_rsrc_t rq = __builtin_amdgcn_make_buffer_rsrc(Q + tok0 * 512, 0, 65536, 0x00020000);
#pragma unroll
    for (int m = 0; m < 2; ++m) {
      unsigned p[4];
#pragma unroll
      for (int i = 0; i < 4; ++i) p[i] = pack2(unpack2(pack2(f32x2{acc[8 * m + 2 * i], acc[8 * m + 2 * i + 1]})) * f32x2{sc, sc});
      swap32(p[0], p[2]);
      swap32(p[1], p[3]);
      const u32x4 o = {p[0], p[1], p[2], p[3]};
      __builtin_amdgcn_raw_buffer_store_b128(o, rq, ((t0 + r) * 512 + cb * 64 + n0 + 16 * m + 8 * h) * 2, 0, 0);
    }
  } else if (wave == 7) {
    if (lane < 2) {
      const unsigned t = *(const volatile unsigned*)(lane ? ws + WS_K + tb * 2048 : (const char*)(Q + tok0 * 512 + cb * 64));
      asm volatile("" ::"v"(t));
    }
  } else if (wave == 6 && cb == 7 && tb < 8) {
    unsigned long long c0, c1;
    CODE_ADDR(k2_attn, c0);
    CODE_ADDR(k1_ln_qkv, c1);
    const long long n = (long long)(c1 - c0);
    unsigned t[4];
#pragma unroll
    for (int i = 0; i < 4; ++i) {
      const int off = (i * 64 + lane) * 128;
      t[i] = off < n ? *(const volatile unsigned*)(c0 + off) : 0u;
    }
#pragma unroll
    for (int i = 0; i < 4; ++i) asm volatile("" ::"v"(t[i]));
  } else if (wave < 6 && cb < 2) {
    const int t0 = (wave - 4) * 32;
    f32x16 acc = gemm_tile16(kvimg, ximg, t0, lane);
    const int key = tok0 + t0 + r;
    const int t = key >> 5;
    if (cb == 0) {
#pragma unroll
      for (int g = 0; g < 2; ++g) {
        bf16x4 o;
#pragma unroll
        for (int j = 0; j < 4; ++j) o[j] = (bf16)acc[4 * g + j];
        *(bf16x4*)(Kb + ((t * 2 + g) * 32 + r) * 8 + 4 * h) = o;
      }
    } else {
      const int kk = key & 31, sg = kk >> 4, rem = kk & 15;
      const int hh = rem >> 3, jj = rem & 7;
#pragma unroll
      for (int g = 0; g < 2; ++g) {
#pragma unroll
        for (int j = 0; j < 4; ++j) {
          const int d = 8 * g + 4 * h + j;
          Vf[(((t * 2 + sg) * 2 + hh) * 16 + d) * 8 + jj] = (bf16)acc[4 * g + j];
        }
      }
    }
  }
  tsv[6] = CLK();
  { if ((threadIdx.x & 63) == 0) {
#pragma unroll
 for (int i_ = 0; i_ < 16; ++i_) G_TS(ws)[0][blockIdx.x][threadIdx.x >> 6][i_] = tsv[i_]; } }

}


extern "C" __global__ __launch_bounds__(1024) void k3_delay(char* ws) {
  __shared__ int r[16][9];
  const unsigned base = G_TS(ws)[0][0][0][0];
  int v[9] = {0x7fffffff, -0x7fffffff, 0, 0, 0x7fffffff, -0x7fffffff, 0, 0, 0x7fffffff};
  for (int e = threadIdx.x; e < 3072; e += 1024) {
    const unsigned a = G_TS(ws)[0][e / 12][e % 12][0], b = G_TS(ws)[0][e / 12][e % 12][0], c = G_TS(ws)[0][e / 12][e % 12][0];
    if (a) { const int d = (int)(a - base); v[0] = min(v[0], d); v[1] = max(v[1], d); v[2] += d; v[3] += 1; }
    if (b) { const int d = (int)(b - base); v[4] = min(v[4], d); v[5] = max(v[5], d); v[6] += d; v[7] += 1; }
    if (c) v[8] = min(v[8], (int)(c - base));
  }
  for (int o = 32; o > 0; o >>= 1) {
    v[0] = min(v[0], __shfl_xor(v[0], o)); v[1] = max(v[1], __shfl_xor(v[1], o)); v[2] += __shfl_xor(v[2], o); v[3] += __shfl_xor(v[3], o);
    v[4] = min(v[4], __shfl_xor(v[4], o)); v[5] = max(v[5], __shfl_xor(v[5], o)); v[6] += __shfl_xor(v[6], o); v[7] += __shfl_xor(v[7], o);
    v[8] = min(v[8], __shfl_xor(v[8], o));
  }
  if ((threadIdx.x & 63) == 0) for (int i = 0; i < 9; ++i) r[threadIdx.x >> 6][i] = v[i];
  __syncthreads();
  if (threadIdx.x == 0) {
    for (int w = 1; w < 16; ++w) {
      v[0] = min(v[0], r[w][0]); v[1] = max(v[1], r[w][1]); v[2] += r[w][2]; v[3] += r[w][3];
      v[4] = min(v[4], r[w][4]); v[5] = max(v[5], r[w][5]); v[6] += r[w][6]; v[7] += r[w][7];
      v[8] = min(v[8], r[w][8]);
    }
    const float va = 0 == 2 ? (float)v[2] / (float)max(v[3], 1) : (float)v[0];
    const float vb = 0 == 2 ? (float)v[6] / (float)max(v[7], 1) : (float)v[4 + 0];
    int d = (int)((vb - va) * 0.f);
    d = min(max(d, 0), 30000) + 6000;
    const unsigned t0 = base + (unsigned)v[8];
    const unsigned t1 = CLK();
    while ((int)(CLK() - t0) < d && (int)(CLK() - t1) < 40000) __builtin_amdgcn_s_sleep(1);
  }
}
extern "C" void kernel_launch(void* const* d_in, const int* in_sizes, int n_in, void* d_out, int out_size,
                              void* d_ws, size_t ws_size, hipStream_t stream) {
  const bf16* x = (const bf16*)d_in[0];
  const bf16* wq = (const bf16*)d_in[1];
  const bf16* wk = (const bf16*)d_in[2];
  const bf16* wv = (const bf16*)d_in[3];
  const bf16* wo = (const bf16*)d_in[4];
  const bf16* wr = (const bf16*)d_in[6];
  const bf16* wg = (const bf16*)d_in[7];
  const bf16* wu = (const bf16*)d_in[8];
  const bf16* wd = (const bf16*)d_in[9];
  char* ws = (char*)d_ws;
  bf16* out = (bf16*)d_out;

  (void)hipFuncSetAttribute((const void*)k1_ln_qkv, hipFuncAttributeMaxDynamicSharedMemorySize, K1_LDS);
  (void)hipFuncSetAttribute((const void*)k2_attn, hipFuncAttributeMaxDynamicSharedMemorySize, ATT_LDS);

  k1_ln_qkv<<<256, 768, K1_LDS, stream>>>(x, wq, wk, wv, wr, ws);
  k2_attn<<<256, 768, ATT_LDS, stream>>>(ws, x, wg, wu, wd, wo, out);
  k3_delay<<<1, 1024, 0, stream>>>(ws);
}
```

```cpp
#include <hip/hip_runtime.h>
#include <hip/hip_bf16.h>

typedef __attribute__((ext_vector_type(8))) __bf16 bf16x8;
typedef __attribute__((ext_vector_type(16))) __bf16 bf16x16;
typedef __attribute__((ext_vector_type(8))) float f32x8;
typedef __attribute__((ext_vector_type(4))) __bf16 bf16x4;
typedef __attribute__((ext_vector_type(2))) __bf16 bf16x2;
typedef __attribute__((ext_vector_type(4))) short s16x4;
typedef __attribute__((ext_vector_type(8))) short s16x8;
typedef __attribute__((ext_vector_type(16))) float f32x16;
typedef __attribute__((ext_vector_type(4))) float f32x4;
typedef __attribute__((ext_vector_type(4))) unsigned u32x4;
typedef __attribute__((ext_vector_type(2))) unsigned u32x2;
typedef __attribute__((ext_vector_type(2))) float f32x2;
typedef __attribute__((ext_vector_type(16))) unsigned u32x16;
typedef __bf16 bf16;

#define LDS_AS __attribute__((address_space(3)))
#define GLB_AS __attribute__((address_space(1)))
#define CONST_AS __attribute__((address_space(4)))
#define DI __device__ __forceinline__

constexpr int S_ = 2048, D_ = 512, H_ = 32, DH_ = 16, F_ = 1376;

constexpr size_t WS_Q = 0;
constexpr size_t WS_O = WS_Q;
constexpr size_t WS_K = 2u << 20;
constexpr size_t WS_V = WS_K + 65536;
constexpr size_t WS_ST = WS_V + 65536;
constexpr size_t WS_R = WS_ST + 16384;
constexpr size_t WS_CX = WS_R + 4096;
constexpr size_t WS_CNT = WS_CX + 131072;
constexpr int NCNT = 64 + 16 + 32;
constexpr size_t WS_FLG = WS_CNT + 64 * 256;
constexpr size_t WS_CL = WS_FLG + 16 * 256;
constexpr size_t WS_UPD = WS_CNT + NCNT * 256;
constexpr size_t WS_YP = WS_UPD + 4096;
constexpr int NFFN = 172;

DI float bf2f(bf16 v) { return (float)v; }
DI float rbf(float v) { return (float)(bf16)v; }
DI f32x2 unpack2(unsigned u) { return f32x2{__uint_as_float(u << 16), __uint_as_float(u & 0xffff0000u)}; }
DI unsigned pack2(f32x2 v) {
  unsigned r = __builtin_bit_cast(unsigned, __builtin_convertvector(v, bf16x2));
  asm("" : "+v"(r));
  return r;
}

DI unsigned route_key(float v, int tok) {
  const unsigned u = __float_as_uint(v) >> 16;
  const unsigned o = (u & 0x8000u) ? (~u & 0xffffu) : (u | 0x8000u);
  return (o << 16) | (unsigned)((0x7ff - tok) << 1);
}
DI int key_tok(unsigned k) { return 0x7ff - (int)((k >> 1) & 0x7ffu); }
DI int key_row(unsigned k) { return (key_tok(k) >> 5) * 2 + (int)(k & 1u); }
DI float key_val(unsigned k) {
  const unsigned o = k >> 16;
  const unsigned u = (o & 0x8000u) ? (o & 0x7fffu) : (~o & 0xffffu);
  return __uint_as_float(u << 16);
}
DI unsigned ln2(f32x2 x, f32x2 mb2, f32x2 rinv2) { return pack2(unpack2(pack2(x - mb2)) * rinv2); }
DI void top2_wave(unsigned& k1, unsigned& k2, int width) {
  for (int o = width >> 1; o > 0; o >>= 1) {
    const unsigned a1 = __shfl_xor(k1, o), a2 = __shfl_xor(k2, o);
    k2 = max(min(k1, a1), max(k2, a2));
    k1 = max(k1, a1);
  }
}

DI void top2_merge(u32x16 (&cv)[4], unsigned& k1, unsigned& k2) {
  asm volatile("" : "+s"(cv[0]), "+s"(cv[1]), "+s"(cv[2]), "+s"(cv[3]));
#pragma unroll
  for (int i = 0; i < 32; ++i) {
    const unsigned a1 = cv[i >> 3][(i & 7) * 2], a2 = cv[i >> 3][(i & 7) * 2 + 1];
    unsigned t;
    asm volatile("s_min_u32 %2, %0, %3\n s_max_u32 %1, %1, %4\n s_max_u32 %0, %0, %3\n s_max_u32 %1, %1, %2"
                 : "+s"(k1), "+s"(k2), "=&s"(t) : "s"(a1), "s"(a2) : "scc");
  }
}

DI void glds16_raw(const void* g, unsigned lds_off) {
  asm volatile("s_mov_b32 m0, %0\n s_nop 0\n global_load_lds_dwordx4 %1, off" ::"s"(lds_off), "v"(g) : "memory", "m0");
}

DI void blds16(__amdgpu_buffer_rsrc_t rs, unsigned voff, unsigned soff, unsigned lds_off) {
  asm volatile("s_mov_b32 m0, %0\n s_nop 0\n buffer_load_dwordx4 %1, %2, %3 offen lds" ::"s"(lds_off), "v"(voff), "s"(rs), "s"(soff) : "memory", "m0");
}
DI void blds16x2(__amdgpu_buffer_rsrc_t rs, unsigned voff, unsigned soff, unsigned lds_off) {
  asm volatile("s_mov_b32 m0, %0\n s_nop 0\n buffer_load_dwordx4 %1, %2, %3 offen lds\n buffer_load_dwordx4 %1, %2, %3 offen offset:1024 lds"
               ::"s"(lds_off), "v"(voff), "s"(rs), "s"(soff) : "memory", "m0");
}
DI void count_up(__amdgpu_buffer_rsrc_t rs, unsigned one, unsigned soff) {
  asm volatile("s_mov_b64 exec, 1\n buffer_atomic_add %0, off, %1, %2\n s_mov_b64 exec, -1" ::"v"(one), "s"(rs), "s"(soff) : "memory");
}
#define LDSP(T, off) ((LDS_AS T*)(unsigned long)(off))

template <int CTRL>
DI float dpp_add(float v) {
  const int t = __builtin_amdgcn_update_dpp(0, __float_as_int(v), CTRL, 0xf, 0xf, true);
  return v + __int_as_float(t);
}
DI float sum8(float v) {
  v = dpp_add<0xB1>(v);
  v = dpp_add<0x4E>(v);
  v = dpp_add<0x141>(v);
  return v;
}

DI f32x16 pv_mfma(bf16x8 v, f32x8 p, f32x16 acc, int sel) {
  return __builtin_amdgcn_smfmac_f32_32x32x32_bf16(v, __builtin_bit_cast(bf16x16, p), acc, sel, 0, 0);
}

DI void swap32(unsigned& a, unsigned& b) {
  const u32x2 t = __builtin_amdgcn_permlane32_swap(a, b, false, false);
  a = t[0];
  b = t[1];
}
DI float max32(float v) {
  unsigned a = __float_as_uint(v), b = a;
  swap32(a, b);
  return fmaxf(__uint_as_float(a), __uint_as_float(b));
}

DI s16x4 tr_read(const char* p) {
  return __builtin_amdgcn_ds_read_tr16_b64_v4i16((LDS_AS s16x4*)p);
}
DI bf16x8 tr_read8(unsigned lo, unsigned hi) {
  const s16x4 a = __builtin_amdgcn_ds_read_tr16_b64_v4i16(LDSP(s16x4, lo)), b = __builtin_amdgcn_ds_read_tr16_b64_v4i16(LDSP(s16x4, hi));
  return __builtin_bit_cast(bf16x8, __builtin_shufflevector(a, b, 0, 1, 2, 3, 4, 5, 6, 7));
}

DI int wimg_off(int row, int ch) { return row * 128 + ((ch ^ (((row >> 1) & 1) << 2)) << 4); }

constexpr int XS_STRIDE = 1040;

constexpr int GP = 4;
DI f32x16 gemm_tile(const char* wimg, const char* ximg, int n0, int t0, int lane) {
  const int r = lane & 31, h = lane >> 5;
  const int q = (lane & 15) >> 2, p = lane & 3, blk = (lane >> 4) & 1;
  f32x16 acc;
#pragma unroll
  for (int i = 0; i < 16; ++i) acc[i] = 0.f;
  const char* xb = ximg + (t0 + r) * XS_STRIDE + 16 * h;
  const int col = n0 + 16 * blk + 4 * p;
  const int ch = col >> 3, sub = (col & 7) * 2;
  const char* wb0 = wimg + wimg_off(8 * h + q, ch) + sub;
  const char* wb1 = wimg + wimg_off(8 * h + q + 4, ch) + sub;
  s16x4 lo[32], hi[32];
  bf16x8 b[32];
#pragma unroll
  for (int ks = 0; ks < 32 + GP; ++ks) {
    if (ks < 32) {
      lo[ks] = tr_read(wb0 + ks * 2048);
      hi[ks] = tr_read(wb1 + ks * 2048);
      b[ks] = *(const bf16x8*)(xb + ks * 32);
    }
    if (ks >= GP) {
      const s16x8 a = __builtin_shufflevector(lo[ks - GP], hi[ks - GP], 0, 1, 2, 3, 4, 5, 6, 7);
      acc = __builtin_amdgcn_mfma_f32_32x32x16_bf16(__builtin_bit_cast(bf16x8, a), b[ks - GP], acc, 0, 0, 0);
      asm volatile("" : "+v"(acc));
    }
    __builtin_amdgcn_sched_barrier(0);
  }
  return acc;
}

DI float dot2(unsigned a, unsigned b, float c) {
  return __builtin_amdgcn_fdot2_f32_bf16(__builtin_bit_cast(bf16x2, a), __builtin_bit_cast(bf16x2, b), c, false);
}
DI void touch(unsigned& t, const void* p) {
  asm volatile("global_load_dword %0, %1, off" : "=v"(t) : "v"(p) : "memory");
}
DI float rbf1(float v) { return __uint_as_float(pack2(f32x2{0.f, v})); }
DI void unpack_add(u32x4 x, float c, f32x2* t) {
  float d[8];
  asm("v_dot2_f32_bf16 %0, %8, %12, %14\n v_dot2_f32_bf16 %1, %8, %13, %14\n"
      "v_dot2_f32_bf16 %2, %9, %12, %14\n v_dot2_f32_bf16 %3, %9, %13, %14\n"
      "v_dot2_f32_bf16 %4, %10, %12, %14\n v_dot2_f32_bf16 %5, %10, %13, %14\n"
      "v_dot2_f32_bf16 %6, %11, %12, %14\n v_dot2_f32_bf16 %7, %11, %13, %14\n s_nop 2"
      : "=&v"(d[0]), "=&v"(d[1]), "=&v"(d[2]), "=&v"(d[3]), "=&v"(d[4]), "=&v"(d[5]), "=&v"(d[6]), "=&v"(d[7])
      : "v"(x[0]), "v"(x[1]), "v"(x[2]), "v"(x[3]), "s"(0x00003f80u), "s"(0x3f800000u), "v"(c));
#pragma unroll
  for (int i = 0; i < 4; ++i) t[i] = f32x2{d[2 * i], d[2 * i + 1]};
}
DI float sum16(float v) { return dpp_add<0x140>(sum8(v)); }
template <int N>
DI void wait_vm() { asm volatile("s_waitcnt vmcnt(%0)" ::"n"(N) : "memory"); }

#define QSTEP(i)                                                                  \
  acc = __builtin_amdgcn_mfma_f32_32x32x16_bf16(a[i], b[i], acc, 0, 0, 0); \
  asm volatile("" : "+v"(acc));                                            \
  __builtin_amdgcn_sched_barrier(0)

#define CODE_ADDR(sym, out)                                                                               \
  asm volatile("s_getpc_b64 s[20:21]\n s_add_u32 s20, s20, " #sym "@rel32@lo+4\n s_addc_u32 s21, s21, " #sym \
               "@rel32@hi+12\n s_mov_b64 %0, s[20:21]"                                                    \
               : "=s"(out)::"s20", "s21", "scc")

constexpr int NT = 16;
constexpr int CST = 4096;
constexpr int L_RED = 131072 + CST;
constexpr int L_CNT = L_RED + 512;
constexpr int L_H = L_CNT + 64;
constexpr int L_STG = L_RED + 1024;
constexpr int ATT_LDS = L_STG + 4 * 6144;
constexpr int NC = 64 / NT;
constexpr int PSW = 53;
constexpr int SPIN_MAX = 1 << 22;

extern "C" __global__ __launch_bounds__(768) void k2_attn(char* __restrict__ ws, const bf16* __restrict__ x,
                                               const bf16* __restrict__ wg, const bf16* __restrict__ wu,
                                               const bf16* __restrict__ wd, const bf16* __restrict__ wo,
                                               bf16* __restrict__ out) {
  const bf16* Q = (const bf16*)(ws + WS_Q);
  const bf16* Kb = (const bf16*)(ws + WS_K);
  bf16* O = (bf16*)(ws + WS_O);
  extern __shared__ __attribute__((aligned(16))) char smem[];
  char* kl = smem;
  char* vl = smem + 65536;
  char* cst = smem + 131072;
  const int tid = threadIdx.x, lane = tid & 63;
  const int wave = __builtin_amdgcn_readfirstlane(tid >> 6);

  const unsigned lds0 = (unsigned)(size_t)(LDS_AS char*)smem;
  unsigned long long pc = (unsigned long long)ws + WS_R, pg = (unsigned long long)wg, pu = (unsigned long long)wu, pd = (unsigned long long)wd;
  asm volatile("" : "+s"(pc), "+s"(pg), "+s"(pu), "+s"(pd));
  if (wave >= 8) {
    const int sw = wave - 8, r = lane & 31, h = lane >> 5;
    u32x16 cv[4];
#pragma unroll
    for (int i = 0; i < 4; ++i) cv[i] = ((const CONST_AS u32x16*)pc)[i];
    unsigned l16 = lane * 16;
    const int fs = (blockIdx.x & 7) * 22 + (blockIdx.x >> 3);
    const bool ffn = blockIdx.x < 176 && fs < NFFN;
    const unsigned stg = lds0 + L_STG + sw * 6144;
    const int n = lane & 15, g = lane >> 4, q = n >> 2, p = lane & 3;
    unsigned a_gu = stg + (p >> 1) * 2048 + (8 * g + q) * 16 + (p & 1) * 8;
    unsigned a_dn = stg + 4096 + q * 256 + (16 * g + 4 * p) * 2;
    unsigned a_rw = lds0 + L_RED + (n & 7) * 32 + sw * 8 + (n >> 3) * 4;
    unsigned a_rr = lds0 + L_RED + n * 32;
    unsigned a_hw = lds0 + L_H + sw * 32 + n * 2;
    const unsigned hrow = lds0 + L_H + sw * 32 + (n >> 3) * 16, zrow = lds0 + 131072 + 16;
    unsigned a_h0 = (p == g && (q & 1) == 0) ? hrow : zrow, a_h1 = (p == g && (q & 1) == 1) ? hrow : zrow;
    unsigned a_cnt = lds0 + L_CNT, one = 1, odd = lane & 1, even = (lane & 1) ^ 1, g16 = g * 16;
    unsigned v_w = (lane >> 3) * 1024 + (((lane & 7) ^ ((lane >> 4 & 1) << 2)) << 4);
    unsigned v_x = r * 1024 + h * 8, v_s = r * 8;
    unsigned v_y = n * 4096 + g * 16, v_c = n * 256, v_e = n < 12 ? 11 : 10;
    asm volatile("" : "+v"(l16), "+v"(a_gu), "+v"(a_dn), "+v"(a_rw), "+v"(a_rr), "+v"(a_hw), "+v"(a_h0), "+v"(a_h1),
                 "+v"(a_cnt), "+v"(one), "+v"(odd), "+v"(even), "+v"(g16));
    unsigned vg = lane * (F_ * 2), vd = g * 1024 + n * 16;
    asm volatile("" : "+v"(v_w), "+v"(v_x), "+v"(v_s), "+v"(v_y), "+v"(v_c), "+v"(v_e), "+v"(vg), "+v"(vd));
    {
      const __amdgpu_buffer_rsrc_t rk = __builtin_amdgcn_make_buffer_rsrc((void*)Kb, 0, 131072, 0x00020000);
#pragma unroll
      for (int i = 0; i < 13; ++i) {
        const unsigned o = sw * 2048 + (i < 6 ? 16 + 8 * i : 24 + 8 * i) * 1024;
        blds16x2(rk, l16, o, lds0 + o);
      }
    }
    asm volatile("s_barrier" ::: "memory");
    unsigned k1 = 0, k2 = 0;
    top2_merge(cv, k1, k2);
#pragma unroll
    for (int i = 0; i < 4; ++i) cv[i] = ((const CONST_AS u32x16*)pc)[4 + i];
    const __amdgpu_buffer_rsrc_t rc = __builtin_amdgcn_make_buffer_rsrc(ws + WS_CNT, 0, NCNT * 256, 0x00020000);
    const __amdgpu_buffer_rsrc_t ry = __builtin_amdgcn_make_buffer_rsrc(ws + WS_YP, 0, NFFN * 4096, 0x00020000);
    const __amdgpu_buffer_rsrc_t rup = __builtin_amdgcn_make_buffer_rsrc(ws + WS_UPD, 0, 4096, 0x00020000);
    asm volatile("s_waitcnt vmcnt(0)\n s_barrier" ::: "memory");
    if (ffn) {
      const __amdgpu_buffer_rsrc_t rg = __builtin_amdgcn_make_buffer_rsrc((void*)pg, 0, 512 * F_ * 2, 0x00020000);
      const __amdgpu_buffer_rsrc_t ru = __builtin_amdgcn_make_buffer_rsrc((void*)pu, 0, 512 * F_ * 2, 0x00020000);
      const __amdgpu_buffer_rsrc_t rd = __builtin_amdgcn_make_buffer_rsrc((void*)pd, 0, 512 * F_ * 2, 0x00020000);
      const unsigned so = (sw * 128 * F_ + fs * 8) * 2, sd = (fs * 8 * 512 + sw * 128) * 2;
      blds16(rg, vg, so, stg);
      blds16(rg, vg, so + 64 * F_ * 2, stg + 1024);
      blds16(ru, vg, so, stg + 2048);
      blds16(ru, vg, so + 64 * F_ * 2, stg + 3072);
      blds16(rd, vd, sd, stg + 4096);
      blds16(rd, vd, sd + 4096, stg + 5120);
    }
    top2_merge(cv, k1, k2);
    if (ffn) {
      const __amdgpu_buffer_rsrc_t rx = __builtin_amdgcn_make_buffer_rsrc(ws + WS_CX, 0, 131072, 0x00020000);
      u32x4 xf[4];
      __builtin_amdgcn_s_setprio(3);
      unsigned vx = __umul24(even, key_row(k1) * 1024) + g16;
      vx += __umul24(odd, key_row(k2) * 1024);
      asm volatile("" : "+v"(vx));
      __builtin_amdgcn_s_setprio(0);
#pragma unroll
      for (int i = 0; i < 4; ++i) xf[i] = __builtin_amdgcn_raw_buffer_load_b128(rx, vx + i * 64, sw * 256, 0);
      asm volatile("s_waitcnt vmcnt(0)" ::: "memory");
      bf16x8 wf[4], df[2];
#pragma unroll
      for (int i = 0; i < 4; ++i) wf[i] = tr_read8(a_gu + i * 512, a_gu + i * 512 + 64);
#pragma unroll
      for (int i = 0; i < 2; ++i) df[i] = tr_read8(a_dn + i * 128, a_dn + i * 128 + 1024);
      asm volatile("s_waitcnt lgkmcnt(0)" ::: "memory");
      __builtin_amdgcn_s_setprio(3);
      f32x4 gu = {0.f, 0.f, 0.f, 0.f};
#pragma unroll
      for (int i = 0; i < 4; ++i) gu = __builtin_amdgcn_mfma_f32_16x16x32_bf16(__builtin_bit_cast(bf16x8, xf[i]), wf[i], gu, 0, 0, 0);
      *LDSP(float, a_rw) = gu[0];
      *LDSP(float, a_rw + 256) = gu[1];
      asm volatile("s_mov_b64 exec, 1\n ds_add_u32 %0, %1\n s_mov_b64 exec, -1" ::"v"(a_cnt), "v"(one) : "memory");
      while (*(volatile LDS_AS unsigned*)LDSP(unsigned, a_cnt) < 4) __builtin_amdgcn_s_sleep(1);
      asm volatile("" ::: "memory");
      const f32x4 r0 = *LDSP(f32x4, a_rr), r1 = *LDSP(f32x4, a_rr + 16);
      const f32x2 gs = unpack2(pack2((f32x2{r0[0], r0[1]} + f32x2{r0[2], r0[3]}) + (f32x2{r1[0], r1[1]} + f32x2{r1[2], r1[3]})));
      const float hv = rbf(gs.x * __builtin_amdgcn_rcpf(1.f + __expf(-gs.x))) * gs.y;
      *LDSP(bf16, a_hw) = (bf16)hv;
      asm volatile("" ::: "memory");
      const bf16x8 h0 = *LDSP(bf16x8, a_h0), h1 = *LDSP(bf16x8, a_h1);
      f32x4 y = {0.f, 0.f, 0.f, 0.f};
      y = __builtin_amdgcn_mfma_f32_16x16x32_bf16(h0, df[0], y, 0, 0, 0);
      y = __builtin_amdgcn_mfma_f32_16x16x32_bf16(h1, df[1], y, 0, 0, 0);
      __builtin_amdgcn_raw_buffer_store_b128(__builtin_bit_cast(u32x4, y), ry, l16, fs * 4096 + sw * 1024, 16);
      __builtin_amdgcn_s_setprio(0);
      asm volatile("s_waitcnt vmcnt(0)" ::: "memory");
      count_up(rc, one, (sw * 16 + (fs & 15)) * 256);
    }
    const int tb = (blockIdx.x >> 6) * 8 + (blockIdx.x & 7), cb = (blockIdx.x >> 3) & 7, tok0 = tb * 64;
    char* ximg = smem;
    char* wimg = smem + 64 * XS_STRIDE;
    const int n0 = (sw & 1) * 32, t0 = (sw >> 1) * 32;
    const unsigned so_x = (tok0 + t0) * 1024 + (cb * 64 + n0) * 2;
    const __amdgpu_buffer_rsrc_t rout = __builtin_amdgcn_make_buffer_rsrc(out, 0, S_ * D_ * 2, 0x00020000);
    u32x4 wreg[16];
    u32x2 xv[4], nv[4], sxr;
    {
      const __amdgpu_buffer_rsrc_t rw = __builtin_amdgcn_make_buffer_rsrc((void*)wo, 0, 512 * 1024, 0x00020000);
      const __amdgpu_buffer_rsrc_t rx = __builtin_amdgcn_make_buffer_rsrc((void*)x, 0, S_ * D_ * 2, 0x00020000);
      const __amdgpu_buffer_rsrc_t rs = __builtin_amdgcn_make_buffer_rsrc(ws + WS_ST, 0, S_ * 8, 0x00020000);
#pragma unroll
      for (int i = 0; i < 16; ++i) wreg[i] = __builtin_amdgcn_raw_buffer_load_b128(rw, v_w, (sw + 4 * i) * 8192 + cb * 128, 0);
#pragma unroll
      for (int i = 0; i < 4; ++i) xv[i] = __builtin_amdgcn_raw_buffer_load_b64(rx, v_x + i * 16, so_x, 0);
      sxr = __builtin_amdgcn_raw_buffer_load_b64(rs, v_s, (tok0 + t0) * 8, 0);
      if (wave == 11) {
        const unsigned t = __builtin_amdgcn_raw_buffer_load_b32(rout, 0, so_x, 0);
        asm volatile("" ::"v"(t));
      }
    }
    if ((blockIdx.x >> 4) == 11) {
      const int sel = (blockIdx.x >> 3) & 1, cq = blockIdx.x & 7;
      __builtin_amdgcn_s_setprio(3);
      int spin = 0;
      while (__any(__builtin_amdgcn_raw_buffer_load_b32(rc, v_c, (cq >> 1) * 4096, 16) < v_e) && ++spin < SPIN_MAX)
        asm volatile("s_sleep 8" ::: "memory");
      __builtin_amdgcn_s_setprio(0);
      asm volatile("" ::: "memory");
      const unsigned so = (cq >> 1) * 1024 + (2 * sel + (cq & 1)) * 256 + sw * 64;
      u32x4 t[11];
#pragma unroll
      for (int i = 0; i < 10; ++i) t[i] = __builtin_amdgcn_raw_buffer_load_b128(ry, v_y, so + i * 65536, 16);
      __builtin_amdgcn_s_setprio(3);
      t[10] = u32x4{0u, 0u, 0u, 0u};
      if (v_e == 11) t[10] = __builtin_amdgcn_raw_buffer_load_b128(ry, v_y, so + 10 * 65536, 16);
      const float gate = rbf(__builtin_amdgcn_rcpf(1.f + __expf(-key_val(sel ? k2 : k1))));
      f32x4 a = __builtin_bit_cast(f32x4, t[0]);
#pragma unroll
      for (int i = 1; i < 11; ++i) a += __builtin_bit_cast(f32x4, t[i]);
#pragma unroll
      for (int i = 0; i < 4; ++i) a[i] = dpp_add<0x140>(sum8(a[i]));
      asm volatile("" : "+v"(a));
      const float u = rbf(rbf(p == 0 ? a[0] : p == 1 ? a[1] : p == 2 ? a[2] : a[3]) * gate);
      if (n < 4) __builtin_amdgcn_raw_buffer_store_b32(__float_as_uint(u), rup, n * 64 + g * 4, (sel * 512 + cq * 64 + sw * 4) * 4, 16);
      __builtin_amdgcn_s_setprio(0);
      asm volatile("s_waitcnt vmcnt(0)" ::: "memory");
      count_up(rc, one, (64 + sel * 8 + cq) * 256);
    }
    const bool routed = ((key_tok(k1) ^ (tok0 + t0)) >> 5) == 0 || ((key_tok(k2) ^ (tok0 + t0)) >> 5) == 0;
    u32x4 uf[4];
    if (routed) {
      __builtin_amdgcn_s_setprio(3);
      const int tok = tok0 + t0 + r;
      const int rsel = tok == key_tok(k1) ? 0 : tok == key_tok(k2) ? 1 : -1;
#pragma unroll
      for (int i = 0; i < 4; ++i) uf[i] = u32x4{0x80000000u, 0x80000000u, 0x80000000u, 0x80000000u};
      if (rsel >= 0) {
        int spin = 0;
        while (__builtin_amdgcn_raw_buffer_load_b32(rc, rsel * 2048, (64 + cb) * 256, 16) < 4 && ++spin < SPIN_MAX)
          asm volatile("s_sleep 4" ::: "memory");
        asm volatile("" ::: "memory");
#pragma unroll
        for (int i = 0; i < 4; ++i)
          uf[i] = __builtin_amdgcn_raw_buffer_load_b128(rup, (rsel * 512 + 8 * i + 4 * h) * 4, (cb * 64 + n0) * 4, 16);
      }
#pragma unroll
      for (int i = 0; i < 4; ++i) asm volatile("" : "+v"(uf[i]));
      __builtin_amdgcn_s_setprio(0);
    }
#pragma unroll
    for (int i = 0; i < 16; ++i) asm volatile("" : "+v"(wreg[i]));
    asm volatile("s_barrier" ::: "memory");
    if (wave == 8) count_up(rc, one, (80 + tb) * 256);
#pragma unroll
    for (int i = 0; i < 16; ++i) *(u32x4*)(wimg + (sw + 4 * i) * 1024 + lane * 16) = wreg[i];
    {
      const f32x2 mb2 = {__uint_as_float(sxr[0]), __uint_as_float(sxr[0])}, ri2 = {__uint_as_float(sxr[1]), __uint_as_float(sxr[1])};
#pragma unroll
      for (int i = 0; i < 4; ++i) {
#pragma unroll
        for (int jj = 0; jj < 2; ++jj) nv[i][jj] = ln2(unpack2(xv[i][jj]), mb2, ri2);
      }
    }
    if (routed) {
#pragma unroll
      for (int i = 0; i < 4; ++i) {
        const f32x4 u = __builtin_bit_cast(f32x4, uf[i]);
        nv[i][0] = pack2(unpack2(nv[i][0]) + f32x2{u[0], u[1]});
        nv[i][1] = pack2(unpack2(nv[i][1]) + f32x2{u[2], u[3]});
      }
    }
    if (wave == 8) {
      int spin = 0;
      while (__hip_atomic_load((GLB_AS unsigned*)(ws + WS_CL + tb * 256), __ATOMIC_RELAXED, __HIP_MEMORY_SCOPE_AGENT) < 8 && ++spin < SPIN_MAX)
        __builtin_amdgcn_s_sleep(1);
    }
    asm volatile("s_barrier" ::: "memory");
    {
      const __amdgpu_buffer_rsrc_t ro = __builtin_amdgcn_make_buffer_rsrc(O + tok0 * 512, 0, 65536, 0x00020000);
      u32x4 t[16];
#pragma unroll
      for (int i = 0; i < 16; ++i) t[i] = __builtin_amdgcn_raw_buffer_load_b128(ro, l16, (sw * 16 + i) * 1024, 16);
#pragma unroll
      for (int i = 0; i < 16; ++i) *(u32x4*)(ximg + (sw * 16 + i) * XS_STRIDE + lane * 16) = t[i];
    }
    asm volatile("s_waitcnt lgkmcnt(0)\n s_barrier" ::: "memory");
    f32x16 acc = gemm_tile(wimg, ximg, n0, t0, lane);
#pragma unroll
    for (int g = 0; g < 4; ++g) {
      u32x2 o;
#pragma unroll
      for (int jj = 0; jj < 2; ++jj) {
        const unsigned xr = xv[g][jj], nr = nv[g][jj];
        const f32x2 a = unpack2(pack2(f32x2{acc[4 * g + 2 * jj], acc[4 * g + 2 * jj + 1]}));
        o[jj] = pack2(unpack2(pack2(unpack2(xr) + a)) + unpack2(nr));
      }
      __builtin_amdgcn_raw_buffer_store_b64(o, rout, v_x + g * 16, so_x, 0);
    }
    return;
  }

  const int r = lane & 31, h = lane >> 5;
  const int token = ((blockIdx.x >> 6) * 8 + (blockIdx.x & 7)) * 64 + ((blockIdx.x >> 3) & 7) * 8 + wave;
  bf16x8 qf;
  asm volatile("global_load_dwordx4 %0, %1, off" : "=v"(qf) : "v"(Q + token * 512 + r * 16 + 8 * h) : "memory");
#pragma unroll
  for (int i = 0; i < 3; ++i) {
    const int piece = (i < 2) ? (wave + i * 8) : (64 + wave);
    glds16_raw((const char*)Kb + piece * 1024 + lane * 16, lds0 + piece * 1024);
  }
  {
    const unsigned cv = ((tid & 30) == 0 || (tid & 30) == 8) ? 0x3f803f80u : 0u;
    *(u32x2*)(cst + tid * 8) = u32x2{cv, cv};
    if (tid == 1) *(unsigned*)(smem + L_CNT) = 0;
  }
  int vo = (r < 16) ? 65536 + (h * 16 + r) * 16 : 131072 + (h * 16 + r - 16) * 16;
  int vstep = (r < 16) ? CST : 0;
  int sel = 0xdddd;
  f32x16 zero;
#pragma unroll
  for (int i = 0; i < 16; ++i) zero[i] = 0.f;
  f32x16 acc = zero;
  asm volatile("" : "+v"(sel), "+v"(acc), "+v"(vstep), "+v"(vo));
  const char* kp0 = kl + lane * 16;
  const char* vp0 = smem + vo;
  asm volatile("s_waitcnt vmcnt(0) lgkmcnt(0)\n s_barrier" ::: "memory");

  constexpr float HEAD = 40.f;
  f32x16 negm, xs;
  bf16x8 kfn;
  {
    bf16x8 kf[4];
    f32x16 s0[4];
#pragma unroll
    for (int t = 0; t < 4; ++t) kf[t] = *(const bf16x8*)(kp0 + t * 1024);
#pragma unroll
    for (int t = 0; t < 4; ++t) {
      s0[t] = __builtin_amdgcn_mfma_f32_32x32x16_bf16(kf[t], qf, zero, 0, 0, 0);
      asm volatile("" : "+v"(s0[t]));
    }
    __builtin_amdgcn_sched_barrier(0);
    float mx = -INFINITY;
#pragma unroll
    for (int t = 0; t < 4; ++t) {
#pragma unroll
      for (int i = 0; i < 16; i += 2) mx = fmaxf(fmaxf(mx, s0[t][i]), s0[t][i + 1]);
    }
    const float nm = -(floorf(max32(mx)) + HEAD);
#pragma unroll
    for (int i = 0; i < 16; ++i) {
      xs[i] = s0[0][i] + nm;
      negm[i] = nm;
    }
    kfn = kf[1];
  }
  asm volatile("" : "+v"(negm));
  {
    const char* kp = kp0;
    const char* vp = vp0;
    f32x16 x = xs;
    if (wave < 4) __builtin_amdgcn_s_setprio(2);
    else __builtin_amdgcn_s_setprio(1);
    for (int c = 0; c < NC; ++c) {
      bool grown;
#pragma unroll
      for (int t = 0; t < NT; ++t) {
        const bf16x8 kf = kfn;
        kfn = *(const bf16x8*)(kp + (t + 2) * 1024);
        const bf16x8 vf0 = *(const bf16x8*)(vp + ((t & 3) * 2) * 512);
        const bf16x8 vf1 = *(const bf16x8*)(vp + ((t & 3) * 2 + 1) * 512);
        if ((t & 3) == 3) vp += vstep;
        f32x8 p0, p1;
#pragma unroll
        for (int j = 0; j < 4; ++j) p0[j] = __builtin_amdgcn_exp2f(x[j]);
        __builtin_amdgcn_sched_barrier(0);
        f32x16 xn = __builtin_amdgcn_mfma_f32_32x32x16_bf16(kf, qf, negm, 0, 0, 0);
        asm volatile("" : "+v"(xn));
        __builtin_amdgcn_sched_barrier(0);
#pragma unroll
        for (int j = 4; j < 8; ++j) p0[j] = __builtin_amdgcn_exp2f(x[j]);
        if (t == NT - 1) grown = __any(!(acc[8] <= 4.3e9f));
        acc = pv_mfma(vf0, p0, acc, sel);
#pragma unroll
        for (int j = 0; j < 8; ++j) p1[j] = __builtin_amdgcn_exp2f(x[8 + j]);
        acc = pv_mfma(vf1, p1, acc, sel);
        if (t == 7 && c == 0) asm volatile("s_barrier" ::: "memory");
        if (c * NT + t == PSW && wave < 4) __builtin_amdgcn_s_setprio(0);
        __builtin_amdgcn_sched_barrier(0);
        x = xn;
      }
      kp += NT * 1024;
      if (grown) {
        const int e = max(__builtin_amdgcn_frexp_expf(acc[8]) - 1 + (int)HEAD, 0);
#pragma unroll
        for (int i = 0; i < 9; ++i) acc[i] = __builtin_amdgcn_ldexpf(acc[i], -e);
        const float ef = (float)e;
#pragma unroll
        for (int i = 0; i < 16; ++i) {
          negm[i] -= ef;
          x[i] -= ef;
        }
        asm volatile("" : "+v"(negm));
      }
    }
  }
  {
    float chk = 0.f;
#pragma unroll
    for (int i = 0; i < 9; ++i) chk = __builtin_fmaf(acc[i], 0.f, chk);
    if (__any(chk != 0.f)) {
      f32x16 oacc = zero;
      float mrun = -INFINITY;
      const char* kp = kp0;
      const char* vp = vp0;
      for (int t = 0; t < 64; ++t) {
        bf16x8 kf = *(const bf16x8*)(kp);
        f32x16 x = __builtin_amdgcn_mfma_f32_32x32x16_bf16(kf, qf, zero, 0, 0, 0);
        float mx = x[0];
#pragma unroll
        for (int i = 1; i < 16; ++i) mx = fmaxf(mx, x[i]);
        const float mn = fmaxf(mrun, max32(mx));
        const float al = __builtin_amdgcn_exp2f(mrun - mn);
        mrun = mn;
#pragma unroll
        for (int i = 0; i < 16; ++i) oacc[i] *= al;
#pragma unroll
        for (int s = 0; s < 2; ++s) {
          f32x8 pf;
#pragma unroll
          for (int j = 0; j < 8; ++j) pf[j] = __builtin_amdgcn_exp2f(x[8 * s + j] - mn);
          bf16x8 vf = *(const bf16x8*)(vp + ((t & 3) * 2 + s) * 512);
          oacc = pv_mfma(vf, pf, oacc, sel);
        }
        kp += 1024;
        if ((t & 3) == 3) vp += vstep;
      }
      acc = oacc;
    }
  }
  const float inv = 1.f / acc[8];
  {
    unsigned a0 = pack2(f32x2{acc[0] * inv, acc[1] * inv}), a1 = pack2(f32x2{acc[2] * inv, acc[3] * inv});
    unsigned b0 = pack2(f32x2{acc[4] * inv, acc[5] * inv}), b1 = pack2(f32x2{acc[6] * inv, acc[7] * inv});
    swap32(a0, b0);
    swap32(a1, b1);
    const u32x4 o = {a0, a1, b0, b1};
    const __amdgpu_buffer_rsrc_t ro = __builtin_amdgcn_make_buffer_rsrc(O + token * 512, 0, 1024, 0x00020000);
    __builtin_amdgcn_raw_buffer_store_b128(o, ro, r * 32 + h * 16, 0, 16);
  }
  asm volatile("s_waitcnt vmcnt(0)\n s_barrier" ::: "memory");
}

constexpr int K1_KV = 32 * XS_STRIDE;
constexpr int K1_RS = K1_KV + 16384;
constexpr int K1_W = K1_RS + 256;
constexpr int NSLOT = 27;
constexpr int NPRE = 24;
constexpr int LA = 4;
constexpr int K1_LDS = K1_W + 4 * NSLOT * 1024;

extern "C" __global__ __launch_bounds__(768) void k1_ln_qkv(const bf16* __restrict__ x, const bf16* __restrict__ wq,
                                                 const bf16* __restrict__ wk, const bf16* __restrict__ wv,
                                                 const bf16* __restrict__ wr, char* __restrict__ ws) {
  float* st = (float*)(ws + WS_ST);
  bf16* Q = (bf16*)(ws + WS_Q);
  unsigned* cand = (unsigned*)(ws + WS_R);
  extern __shared__ __attribute__((aligned(16))) char smem[];
  char* ximg = smem;
  float* rs = (float*)(smem + K1_RS);
  const unsigned lds0 = (unsigned)(size_t)(LDS_AS char*)smem;
  const int cb = blockIdx.x >> 6, tb = ((blockIdx.x >> 4 & 3) * 8 + (blockIdx.x & 7)) * 2 + (blockIdx.x >> 3 & 1);
  const int tid = threadIdx.x, lane = tid & 63;
  const int wave = __builtin_amdgcn_readfirstlane(tid >> 6);
  const int tok0 = tb * 32;
  const int r = lane & 31, h = lane >> 5;
  const int n = lane & 15, g = lane >> 4, q = n >> 2, p = lane & 3;

  if (wave >= 8) {
    const int sw = wave - 8;
    const unsigned ring = lds0 + K1_W + sw * NSLOT * 1024;
    const __amdgpu_buffer_rsrc_t rw = __builtin_amdgcn_make_buffer_rsrc((void*)wq, 0, 512 * 1024, 0x00020000);
    const __amdgpu_buffer_rsrc_t rkv = __builtin_amdgcn_make_buffer_rsrc((void*)((cb & 1) ? wv : wk), 0, 16384, 0x00020000);
    const unsigned vw = (lane >> 2) * 1024 + (lane & 3) * 16, sw0 = (cb * 128 + sw * 32) * 2;
#pragma unroll
    for (int ks = 0; ks < NSLOT; ++ks) {
      blds16(rw, vw, sw0 + ks * 16384, ring + ks * 1024);
      if (ks == 32 - NSLOT - 1) {
        blds16x2(rkv, lane * 16, sw * 4096, lds0 + K1_KV + sw * 4096);
        blds16x2(rkv, lane * 16, sw * 4096 + 2048, lds0 + K1_KV + sw * 4096 + 2048);
      }
    }
    const unsigned wa = ring + (8 * h + q) * 64 + (g & 1) * 32 + p * 8;
    bf16x8 a[32], b[32];
    wait_vm<NSLOT + 4 - (32 - NSLOT)>();
#pragma unroll
    for (int ks = 0; ks < 32 - NSLOT; ++ks) a[ks] = tr_read8(wa + ks * 1024, wa + ks * 1024 + 256);
#pragma unroll
    for (int ks = 0; ks < 32 - NSLOT; ++ks) asm volatile("" : "+v"(a[ks]));
#pragma unroll
    for (int ks = NSLOT; ks < 32; ++ks) blds16(rw, vw, sw0 + ks * 16384, ring + (ks - NSLOT) * 1024);
    wait_vm<23>();
#pragma unroll
    for (int ks = 5; ks < 9; ++ks) a[ks] = tr_read8(wa + ks * 1024, wa + ks * 1024 + 256);
    wait_vm<19>();
#pragma unroll
    for (int ks = 9; ks < 13; ++ks) a[ks] = tr_read8(wa + ks * 1024, wa + ks * 1024 + 256);
    wait_vm<15>();
#pragma unroll
    for (int ks = 13; ks < 17; ++ks) a[ks] = tr_read8(wa + ks * 1024, wa + ks * 1024 + 256);
    wait_vm<11>();
#pragma unroll
    for (int ks = 17; ks < 21; ++ks) a[ks] = tr_read8(wa + ks * 1024, wa + ks * 1024 + 256);
    wait_vm<8>();
#pragma unroll
    for (int ks = 21; ks < 24; ++ks) a[ks] = tr_read8(wa + ks * 1024, wa + ks * 1024 + 256);
#pragma unroll
    for (int ks = 32 - NSLOT; ks < NPRE; ++ks) asm volatile("" : "+v"(a[ks]));
    asm volatile("s_barrier" ::: "memory");
    f32x16 acc;
#pragma unroll
    for (int i = 0; i < 16; ++i) acc[i] = 0.f;
    const char* xb = ximg + r * XS_STRIDE + 16 * h;
    b[0] = *(const bf16x8*)(xb + 0);
    __builtin_amdgcn_sched_barrier(0);
    b[1] = *(const bf16x8*)(xb + 32);
    __builtin_amdgcn_sched_barrier(0);
    b[2] = *(const bf16x8*)(xb + 64);
    __builtin_amdgcn_sched_barrier(0);
    b[3] = *(const bf16x8*)(xb + 96);
    __builtin_amdgcn_sched_barrier(0);
    b[4] = *(const bf16x8*)(xb + 128);
    QSTEP(0);
    b[5] = *(const bf16x8*)(xb + 160);
    QSTEP(1);
    b[6] = *(const bf16x8*)(xb + 192);
    QSTEP(2);
    b[7] = *(const bf16x8*)(xb + 224);
    QSTEP(3);
    b[8] = *(const bf16x8*)(xb + 256);
    QSTEP(4);
    b[9] = *(const bf16x8*)(xb + 288);
    QSTEP(5);
    b[10] = *(const bf16x8*)(xb + 320);
    QSTEP(6);
    b[11] = *(const bf16x8*)(xb + 352);
    QSTEP(7);
    b[12] = *(const bf16x8*)(xb + 384);
    QSTEP(8);
    b[13] = *(const bf16x8*)(xb + 416);
    QSTEP(9);
    b[14] = *(const bf16x8*)(xb + 448);
    QSTEP(10);
    b[15] = *(const bf16x8*)(xb + 480);
    QSTEP(11);
    b[16] = *(const bf16x8*)(xb + 512);
    QSTEP(12);
    b[17] = *(const bf16x8*)(xb + 544);
    QSTEP(13);
    b[18] = *(const bf16x8*)(xb + 576);
    QSTEP(14);
    b[19] = *(const bf16x8*)(xb + 608);
    QSTEP(15);
    b[20] = *(const bf16x8*)(xb + 640);
    wait_vm<7>();
    a[24] = tr_read8(wa + 24576, wa + 24832);
    QSTEP(16);
    b[21] = *(const bf16x8*)(xb + 672);
    wait_vm<6>();
    a[25] = tr_read8(wa + 25600, wa + 25856);
    QSTEP(17);
    b[22] = *(const bf16x8*)(xb + 704);
    wait_vm<5>();
    a[26] = tr_read8(wa + 26624, wa + 26880);
    QSTEP(18);
    b[23] = *(const bf16x8*)(xb + 736);
    wait_vm<4>();
    a[27] = tr_read8(wa + 0, wa + 256);
    QSTEP(19);
    b[24] = *(const bf16x8*)(xb + 768);
    wait_vm<3>();
    a[28] = tr_read8(wa + 1024, wa + 1280);
    QSTEP(20);
    b[25] = *(const bf16x8*)(xb + 800);
    wait_vm<2>();
    a[29] = tr_read8(wa + 2048, wa + 2304);
    QSTEP(21);
    b[26] = *(const bf16x8*)(xb + 832);
    wait_vm<1>();
    a[30] = tr_read8(wa + 3072, wa + 3328);
    QSTEP(22);
    b[27] = *(const bf16x8*)(xb + 864);
    wait_vm<0>();
    a[31] = tr_read8(wa + 4096, wa + 4352);
    QSTEP(23);
    b[28] = *(const bf16x8*)(xb + 896);
    QSTEP(24);
    b[29] = *(const bf16x8*)(xb + 928);
    QSTEP(25);
    b[30] = *(const bf16x8*)(xb + 960);
    QSTEP(26);
    b[31] = *(const bf16x8*)(xb + 992);
    QSTEP(27);
    QSTEP(28);
    QSTEP(29);
    QSTEP(30);
    QSTEP(31);
    const float sc = 0.25f * 1.4426950408889634f;
    const __amdgpu_buffer_rsrc_t rq = __builtin_amdgcn_make_buffer_rsrc(Q + tok0 * 512, 0, 32768, 0x00020000);
#pragma unroll
    for (int m = 0; m < 2; ++m) {
      unsigned o[4];
#pragma unroll
      for (int i = 0; i < 4; ++i)
        o[i] = pack2(f32x2{rbf1(acc[8 * m + 2 * i]), rbf1(acc[8 * m + 2 * i + 1])} * f32x2{sc, sc});
      swap32(o[0], o[2]);
      swap32(o[1], o[3]);
      __builtin_amdgcn_raw_buffer_store_b128(u32x4{o[0], o[1], o[2], o[3]}, rq, r * 1024 + (16 * m + 8 * h) * 2, sw0, 0);
    }
    return;
  }

  const int tl = wave * 4 + g;
  u32x4 xv[4], wrv[4];
#pragma unroll
  for (int i = 0; i < 4; ++i) xv[i] = *(const u32x4*)(x + (tok0 + tl) * 512 + (n + 16 * i) * 8);
  if (cb == 3) {
#pragma unroll
    for (int i = 0; i < 4; ++i) wrv[i] = *(const u32x4*)(wr + (n + 16 * i) * 8);
  }
  float s[4] = {0.f, 0.f, 0.f, 0.f};
#pragma unroll
  for (int i = 0; i < 16; ++i) s[i & 3] = dot2(xv[i >> 2][i & 3], 0x3f803f80u, s[i & 3]);
  float ssum = (s[0] + s[1]) + (s[2] + s[3]);
  if (cb == 3) asm volatile("" : "+v"(wrv[0]), "+v"(wrv[1]), "+v"(wrv[2]), "+v"(wrv[3]));
  asm volatile("" : "+v"(ssum));
  unsigned pf[4] = {0u, 0u, 0u, 0u};
  if (wave == 6) {
    touch(pf[0], (lane & 1) ? ws + WS_K + tb * 1024 : (const char*)(Q + tok0 * 512 + cb * 128));
  } else if (wave == 5 && (blockIdx.x >> 3) == 16) {
    unsigned long long c0, c1;
    CODE_ADDR(k2_attn, c0);
    CODE_ADDR(k1_ln_qkv, c1);
    const long long len = (long long)(c1 - c0);
#pragma unroll
    for (int i = 0; i < 4; ++i) {
      const int off = (i * 64 + lane) * 128;
      touch(pf[i], (const char*)c0 + (off < len ? off : 0));
    }
  }
  const float mean = sum16(ssum) * (1.f / 512.f);
  const float mb = rbf1(mean), nmb = -mb;
  f32x2 t[16], v2[2] = {{0.f, 0.f}, {0.f, 0.f}};
#pragma unroll
  for (int i = 0; i < 4; ++i) unpack_add(xv[i], nmb, t + 4 * i);
#pragma unroll
  for (int i = 0; i < 16; ++i) v2[i & 1] += t[i] * t[i];
  const float dm = mean - mb;
  const float var = fmaxf(sum16((v2[0].x + v2[0].y) + (v2[1].x + v2[1].y)) * (1.f / 512.f) - dm * dm, 0.f);
#pragma unroll
  for (int i = 0; i < 16; ++i) t[i] = f32x2{rbf1(t[i].x), rbf1(t[i].y)};
  const float den = rbf1(sqrtf(rbf1(rbf1(var) + 1e-5f)));
  const float rinv = __builtin_amdgcn_rcpf(den);
  const f32x2 rinv2 = {rinv, rinv};
  u32x4 o[4];
#pragma unroll
  for (int i = 0; i < 4; ++i) {
#pragma unroll
    for (int j = 0; j < 4; ++j) o[i][j] = pack2(t[4 * i + j] * rinv2);
    *(u32x4*)(ximg + tl * XS_STRIDE + (n + 16 * i) * 16) = o[i];
  }
  if (cb == 3) {
    float rd = 0.f;
#pragma unroll
    for (int i = 0; i < 4; ++i) {
#pragma unroll
      for (int j = 0; j < 4; ++j) {
        rd = dot2(o[i][j], wrv[i][j], rd);
      }
    }
    rd = sum16(rd);
    if (n == 0) rs[tl] = rbf1(rd);
  }
  asm volatile("s_waitcnt lgkmcnt(0)\n s_barrier" ::: "memory");
  if (cb == 2 && n == 0) *(f32x2*)(st + (tok0 + tl) * 2) = f32x2{mb, rinv};
  if (wave == 7) {
    const int half = cb >> 1;
    const char* xb = ximg + (16 * half + n) * XS_STRIDE + 16 * g;
    const unsigned wa = lds0 + K1_KV + (8 * g + q) * 32 + p * 8;
    bf16x8 xf[16], wf[16];
#pragma unroll
    for (int i = 0; i < 16; ++i) {
      xf[i] = *(const bf16x8*)(xb + i * 64);
      wf[i] = tr_read8(wa + i * 1024, wa + i * 1024 + 128);
    }
    f32x4 c0 = {0.f, 0.f, 0.f, 0.f}, c1 = c0;
    unsigned off;
    if (cb & 1) {
#pragma unroll
      for (int i = 0; i < 16; i += 2) {
        c0 = __builtin_amdgcn_mfma_f32_16x16x32_bf16(xf[i], wf[i], c0, 0, 0, 0);
        c1 = __builtin_amdgcn_mfma_f32_16x16x32_bf16(xf[i + 1], wf[i + 1], c1, 0, 0, 0);
      }
      off = 65536 + ((half * 2 + (g >> 1)) * 16 + n) * 16;
    } else {
#pragma unroll
      for (int i = 0; i < 16; i += 2) {
        c0 = __builtin_amdgcn_mfma_f32_16x16x32_bf16(wf[i], xf[i], c0, 0, 0, 0);
        c1 = __builtin_amdgcn_mfma_f32_16x16x32_bf16(wf[i + 1], xf[i + 1], c1, 0, 0, 0);
      }
      off = ((g >> 1) * 32 + 16 * half + n) * 16;
    }
    c0 += c1;
    *(u32x2*)(ws + WS_K + tb * 1024 + off + (g & 1) * 8) = u32x2{pack2(f32x2{c0[0], c0[1]}), pack2(f32x2{c0[2], c0[3]})};
  } else if (wave == 4 && cb == 3) {
    unsigned k1 = route_key(rs[r], tok0 + r), k2 = 0;
    top2_wave(k1, k2, 32);
    if (lane == 0) *(u32x2*)(cand + tb * 2) = u32x2{k1, k2 | 1u};
    const u32x4 a = *(const u32x4*)(ximg + (key_tok(k1) & 31) * XS_STRIDE + lane * 16);
    const u32x4 b = *(const u32x4*)(ximg + (key_tok(k2) & 31) * XS_STRIDE + lane * 16);
    *(u32x4*)(ws + WS_CX + tb * 2048 + lane * 16) = a;
    *(u32x4*)(ws + WS_CX + tb * 2048 + 1024 + lane * 16) = b;
  } else if (blockIdx.x == 0 && tid < NCNT) {
    ((unsigned*)(ws + WS_CNT))[tid * 64] = 0;
  }
  asm volatile("s_waitcnt vmcnt(0)" ::"v"(pf[0]), "v"(pf[1]), "v"(pf[2]), "v"(pf[3]));
}

extern "C" void kernel_launch(void* const* d_in, const int* in_sizes, int n_in, void* d_out, int out_size,
                              void* d_ws, size_t ws_size, hipStream_t stream) {
  const bf16* x = (const bf16*)d_in[0];
  const bf16* wq = (const bf16*)d_in[1];
  const bf16* wk = (const bf16*)d_in[2];
  const bf16* wv = (const bf16*)d_in[3];
  const bf16* wo = (const bf16*)d_in[4];
  const bf16* wr = (const bf16*)d_in[6];
  const bf16* wg = (const bf16*)d_in[7];
  const bf16* wu = (const bf16*)d_in[8];
  const bf16* wd = (const bf16*)d_in[9];
  char* ws = (char*)d_ws;
  bf16* out = (bf16*)d_out;

  (void)hipFuncSetAttribute((const void*)k1_ln_qkv, hipFuncAttributeMaxDynamicSharedMemorySize, K1_LDS);
  (void)hipFuncSetAttribute((const void*)k2_attn, hipFuncAttributeMaxDynamicSharedMemorySize, ATT_LDS);

  k1_ln_qkv<<<256, 768, K1_LDS, stream>>>(x, wq, wk, wv, wr, ws);
  k2_attn<<<256, 768, ATT_LDS, stream>>>(ws, x, wg, wu, wd, wo, out);
}
```

```cpp
#include <hip/hip_runtime.h>
#include <hip/hip_bf16.h>

typedef __attribute__((ext_vector_type(8))) __bf16 bf16x8;
typedef __attribute__((ext_vector_type(16))) __bf16 bf16x16;
typedef __attribute__((ext_vector_type(8))) float f32x8;
typedef __attribute__((ext_vector_type(4))) __bf16 bf16x4;
typedef __attribute__((ext_vector_type(2))) __bf16 bf16x2;
typedef __attribute__((ext_vector_type(4))) short s16x4;
typedef __attribute__((ext_vector_type(8))) short s16x8;
typedef __attribute__((ext_vector_type(16))) float f32x16;
typedef __attribute__((ext_vector_type(4))) float f32x4;
typedef __attribute__((ext_vector_type(4))) unsigned u32x4;
typedef __attribute__((ext_vector_type(2))) unsigned u32x2;
typedef __attribute__((ext_vector_type(2))) float f32x2;
typedef __attribute__((ext_vector_type(16))) unsigned u32x16;
typedef __bf16 bf16;

#define LDS_AS __attribute__((address_space(3)))
#define GLB_AS __attribute__((address_space(1)))
#define CONST_AS __attribute__((address_space(4)))
#define DI __device__ __forceinline__

constexpr int S_ = 2048, D_ = 512, H_ = 32, DH_ = 16, F_ = 1376;

constexpr size_t WS_Q = 0;
constexpr size_t WS_O = WS_Q;
constexpr size_t WS_K = 2u << 20;
constexpr size_t WS_V = WS_K + 65536;
constexpr size_t WS_ST = WS_V + 65536;
constexpr size_t WS_R = WS_ST + 16384;
constexpr size_t WS_CX = WS_R + 4096;
constexpr size_t WS_CNT = WS_CX + 131072;
constexpr int NCNT = 64 + 16 + 32;
constexpr size_t WS_FLG = WS_CNT + 64 * 256;
constexpr size_t WS_CL = WS_FLG + 16 * 256;
constexpr size_t WS_UPD = WS_CNT + NCNT * 256;
constexpr size_t WS_YP = WS_UPD + 4096;
constexpr int NFFN = 172;

DI float bf2f(bf16 v) { return (float)v; }
DI float rbf(float v) { return (float)(bf16)v; }
DI f32x2 unpack2(unsigned u) { return f32x2{__uint_as_float(u << 16), __uint_as_float(u & 0xffff0000u)}; }
DI unsigned pack2(f32x2 v) {
  unsigned r = __builtin_bit_cast(unsigned, __builtin_convertvector(v, bf16x2));
  asm("" : "+v"(r));
  return r;
}

DI unsigned route_key(float v, int tok) {
  const unsigned u = __float_as_uint(v) >> 16;
  const unsigned o = (u & 0x8000u) ? (~u & 0xffffu) : (u | 0x8000u);
  return (o << 16) | (unsigned)((0x7ff - tok) << 1);
}
DI int key_tok(unsigned k) { return 0x7ff - (int)((k >> 1) & 0x7ffu); }
DI int key_row(unsigned k) { return (key_tok(k) >> 5) * 2 + (int)(k & 1u); }
DI float key_val(unsigned k) {
  const unsigned o = k >> 16;
  const unsigned u = (o & 0x8000u) ? (o & 0x7fffu) : (~o & 0xffffu);
  return __uint_as_float(u << 16);
}
DI unsigned ln2(f32x2 x, f32x2 mb2, f32x2 rinv2) { return pack2(unpack2(pack2(x - mb2)) * rinv2); }
DI void top2_wave(unsigned& k1, unsigned& k2, int width) {
  for (int o = width >> 1; o > 0; o >>= 1) {
    const unsigned a1 = __shfl_xor(k1, o), a2 = __shfl_xor(k2, o);
    k2 = max(min(k1, a1), max(k2, a2));
    k1 = max(k1, a1);
  }
}

DI void top2_merge(u32x16 (&cv)[4], unsigned& k1, unsigned& k2) {
  asm volatile("" : "+s"(cv[0]), "+s"(cv[1]), "+s"(cv[2]), "+s"(cv[3]));
#pragma unroll
  for (int i = 0; i < 32; ++i) {
    const unsigned a1 = cv[i >> 3][(i & 7) * 2], a2 = cv[i >> 3][(i & 7) * 2 + 1];
    unsigned t;
    asm volatile("s_min_u32 %2, %0, %3\n s_max_u32 %1, %1, %4\n s_max_u32 %0, %0, %3\n s_max_u32 %1, %1, %2"
                 : "+s"(k1), "+s"(k2), "=&s"(t) : "s"(a1), "s"(a2) : "scc");
  }
}

DI void glds16_raw(const void* g, unsigned lds_off) {
  asm volatile("s_mov_b32 m0, %0\n s_nop 0\n global_load_lds_dwordx4 %1, off" ::"s"(lds_off), "v"(g) : "memory", "m0");
}

DI void blds16(__amdgpu_buffer_rsrc_t rs, unsigned voff, unsigned soff, unsigned lds_off) {
  asm volatile("s_mov_b32 m0, %0\n s_nop 0\n buffer_load_dwordx4 %1, %2, %3 offen lds" ::"s"(lds_off), "v"(voff), "s"(rs), "s"(soff) : "memory", "m0");
}
DI void blds16x2(__amdgpu_buffer_rsrc_t rs, unsigned voff, unsigned soff, unsigned lds_off) {
  asm volatile("s_mov_b32 m0, %0\n s_nop 0\n buffer_load_dwordx4 %1, %2, %3 offen lds\n buffer_load_dwordx4 %1, %2, %3 offen offset:1024 lds"
               ::"s"(lds_off), "v"(voff), "s"(rs), "s"(soff) : "memory", "m0");
}
DI void count_up(__amdgpu_buffer_rsrc_t rs, unsigned one, unsigned soff) {
  asm volatile("s_mov_b64 exec, 1\n buffer_atomic_add %0, off, %1, %2\n s_mov_b64 exec, -1" ::"v"(one), "s"(rs), "s"(soff) : "memory");
}
#define LDSP(T, off) ((LDS_AS T*)(unsigned long)(off))

template <int CTRL>
DI float dpp_add(float v) {
  const int t = __builtin_amdgcn_update_dpp(0, __float_as_int(v), CTRL, 0xf, 0xf, true);
  return v + __int_as_float(t);
}
DI float sum8(float v) {
  v = dpp_add<0xB1>(v);
  v = dpp_add<0x4E>(v);
  v = dpp_add<0x141>(v);
  return v;
}

DI f32x16 pv_mfma(bf16x8 v, f32x8 p, f32x16 acc, int sel) {
  return __builtin_amdgcn_smfmac_f32_32x32x32_bf16(v, __builtin_bit_cast(bf16x16, p), acc, sel, 0, 0);
}

DI void swap32(unsigned& a, unsigned& b) {
  const u32x2 t = __builtin_amdgcn_permlane32_swap(a, b, false, false);
  a = t[0];
  b = t[1];
}
DI float max32(float v) {
  unsigned a = __float_as_uint(v), b = a;
  swap32(a, b);
  return fmaxf(__uint_as_float(a), __uint_as_float(b));
}

DI s16x4 tr_read(const char* p) {
  return __builtin_amdgcn_ds_read_tr16_b64_v4i16((LDS_AS s16x4*)p);
}
DI bf16x8 tr_read8(unsigned lo, unsigned hi) {
  const s16x4 a = __builtin_amdgcn_ds_read_tr16_b64_v4i16(LDSP(s16x4, lo)), b = __builtin_amdgcn_ds_read_tr16_b64_v4i16(LDSP(s16x4, hi));
  return __builtin_bit_cast(bf16x8, __builtin_shufflevector(a, b, 0, 1, 2, 3, 4, 5, 6, 7));
}

DI int wimg_off(int row, int ch) { return row * 128 + ((ch ^ (((row >> 1) & 1) << 2)) << 4); }

constexpr int XS_STRIDE = 1040;

constexpr int GP = 4;
DI f32x16 gemm_tile(const char* wimg, const char* ximg, int n0, int t0, int lane) {
  const int r = lane & 31, h = lane >> 5;
  const int q = (lane & 15) >> 2, p = lane & 3, blk = (lane >> 4) & 1;
  f32x16 acc;
#pragma unroll
  for (int i = 0; i < 16; ++i) acc[i] = 0.f;
  const char* xb = ximg + (t0 + r) * XS_STRIDE + 16 * h;
  const int col = n0 + 16 * blk + 4 * p;
  const int ch = col >> 3, sub = (col & 7) * 2;
  const char* wb0 = wimg + wimg_off(8 * h + q, ch) + sub;
  const char* wb1 = wimg + wimg_off(8 * h + q + 4, ch) + sub;
  s16x4 lo[32], hi[32];
  bf16x8 b[32];
#pragma unroll
  for (int ks = 0; ks < 32 + GP; ++ks) {
    if (ks < 32) {
      lo[ks] = tr_read(wb0 + ks * 2048);
      hi[ks] = tr_read(wb1 + ks * 2048);
      b[ks] = *(const bf16x8*)(xb + ks * 32);
    }
    if (ks >= GP) {
      const s16x8 a = __builtin_shufflevector(lo[ks - GP], hi[ks - GP], 0, 1, 2, 3, 4, 5, 6, 7);
      acc = __builtin_amdgcn_mfma_f32_32x32x16_bf16(__builtin_bit_cast(bf16x8, a), b[ks - GP], acc, 0, 0, 0);
      asm volatile("" : "+v"(acc));
    }
    __builtin_amdgcn_sched_barrier(0);
  }
  return acc;
}

DI float dot2(unsigned a, unsigned b, float c) {
  return __builtin_amdgcn_fdot2_f32_bf16(__builtin_bit_cast(bf16x2, a), __builtin_bit_cast(bf16x2, b), c, false);
}
DI void touch(unsigned& t, const void* p) {
  asm volatile("global_load_dword %0, %1, off" : "=v"(t) : "v"(p) : "memory");
}
DI float rbf1(float v) { return __uint_as_float(pack2(f32x2{0.f, v})); }
DI void unpack_add(u32x4 x, float c, f32x2* t) {
  float d[8];
  asm("v_dot2_f32_bf16 %0, %8, %12, %14\n v_dot2_f32_bf16 %1, %8, %13, %14\n"
      "v_dot2_f32_bf16 %2, %9, %12, %14\n v_dot2_f32_bf16 %3, %9, %13, %14\n"
      "v_dot2_f32_bf16 %4, %10, %12, %14\n v_dot2_f32_bf16 %5, %10, %13, %14\n"
      "v_dot2_f32_bf16 %6, %11, %12, %14\n v_dot2_f32_bf16 %7, %11, %13, %14\n s_nop 2"
      : "=&v"(d[0]), "=&v"(d[1]), "=&v"(d[2]), "=&v"(d[3]), "=&v"(d[4]), "=&v"(d[5]), "=&v"(d[6]), "=&v"(d[7])
      : "v"(x[0]), "v"(x[1]), "v"(x[2]), "v"(x[3]), "s"(0x00003f80u), "s"(0x3f800000u), "v"(c));
#pragma unroll
  for (int i = 0; i < 4; ++i) t[i] = f32x2{d[2 * i], d[2 * i + 1]};
}
DI float sum16(float v) { return dpp_add<0x140>(sum8(v)); }
template <int N>
DI void wait_vm() { asm volatile("s_waitcnt vmcnt(%0)" ::"n"(N) : "memory"); }

#define QSTEP(i)                                                                  \
  acc = __builtin_amdgcn_mfma_f32_32x32x16_bf16(a[i], b[i], acc, 0, 0, 0); \
  asm volatile("" : "+v"(acc));                                            \
  __builtin_amdgcn_sched_barrier(0)

#define CODE_ADDR(sym, out)                                                                               \
  asm volatile("s_getpc_b64 s[20:21]\n s_add_u32 s20, s20, " #sym "@rel32@lo+4\n s_addc_u32 s21, s21, " #sym \
               "@rel32@hi+12\n s_mov_b64 %0, s[20:21]"                                                    \
               : "=s"(out)::"s20", "s21", "scc")

constexpr int NT = 16;
constexpr int CST = 4096;
constexpr int L_RED = 131072 + CST;
constexpr int L_CNT = L_RED + 512;
constexpr int L_H = L_CNT + 64;
constexpr int L_STG = L_RED + 1024;
constexpr int ATT_LDS = L_STG + 4 * 6144;
constexpr int NC = 64 / NT;
constexpr int PSW = 53;
constexpr int SPIN_MAX = 1 << 22;

extern "C" __global__ __launch_bounds__(768) void k2_attn(char* __restrict__ ws, const bf16* __restrict__ x,
                                               const bf16* __restrict__ wg, const bf16* __restrict__ wu,
                                               const bf16* __restrict__ wd, const bf16* __restrict__ wo,
                                               bf16* __restrict__ out) {
  const bf16* Q = (const bf16*)(ws + WS_Q);
  const bf16* Kb = (const bf16*)(ws + WS_K);
  bf16* O = (bf16*)(ws + WS_O);
  extern __shared__ __attribute__((aligned(16))) char smem[];
  char* kl = smem;
  char* vl = smem + 65536;
  char* cst = smem + 131072;
  const int tid = threadIdx.x, lane = tid & 63;
  const int wave = __builtin_amdgcn_readfirstlane(tid >> 6);

  const unsigned lds0 = (unsigned)(size_t)(LDS_AS char*)smem;
  unsigned long long pc = (unsigned long long)ws + WS_R, pg = (unsigned long long)wg, pu = (unsigned long long)wu, pd = (unsigned long long)wd;
  asm volatile("" : "+s"(pc), "+s"(pg), "+s"(pu), "+s"(pd));
  if (wave >= 8) {
    const int sw = wave - 8, r = lane & 31, h = lane >> 5;
    u32x16 cv[4];
#pragma unroll
    for (int i = 0; i < 4; ++i) cv[i] = ((const CONST_AS u32x16*)pc)[i];
    unsigned l16 = lane * 16;
    const int fs = (blockIdx.x & 7) * 22 + (blockIdx.x >> 3);
    const bool ffn = blockIdx.x < 176 && fs < NFFN;
    const unsigned stg = lds0 + L_STG + sw * 6144;
    const int n = lane & 15, g = lane >> 4, q = n >> 2, p = lane & 3;
    unsigned a_gu = stg + (p >> 1) * 2048 + (8 * g + q) * 16 + (p & 1) * 8;
    unsigned a_dn = stg + 4096 + q * 256 + (16 * g + 4 * p) * 2;
    unsigned a_rw = lds0 + L_RED + (n & 7) * 32 + sw * 8 + (n >> 3) * 4;
    unsigned a_rr = lds0 + L_RED + n * 32;
    unsigned a_hw = lds0 + L_H + sw * 32 + n * 2;
    const unsigned hrow = lds0 + L_H + sw * 32 + (n >> 3) * 16, zrow = lds0 + 131072 + 16;
    unsigned a_h0 = (p == g && (q & 1) == 0) ? hrow : zrow, a_h1 = (p == g && (q & 1) == 1) ? hrow : zrow;
    unsigned a_cnt = lds0 + L_CNT, one = 1, odd = lane & 1, even = (lane & 1) ^ 1, g16 = g * 16;
    unsigned v_w = (lane >> 3) * 1024 + (((lane & 7) ^ ((lane >> 4 & 1) << 2)) << 4);
    unsigned v_x = r * 1024 + h * 8, v_s = r * 8;
    unsigned v_y = n * 4096 + g * 16, v_c = n * 256, v_e = n < 12 ? 11 : 10;
    asm volatile("" : "+v"(l16), "+v"(a_gu), "+v"(a_dn), "+v"(a_rw), "+v"(a_rr), "+v"(a_hw), "+v"(a_h0), "+v"(a_h1),
                 "+v"(a_cnt), "+v"(one), "+v"(odd), "+v"(even), "+v"(g16));
    unsigned vg = lane * (F_ * 2), vd = g * 1024 + n * 16;
    asm volatile("" : "+v"(v_w), "+v"(v_x), "+v"(v_s), "+v"(v_y), "+v"(v_c), "+v"(v_e), "+v"(vg), "+v"(vd));
    {
      const __amdgpu_buffer_rsrc_t rk = __builtin_amdgcn_make_buffer_rsrc((void*)Kb, 0, 131072, 0x00020000);
#pragma unroll
      for (int i = 0; i < 13; ++i) {
        const unsigned o = sw * 2048 + (i < 6 ? 16 + 8 * i : 24 + 8 * i) * 1024;
        blds16x2(rk, l16, o, lds0 + o);
      }
    }
    asm volatile("s_barrier" ::: "memory");
    unsigned k1 = 0, k2 = 0;
    top2_merge(cv, k1, k2);
#pragma unroll
    for (int i = 0; i < 4; ++i) cv[i] = ((const CONST_AS u32x16*)pc)[4 + i];
    const __amdgpu_buffer_rsrc_t rc = __builtin_amdgcn_make_buffer_rsrc(ws + WS_CNT, 0, NCNT * 256, 0x00020000);
    const __amdgpu_buffer_rsrc_t ry = __builtin_amdgcn_make_buffer_rsrc(ws + WS_YP, 0, NFFN * 4096, 0x00020000);
    const __amdgpu_buffer_rsrc_t rup = __builtin_amdgcn_make_buffer_rsrc(ws + WS_UPD, 0, 4096, 0x00020000);
    asm volatile("s_waitcnt vmcnt(0)\n s_barrier" ::: "memory");
    if (ffn) {
      const __amdgpu_buffer_rsrc_t rg = __builtin_amdgcn_make_buffer_rsrc((void*)pg, 0, 512 * F_ * 2, 0x00020000);
      const __amdgpu_buffer_rsrc_t ru = __builtin_amdgcn_make_buffer_rsrc((void*)pu, 0, 512 * F_ * 2, 0x00020000);
      const __amdgpu_buffer_rsrc_t rd = __builtin_amdgcn_make_buffer_rsrc((void*)pd, 0, 512 * F_ * 2, 0x00020000);
      const unsigned so = (sw * 128 * F_ + fs * 8) * 2, sd = (fs * 8 * 512 + sw * 128) * 2;
      blds16(rg, vg, so, stg);
      blds16(rg, vg, so + 64 * F_ * 2, stg + 1024);
      blds16(ru, vg, so, stg + 2048);
      blds16(ru, vg, so + 64 * F_ * 2, stg + 3072);
      blds16(rd, vd, sd, stg + 4096);
      blds16(rd, vd, sd + 4096, stg + 5120);
    }
    top2_merge(cv, k1, k2);
    if (ffn) {
      const __amdgpu_buffer_rsrc_t rx = __builtin_amdgcn_make_buffer_rsrc(ws + WS_CX, 0, 131072, 0x00020000);
      u32x4 xf[4];
      __builtin_amdgcn_s_setprio(3);
      unsigned vx = __umul24(even, key_row(k1) * 1024) + g16;
      vx += __umul24(odd, key_row(k2) * 1024);
      asm volatile("" : "+v"(vx));
      __builtin_amdgcn_s_setprio(0);
#pragma unroll
      for (int i = 0; i < 4; ++i) xf[i] = __builtin_amdgcn_raw_buffer_load_b128(rx, vx + i * 64, sw * 256, 0);
      asm volatile("s_waitcnt vmcnt(0)" ::: "memory");
      bf16x8 wf[4], df[2];
#pragma unroll
      for (int i = 0; i < 4; ++i) wf[i] = tr_read8(a_gu + i * 512, a_gu + i * 512 + 64);
#pragma unroll
      for (int i = 0; i < 2; ++i) df[i] = tr_read8(a_dn + i * 128, a_dn + i * 128 + 1024);
      asm volatile("s_waitcnt lgkmcnt(0)" ::: "memory");
      __builtin_amdgcn_s_setprio(3);
      f32x4 gu = {0.f, 0.f, 0.f, 0.f};
#pragma unroll
      for (int i = 0; i < 4; ++i) gu = __builtin_amdgcn_mfma_f32_16x16x32_bf16(__builtin_bit_cast(bf16x8, xf[i]), wf[i], gu, 0, 0, 0);
      *LDSP(float, a_rw) = gu[0];
      *LDSP(float, a_rw + 256) = gu[1];
      asm volatile("s_mov_b64 exec, 1\n ds_add_u32 %0, %1\n s_mov_b64 exec, -1" ::"v"(a_cnt), "v"(one) : "memory");
      while (*(volatile LDS_AS unsigned*)LDSP(unsigned, a_cnt) < 4) __builtin_amdgcn_s_sleep(1);
      asm volatile("" ::: "memory");
      const f32x4 r0 = *LDSP(f32x4, a_rr), r1 = *LDSP(f32x4, a_rr + 16);
      const f32x2 gs = unpack2(pack2((f32x2{r0[0], r0[1]} + f32x2{r0[2], r0[3]}) + (f32x2{r1[0], r1[1]} + f32x2{r1[2], r1[3]})));
      const float hv = rbf(gs.x * __builtin_amdgcn_rcpf(1.f + __expf(-gs.x))) * gs.y;
      *LDSP(bf16, a_hw) = (bf16)hv;
      asm volatile("" ::: "memory");
      const bf16x8 h0 = *LDSP(bf16x8, a_h0), h1 = *LDSP(bf16x8, a_h1);
      f32x4 y = {0.f, 0.f, 0.f, 0.f};
      y = __builtin_amdgcn_mfma_f32_16x16x32_bf16(h0, df[0], y, 0, 0, 0);
      y = __builtin_amdgcn_mfma_f32_16x16x32_bf16(h1, df[1], y, 0, 0, 0);
      __builtin_amdgcn_raw_buffer_store_b128(__builtin_bit_cast(u32x4, y), ry, l16, fs * 4096 + sw * 1024, 16);
      __builtin_amdgcn_s_setprio(0);
      asm volatile("s_waitcnt vmcnt(0)" ::: "memory");
      count_up(rc, one, (sw * 16 + (fs & 15)) * 256);
    }
    const int tb = (blockIdx.x >> 6) * 8 + (blockIdx.x & 7), cb = (blockIdx.x >> 3) & 7, tok0 = tb * 64;
    char* ximg = smem;
    char* wimg = smem + 64 * XS_STRIDE;
    const int n0 = (sw & 1) * 32, t0 = (sw >> 1) * 32;
    const unsigned so_x = (tok0 + t0) * 1024 + (cb * 64 + n0) * 2;
    const __amdgpu_buffer_rsrc_t rout = __builtin_amdgcn_make_buffer_rsrc(out, 0, S_ * D_ * 2, 0x00020000);
    u32x4 wreg[16];
    u32x2 xv[4], nv[4], sxr;
    {
      const __amdgpu_buffer_rsrc_t rw = __builtin_amdgcn_make_buffer_rsrc((void*)wo, 0, 512 * 1024, 0x00020000);
      const __amdgpu_buffer_rsrc_t rx = __builtin_amdgcn_make_buffer_rsrc((void*)x, 0, S_ * D_ * 2, 0x00020000);
      const __amdgpu_buffer_rsrc_t rs = __builtin_amdgcn_make_buffer_rsrc(ws + WS_ST, 0, S_ * 8, 0x00020000);
#pragma unroll
      for (int i = 0; i < 16; ++i) wreg[i] = __builtin_amdgcn_raw_buffer_load_b128(rw, v_w, (sw + 4 * i) * 8192 + cb * 128, 0);
#pragma unroll
      for (int i = 0; i < 4; ++i) xv[i] = __builtin_amdgcn_raw_buffer_load_b64(rx, v_x + i * 16, so_x, 0);
      sxr = __builtin_amdgcn_raw_buffer_load_b64(rs, v_s, (tok0 + t0) * 8, 0);
      if (wave == 11) {
        const unsigned t = __builtin_amdgcn_raw_buffer_load_b32(rout, 0, so_x, 0);
        asm volatile("" ::"v"(t));
      }
    }
    if ((blockIdx.x >> 4) == 11) {
      const int sel = (blockIdx.x >> 3) & 1, cq = blockIdx.x & 7;
      __builtin_amdgcn_s_setprio(3);
      int spin = 0;
      while (__any(__builtin_amdgcn_raw_buffer_load_b32(rc, v_c, (cq >> 1) * 4096, 16) < v_e) && ++spin < SPIN_MAX)
        asm volatile("s_sleep 8" ::: "memory");
      __builtin_amdgcn_s_setprio(0);
      asm volatile("" ::: "memory");
      const unsigned so = (cq >> 1) * 1024 + (2 * sel + (cq & 1)) * 256 + sw * 64;
      u32x4 t[11];
#pragma unroll
      for (int i = 0; i < 10; ++i) t[i] = __builtin_amdgcn_raw_buffer_load_b128(ry, v_y, so + i * 65536, 16);
      __builtin_amdgcn_s_setprio(3);
      t[10] = u32x4{0u, 0u, 0u, 0u};
      if (v_e == 11) t[10] = __builtin_amdgcn_raw_buffer_load_b128(ry, v_y, so + 10 * 65536, 16);
      const float gate = rbf(__builtin_amdgcn_rcpf(1.f + __expf(-key_val(sel ? k2 : k1))));
      f32x4 a = __builtin_bit_cast(f32x4, t[0]);
#pragma unroll
      for (int i = 1; i < 11; ++i) a += __builtin_bit_cast(f32x4, t[i]);
#pragma unroll
      for (int i = 0; i < 4; ++i) a[i] = dpp_add<0x140>(sum8(a[i]));
      asm volatile("" : "+v"(a));
      const float u = rbf(rbf(p == 0 ? a[0] : p == 1 ? a[1] : p == 2 ? a[2] : a[3]) * gate);
      if (n < 4) __builtin_amdgcn_raw_buffer_store_b32(__float_as_uint(u), rup, n * 64 + g * 4, (sel * 512 + cq * 64 + sw * 4) * 4, 16);
      __builtin_amdgcn_s_setprio(0);
      asm volatile("s_waitcnt vmcnt(0)" ::: "memory");
      count_up(rc, one, (64 + sel * 8 + cq) * 256);
    }
    const bool routed = ((key_tok(k1) ^ (tok0 + t0)) >> 5) == 0 || ((key_tok(k2) ^ (tok0 + t0)) >> 5) == 0;
    u32x4 uf[4];
    if (routed) {
      __builtin_amdgcn_s_setprio(3);
      const int tok = tok0 + t0 + r;
      const int rsel = tok == key_tok(k1) ? 0 : tok == key_tok(k2) ? 1 : -1;
#pragma unroll
      for (int i = 0; i < 4; ++i) uf[i] = u32x4{0x80000000u, 0x80000000u, 0x80000000u, 0x80000000u};
      if (rsel >= 0) {
        int spin = 0;
        while (__builtin_amdgcn_raw_buffer_load_b32(rc, rsel * 2048, (64 + cb) * 256, 16) < 4 && ++spin < SPIN_MAX)
          asm volatile("s_sleep 4" ::: "memory");
        asm volatile("" ::: "memory");
#pragma unroll
        for (int i = 0; i < 4; ++i)
          uf[i] = __builtin_amdgcn_raw_buffer_load_b128(rup, (rsel * 512 + 8 * i + 4 * h) * 4, (cb * 64 + n0) * 4, 16);
      }
#pragma unroll
      for (int i = 0; i < 4; ++i) asm volatile("" : "+v"(uf[i]));
      __builtin_amdgcn_s_setprio(0);
    }
#pragma unroll
    for (int i = 0; i < 16; ++i) asm volatile("" : "+v"(wreg[i]));
    asm volatile("s_barrier" ::: "memory");
    if (wave == 8) count_up(rc, one, (80 + tb) * 256);
#pragma unroll
    for (int i = 0; i < 16; ++i) *(u32x4*)(wimg + (sw + 4 * i) * 1024 + lane * 16) = wreg[i];
    {
      const f32x2 mb2 = {__uint_as_float(sxr[0]), __uint_as_float(sxr[0])}, ri2 = {__uint_as_float(sxr[1]), __uint_as_float(sxr[1])};
#pragma unroll
      for (int i = 0; i < 4; ++i) {
#pragma unroll
        for (int jj = 0; jj < 2; ++jj) nv[i][jj] = ln2(unpack2(xv[i][jj]), mb2, ri2);
      }
    }
    if (routed) {
#pragma unroll
      for (int i = 0; i < 4; ++i) {
        const f32x4 u = __builtin_bit_cast(f32x4, uf[i]);
        nv[i][0] = pack2(unpack2(nv[i][0]) + f32x2{u[0], u[1]});
        nv[i][1] = pack2(unpack2(nv[i][1]) + f32x2{u[2], u[3]});
      }
    }
    if (wave == 8) {
      int spin = 0;
      while (__hip_atomic_load((GLB_AS unsigned*)(ws + WS_CL + tb * 256), __ATOMIC_RELAXED, __HIP_MEMORY_SCOPE_AGENT) < 8 && ++spin < SPIN_MAX)
        __builtin_amdgcn_s_sleep(1);
    }
    asm volatile("s_barrier" ::: "memory");
    {
      const __amdgpu_buffer_rsrc_t ro = __builtin_amdgcn_make_buffer_rsrc(O + tok0 * 512, 0, 65536, 0x00020000);
      u32x4 t[16];
#pragma unroll
      for (int i = 0; i < 16; ++i) t[i] = __builtin_amdgcn_raw_buffer_load_b128(ro, l16, (sw * 16 + i) * 1024, 16);
#pragma unroll
      for (int i = 0; i < 16; ++i) *(u32x4*)(ximg + (sw * 16 + i) * XS_STRIDE + lane * 16) = t[i];
    }
    asm volatile("s_waitcnt lgkmcnt(0)\n s_barrier" ::: "memory");
    f32x16 acc = gemm_tile(wimg, ximg, n0, t0, lane);
#pragma unroll
    for (int g = 0; g < 4; ++g) {
      u32x2 o;
#pragma unroll
      for (int jj = 0; jj < 2; ++jj) {
        const unsigned xr = xv[g][jj], nr = nv[g][jj];
        const f32x2 a = unpack2(pack2(f32x2{acc[4 * g + 2 * jj], acc[4 * g + 2 * jj + 1]}));
        o[jj] = pack2(unpack2(pack2(unpack2(xr) + a)) + unpack2(nr));
      }
      __builtin_amdgcn_raw_buffer_store_b64(o, rout, v_x + g * 16, so_x, 0);
    }
    return;
  }

  const int r = lane & 31, h = lane >> 5;
  const int token = ((blockIdx.x >> 6) * 8 + (blockIdx.x & 7)) * 64 + ((blockIdx.x >> 3) & 7) * 8 + wave;
  bf16x8 qf;
  asm volatile("global_load_dwordx4 %0, %1, off" : "=v"(qf) : "v"(Q + token * 512 + r * 16 + 8 * h) : "memory");
#pragma unroll
  for (int i = 0; i < 3; ++i) {
    const int piece = (i < 2) ? (wave + i * 8) : (64 + wave);
    glds16_raw((const char*)Kb + piece * 1024 + lane * 16, lds0 + piece * 1024);
  }
  {
    const unsigned cv = ((tid & 30) == 0 || (tid & 30) == 8) ? 0x3f803f80u : 0u;
    *(u32x2*)(cst + tid * 8) = u32x2{cv, cv};
    if (tid == 1) *(unsigned*)(smem + L_CNT) = 0;
  }
  int vo = (r < 16) ? 65536 + (h * 16 + r) * 16 : 131072 + ((r & 11) == 0 ? 0 : 16);
  int vstep = (r < 16) ? CST : 0;
  int sel = 0xdddd;
  f32x16 zero;
#pragma unroll
  for (int i = 0; i < 16; ++i) zero[i] = 0.f;
  f32x16 acc = zero;
  asm volatile("" : "+v"(sel), "+v"(acc), "+v"(vstep), "+v"(vo));
  const char* kp0 = kl + lane * 16;
  const char* vp0 = smem + vo;
  asm volatile("s_waitcnt vmcnt(0) lgkmcnt(0)\n s_barrier" ::: "memory");

  constexpr float HEAD = 90.f;
  f32x16 negm, xs;
  bf16x8 kfn;
  {
    bf16x8 kf[4];
    f32x16 s0[4];
#pragma unroll
    for (int t = 0; t < 4; ++t) kf[t] = *(const bf16x8*)(kp0 + t * 1024);
#pragma unroll
    for (int t = 0; t < 4; ++t) {
      s0[t] = __builtin_amdgcn_mfma_f32_32x32x16_bf16(kf[t], qf, zero, 0, 0, 0);
      asm volatile("" : "+v"(s0[t]));
    }
    __builtin_amdgcn_sched_barrier(0);
    float mx = -INFINITY;
#pragma unroll
    for (int t = 0; t < 4; ++t) {
#pragma unroll
      for (int i = 0; i < 16; i += 2) mx = fmaxf(fmaxf(mx, s0[t][i]), s0[t][i + 1]);
    }
    const float nm = -(floorf(max32(mx)) + HEAD);
#pragma unroll
    for (int i = 0; i < 16; ++i) {
      xs[i] = s0[0][i] + nm;
      negm[i] = nm;
    }
    kfn = kf[1];
  }
  asm volatile("" : "+v"(negm));
  {
    const char* kp = kp0;
    const char* vp = vp0;
    f32x16 x = xs;
    if (wave < 4) __builtin_amdgcn_s_setprio(2);
    else __builtin_amdgcn_s_setprio(1);
    for (int c = 0; c < NC; ++c) {
      bool grown;
#pragma unroll
      for (int t = 0; t < NT; ++t) {
        const bf16x8 kf = kfn;
        kfn = *(const bf16x8*)(kp + (t + 2) * 1024);
        const bf16x8 vf0 = *(const bf16x8*)(vp + ((t & 3) * 2) * 512);
        const bf16x8 vf1 = *(const bf16x8*)(vp + ((t & 3) * 2 + 1) * 512);
        if ((t & 3) == 3) vp += vstep;
        f32x8 p0, p1;
#pragma unroll
        for (int j = 0; j < 4; ++j) p0[j] = __builtin_amdgcn_exp2f(x[j]);
        __builtin_amdgcn_sched_barrier(0);
        f32x16 xn = __builtin_amdgcn_mfma_f32_32x32x16_bf16(kf, qf, negm, 0, 0, 0);
        asm volatile("" : "+v"(xn));
        __builtin_amdgcn_sched_barrier(0);
#pragma unroll
        for (int j = 4; j < 8; ++j) p0[j] = __builtin_amdgcn_exp2f(x[j]);
        if (t == NT - 1) grown = __any(!(acc[8] <= 4.3e9f));
        acc = pv_mfma(vf0, p0, acc, sel);
#pragma unroll
        for (int j = 0; j < 8; ++j) p1[j] = __builtin_amdgcn_exp2f(x[8 + j]);
        acc = pv_mfma(vf1, p1, acc, sel);
        if (t == 7 && c == 0) asm volatile("s_barrier" ::: "memory");
        if (c * NT + t == PSW && wave < 4) __builtin_amdgcn_s_setprio(0);
        __builtin_amdgcn_sched_barrier(0);
        x = xn;
      }
      kp += NT * 1024;
      if (grown) {
        const int e = max(__builtin_amdgcn_frexp_expf(acc[8]) - 1 + (int)HEAD, 0);
#pragma unroll
        for (int i = 0; i < 9; ++i) acc[i] = __builtin_amdgcn_ldexpf(acc[i], -e);
        const float ef = (float)e;
#pragma unroll
        for (int i = 0; i < 16; ++i) {
          negm[i] -= ef;
          x[i] -= ef;
        }
        asm volatile("" : "+v"(negm));
      }
    }
  }
  {
    float chk = 0.f;
#pragma unroll
    for (int i = 0; i < 9; ++i) chk = __builtin_fmaf(acc[i], 0.f, chk);
    if (__any(chk != 0.f)) {
      f32x16 oacc = zero;
      float mrun = -INFINITY;
      const char* kp = kp0;
      const char* vp = vp0;
      for (int t = 0; t < 64; ++t) {
        bf16x8 kf = *(const bf16x8*)(kp);
        f32x16 x = __builtin_amdgcn_mfma_f32_32x32x16_bf16(kf, qf, zero, 0, 0, 0);
        float mx = x[0];
#pragma unroll
        for (int i = 1; i < 16; ++i) mx = fmaxf(mx, x[i]);
        const float mn = fmaxf(mrun, max32(mx));
        const float al = __builtin_amdgcn_exp2f(mrun - mn);
        mrun = mn;
#pragma unroll
        for (int i = 0; i < 16; ++i) oacc[i] *= al;
#pragma unroll
        for (int s = 0; s < 2; ++s) {
          f32x8 pf;
#pragma unroll
          for (int j = 0; j < 8; ++j) pf[j] = __builtin_amdgcn_exp2f(x[8 * s + j] - mn);
          bf16x8 vf = *(const bf16x8*)(vp + ((t & 3) * 2 + s) * 512);
          oacc = pv_mfma(vf, pf, oacc, sel);
        }
        kp += 1024;
        if ((t & 3) == 3) vp += vstep;
      }
      acc = oacc;
    }
  }
  const float inv = 1.f / acc[8];
  {
    unsigned a0 = pack2(f32x2{acc[0] * inv, acc[1] * inv}), a1 = pack2(f32x2{acc[2] * inv, acc[3] * inv});
    unsigned b0 = pack2(f32x2{acc[4] * inv, acc[5] * inv}), b1 = pack2(f32x2{acc[6] * inv, acc[7] * inv});
    swap32(a0, b0);
    swap32(a1, b1);
    const u32x4 o = {a0, a1, b0, b1};
    const __amdgpu_buffer_rsrc_t ro = __builtin_amdgcn_make_buffer_rsrc(O + token * 512, 0, 1024, 0x00020000);
    __builtin_amdgcn_raw_buffer_store_b128(o, ro, r * 32 + h * 16, 0, 16);
  }
  asm volatile("s_waitcnt vmcnt(0)\n s_barrier" ::: "memory");
}

constexpr int K1_KV = 32 * XS_STRIDE;
constexpr int K1_RS = K1_KV + 16384;
constexpr int K1_W = K1_RS + 256;
constexpr int NSLOT = 27;
constexpr int NPRE = 16;
constexpr int LA = 4;
constexpr int K1_LDS = K1_W + 4 * NSLOT * 1024;

extern "C" __global__ __launch_bounds__(768) void k1_ln_qkv(const bf16* __restrict__ x, const bf16* __restrict__ wq,
                                                 const bf16* __restrict__ wk, const bf16* __restrict__ wv,
                                                 const bf16* __restrict__ wr, char* __restrict__ ws) {
  float* st = (float*)(ws + WS_ST);
  bf16* Q = (bf16*)(ws + WS_Q);
  unsigned* cand = (unsigned*)(ws + WS_R);
  extern __shared__ __attribute__((aligned(16))) char smem[];
  char* ximg = smem;
  float* rs = (float*)(smem + K1_RS);
  const unsigned lds0 = (unsigned)(size_t)(LDS_AS char*)smem;
  const int cb = blockIdx.x >> 6, tb = ((blockIdx.x >> 4 & 3) * 8 + (blockIdx.x & 7)) * 2 + (blockIdx.x >> 3 & 1);
  const int tid = threadIdx.x, lane = tid & 63;
  const int wave = __builtin_amdgcn_readfirstlane(tid >> 6);
  const int tok0 = tb * 32;
  const int r = lane & 31, h = lane >> 5;
  const int n = lane & 15, g = lane >> 4, q = n >> 2, p = lane & 3;

  if (wave >= 8) {
    const int sw = wave - 8;
    const unsigned ring = lds0 + K1_W + sw * NSLOT * 1024;
    const __amdgpu_buffer_rsrc_t rw = __builtin_amdgcn_make_buffer_rsrc((void*)wq, 0, 512 * 1024, 0x00020000);
    const __amdgpu_buffer_rsrc_t rkv = __builtin_amdgcn_make_buffer_rsrc((void*)((cb & 1) ? wv : wk), 0, 16384, 0x00020000);
    const unsigned vw = (lane >> 2) * 1024 + (lane & 3) * 16, sw0 = (cb * 128 + sw * 32) * 2;
#pragma unroll
    for (int ks = 0; ks < NSLOT; ++ks) {
      blds16(rw, vw, sw0 + ks * 16384, ring + ks * 1024);
      if (ks == 32 - NSLOT - 1) {
        blds16x2(rkv, lane * 16, sw * 4096, lds0 + K1_KV + sw * 4096);
        blds16x2(rkv, lane * 16, sw * 4096 + 2048, lds0 + K1_KV + sw * 4096 + 2048);
      }
    }
    const unsigned wa = ring + (8 * h + q) * 64 + (g & 1) * 32 + p * 8;
    bf16x8 a[32], b[32];
    wait_vm<NSLOT + 4 - (32 - NSLOT)>();
#pragma unroll
    for (int ks = 0; ks < 32 - NSLOT; ++ks) a[ks] = tr_read8(wa + ks * 1024, wa + ks * 1024 + 256);
#pragma unroll
    for (int ks = 0; ks < 32 - NSLOT; ++ks) asm volatile("" : "+v"(a[ks]));
#pragma unroll
    for (int ks = NSLOT; ks < 32; ++ks) blds16(rw, vw, sw0 + ks * 16384, ring + (ks - NSLOT) * 1024);
    wait_vm<23>();
#pragma unroll
    for (int ks = 5; ks < 9; ++ks) a[ks] = tr_read8(wa + ks * 1024, wa + ks * 1024 + 256);
    wait_vm<19>();
#pragma unroll
    for (int ks = 9; ks < 13; ++ks) a[ks] = tr_read8(wa + ks * 1024, wa + ks * 1024 + 256);
    wait_vm<16>();
#pragma unroll
    for (int ks = 13; ks < 16; ++ks) a[ks] = tr_read8(wa + ks * 1024, wa + ks * 1024 + 256);
#pragma unroll
    for (int ks = 32 - NSLOT; ks < NPRE; ++ks) asm volatile("" : "+v"(a[ks]));
    asm volatile("s_barrier" ::: "memory");
    f32x16 acc;
#pragma unroll
    for (int i = 0; i < 16; ++i) acc[i] = 0.f;
    const char* xb = ximg + r * XS_STRIDE + 16 * h;
    b[0] = *(const bf16x8*)(xb + 0);
    __builtin_amdgcn_sched_barrier(0);
    b[1] = *(const bf16x8*)(xb + 32);
    __builtin_amdgcn_sched_barrier(0);
    b[2] = *(const bf16x8*)(xb + 64);
    __builtin_amdgcn_sched_barrier(0);
    b[3] = *(const bf16x8*)(xb + 96);
    __builtin_amdgcn_sched_barrier(0);
    b[4] = *(const bf16x8*)(xb + 128);
    QSTEP(0);
    b[5] = *(const bf16x8*)(xb + 160);
    QSTEP(1);
    b[6] = *(const bf16x8*)(xb + 192);
    QSTEP(2);
    b[7] = *(const bf16x8*)(xb + 224);
    QSTEP(3);
    b[8] = *(const bf16x8*)(xb + 256);
    QSTEP(4);
    b[9] = *(const bf16x8*)(xb + 288);
    QSTEP(5);
    b[10] = *(const bf16x8*)(xb + 320);
    QSTEP(6);
    b[11] = *(const bf16x8*)(xb + 352);
    QSTEP(7);
    b[12] = *(const bf16x8*)(xb + 384);
    wait_vm<15>();
    a[16] = tr_read8(wa + 16384, wa + 16640);
    QSTEP(8);
    b[13] = *(const bf16x8*)(xb + 416);
    wait_vm<14>();
    a[17] = tr_read8(wa + 17408, wa + 17664);
    QSTEP(9);
    b[14] = *(const bf16x8*)(xb + 448);
    wait_vm<13>();
    a[18] = tr_read8(wa + 18432, wa + 18688);
    QSTEP(10);
    b[15] = *(const bf16x8*)(xb + 480);
    wait_vm<12>();
    a[19] = tr_read8(wa + 19456, wa + 19712);
    QSTEP(11);
    b[16] = *(const bf16x8*)(xb + 512);
    wait_vm<11>();
    a[20] = tr_read8(wa + 20480, wa + 20736);
    QSTEP(12);
    b[17] = *(const bf16x8*)(xb + 544);
    wait_vm<10>();
    a[21] = tr_read8(wa + 21504, wa + 21760);
    QSTEP(13);
    b[18] = *(const bf16x8*)(xb + 576);
    wait_vm<9>();
    a[22] = tr_read8(wa + 22528, wa + 22784);
    QSTEP(14);
    b[19] = *(const bf16x8*)(xb + 608);
    wait_vm<8>();
    a[23] = tr_read8(wa + 23552, wa + 23808);
    QSTEP(15);
    b[20] = *(const bf16x8*)(xb + 640);
    wait_vm<7>();
    a[24] = tr_read8(wa + 24576, wa + 24832);
    QSTEP(16);
    b[21] = *(const bf16x8*)(xb + 672);
    wait_vm<6>();
    a[25] = tr_read8(wa + 25600, wa + 25856);
    QSTEP(17);
    b[22] = *(const bf16x8*)(xb + 704);
    wait_vm<5>();
    a[26] = tr_read8(wa + 26624, wa + 26880);
    QSTEP(18);
    b[23] = *(const bf16x8*)(xb + 736);
    wait_vm<4>();
    a[27] = tr_read8(wa + 0, wa + 256);
    QSTEP(19);
    b[24] = *(const bf16x8*)(xb + 768);
    wait_vm<3>();
    a[28] = tr_read8(wa + 1024, wa + 1280);
    QSTEP(20);
    b[25] = *(const bf16x8*)(xb + 800);
    wait_vm<2>();
    a[29] = tr_read8(wa + 2048, wa + 2304);
    QSTEP(21);
    b[26] = *(const bf16x8*)(xb + 832);
    wait_vm<1>();
    a[30] = tr_read8(wa + 3072, wa + 3328);
    QSTEP(22);
    b[27] = *(const bf16x8*)(xb + 864);
    wait_vm<0>();
    a[31] = tr_read8(wa + 4096, wa + 4352);
    QSTEP(23);
    b[28] = *(const bf16x8*)(xb + 896);
    QSTEP(24);
    b[29] = *(const bf16x8*)(xb + 928);
    QSTEP(25);
    b[30] = *(const bf16x8*)(xb + 960);
    QSTEP(26);
    b[31] = *(const bf16x8*)(xb + 992);
    QSTEP(27);
    QSTEP(28);
    QSTEP(29);
    QSTEP(30);
    QSTEP(31);
    const float sc = 0.25f * 1.4426950408889634f;
    const __amdgpu_buffer_rsrc_t rq = __builtin_amdgcn_make_buffer_rsrc(Q + tok0 * 512, 0, 32768, 0x00020000);
#pragma unroll
    for (int m = 0; m < 2; ++m) {
      unsigned o[4];
#pragma unroll
      for (int i = 0; i < 4; ++i)
        o[i] = pack2(f32x2{rbf1(acc[8 * m + 2 * i]), rbf1(acc[8 * m + 2 * i + 1])} * f32x2{sc, sc});
      swap32(o[0], o[2]);
      swap32(o[1], o[3]);
      __builtin_amdgcn_raw_buffer_store_b128(u32x4{o[0], o[1], o[2], o[3]}, rq, r * 1024 + (16 * m + 8 * h) * 2, sw0, 0);
    }
    return;
  }

  const int tl = wave * 4 + g;
  u32x4 xv[4], wrv[4];
#pragma unroll
  for (int i = 0; i < 4; ++i) xv[i] = *(const u32x4*)(x + (tok0 + tl) * 512 + (n + 16 * i) * 8);
  if (cb == 3) {
#pragma unroll
    for (int i = 0; i < 4; ++i) wrv[i] = *(const u32x4*)(wr + (n + 16 * i) * 8);
  }
  float s[4] = {0.f, 0.f, 0.f, 0.f};
#pragma unroll
  for (int i = 0; i < 16; ++i) s[i & 3] = dot2(xv[i >> 2][i & 3], 0x3f803f80u, s[i & 3]);
  float ssum = (s[0] + s[1]) + (s[2] + s[3]);
  if (cb == 3) asm volatile("" : "+v"(wrv[0]), "+v"(wrv[1]), "+v"(wrv[2]), "+v"(wrv[3]));
  asm volatile("" : "+v"(ssum));
  unsigned pf[4] = {0u, 0u, 0u, 0u};
  if (wave == 6) {
    touch(pf[0], (lane & 1) ? ws + WS_K + tb * 1024 : (const char*)(Q + tok0 * 512 + cb * 128));
  } else if (wave == 5 && (blockIdx.x >> 3) == 16) {
    unsigned long long c0, c1;
    CODE_ADDR(k2_attn, c0);
    CODE_ADDR(k1_ln_qkv, c1);
    const long long len = (long long)(c1 - c0);
#pragma unroll
    for (int i = 0; i < 4; ++i) {
      const int off = (i * 64 + lane) * 128;
      touch(pf[i], (const char*)c0 + (off < len ? off : 0));
    }
  }
  const float mean = sum16(ssum) * (1.f / 512.f);
  const float mb = rbf1(mean), nmb = -mb;
  f32x2 t[16], v2[2] = {{0.f, 0.f}, {0.f, 0.f}};
#pragma unroll
  for (int i = 0; i < 4; ++i) unpack_add(xv[i], nmb, t + 4 * i);
#pragma unroll
  for (int i = 0; i < 16; ++i) v2[i & 1] += t[i] * t[i];
  const float dm = mean - mb;
  const float var = fmaxf(sum16((v2[0].x + v2[0].y) + (v2[1].x + v2[1].y)) * (1.f / 512.f) - dm * dm, 0.f);
#pragma unroll
  for (int i = 0; i < 16; ++i) t[i] = f32x2{rbf1(t[i].x), rbf1(t[i].y)};
  const float den = rbf1(sqrtf(rbf1(rbf1(var) + 1e-5f)));
  const float rinv = __builtin_amdgcn_rcpf(den);
  const f32x2 rinv2 = {rinv, rinv};
  u32x4 o[4];
#pragma unroll
  for (int i = 0; i < 4; ++i) {
#pragma unroll
    for (int j = 0; j < 4; ++j) o[i][j] = pack2(t[4 * i + j] * rinv2);
    *(u32x4*)(ximg + tl * XS_STRIDE + (n + 16 * i) * 16) = o[i];
  }
  if (cb == 3) {
    float rd = 0.f;
#pragma unroll
    for (int i = 0; i < 4; ++i) {
#pragma unroll
      for (int j = 0; j < 4; ++j) {
        rd = dot2(o[i][j], wrv[i][j], rd);
      }
    }
    rd = sum16(rd);
    if (n == 0) rs[tl] = rbf1(rd);
  }
  asm volatile("s_waitcnt lgkmcnt(0)\n s_barrier" ::: "memory");
  if (cb == 2 && n == 0) *(f32x2*)(st + (tok0 + tl) * 2) = f32x2{mb, rinv};
  if (wave == 7) {
    const int half = cb >> 1;
    const char* xb = ximg + (16 * half + n) * XS_STRIDE + 16 * g;
    const unsigned wa = lds0 + K1_KV + (8 * g + q) * 32 + p * 8;
    bf16x8 xf[16], wf[16];
#pragma unroll
    for (int i = 0; i < 16; ++i) {
      xf[i] = *(const bf16x8*)(xb + i * 64);
      wf[i] = tr_read8(wa + i * 1024, wa + i * 1024 + 128);
    }
    f32x4 c0 = {0.f, 0.f, 0.f, 0.f}, c1 = c0;
    unsigned off;
    if (cb & 1) {
#pragma unroll
      for (int i = 0; i < 16; i += 2) {
        c0 = __builtin_amdgcn_mfma_f32_16x16x32_bf16(xf[i], wf[i], c0, 0, 0, 0);
        c1 = __builtin_amdgcn_mfma_f32_16x16x32_bf16(xf[i + 1], wf[i + 1], c1, 0, 0, 0);
      }
      off = 65536 + ((half * 2 + (g >> 1)) * 16 + n) * 16;
    } else {
#pragma unroll
      for (int i = 0; i < 16; i += 2) {
        c0 = __builtin_amdgcn_mfma_f32_16x16x32_bf16(wf[i], xf[i], c0, 0, 0, 0);
        c1 = __builtin_amdgcn_mfma_f32_16x16x32_bf16(wf[i + 1], xf[i + 1], c1, 0, 0, 0);
      }
      off = ((g >> 1) * 32 + 16 * half + n) * 16;
    }
    c0 += c1;
    *(u32x2*)(ws + WS_K + tb * 1024 + off + (g & 1) * 8) = u32x2{pack2(f32x2{c0[0], c0[1]}), pack2(f32x2{c0[2], c0[3]})};
  } else if (wave == 4 && cb == 3) {
    unsigned k1 = route_key(rs[r], tok0 + r), k2 = 0;
    top2_wave(k1, k2, 32);
    if (lane == 0) *(u32x2*)(cand + tb * 2) = u32x2{k1, k2 | 1u};
    const u32x4 a = *(const u32x4*)(ximg + (key_tok(k1) & 31) * XS_STRIDE + lane * 16);
    const u32x4 b = *(const u32x4*)(ximg + (key_tok(k2) & 31) * XS_STRIDE + lane * 16);
    *(u32x4*)(ws + WS_CX + tb * 2048 + lane * 16) = a;
    *(u32x4*)(ws + WS_CX + tb * 2048 + 1024 + lane * 16) = b;
  } else if (blockIdx.x == 0 && tid < NCNT) {
    ((unsigned*)(ws + WS_CNT))[tid * 64] = 0;
  }
  asm volatile("s_waitcnt vmcnt(0)" ::"v"(pf[0]), "v"(pf[1]), "v"(pf[2]), "v"(pf[3]));
}

extern "C" void kernel_launch(void* const* d_in, const int* in_sizes, int n_in, void* d_out, int out_size,
                              void* d_ws, size_t ws_size, hipStream_t stream) {
  const bf16* x = (const bf16*)d_in[0];
  const bf16* wq = (const bf16*)d_in[1];
  const bf16* wk = (const bf16*)d_in[2];
  const bf16* wv = (const bf16*)d_in[3];
  const bf16* wo = (const bf16*)d_in[4];
  const bf16* wr = (const bf16*)d_in[6];
  const bf16* wg = (const bf16*)d_in[7];
  const bf16* wu = (const bf16*)d_in[8];
  const bf16* wd = (const bf16*)d_in[9];
  char* ws = (char*)d_ws;
  bf16* out = (bf16*)d_out;

  (void)hipFuncSetAttribute((const void*)k1_ln_qkv, hipFuncAttributeMaxDynamicSharedMemorySize, K1_LDS);
  (void)hipFuncSetAttribute((const void*)k2_attn, hipFuncAttributeMaxDynamicSharedMemorySize, ATT_LDS);

  k1_ln_qkv<<<256, 768, K1_LDS, stream>>>(x, wq, wk, wv, wr, ws);
  k2_attn<<<256, 768, ATT_LDS, stream>>>(ws, x, wg, wu, wd, wo, out);
}
```

```cpp
#include <hip/hip_runtime.h>
#include <hip/hip_bf16.h>

typedef __attribute__((ext_vector_type(8))) __bf16 bf16x8;
typedef __attribute__((ext_vector_type(16))) __bf16 bf16x16;
typedef __attribute__((ext_vector_type(8))) float f32x8;
typedef __attribute__((ext_vector_type(4))) __bf16 bf16x4;
typedef __attribute__((ext_vector_type(2))) __bf16 bf16x2;
typedef __attribute__((ext_vector_type(4))) short s16x4;
typedef __attribute__((ext_vector_type(8))) short s16x8;
typedef __attribute__((ext_vector_type(16))) float f32x16;
typedef __attribute__((ext_vector_type(4))) float f32x4;
typedef __attribute__((ext_vector_type(4))) unsigned u32x4;
typedef __attribute__((ext_vector_type(2))) unsigned u32x2;
typedef __attribute__((ext_vector_type(2))) float f32x2;
typedef __attribute__((ext_vector_type(16))) unsigned u32x16;
typedef __bf16 bf16;

#define LDS_AS __attribute__((address_space(3)))
#define GLB_AS __attribute__((address_space(1)))
#define CONST_AS __attribute__((address_space(4)))
#define DI __device__ __forceinline__

constexpr int S_ = 2048, D_ = 512, H_ = 32, DH_ = 16, F_ = 1376;

constexpr size_t WS_Q = 0;
constexpr size_t WS_O = WS_Q;
constexpr size_t WS_K = 2u << 20;
constexpr size_t WS_V = WS_K + 65536;
constexpr size_t WS_ST = WS_V + 65536;
constexpr size_t WS_R = WS_ST + 16384;
constexpr size_t WS_LOW = WS_R + 512;
constexpr size_t WS_CX = WS_R + 4096;
constexpr size_t WS_CNT = WS_CX + 131072;
constexpr int NCNT = 64 + 16 + 256;
constexpr size_t WS_FLG = WS_CNT + 64 * 256;
constexpr size_t WS_CL = WS_FLG + 16 * 256;
constexpr size_t WS_UPD = WS_CNT + NCNT * 256;
constexpr size_t WS_YP = WS_UPD + 4096;
constexpr int NFFN = 172;
constexpr size_t WS_END = WS_YP + NFFN * 4096;

DI float bf2f(bf16 v) { return (float)v; }
DI float rbf(float v) { return (float)(bf16)v; }
DI f32x2 unpack2(unsigned u) { return f32x2{__uint_as_float(u << 16), __uint_as_float(u & 0xffff0000u)}; }
DI unsigned pack2(f32x2 v) {
  unsigned r = __builtin_bit_cast(unsigned, __builtin_convertvector(v, bf16x2));
  asm("" : "+v"(r));
  return r;
}

DI unsigned route_key(float v, int tok) {
  const unsigned u = __float_as_uint(v) >> 16;
  const unsigned o = (u & 0x8000u) ? (~u & 0xffffu) : (u | 0x8000u);
  return (o << 16) | (unsigned)((0x7ff - tok) << 1);
}
DI int key_tok(unsigned k) { return 0x7ff - (int)((k >> 1) & 0x7ffu); }
DI int key_row(unsigned k) { return (key_tok(k) >> 5) * 2 + (int)(k & 1u); }
DI float key_val(unsigned k) {
  const unsigned o = k >> 16;
  const unsigned u = (o & 0x8000u) ? (o & 0x7fffu) : (~o & 0xffffu);
  return __uint_as_float(u << 16);
}
DI unsigned ln2(f32x2 x, f32x2 mb2, f32x2 rinv2) { return pack2(unpack2(pack2(x - mb2)) * rinv2); }
DI void top2_wave(unsigned& k1, unsigned& k2, int width) {
  for (int o = width >> 1; o > 0; o >>= 1) {
    const unsigned a1 = __shfl_xor(k1, o), a2 = __shfl_xor(k2, o);
    k2 = max(min(k1, a1), max(k2, a2));
    k1 = max(k1, a1);
  }
}

DI void top2_merge(u32x16 (&cv)[4], unsigned& k1, unsigned& k2) {
  asm volatile("" : "+s"(cv[0]), "+s"(cv[1]), "+s"(cv[2]), "+s"(cv[3]));
#pragma unroll
  for (int i = 0; i < 32; ++i) {
    const unsigned a1 = cv[i >> 3][(i & 7) * 2], a2 = cv[i >> 3][(i & 7) * 2 + 1];
    unsigned t;
    asm volatile("s_min_u32 %2, %0, %3\n s_max_u32 %1, %1, %4\n s_max_u32 %0, %0, %3\n s_max_u32 %1, %1, %2"
                 : "+s"(k1), "+s"(k2), "=&s"(t) : "s"(a1), "s"(a2) : "scc");
  }
}

DI void glds16_raw(const void* g, unsigned lds_off) {
  asm volatile("s_mov_b32 m0, %0\n s_nop 0\n global_load_lds_dwordx4 %1, off" ::"s"(lds_off), "v"(g) : "memory", "m0");
}

DI void blds16(__amdgpu_buffer_rsrc_t rs, unsigned voff, unsigned soff, unsigned lds_off) {
  asm volatile("s_mov_b32 m0, %0\n s_nop 0\n buffer_load_dwordx4 %1, %2, %3 offen lds" ::"s"(lds_off), "v"(voff), "s"(rs), "s"(soff) : "memory", "m0");
}
DI void blds16x2(__amdgpu_buffer_rsrc_t rs, unsigned voff, unsigned soff, unsigned lds_off) {
  asm volatile("s_mov_b32 m0, %0\n s_nop 0\n buffer_load_dwordx4 %1, %2, %3 offen lds\n buffer_load_dwordx4 %1, %2, %3 offen offset:1024 lds"
               ::"s"(lds_off), "v"(voff), "s"(rs), "s"(soff) : "memory", "m0");
}
DI void count_up(__amdgpu_buffer_rsrc_t rs, unsigned one, unsigned soff) {
  asm volatile("s_mov_b64 exec, 1\n buffer_atomic_add %0, off, %1, %2\n s_mov_b64 exec, -1" ::"v"(one), "s"(rs), "s"(__builtin_amdgcn_readfirstlane(soff)) : "memory");
}
DI void fetch8(u32x4* t, unsigned voff, __amdgpu_buffer_rsrc_t rs, unsigned soff) {
  asm volatile("buffer_load_dwordx4 %0, %8, %9, %10 offen sc1\n buffer_load_dwordx4 %1, %8, %9, %10 offen offset:1024 sc1\n"
               "buffer_load_dwordx4 %2, %8, %9, %10 offen offset:2048 sc1\n buffer_load_dwordx4 %3, %8, %9, %10 offen offset:3072 sc1\n"
               "buffer_load_dwordx4 %4, %8, %9, %11 offen sc1\n buffer_load_dwordx4 %5, %8, %9, %11 offen offset:1024 sc1\n"
               "buffer_load_dwordx4 %6, %8, %9, %11 offen offset:2048 sc1\n buffer_load_dwordx4 %7, %8, %9, %11 offen offset:3072 sc1"
               : "+v"(t[0]), "+v"(t[1]), "+v"(t[2]), "+v"(t[3]), "+v"(t[4]), "+v"(t[5]), "+v"(t[6]), "+v"(t[7])
               : "v"(voff), "s"(rs), "s"(soff), "s"(soff + 4096) : "memory");
}
#define LDSP(T, off) ((LDS_AS T*)(unsigned long)(off))

template <int CTRL>
DI float dpp_add(float v) {
  const int t = __builtin_amdgcn_update_dpp(0, __float_as_int(v), CTRL, 0xf, 0xf, true);
  return v + __int_as_float(t);
}
DI float sum8(float v) {
  v = dpp_add<0xB1>(v);
  v = dpp_add<0x4E>(v);
  v = dpp_add<0x141>(v);
  return v;
}

DI f32x16 pv_mfma(bf16x8 v, f32x8 p, f32x16 acc, int sel) {
  return __builtin_amdgcn_smfmac_f32_32x32x32_bf16(v, __builtin_bit_cast(bf16x16, p), acc, sel, 0, 0);
}

DI void swap32(unsigned& a, unsigned& b) {
  const u32x2 t = __builtin_amdgcn_permlane32_swap(a, b, false, false);
  a = t[0];
  b = t[1];
}
DI float max32(float v) {
  unsigned a = __float_as_uint(v), b = a;
  swap32(a, b);
  return fmaxf(__uint_as_float(a), __uint_as_float(b));
}

DI s16x4 tr_read(const char* p) {
  return __builtin_amdgcn_ds_read_tr16_b64_v4i16((LDS_AS s16x4*)p);
}
DI bf16x8 tr_read8(unsigned lo, unsigned hi) {
  const s16x4 a = __builtin_amdgcn_ds_read_tr16_b64_v4i16(LDSP(s16x4, lo)), b = __builtin_amdgcn_ds_read_tr16_b64_v4i16(LDSP(s16x4, hi));
  return __builtin_bit_cast(bf16x8, __builtin_shufflevector(a, b, 0, 1, 2, 3, 4, 5, 6, 7));
}

DI int wimg_off(int row, int ch) { return row * 128 + ((ch ^ (((row >> 1) & 1) << 2)) << 4); }

constexpr int XS_STRIDE = 1040;

constexpr int GP = 4;
DI f32x16 gemm_tile(const char* wimg, const char* ximg, int n0, int t0, int lane) {
  const int r = lane & 31, h = lane >> 5;
  const int q = (lane & 15) >> 2, p = lane & 3, blk = (lane >> 4) & 1;
  f32x16 acc;
#pragma unroll
  for (int i = 0; i < 16; ++i) acc[i] = 0.f;
  const char* xb = ximg + (t0 + r) * XS_STRIDE + 16 * h;
  const int col = n0 + 16 * blk + 4 * p;
  const int ch = col >> 3, sub = (col & 7) * 2;
  const char* wb0 = wimg + wimg_off(8 * h + q, ch) + sub;
  const char* wb1 = wimg + wimg_off(8 * h + q + 4, ch) + sub;
  s16x4 lo[32], hi[32];
  bf16x8 b[32];
#pragma unroll
  for (int ks = 0; ks < 32 + GP; ++ks) {
    if (ks < 32) {
      lo[ks] = tr_read(wb0 + ks * 2048);
      hi[ks] = tr_read(wb1 + ks * 2048);
      b[ks] = *(const bf16x8*)(xb + ks * 32);
    }
    if (ks >= GP) {
      const s16x8 a = __builtin_shufflevector(lo[ks - GP], hi[ks - GP], 0, 1, 2, 3, 4, 5, 6, 7);
      acc = __builtin_amdgcn_mfma_f32_32x32x16_bf16(__builtin_bit_cast(bf16x8, a), b[ks - GP], acc, 0, 0, 0);
      asm volatile("" : "+v"(acc));
    }
    __builtin_amdgcn_sched_barrier(0);
  }
  return acc;
}

DI float dot2(unsigned a, unsigned b, float c) {
  return __builtin_amdgcn_fdot2_f32_bf16(__builtin_bit_cast(bf16x2, a), __builtin_bit_cast(bf16x2, b), c, false);
}
DI void touch(unsigned& t, const void* p) {
  asm volatile("global_load_dword %0, %1, off" : "=v"(t) : "v"(p) : "memory");
}
DI float rbf1(float v) { return __uint_as_float(pack2(f32x2{0.f, v})); }
DI void unpack_add(u32x4 x, float c, f32x2* t) {
  float d[8];
  asm("v_dot2_f32_bf16 %0, %8, %12, %14\n v_dot2_f32_bf16 %1, %8, %13, %14\n"
      "v_dot2_f32_bf16 %2, %9, %12, %14\n v_dot2_f32_bf16 %3, %9, %13, %14\n"
      "v_dot2_f32_bf16 %4, %10, %12, %14\n v_dot2_f32_bf16 %5, %10, %13, %14\n"
      "v_dot2_f32_bf16 %6, %11, %12, %14\n v_dot2_f32_bf16 %7, %11, %13, %14\n s_nop 2"
      : "=&v"(d[0]), "=&v"(d[1]), "=&v"(d[2]), "=&v"(d[3]), "=&v"(d[4]), "=&v"(d[5]), "=&v"(d[6]), "=&v"(d[7])
      : "v"(x[0]), "v"(x[1]), "v"(x[2]), "v"(x[3]), "s"(0x00003f80u), "s"(0x3f800000u), "v"(c));
#pragma unroll
  for (int i = 0; i < 4; ++i) t[i] = f32x2{d[2 * i], d[2 * i + 1]};
}
DI float sum16(float v) { return dpp_add<0x140>(sum8(v)); }
template <int N>
DI void wait_vm() { asm volatile("s_waitcnt vmcnt(%0)" ::"n"(N) : "memory"); }

#define QSTEP(i)                                                                  \
  acc = __builtin_amdgcn_mfma_f32_32x32x16_bf16(a[i], b[i], acc, 0, 0, 0); \
  asm volatile("" : "+v"(acc));                                            \
  __builtin_amdgcn_sched_barrier(0)

#define CODE_ADDR(sym, out)                                                                               \
  asm volatile("s_getpc_b64 s[20:21]\n s_add_u32 s20, s20, " #sym "@rel32@lo+4\n s_addc_u32 s21, s21, " #sym \
               "@rel32@hi+12\n s_mov_b64 %0, s[20:21]"                                                    \
               : "=s"(out)::"s20", "s21", "scc")

constexpr int NT = 16;
constexpr int CST = 4096;
constexpr int L_RED = 131072 + CST;
constexpr int L_CNT = L_RED + 512;
constexpr int L_H = L_CNT + 64;
constexpr int L_STG = L_RED + 1024;
constexpr int ATT_LDS = L_STG + 4 * 6144;
constexpr int NC = 64 / NT;
constexpr int PSW = 53;
constexpr int SPIN_MAX = 1 << 22;

extern "C" __global__ __launch_bounds__(768) void k2_attn(char* __restrict__ ws, const bf16* __restrict__ x,
                                               const bf16* __restrict__ wg, const bf16* __restrict__ wu,
                                               const bf16* __restrict__ wd, const bf16* __restrict__ wo,
                                               bf16* __restrict__ out) {
  const bf16* Q = (const bf16*)(ws + WS_Q);
  const bf16* Kb = (const bf16*)(ws + WS_K);
  bf16* O = (bf16*)(ws + WS_O);
  extern __shared__ __attribute__((aligned(16))) char smem[];
  char* kl = smem;
  char* vl = smem + 65536;
  char* cst = smem + 131072;
  const int tid = threadIdx.x, lane = tid & 63;
  const int wave = __builtin_amdgcn_readfirstlane(tid >> 6);

  const unsigned lds0 = (unsigned)(size_t)(LDS_AS char*)smem;
  unsigned long long pc = (unsigned long long)ws + WS_R, pg = (unsigned long long)wg, pu = (unsigned long long)wu, pd = (unsigned long long)wd;
  asm volatile("" : "+s"(pc), "+s"(pg), "+s"(pu), "+s"(pd));
  if (wave >= 8) {
    const int sw = wave - 8, r = lane & 31, h = lane >> 5;
    u32x16 cv[4];
#pragma unroll
    for (int i = 0; i < 4; ++i) cv[i] = ((const CONST_AS u32x16*)pc)[i];
    unsigned l16 = lane * 16;
    const int fs = (blockIdx.x & 7) * 22 + (blockIdx.x >> 3);
    const bool ffn = blockIdx.x < 176 && fs < NFFN;
    const unsigned stg = lds0 + L_STG + sw * 6144;
    const int n = lane & 15, g = lane >> 4, q = n >> 2, p = lane & 3;
    unsigned a_gu = stg + (p >> 1) * 2048 + (8 * g + q) * 16 + (p & 1) * 8;
    unsigned a_dn = stg + 4096 + q * 256 + (16 * g + 4 * p) * 2;
    unsigned a_rw = lds0 + L_RED + (n & 7) * 32 + sw * 8 + (n >> 3) * 4;
    unsigned a_rr = lds0 + L_RED + n * 32;
    unsigned a_hw = lds0 + L_H + sw * 32 + n * 2;
    const unsigned hrow = lds0 + L_H + sw * 32 + (n >> 3) * 16, zrow = lds0 + 131072 + 16;
    unsigned a_h0 = (p == g && (q & 1) == 0) ? hrow : zrow, a_h1 = (p == g && (q & 1) == 1) ? hrow : zrow;
    unsigned a_cnt = lds0 + L_CNT, one = 1, odd = lane & 1, even = (lane & 1) ^ 1, g16 = g * 16;
    unsigned v_w = (lane >> 3) * 1024 + (((lane & 7) ^ ((lane >> 4 & 1) << 2)) << 4);
    unsigned v_x = r * 1024 + h * 8, v_s = r * 8;
    unsigned v_y = n * 4096 + g * 16, v_c = n * 256, v_e = n < 12 ? 11 : 10;
    asm volatile("" : "+v"(l16), "+v"(a_gu), "+v"(a_dn), "+v"(a_rw), "+v"(a_rr), "+v"(a_hw), "+v"(a_h0), "+v"(a_h1),
                 "+v"(a_cnt), "+v"(one), "+v"(odd), "+v"(even), "+v"(g16));
    unsigned vg = lane * (F_ * 2), vd = g * 1024 + n * 16;
    asm volatile("" : "+v"(v_w), "+v"(v_x), "+v"(v_s), "+v"(v_y), "+v"(v_c), "+v"(v_e), "+v"(vg), "+v"(vd));
    const __amdgpu_buffer_rsrc_t rws = __builtin_amdgcn_make_buffer_rsrc(ws, 0, WS_END, 0x00020000);
#pragma unroll
    for (int i = 0; i < 13; ++i) {
      const unsigned o = sw * 2048 + (i < 6 ? 16 + 8 * i : 24 + 8 * i) * 1024;
      blds16x2(rws, l16, WS_K + o, lds0 + o);
    }
    asm volatile("s_barrier" ::: "memory");
    unsigned k1 = 0, k2 = 0;
    top2_merge(cv, k1, k2);
#pragma unroll
    for (int i = 0; i < 4; ++i) cv[i] = ((const CONST_AS u32x16*)pc)[4 + i];
    asm volatile("s_waitcnt vmcnt(0)\n s_barrier" ::: "memory");
    if (ffn) {
      const __amdgpu_buffer_rsrc_t rg = __builtin_amdgcn_make_buffer_rsrc((void*)pg, 0, 512 * F_ * 2, 0x00020000);
      const __amdgpu_buffer_rsrc_t ru = __builtin_amdgcn_make_buffer_rsrc((void*)pu, 0, 512 * F_ * 2, 0x00020000);
      const __amdgpu_buffer_rsrc_t rd = __builtin_amdgcn_make_buffer_rsrc((void*)pd, 0, 512 * F_ * 2, 0x00020000);
      const unsigned so = (sw * 128 * F_ + fs * 8) * 2, sd = (fs * 8 * 512 + sw * 128) * 2;
      blds16(rg, vg, so, stg);
      blds16(rg, vg, so + 64 * F_ * 2, stg + 1024);
      blds16(ru, vg, so, stg + 2048);
      blds16(ru, vg, so + 64 * F_ * 2, stg + 3072);
      blds16(rd, vd, sd, stg + 4096);
      blds16(rd, vd, sd + 4096, stg + 5120);
    }
    top2_merge(cv, k1, k2);
    const CONST_AS unsigned long long* ka = (const CONST_AS unsigned long long*)__builtin_amdgcn_kernarg_segment_ptr();
    asm volatile("" : "+s"(ka));
    const unsigned long long px = ka[1], po = ka[5], pout = ka[6];
    if (ffn) {
      u32x4 xf[4];
      __builtin_amdgcn_s_setprio(3);
      unsigned vx = __umul24(even, key_row(k1) * 1024) + g16;
      vx += __umul24(odd, key_row(k2) * 1024);
      asm volatile("" : "+v"(vx));
      __builtin_amdgcn_s_setprio(0);
#pragma unroll
      for (int i = 0; i < 4; ++i) xf[i] = __builtin_amdgcn_raw_buffer_load_b128(rws, vx + i * 64, WS_CX + sw * 256, 0);
      asm volatile("s_waitcnt vmcnt(0)" ::: "memory");
      bf16x8 wf[4], df[2];
#pragma unroll
      for (int i = 0; i < 4; ++i) wf[i] = tr_read8(a_gu + i * 512, a_gu + i * 512 + 64);
#pragma unroll
      for (int i = 0; i < 2; ++i) df[i] = tr_read8(a_dn + i * 128, a_dn + i * 128 + 1024);
      asm volatile("s_waitcnt lgkmcnt(0)" ::: "memory");
      __builtin_amdgcn_s_setprio(3);
      f32x4 gu = {0.f, 0.f, 0.f, 0.f};
#pragma unroll
      for (int i = 0; i < 4; ++i) gu = __builtin_amdgcn_mfma_f32_16x16x32_bf16(__builtin_bit_cast(bf16x8, xf[i]), wf[i], gu, 0, 0, 0);
      *LDSP(float, a_rw) = gu[0];
      *LDSP(float, a_rw + 256) = gu[1];
      asm volatile("s_mov_b64 exec, 1\n ds_add_u32 %0, %1\n s_mov_b64 exec, -1" ::"v"(a_cnt), "v"(one) : "memory");
      while (*(volatile LDS_AS unsigned*)LDSP(unsigned, a_cnt) < 4) __builtin_amdgcn_s_sleep(1);
      asm volatile("" ::: "memory");
      const f32x4 r0 = *LDSP(f32x4, a_rr), r1 = *LDSP(f32x4, a_rr + 16);
      const f32x2 gs = unpack2(pack2((f32x2{r0[0], r0[1]} + f32x2{r0[2], r0[3]}) + (f32x2{r1[0], r1[1]} + f32x2{r1[2], r1[3]})));
      const float hv = rbf(gs.x * __builtin_amdgcn_rcpf(1.f + __expf(-gs.x))) * gs.y;
      *LDSP(bf16, a_hw) = (bf16)hv;
      asm volatile("" ::: "memory");
      const bf16x8 h0 = *LDSP(bf16x8, a_h0), h1 = *LDSP(bf16x8, a_h1);
      f32x4 y = {0.f, 0.f, 0.f, 0.f};
      y = __builtin_amdgcn_mfma_f32_16x16x32_bf16(h0, df[0], y, 0, 0, 0);
      y = __builtin_amdgcn_mfma_f32_16x16x32_bf16(h1, df[1], y, 0, 0, 0);
      __builtin_amdgcn_raw_buffer_store_b128(__builtin_bit_cast(u32x4, y), rws, l16, WS_YP + fs * 4096 + sw * 1024, 16);
      __builtin_amdgcn_s_setprio(0);
      asm volatile("s_waitcnt vmcnt(0)" ::: "memory");
      count_up(rws, one, WS_CNT + (sw * 16 + (fs & 15)) * 256);
    }
    const int tb = (blockIdx.x >> 6) * 8 + (blockIdx.x & 7), cb = (blockIdx.x >> 3) & 7, tok0 = tb * 64;
    char* ximg = smem;
    char* wimg = smem + 64 * XS_STRIDE;
    const int n0 = (sw & 1) * 32, t0 = (sw >> 1) * 32;
    const unsigned so_x = (tok0 + t0) * 1024 + (cb * 64 + n0) * 2;
    const __amdgpu_buffer_rsrc_t rout = __builtin_amdgcn_make_buffer_rsrc((void*)pout, 0, S_ * D_ * 2, 0x00020000);
    u32x4 wreg[16];
    u32x2 xv[4], nv[4], sxr;
    {
      const __amdgpu_buffer_rsrc_t rw = __builtin_amdgcn_make_buffer_rsrc((void*)po, 0, 512 * 1024, 0x00020000);
      const __amdgpu_buffer_rsrc_t rx = __builtin_amdgcn_make_buffer_rsrc((void*)px, 0, S_ * D_ * 2, 0x00020000);
#pragma unroll
      for (int i = 0; i < 16; ++i) wreg[i] = __builtin_amdgcn_raw_buffer_load_b128(rw, v_w, (sw + 4 * i) * 8192 + cb * 128, 0);
#pragma unroll
      for (int i = 0; i < 4; ++i) xv[i] = __builtin_amdgcn_raw_buffer_load_b64(rx, v_x + i * 16, so_x, 0);
      sxr = __builtin_amdgcn_raw_buffer_load_b64(rws, v_s, WS_ST + (tok0 + t0) * 8, 0);
      if (wave == 11) {
        const unsigned t = __builtin_amdgcn_raw_buffer_load_b32(rout, 0, so_x, 0);
        const unsigned t2 = __builtin_amdgcn_raw_buffer_load_b32(rws, v_c, WS_CL + tb * 2048, 16);
        asm volatile("" ::"v"(t), "v"(t2));
      }
    }
    if ((blockIdx.x >> 4) == 11) {
      const int sel = (blockIdx.x >> 3) & 1, cq = blockIdx.x & 7;
      __builtin_amdgcn_s_setprio(3);
      int spin = 0;
      while (__any(__builtin_amdgcn_raw_buffer_load_b32(rws, v_c, WS_CNT + (cq >> 1) * 4096, 16) < v_e) && ++spin < SPIN_MAX)
        asm volatile("s_sleep 8" ::: "memory");
      __builtin_amdgcn_s_setprio(0);
      asm volatile("" ::: "memory");
      const unsigned so = WS_YP + (cq >> 1) * 1024 + (2 * sel + (cq & 1)) * 256 + sw * 64;
      u32x4 t[11];
#pragma unroll
      for (int i = 0; i < 10; ++i) t[i] = __builtin_amdgcn_raw_buffer_load_b128(rws, v_y, so + i * 65536, 16);
      __builtin_amdgcn_s_setprio(3);
      t[10] = u32x4{0u, 0u, 0u, 0u};
      if (v_e == 11) t[10] = __builtin_amdgcn_raw_buffer_load_b128(rws, v_y, so + 10 * 65536, 16);
      const float gate = rbf(__builtin_amdgcn_rcpf(1.f + __expf(-key_val(sel ? k2 : k1))));
      f32x4 a = __builtin_bit_cast(f32x4, t[0]);
#pragma unroll
      for (int i = 1; i < 11; ++i) a += __builtin_bit_cast(f32x4, t[i]);
#pragma unroll
      for (int i = 0; i < 4; ++i) a[i] = dpp_add<0x140>(sum8(a[i]));
      asm volatile("" : "+v"(a));
      const float u = rbf(rbf(p == 0 ? a[0] : p == 1 ? a[1] : p == 2 ? a[2] : a[3]) * gate);
      if (n < 4) __builtin_amdgcn_raw_buffer_store_b32(__float_as_uint(u), rws, n * 64 + g * 4, WS_UPD + (sel * 512 + cq * 64 + sw * 4) * 4, 16);
      __builtin_amdgcn_s_setprio(0);
      asm volatile("s_waitcnt vmcnt(0)" ::: "memory");
      count_up(rws, one, WS_FLG + (sel * 8 + cq) * 256);
    }
    const bool routed = ((key_tok(k1) ^ (tok0 + t0)) >> 5) == 0 || ((key_tok(k2) ^ (tok0 + t0)) >> 5) == 0;
    u32x4 uf[4];
    if (routed) {
      __builtin_amdgcn_s_setprio(3);
      const int tok = tok0 + t0 + r;
      const int rsel = tok == key_tok(k1) ? 0 : tok == key_tok(k2) ? 1 : -1;
#pragma unroll
      for (int i = 0; i < 4; ++i) uf[i] = u32x4{0x80000000u, 0x80000000u, 0x80000000u, 0x80000000u};
      if (rsel >= 0) {
        int spin = 0;
        while (__builtin_amdgcn_raw_buffer_load_b32(rws, rsel * 2048, WS_FLG + cb * 256, 16) < 4 && ++spin < SPIN_MAX)
          asm volatile("s_sleep 4" ::: "memory");
        asm volatile("" ::: "memory");
#pragma unroll
        for (int i = 0; i < 4; ++i)
          uf[i] = __builtin_amdgcn_raw_buffer_load_b128(rws, (rsel * 512 + 8 * i + 4 * h) * 4, WS_UPD + (cb * 64 + n0) * 4, 16);
      }
#pragma unroll
      for (int i = 0; i < 4; ++i) asm volatile("" : "+v"(uf[i]));
      __builtin_amdgcn_s_setprio(0);
    }
#pragma unroll
    for (int i = 0; i < 16; ++i) asm volatile("" : "+v"(wreg[i]));
    asm volatile("s_barrier" ::: "memory");
#pragma unroll
    for (int i = 0; i < 16; ++i) *(u32x4*)(wimg + (sw + 4 * i) * 1024 + lane * 16) = wreg[i];
    {
      const f32x2 mb2 = {__uint_as_float(sxr[0]), __uint_as_float(sxr[0])}, ri2 = {__uint_as_float(sxr[1]), __uint_as_float(sxr[1])};
#pragma unroll
      for (int i = 0; i < 4; ++i) {
#pragma unroll
        for (int jj = 0; jj < 2; ++jj) nv[i][jj] = ln2(unpack2(xv[i][jj]), mb2, ri2);
      }
    }
    if (routed) {
#pragma unroll
      for (int i = 0; i < 4; ++i) {
        const f32x4 u = __builtin_bit_cast(f32x4, uf[i]);
        nv[i][0] = pack2(unpack2(nv[i][0]) + f32x2{u[0], u[1]});
        nv[i][1] = pack2(unpack2(nv[i][1]) + f32x2{u[2], u[3]});
      }
    }
    asm volatile("s_waitcnt lgkmcnt(0)\n s_barrier" ::: "memory");
    f32x16 acc = gemm_tile(wimg, ximg, n0, t0, lane);
#pragma unroll
    for (int g = 0; g < 4; ++g) {
      u32x2 o;
#pragma unroll
      for (int jj = 0; jj < 2; ++jj) {
        const unsigned xr = xv[g][jj], nr = nv[g][jj];
        const f32x2 a = unpack2(pack2(f32x2{acc[4 * g + 2 * jj], acc[4 * g + 2 * jj + 1]}));
        o[jj] = pack2(unpack2(pack2(unpack2(xr) + a)) + unpack2(nr));
      }
      __builtin_amdgcn_raw_buffer_store_b64(o, rout, v_x + g * 16, so_x, 0);
    }
    return;
  }

  const int r = lane & 31, h = lane >> 5;
  const int token = ((blockIdx.x >> 6) * 8 + (blockIdx.x & 7)) * 64 + ((blockIdx.x >> 3) & 7) * 8 + wave;
  unsigned low = *(const CONST_AS unsigned*)(pc + (WS_LOW - WS_R));
  bf16x8 qf;
  asm volatile("global_load_dwordx4 %0, %1, off" : "=v"(qf) : "v"(Q + token * 512 + r * 16 + 8 * h) : "memory");
#pragma unroll
  for (int i = 0; i < 3; ++i) {
    const int piece = (i < 2) ? (wave + i * 8) : (64 + wave);
    glds16_raw((const char*)Kb + piece * 1024 + lane * 16, lds0 + piece * 1024);
  }
  {
    const unsigned cv = ((tid & 30) == 0 || (tid & 30) == 8) ? 0x3f803f80u : 0u;
    *(u32x2*)(cst + tid * 8) = u32x2{cv, cv};
    if (tid == 1) *(unsigned*)(smem + L_CNT) = 0;
  }
  int vo = (r < 16) ? 65536 + (h * 16 + r) * 16 : 131072 + ((r & 11) == 0 ? 0 : 16);
  int vstep = (r < 16) ? CST : 0;
  int sel = 0xdddd;
  f32x16 zero;
#pragma unroll
  for (int i = 0; i < 16; ++i) zero[i] = 0.f;
  f32x16 acc = zero;
  asm volatile("" : "+v"(sel), "+v"(acc), "+v"(vstep), "+v"(vo), "+s"(low));
  const char* kp0 = kl + lane * 16;
  const char* vp0 = smem + vo;
  asm volatile("s_waitcnt vmcnt(0) lgkmcnt(0)\n s_barrier" ::: "memory");

  constexpr float HEAD = 90.f;
  f32x16 negm, xs;
  bf16x8 kfn;
  {
    bf16x8 kf[4];
    f32x16 s0[4];
#pragma unroll
    for (int t = 0; t < 4; ++t) kf[t] = *(const bf16x8*)(kp0 + t * 1024);
#pragma unroll
    for (int t = 0; t < 4; ++t) {
      s0[t] = __builtin_amdgcn_mfma_f32_32x32x16_bf16(kf[t], qf, zero, 0, 0, 0);
      asm volatile("" : "+v"(s0[t]));
    }
    __builtin_amdgcn_sched_barrier(0);
    float mx = -INFINITY;
#pragma unroll
    for (int t = 0; t < 4; ++t) {
#pragma unroll
      for (int i = 0; i < 16; i += 2) mx = fmaxf(fmaxf(mx, s0[t][i]), s0[t][i + 1]);
    }
    const float nm = -(floorf(max32(mx)) + HEAD);
#pragma unroll
    for (int i = 0; i < 16; ++i) {
      xs[i] = s0[0][i] + nm;
      negm[i] = nm;
    }
    kfn = kf[1];
  }
  asm volatile("" : "+v"(negm));
  {
    const char* kp = kp0;
    const char* vp = vp0;
    f32x16 x = xs;
    if (wave < 4) __builtin_amdgcn_s_setprio(2);
    else __builtin_amdgcn_s_setprio(1);
    for (int c = 0; c < NC; ++c) {
      bool grown;
#pragma unroll
      for (int t = 0; t < NT; ++t) {
        const bf16x8 kf = kfn;
        kfn = *(const bf16x8*)(kp + (t + 2) * 1024);
        const bf16x8 vf0 = *(const bf16x8*)(vp + ((t & 3) * 2) * 512);
        const bf16x8 vf1 = *(const bf16x8*)(vp + ((t & 3) * 2 + 1) * 512);
        if ((t & 3) == 3) vp += vstep;
        f32x8 p0, p1;
#pragma unroll
        for (int j = 0; j < 4; ++j) p0[j] = __builtin_amdgcn_exp2f(x[j]);
        __builtin_amdgcn_sched_barrier(0);
        f32x16 xn = __builtin_amdgcn_mfma_f32_32x32x16_bf16(kf, qf, negm, 0, 0, 0);
        asm volatile("" : "+v"(xn));
        __builtin_amdgcn_sched_barrier(0);
#pragma unroll
        for (int j = 4; j < 8; ++j) p0[j] = __builtin_amdgcn_exp2f(x[j]);
        if (t == NT - 1) grown = __any(!(acc[8] <= 4.3e9f));
        acc = pv_mfma(vf0, p0, acc, sel);
#pragma unroll
        for (int j = 0; j < 8; ++j) p1[j] = __builtin_amdgcn_exp2f(x[8 + j]);
        acc = pv_mfma(vf1, p1, acc, sel);
        if (t == 7 && c == 0) asm volatile("s_barrier" ::: "memory");
        if (c * NT + t == PSW && wave < 4) __builtin_amdgcn_s_setprio(0);
        __builtin_amdgcn_sched_barrier(0);
        x = xn;
      }
      kp += NT * 1024;
      if (grown) {
        const int e = max(__builtin_amdgcn_frexp_expf(acc[8]) - 1 + (int)HEAD, 0);
#pragma unroll
        for (int i = 0; i < 9; ++i) acc[i] = __builtin_amdgcn_ldexpf(acc[i], -e);
        const float ef = (float)e;
#pragma unroll
        for (int i = 0; i < 16; ++i) {
          negm[i] -= ef;
          x[i] -= ef;
        }
        asm volatile("" : "+v"(negm));
      }
    }
  }
  {
    float chk = ((fabsf(acc[0]) + fabsf(acc[1])) + (fabsf(acc[2]) + fabsf(acc[3]))) +
                ((fabsf(acc[4]) + fabsf(acc[5])) + (fabsf(acc[6]) + fabsf(acc[7])));
    chk = __builtin_fmaf(acc[8], 0.f, chk);
    if (__any(__float_as_uint(chk) - low >= 0x7f800000u - low)) {
      f32x16 oacc = zero;
      float mrun = -INFINITY;
      const char* kp = kp0;
      const char* vp = vp0;
      for (int t = 0; t < 64; ++t) {
        bf16x8 kf = *(const bf16x8*)(kp);
        f32x16 x = __builtin_amdgcn_mfma_f32_32x32x16_bf16(kf, qf, zero, 0, 0, 0);
        float mx = x[0];
#pragma unroll
        for (int i = 1; i < 16; ++i) mx = fmaxf(mx, x[i]);
        const float mn = fmaxf(mrun, max32(mx));
        const float al = __builtin_amdgcn_exp2f(mrun - mn);
        mrun = mn;
#pragma unroll
        for (int i = 0; i < 16; ++i) oacc[i] *= al;
#pragma unroll
        for (int s = 0; s < 2; ++s) {
          f32x8 pf;
#pragma unroll
          for (int j = 0; j < 8; ++j) pf[j] = __builtin_amdgcn_exp2f(x[8 * s + j] - mn);
          bf16x8 vf = *(const bf16x8*)(vp + ((t & 3) * 2 + s) * 512);
          oacc = pv_mfma(vf, pf, oacc, sel);
        }
        kp += 1024;
        if ((t & 3) == 3) vp += vstep;
      }
      acc = oacc;
    }
  }
  const float inv = 1.f / acc[8];
  {
    unsigned a0 = pack2(f32x2{acc[0] * inv, acc[1] * inv}), a1 = pack2(f32x2{acc[2] * inv, acc[3] * inv});
    unsigned b0 = pack2(f32x2{acc[4] * inv, acc[5] * inv}), b1 = pack2(f32x2{acc[6] * inv, acc[7] * inv});
    swap32(a0, b0);
    swap32(a1, b1);
    const u32x4 o = {a0, a1, b0, b1};
    const __amdgpu_buffer_rsrc_t rws = __builtin_amdgcn_make_buffer_rsrc(ws, 0, WS_END, 0x00020000);
    __builtin_amdgcn_raw_buffer_store_b128(o, rws, r * 32 + h * 16, WS_O + token * 1024, 16);
    const int src = (token & ~63) + wave * 8;
    const bool own = ((token ^ src) >> 3) == 0;
    const unsigned so_f = WS_CL + (src >> 3) * 256;
    const unsigned l16 = lane * 16;
    unsigned f = __builtin_amdgcn_raw_buffer_load_b32(rws, 0, so_f, 16);
    asm volatile("s_waitcnt vmcnt(0)" : "+v"(f)::"memory");
    const bool early = __builtin_amdgcn_readfirstlane(f) != 0;
    u32x4 t[8];
#pragma unroll
    for (int i = 0; i < 8; ++i) asm volatile("" : "=v"(t[i]));
    if (early) fetch8(t, l16, rws, WS_O + src * 1024);
    asm volatile("s_barrier" ::: "memory");
    if (own) {
      if (lane == 0) __builtin_amdgcn_raw_buffer_store_b32(1u, rws, 0, so_f, 16);
    }
    *(u32x4*)(smem + (token & 63) * XS_STRIDE + r * 32 + h * 16) = o;
    if (!own) {
      if (!early) {
        int spin = 0;
        while (__builtin_amdgcn_readfirstlane(__builtin_amdgcn_raw_buffer_load_b32(rws, 0, so_f, 16)) == 0 && ++spin < SPIN_MAX)
          asm volatile("s_sleep 1" ::: "memory");
        asm volatile("" ::: "memory");
        fetch8(t, l16, rws, WS_O + src * 1024);
      }
      char* dst = smem + wave * 8 * XS_STRIDE + lane * 16;
      wait_vm<7>(); asm volatile("" : "+v"(t[0])); *(u32x4*)(dst) = t[0];
      wait_vm<6>(); asm volatile("" : "+v"(t[1])); *(u32x4*)(dst + XS_STRIDE) = t[1];
      wait_vm<5>(); asm volatile("" : "+v"(t[2])); *(u32x4*)(dst + 2 * XS_STRIDE) = t[2];
      wait_vm<4>(); asm volatile("" : "+v"(t[3])); *(u32x4*)(dst + 3 * XS_STRIDE) = t[3];
      wait_vm<3>(); asm volatile("" : "+v"(t[4])); *(u32x4*)(dst + 4 * XS_STRIDE) = t[4];
      wait_vm<2>(); asm volatile("" : "+v"(t[5])); *(u32x4*)(dst + 5 * XS_STRIDE) = t[5];
      wait_vm<1>(); asm volatile("" : "+v"(t[6])); *(u32x4*)(dst + 6 * XS_STRIDE) = t[6];
      wait_vm<0>(); asm volatile("" : "+v"(t[7])); *(u32x4*)(dst + 7 * XS_STRIDE) = t[7];
    }
  }
  asm volatile("s_waitcnt lgkmcnt(0)" ::: "memory");
}

constexpr int K1_KV = 32 * XS_STRIDE;
constexpr int K1_RS = K1_KV + 16384;
constexpr int K1_W = K1_RS + 256;
constexpr int NSLOT = 27;
constexpr int NPRE = 16;
constexpr int LA = 4;
constexpr int K1_LDS = K1_W + 4 * NSLOT * 1024;

extern "C" __global__ __launch_bounds__(768) void k1_ln_qkv(const bf16* __restrict__ x, const bf16* __restrict__ wq,
                                                 const bf16* __restrict__ wk, const bf16* __restrict__ wv,
                                                 const bf16* __restrict__ wr, char* __restrict__ ws) {
  float* st = (float*)(ws + WS_ST);
  bf16* Q = (bf16*)(ws + WS_Q);
  unsigned* cand = (unsigned*)(ws + WS_R);
  extern __shared__ __attribute__((aligned(16))) char smem[];
  char* ximg = smem;
  float* rs = (float*)(smem + K1_RS);
  const unsigned lds0 = (unsigned)(size_t)(LDS_AS char*)smem;
  const int cb = blockIdx.x >> 6, tb = ((blockIdx.x >> 4 & 3) * 8 + (blockIdx.x & 7)) * 2 + (blockIdx.x >> 3 & 1);
  const int tid = threadIdx.x, lane = tid & 63;
  const int wave = __builtin_amdgcn_readfirstlane(tid >> 6);
  const int tok0 = tb * 32;
  const int r = lane & 31, h = lane >> 5;
  const int n = lane & 15, g = lane >> 4, q = n >> 2, p = lane & 3;

  if (wave >= 8) {
    const int sw = wave - 8;
    const unsigned ring = lds0 + K1_W + sw * NSLOT * 1024;
    const __amdgpu_buffer_rsrc_t rw = __builtin_amdgcn_make_buffer_rsrc((void*)wq, 0, 512 * 1024, 0x00020000);
    const __amdgpu_buffer_rsrc_t rkv = __builtin_amdgcn_make_buffer_rsrc((void*)((cb & 1) ? wv : wk), 0, 16384, 0x00020000);
    const unsigned vw = (lane >> 2) * 1024 + (lane & 3) * 16, sw0 = (cb * 128 + sw * 32) * 2;
#pragma unroll
    for (int ks = 0; ks < NSLOT; ++ks) {
      blds16(rw, vw, sw0 + ks * 16384, ring + ks * 1024);
      if (ks == 32 - NSLOT - 1) {
        blds16x2(rkv, lane * 16, sw * 4096, lds0 + K1_KV + sw * 4096);
        blds16x2(rkv, lane * 16, sw * 4096 + 2048, lds0 + K1_KV + sw * 4096 + 2048);
      }
    }
    const unsigned wa = ring + (8 * h + q) * 64 + (g & 1) * 32 + p * 8;
    bf16x8 a[32], b[32];
    wait_vm<NSLOT + 4 - (32 - NSLOT)>();
#pragma unroll
    for (int ks = 0; ks < 32 - NSLOT; ++ks) a[ks] = tr_read8(wa + ks * 1024, wa + ks * 1024 + 256);
#pragma unroll
    for (int ks = 0; ks < 32 - NSLOT; ++ks) asm volatile("" : "+v"(a[ks]));
#pragma unroll
    for (int ks = NSLOT; ks < 32; ++ks) blds16(rw, vw, sw0 + ks * 16384, ring + (ks - NSLOT) * 1024);
    wait_vm<23>();
#pragma unroll
    for (int ks = 5; ks < 9; ++ks) a[ks] = tr_read8(wa + ks * 1024, wa + ks * 1024 + 256);
    wait_vm<19>();
#pragma unroll
    for (int ks = 9; ks < 13; ++ks) a[ks] = tr_read8(wa + ks * 1024, wa + ks * 1024 + 256);
    wait_vm<16>();
#pragma unroll
    for (int ks = 13; ks < 16; ++ks) a[ks] = tr_read8(wa + ks * 1024, wa + ks * 1024 + 256);
#pragma unroll
    for (int ks = 32 - NSLOT; ks < NPRE; ++ks) asm volatile("" : "+v"(a[ks]));
    asm volatile("s_barrier" ::: "memory");
    f32x16 acc;
#pragma unroll
    for (int i = 0; i < 16; ++i) acc[i] = 0.f;
    const char* xb = ximg + r * XS_STRIDE + 16 * h;
    b[0] = *(const bf16x8*)(xb + 0);
    __builtin_amdgcn_sched_barrier(0);
    b[1] = *(const bf16x8*)(xb + 32);
    __builtin_amdgcn_sched_barrier(0);
    b[2] = *(const bf16x8*)(xb + 64);
    __builtin_amdgcn_sched_barrier(0);
    b[3] = *(const bf16x8*)(xb + 96);
    __builtin_amdgcn_sched_barrier(0);
    b[4] = *(const bf16x8*)(xb + 128);
    QSTEP(0);
    b[5] = *(const bf16x8*)(xb + 160);
    QSTEP(1);
    b[6] = *(const bf16x8*)(xb + 192);
    QSTEP(2);
    b[7] = *(const bf16x8*)(xb + 224);
    QSTEP(3);
    b[8] = *(const bf16x8*)(xb + 256);
    QSTEP(4);
    b[9] = *(const bf16x8*)(xb + 288);
    QSTEP(5);
    b[10] = *(const bf16x8*)(xb + 320);
    QSTEP(6);
    b[11] = *(const bf16x8*)(xb + 352);
    QSTEP(7);
    b[12] = *(const bf16x8*)(xb + 384);
    wait_vm<15>();
    a[16] = tr_read8(wa + 16384, wa + 16640);
    QSTEP(8);
    b[13] = *(const bf16x8*)(xb + 416);
    wait_vm<14>();
    a[17] = tr_read8(wa + 17408, wa + 17664);
    QSTEP(9);
    b[14] = *(const bf16x8*)(xb + 448);
    wait_vm<13>();
    a[18] = tr_read8(wa + 18432, wa + 18688);
    QSTEP(10);
    b[15] = *(const bf16x8*)(xb + 480);
    wait_vm<12>();
    a[19] = tr_read8(wa + 19456, wa + 19712);
    QSTEP(11);
    b[16] = *(const bf16x8*)(xb + 512);
    wait_vm<11>();
    a[20] = tr_read8(wa + 20480, wa + 20736);
    QSTEP(12);
    b[17] = *(const bf16x8*)(xb + 544);
    wait_vm<10>();
    a[21] = tr_read8(wa + 21504, wa + 21760);
    QSTEP(13);
    b[18] = *(const bf16x8*)(xb + 576);
    wait_vm<9>();
    a[22] = tr_read8(wa + 22528, wa + 22784);
    QSTEP(14);
    b[19] = *(const bf16x8*)(xb + 608);
    wait_vm<8>();
    a[23] = tr_read8(wa + 23552, wa + 23808);
    QSTEP(15);
    b[20] = *(const bf16x8*)(xb + 640);
    wait_vm<7>();
    a[24] = tr_read8(wa + 24576, wa + 24832);
    QSTEP(16);
    b[21] = *(const bf16x8*)(xb + 672);
    wait_vm<6>();
    a[25] = tr_read8(wa + 25600, wa + 25856);
    QSTEP(17);
    b[22] = *(const bf16x8*)(xb + 704);
    wait_vm<5>();
    a[26] = tr_read8(wa + 26624, wa + 26880);
    QSTEP(18);
    b[23] = *(const bf16x8*)(xb + 736);
    wait_vm<4>();
    a[27] = tr_read8(wa + 0, wa + 256);
    QSTEP(19);
    b[24] = *(const bf16x8*)(xb + 768);
    wait_vm<3>();
    a[28] = tr_read8(wa + 1024, wa + 1280);
    QSTEP(20);
    b[25] = *(const bf16x8*)(xb + 800);
    wait_vm<2>();
    a[29] = tr_read8(wa + 2048, wa + 2304);
    QSTEP(21);
    b[26] = *(const bf16x8*)(xb + 832);
    wait_vm<1>();
    a[30] = tr_read8(wa + 3072, wa + 3328);
    QSTEP(22);
    b[27] = *(const bf16x8*)(xb + 864);
    wait_vm<0>();
    a[31] = tr_read8(wa + 4096, wa + 4352);
    QSTEP(23);
    b[28] = *(const bf16x8*)(xb + 896);
    QSTEP(24);
    b[29] = *(const bf16x8*)(xb + 928);
    QSTEP(25);
    b[30] = *(const bf16x8*)(xb + 960);
    QSTEP(26);
    b[31] = *(const bf16x8*)(xb + 992);
    QSTEP(27);
    QSTEP(28);
    QSTEP(29);
    QSTEP(30);
    QSTEP(31);
    const float sc = 0.25f * 1.4426950408889634f;
    const __amdgpu_buffer_rsrc_t rq = __builtin_amdgcn_make_buffer_rsrc(Q + tok0 * 512, 0, 32768, 0x00020000);
#pragma unroll
    for (int m = 0; m < 2; ++m) {
      unsigned o[4];
#pragma unroll
      for (int i = 0; i < 4; ++i)
        o[i] = pack2(f32x2{rbf1(acc[8 * m + 2 * i]), rbf1(acc[8 * m + 2 * i + 1])} * f32x2{sc, sc});
      swap32(o[0], o[2]);
      swap32(o[1], o[3]);
      __builtin_amdgcn_raw_buffer_store_b128(u32x4{o[0], o[1], o[2], o[3]}, rq, r * 1024 + (16 * m + 8 * h) * 2, sw0, 0);
    }
    return;
  }

  const int tl = wave * 4 + g;
  u32x4 xv[4], wrv[4];
#pragma unroll
  for (int i = 0; i < 4; ++i) xv[i] = *(const u32x4*)(x + (tok0 + tl) * 512 + (n + 16 * i) * 8);
  if (cb == 3) {
#pragma unroll
    for (int i = 0; i < 4; ++i) wrv[i] = *(const u32x4*)(wr + (n + 16 * i) * 8);
  }
  float s[4] = {0.f, 0.f, 0.f, 0.f};
#pragma unroll
  for (int i = 0; i < 16; ++i) s[i & 3] = dot2(xv[i >> 2][i & 3], 0x3f803f80u, s[i & 3]);
  float ssum = (s[0] + s[1]) + (s[2] + s[3]);
  if (cb == 3) asm volatile("" : "+v"(wrv[0]), "+v"(wrv[1]), "+v"(wrv[2]), "+v"(wrv[3]));
  asm volatile("" : "+v"(ssum));
  unsigned pf[4] = {0u, 0u, 0u, 0u};
  if (wave == 6) {
    touch(pf[0], (lane & 1) ? ws + WS_K + tb * 1024 : (const char*)(Q + tok0 * 512 + cb * 128));
  } else if (wave == 5 && (blockIdx.x >> 3) == 16) {
    unsigned long long c0, c1;
    CODE_ADDR(k2_attn, c0);
    CODE_ADDR(k1_ln_qkv, c1);
    const long long len = (long long)(c1 - c0);
#pragma unroll
    for (int i = 0; i < 4; ++i) {
      const int off = (i * 64 + lane) * 128;
      touch(pf[i], (const char*)c0 + (off < len ? off : 0));
    }
  }
  const float mean = sum16(ssum) * (1.f / 512.f);
  const float mb = rbf1(mean), nmb = -mb;
  f32x2 t[16], v2[2] = {{0.f, 0.f}, {0.f, 0.f}};
#pragma unroll
  for (int i = 0; i < 4; ++i) unpack_add(xv[i], nmb, t + 4 * i);
#pragma unroll
  for (int i = 0; i < 16; ++i) v2[i & 1] += t[i] * t[i];
  const float dm = mean - mb;
  const float var = fmaxf(sum16((v2[0].x + v2[0].y) + (v2[1].x + v2[1].y)) * (1.f / 512.f) - dm * dm, 0.f);
#pragma unroll
  for (int i = 0; i < 16; ++i) t[i] = f32x2{rbf1(t[i].x), rbf1(t[i].y)};
  const float den = rbf1(sqrtf(rbf1(rbf1(var) + 1e-5f)));
  const float rinv = __builtin_amdgcn_rcpf(den);
  const f32x2 rinv2 = {rinv, rinv};
  u32x4 o[4];
#pragma unroll
  for (int i = 0; i < 4; ++i) {
#pragma unroll
    for (int j = 0; j < 4; ++j) o[i][j] = pack2(t[4 * i + j] * rinv2);
    *(u32x4*)(ximg + tl * XS_STRIDE + (n + 16 * i) * 16) = o[i];
  }
  if (cb == 3) {
    float rd = 0.f;
#pragma unroll
    for (int i = 0; i < 4; ++i) {
#pragma unroll
      for (int j = 0; j < 4; ++j) {
        rd = dot2(o[i][j], wrv[i][j], rd);
      }
    }
    rd = sum16(rd);
    if (n == 0) rs[tl] = rbf1(rd);
  }
  asm volatile("s_waitcnt lgkmcnt(0)\n s_barrier" ::: "memory");
  if (cb == 2 && n == 0) *(f32x2*)(st + (tok0 + tl) * 2) = f32x2{mb, rinv};
  if (wave == 7) {
    const int half = cb >> 1;
    const char* xb = ximg + (16 * half + n) * XS_STRIDE + 16 * g;
    const unsigned wa = lds0 + K1_KV + (8 * g + q) * 32 + p * 8;
    bf16x8 xf[16], wf[16];
#pragma unroll
    for (int i = 0; i < 16; ++i) {
      xf[i] = *(const bf16x8*)(xb + i * 64);
      wf[i] = tr_read8(wa + i * 1024, wa + i * 1024 + 128);
    }
    f32x4 c0 = {0.f, 0.f, 0.f, 0.f}, c1 = c0;
    unsigned off;
    if (cb & 1) {
#pragma unroll
      for (int i = 0; i < 16; i += 2) {
        c0 = __builtin_amdgcn_mfma_f32_16x16x32_bf16(xf[i], wf[i], c0, 0, 0, 0);
        c1 = __builtin_amdgcn_mfma_f32_16x16x32_bf16(xf[i + 1], wf[i + 1], c1, 0, 0, 0);
      }
      off = 65536 + ((half * 2 + (g >> 1)) * 16 + n) * 16;
    } else {
#pragma unroll
      for (int i = 0; i < 16; i += 2) {
        c0 = __builtin_amdgcn_mfma_f32_16x16x32_bf16(wf[i], xf[i], c0, 0, 0, 0);
        c1 = __builtin_amdgcn_mfma_f32_16x16x32_bf16(wf[i + 1], xf[i + 1], c1, 0, 0, 0);
      }
      off = ((g >> 1) * 32 + 16 * half + n) * 16;
    }
    c0 += c1;
    *(u32x2*)(ws + WS_K + tb * 1024 + off + (g & 1) * 8) = u32x2{pack2(f32x2{c0[0], c0[1]}), pack2(f32x2{c0[2], c0[3]})};
  } else if (wave == 4 && cb == 3) {
    unsigned k1 = route_key(rs[r], tok0 + r), k2 = 0;
    top2_wave(k1, k2, 32);
    if (lane == 0) *(u32x2*)(cand + tb * 2) = u32x2{k1, k2 | 1u};
    const u32x4 a = *(const u32x4*)(ximg + (key_tok(k1) & 31) * XS_STRIDE + lane * 16);
    const u32x4 b = *(const u32x4*)(ximg + (key_tok(k2) & 31) * XS_STRIDE + lane * 16);
    *(u32x4*)(ws + WS_CX + tb * 2048 + lane * 16) = a;
    *(u32x4*)(ws + WS_CX + tb * 2048 + 1024 + lane * 16) = b;
  } else if (wave == 3 && blockIdx.x == 64) {
    float q2[4] = {0.f, 0.f, 0.f, 0.f};
#pragma unroll
    for (int i = 0; i < 16; ++i) {
      const u32x4 w = *(const u32x4*)(smem + K1_KV + i * 1024 + lane * 16);
#pragma unroll
      for (int j = 0; j < 4; ++j) q2[j] = dot2(w[j], w[j], q2[j]);
    }
    float t2 = sum16((q2[0] + q2[1]) + (q2[2] + q2[3]));
    t2 += __shfl_xor(t2, 16);
    t2 += __shfl_xor(t2, 32);
    const float b = 34.f * sqrtf(t2);
    const unsigned lo = b < 1e30f ? __float_as_uint(fmaxf(b, 8.f) * 0x1p-103f) : 0x7f800000u;
    if (lane == 0) *(unsigned*)(ws + WS_LOW) = lo;
  } else if (blockIdx.x == 0 && tid < NCNT) {
    ((unsigned*)(ws + WS_CNT))[tid * 64] = 0;
  }
  asm volatile("s_waitcnt vmcnt(0)" ::"v"(pf[0]), "v"(pf[1]), "v"(pf[2]), "v"(pf[3]));
}

extern "C" void kernel_launch(void* const* d_in, const int* in_sizes, int n_in, void* d_out, int out_size,
                              void* d_ws, size_t ws_size, hipStream_t stream) {
  const bf16* x = (const bf16*)d_in[0];
  const bf16* wq = (const bf16*)d_in[1];
  const bf16* wk = (const bf16*)d_in[2];
  const bf16* wv = (const bf16*)d_in[3];
  const bf16* wo = (const bf16*)d_in[4];
  const bf16* wr = (const bf16*)d_in[6];
  const bf16* wg = (const bf16*)d_in[7];
  const bf16* wu = (const bf16*)d_in[8];
  const bf16* wd = (const bf16*)d_in[9];
  char* ws = (char*)d_ws;
  bf16* out = (bf16*)d_out;

  (void)hipFuncSetAttribute((const void*)k1_ln_qkv, hipFuncAttributeMaxDynamicSharedMemorySize, K1_LDS);
  (void)hipFuncSetAttribute((const void*)k2_attn, hipFuncAttributeMaxDynamicSharedMemorySize, ATT_LDS);

  k1_ln_qkv<<<256, 768, K1_LDS, stream>>>(x, wq, wk, wv, wr, ws);
  k2_attn<<<256, 768, ATT_LDS, stream>>>(ws, x, wg, wu, wd, wo, out);
}
```

```cpp
#include <hip/hip_runtime.h>
#include <hip/hip_bf16.h>

typedef __attribute__((ext_vector_type(8))) __bf16 bf16x8;
typedef __attribute__((ext_vector_type(16))) __bf16 bf16x16;
typedef __attribute__((ext_vector_type(8))) float f32x8;
typedef __attribute__((ext_vector_type(4))) __bf16 bf16x4;
typedef __attribute__((ext_vector_type(2))) __bf16 bf16x2;
typedef __attribute__((ext_vector_type(4))) short s16x4;
typedef __attribute__((ext_vector_type(8))) short s16x8;
typedef __attribute__((ext_vector_type(16))) float f32x16;
typedef __attribute__((ext_vector_type(4))) float f32x4;
typedef __attribute__((ext_vector_type(4))) unsigned u32x4;
typedef __attribute__((ext_vector_type(2))) unsigned u32x2;
typedef __attribute__((ext_vector_type(2))) float f32x2;
typedef __attribute__((ext_vector_type(16))) unsigned u32x16;
typedef __attribute__((ext_vector_type(8))) unsigned u32x8;
typedef __bf16 bf16;
#define PTR(lo, hi) (((unsigned long long)(hi) << 32) | (lo))

#define LDS_AS __attribute__((address_space(3)))
#define GLB_AS __attribute__((address_space(1)))
#define CONST_AS __attribute__((address_space(4)))
#define DI __device__ __forceinline__

constexpr int S_ = 2048, D_ = 512, H_ = 32, DH_ = 16, F_ = 1376;

constexpr size_t WS_Q = 0;
constexpr size_t WS_O = WS_Q;
constexpr size_t WS_K = 2u << 20;
constexpr size_t WS_V = WS_K + 65536;
constexpr size_t WS_ST = WS_V + 65536;
constexpr size_t WS_R = WS_ST + 16384;
constexpr size_t WS_CX = WS_R + 4096;
constexpr size_t WS_CNT = WS_CX + 131072;
constexpr int NCNT = 64 + 16;
constexpr size_t WS_FLG = WS_CNT + 64 * 256;
constexpr size_t WS_UPD = WS_CNT + NCNT * 256;
constexpr size_t WS_YP = WS_UPD + 4096;
constexpr int NFFN = 172;
constexpr size_t WS_CL = WS_YP + NFFN * 4096;
constexpr size_t WS_G = WS_CL + 256 * 256;
constexpr int NREP = 16;
constexpr size_t WS_END = WS_G + NREP * 2048;

DI float bf2f(bf16 v) { return (float)v; }
DI float rbf(float v) { return (float)(bf16)v; }
DI f32x2 unpack2(unsigned u) { return f32x2{__uint_as_float(u << 16), __uint_as_float(u & 0xffff0000u)}; }
DI unsigned pack2(f32x2 v) {
  unsigned r = __builtin_bit_cast(unsigned, __builtin_convertvector(v, bf16x2));
  asm("" : "+v"(r));
  return r;
}

DI unsigned route_key(float v, int tok) {
  const unsigned u = __float_as_uint(v) >> 16;
  const unsigned o = (u & 0x8000u) ? (~u & 0xffffu) : (u | 0x8000u);
  return (o << 16) | (unsigned)((0x7ff - tok) << 1);
}
DI int key_tok(unsigned k) { return 0x7ff - (int)((k >> 1) & 0x7ffu); }
DI int key_row(unsigned k) { return (key_tok(k) >> 5) * 2 + (int)(k & 1u); }
DI float key_val(unsigned k) {
  const unsigned o = k >> 16;
  const unsigned u = (o & 0x8000u) ? (o & 0x7fffu) : (~o & 0xffffu);
  return __uint_as_float(u << 16);
}
DI unsigned ln2(f32x2 x, f32x2 mb2, f32x2 rinv2) { return pack2(unpack2(pack2(x - mb2)) * rinv2); }
DI void top2_wave(unsigned& k1, unsigned& k2, int width) {
  for (int o = width >> 1; o > 0; o >>= 1) {
    const unsigned a1 = __shfl_xor(k1, o), a2 = __shfl_xor(k2, o);
    k2 = max(min(k1, a1), max(k2, a2));
    k1 = max(k1, a1);
  }
}

DI void glds16_raw(const void* g, unsigned lds_off) {
  asm volatile("s_mov_b32 m0, %0\n s_nop 0\n global_load_lds_dwordx4 %1, off" ::"s"(lds_off), "v"(g) : "memory", "m0");
}

DI void blds16(__amdgpu_buffer_rsrc_t rs, unsigned voff, unsigned soff, unsigned lds_off) {
  asm volatile("s_mov_b32 m0, %0\n s_nop 0\n buffer_load_dwordx4 %1, %2, %3 offen lds" ::"s"(lds_off), "v"(voff), "s"(rs), "s"(soff) : "memory", "m0");
}
DI void blds16x2(__amdgpu_buffer_rsrc_t rs, unsigned voff, unsigned soff, unsigned lds_off) {
  asm volatile("s_mov_b32 m0, %0\n s_nop 0\n buffer_load_dwordx4 %1, %2, %3 offen lds\n buffer_load_dwordx4 %1, %2, %3 offen offset:1024 lds"
               ::"s"(lds_off), "v"(voff), "s"(rs), "s"(soff) : "memory", "m0");
}
DI void blds16x4(__amdgpu_buffer_rsrc_t rs, unsigned voff, unsigned lds_off) {
  asm volatile("s_mov_b32 m0, %0\n s_nop 0\n buffer_load_dwordx4 %1, %2, 0 offen lds\n buffer_load_dwordx4 %1, %2, 0 offen offset:1024 lds\n"
               "buffer_load_dwordx4 %1, %2, 0 offen offset:2048 lds\n buffer_load_dwordx4 %1, %2, 0 offen offset:3072 lds"
               ::"s"(lds_off), "v"(voff), "s"(rs) : "memory", "m0");
}
DI void blds16x2c(__amdgpu_buffer_rsrc_t rs, unsigned voff, unsigned soff, unsigned lds_off) {
  asm volatile("s_mov_b32 m0, %0\n s_nop 0\n buffer_load_dwordx4 %1, %2, %3 offen sc1 lds\n buffer_load_dwordx4 %1, %2, %3 offen offset:1024 sc1 lds"
               ::"s"(lds_off), "v"(voff), "s"(rs), "s"(soff) : "memory", "m0");
}
DI void blds16c(__amdgpu_buffer_rsrc_t rs, unsigned voff, unsigned soff, unsigned lds_off) {
  asm volatile("s_mov_b32 m0, %0\n s_nop 0\n buffer_load_dwordx4 %1, %2, %3 offen sc1 lds" ::"s"(lds_off), "v"(voff), "s"(rs), "s"(soff) : "memory", "m0");
}
DI void count_up(__amdgpu_buffer_rsrc_t rs, unsigned one, unsigned soff) {
  asm volatile("s_mov_b64 exec, 1\n buffer_atomic_add %0, off, %1, %2\n s_mov_b64 exec, -1" ::"v"(one), "s"(rs), "s"(__builtin_amdgcn_readfirstlane(soff)) : "memory");
}
DI void fetch4(u32x4* t, unsigned voff, __amdgpu_buffer_rsrc_t rs, unsigned soff) {
  asm volatile("buffer_load_dwordx4 %0, %4, %5, %6 offen sc1\n buffer_load_dwordx4 %1, %4, %5, %6 offen offset:1024 sc1\n"
               "buffer_load_dwordx4 %2, %4, %5, %6 offen offset:2048 sc1\n buffer_load_dwordx4 %3, %4, %5, %6 offen offset:3072 sc1"
               : "+v"(t[0]), "+v"(t[1]), "+v"(t[2]), "+v"(t[3])
               : "v"(voff), "s"(rs), "s"(soff) : "memory");
}
#define LDSP(T, off) ((LDS_AS T*)(unsigned long)(off))

template <int CTRL>
DI float dpp_add(float v) {
  const int t = __builtin_amdgcn_update_dpp(0, __float_as_int(v), CTRL, 0xf, 0xf, true);
  return v + __int_as_float(t);
}
DI float sum8(float v) {
  v = dpp_add<0xB1>(v);
  v = dpp_add<0x4E>(v);
  v = dpp_add<0x141>(v);
  return v;
}

DI f32x16 pv_mfma(bf16x8 v, f32x8 p, f32x16 acc, int sel) {
  return __builtin_amdgcn_smfmac_f32_32x32x32_bf16(v, __builtin_bit_cast(bf16x16, p), acc, sel, 0, 0);
}

DI void swap32(unsigned& a, unsigned& b) {
  const u32x2 t = __builtin_amdgcn_permlane32_swap(a, b, false, false);
  a = t[0];
  b = t[1];
}
DI float max32(float v) {
  unsigned a = __float_as_uint(v), b = a;
  swap32(a, b);
  return fmaxf(__uint_as_float(a), __uint_as_float(b));
}

DI s16x4 tr_read(const char* p) {
  return __builtin_amdgcn_ds_read_tr16_b64_v4i16((LDS_AS s16x4*)p);
}
DI bf16x8 tr_read8(unsigned lo, unsigned hi) {
  const s16x4 a = __builtin_amdgcn_ds_read_tr16_b64_v4i16(LDSP(s16x4, lo)), b = __builtin_amdgcn_ds_read_tr16_b64_v4i16(LDSP(s16x4, hi));
  return __builtin_bit_cast(bf16x8, __builtin_shufflevector(a, b, 0, 1, 2, 3, 4, 5, 6, 7));
}

constexpr int XS_STRIDE = 1040;

DI float dot2(unsigned a, unsigned b, float c) {
  return __builtin_amdgcn_fdot2_f32_bf16(__builtin_bit_cast(bf16x2, a), __builtin_bit_cast(bf16x2, b), c, false);
}
DI void touch(unsigned& t, const void* p) {
  asm volatile("global_load_dword %0, %1, off" : "=v"(t) : "v"(p) : "memory");
}
DI float rbf1(float v) { return __uint_as_float(pack2(f32x2{0.f, v})); }
DI void unpack_add(u32x4 x, float c, f32x2* t) {
  float d[8];
  asm("v_dot2_f32_bf16 %0, %8, %12, %14\n v_dot2_f32_bf16 %1, %8, %13, %14\n"
      "v_dot2_f32_bf16 %2, %9, %12, %14\n v_dot2_f32_bf16 %3, %9, %13, %14\n"
      "v_dot2_f32_bf16 %4, %10, %12, %14\n v_dot2_f32_bf16 %5, %10, %13, %14\n"
      "v_dot2_f32_bf16 %6, %11, %12, %14\n v_dot2_f32_bf16 %7, %11, %13, %14\n s_nop 2"
      : "=&v"(d[0]), "=&v"(d[1]), "=&v"(d[2]), "=&v"(d[3]), "=&v"(d[4]), "=&v"(d[5]), "=&v"(d[6]), "=&v"(d[7])
      : "v"(x[0]), "v"(x[1]), "v"(x[2]), "v"(x[3]), "s"(0x00003f80u), "s"(0x3f800000u), "v"(c));
#pragma unroll
  for (int i = 0; i < 4; ++i) t[i] = f32x2{d[2 * i], d[2 * i + 1]};
}
DI float sum16(float v) { return dpp_add<0x140>(sum8(v)); }
template <int N>
DI void wait_vm() { asm volatile("s_waitcnt vmcnt(%0)" ::"n"(N) : "memory"); }

#define CODE_ADDR(sym, out)                                                                               \
  asm volatile("s_getpc_b64 s[20:21]\n s_add_u32 s20, s20, " #sym "@rel32@lo+4\n s_addc_u32 s21, s21, " #sym \
               "@rel32@hi+12\n s_mov_b64 %0, s[20:21]"                                                    \
               : "=s"(out)::"s20", "s21", "scc")

constexpr int NT = 16;
constexpr int L_RED = 131072;
constexpr int L_H = L_RED + 512;
constexpr int L_STG = L_RED + 1024;
constexpr int NC = 64 / NT;
constexpr int PSW = 50;
constexpr int NWR = 26, GP = 4;
constexpr int SPIN_MAX = 1 << 22;

constexpr int K1_RS = 60 * 1024;
constexpr int K1_X = 122 * 1024;
constexpr int L_CST = K1_X + 32 * XS_STRIDE;
constexpr int L_ST = L_CST + 64;
constexpr int L_C1 = L_CST + 320;
constexpr int L_CNT = L_C1 + 4;
constexpr int L_LOW = L_C1 + 16;
constexpr int L_F = L_CST + 576;
constexpr int F_POLL = 8, F_RS = 12, F_ST = 16;
constexpr int ALL_LDS = L_CST + 7 * 512 + 32;
static_assert(L_STG + 4 * 6144 <= L_CST && ALL_LDS <= 163840, "");
constexpr int CODE_LEN = 27000;
constexpr int QPOS = 20;
DI int list0(int sw) { return 10 + (sw & 1) * 27 + (sw >> 1) * 62 + (sw == 3); }

extern "C" __device__ unsigned long long dispatch_id_() __asm("llvm.amdgcn.dispatch.id");
template <int STEP>
DI void blds16w(__amdgpu_buffer_rsrc_t rs, unsigned voff, unsigned& soff, unsigned& lds_off) {
  asm volatile("s_mov_b32 m0, %1\n s_add_u32 %1, %1, %4\n buffer_load_dwordx4 %2, %3, %0 offen sc1 lds\n s_add_u32 %0, %0, %4\n s_and_b32 %0, %0, 0xffff"
               : "+s"(soff), "+s"(lds_off) : "v"(voff), "s"(rs), "n"(STEP) : "memory", "m0", "scc");
}
DI void lds_add(unsigned addr, unsigned v) {
  asm volatile("s_mov_b64 exec, 1\n ds_add_u32 %0, %1\n s_mov_b64 exec, -1" ::"v"(addr), "v"(v) : "memory");
}
DI void lds_wait(unsigned addr, unsigned mask, unsigned val) {
  int spin = 0;
  while ((__builtin_amdgcn_readfirstlane(*(volatile LDS_AS unsigned*)LDSP(unsigned, addr)) & mask) != val && ++spin < (1 << 28)) __builtin_amdgcn_s_sleep(1);
  asm volatile("" ::: "memory");
}

DI void flag_up(unsigned addr, u32x2 tag) {
  asm volatile("s_mov_b64 exec, 1\n ds_write_b64 %0, %1\n s_mov_b64 exec, -1" ::"v"(addr), "v"(tag) : "memory");
}
DI void flag_wait(unsigned addr, unsigned mask, u32x2 tag) {
  int spin = 0;
  for (;;) {
    const unsigned long long v = *(volatile LDS_AS unsigned long long*)LDSP(unsigned long long, addr);
    const unsigned up = (unsigned)__builtin_amdgcn_ballot_w64(v == __builtin_bit_cast(unsigned long long, tag));
    if ((up & mask) == mask || ++spin >= (1 << 28)) break;
    __builtin_amdgcn_s_sleep(1);
  }
  asm volatile("" ::: "memory");
}

extern "C" __global__ __launch_bounds__(768) void k_all(char* __restrict__ ws, const bf16* __restrict__ x,
                                               const bf16* __restrict__ wq, const bf16* __restrict__ wk,
                                               const bf16* __restrict__ wv, const bf16* __restrict__ wr,
                                               const bf16* __restrict__ wg, const bf16* __restrict__ wu,
                                               const bf16* __restrict__ wd, const bf16* __restrict__ wo,
                                               bf16* __restrict__ out) {
  u32x8 ka0;
  u32x4 ka1;
  asm volatile("s_load_dwordx8 %0, %2, 0x0\n s_load_dwordx4 %1, %2, 0x20" : "=&s"(ka0), "=&s"(ka1) : "s"(__builtin_amdgcn_kernarg_segment_ptr()));
  __builtin_amdgcn_sched_barrier(0);
  extern __shared__ __attribute__((aligned(16))) char smem[];
  char* ximg = smem + K1_X;
  float* rs = (float*)(smem + K1_RS);
  char* kl = smem;
  const unsigned lds0 = (unsigned)(size_t)(LDS_AS char*)smem;
  const int cb = blockIdx.x >> 6, tb = blockIdx.x & 63;
  const int tid = threadIdx.x, lane = tid & 63;
  const int wave = __builtin_amdgcn_readfirstlane(tid >> 6);
  const int tok0 = tb * 32;
  const int half = cb >> 1;
  u32x4 xv[4];
  unsigned pc0, pc1, pkv0, pkv1;
  unsigned long long xbase;
  {
    const unsigned cv = (lane & 49) ? 0u : 0x3f803f80u;
    *(u32x4*)(smem + (lane < 16 ? L_CST + (lane >> 1) * 512 + (lane & 1) * 16 : L_C1)) = u32x4{cv, cv, cv, cv};
    const unsigned m = -(wave >> 3);
    const unsigned row = ((tok0 + ((wave + 4 * half) & 7) * 4) & ~m) | (((blockIdx.x >> 3) * 16 + (wave & 3) * 4) & m);
    unsigned vo = (row + (lane >> 4)) * 1024 + (lane & 15) * 16;
    unsigned long long c0;
    CODE_ADDR(k_all, c0);
    c0 += min(((blockIdx.x >> 3) * 12 + wave) * 128u, CODE_LEN - 128u);
    asm volatile("s_waitcnt lgkmcnt(0)" : "+s"(ka0), "+s"(ka1), "+s"(c0), "+v"(vo)::"memory");
    ws = (char*)(GLB_AS char*)PTR(ka0[0], ka0[1]);
    wq = (const bf16*)(const GLB_AS bf16*)PTR(ka0[4], ka0[5]);
    wk = (const bf16*)(const GLB_AS bf16*)PTR(ka0[6], ka0[7]);
    wv = (const bf16*)(const GLB_AS bf16*)PTR(ka1[0], ka1[1]);
    wr = (const bf16*)(const GLB_AS bf16*)PTR(ka1[2], ka1[3]);
    const unsigned long long base = PTR((ka0[2] & ~m) | (ka0[4] & m), (ka0[3] & ~m) | (ka0[5] & m));
    xbase = base;
    asm volatile("global_load_dwordx4 %0, %6, %7\n global_load_dwordx4 %1, %6, %7 offset:256\n"
                 "global_load_dwordx4 %2, %6, %7 offset:512\n global_load_dwordx4 %3, %6, %7 offset:768\n"
                 "s_load_dword %4, %8, 0x0\n s_load_dword %5, %8, 0x40"
                 : "=&v"(xv[0]), "=&v"(xv[1]), "=&v"(xv[2]), "=&v"(xv[3]), "=&s"(pc0), "=&s"(pc1)
                 : "v"(vo), "s"(base), "s"(c0) : "memory");
    asm volatile("s_bitcmp1_b32 %6, 0\n s_cselect_b32 %0, %4, %2\n s_cselect_b32 %1, %5, %3"
                 : "=&s"(pkv0), "=&s"(pkv1) : "s"(ka0[6]), "s"(ka0[7]), "s"(ka1[0]), "s"(ka1[1]), "s"(cb) : "scc");
    __builtin_amdgcn_sched_barrier(0);
  }
  const int r = lane & 31, h = lane >> 5;
  const int n = lane & 15, g = lane >> 4, q = n >> 2, p = lane & 3;
  const __amdgpu_buffer_rsrc_t rws = __builtin_amdgcn_make_buffer_rsrc(ws, 0, WS_END, 0x00020000);
  const unsigned tag1 = (unsigned)dispatch_id_() + 0x5bd1e995u, tag0 = (unsigned)((unsigned long long)__builtin_amdgcn_dispatch_ptr() >> 6);
  const unsigned tagr = tag1 + (tag0 >> 18) * 0x9e3779b1u;
  const unsigned a_c1 = lds0 + L_C1, a_f0 = lds0 + L_F, a_f = a_f0 + r * 8;
  const u32x2 tag = {tag0, tag1};
  const int rep = ((tb & 3) * 4 + cb) * 2048;

  if (__builtin_expect(wave >= 8, 0)) {
    const int sw = wave - 8;
    u32x4 qo[2];
    u32x2 fv;
    unsigned v_f = ((list0(sw) + tb + min(lane >> 1, 26 + (sw >> 1))) & 63) * 16 + (lane & 1) * 8 + (sw >> 1) * 1024 + rep;
    unsigned v_kv = lane * 16 + WS_K + (sw >> 1) * 65536;
    unsigned v_k = lane * 64, a_q = lds0 + (list0(sw) + QPOS) * 1024 + (g >> 1) * 1024 + n * 16 + (g & 1) * 8;
    {
    const __amdgpu_buffer_rsrc_t rw = __builtin_amdgcn_make_buffer_rsrc((void*)wq, 0, 512 * 1024, 0x00020000);
    const unsigned vw = g * 8192 + n * 4, sw0 = (cb * 128 + sw * 32) * 2;
    unsigned w[16][8];
    u32x4 a[16][2];
    f32x4 acc[2][2];
#pragma unroll
    for (int i = 0; i < 4; ++i) acc[i >> 1][i & 1] = f32x4{0.f, 0.f, 0.f, 0.f};
    const char* xb = ximg + n * XS_STRIDE + 16 * g;
    bf16x8 b[16][2];
    auto fetch = [&](int ks, int i0, int i1) __attribute__((always_inline)) {
#pragma unroll
      for (int i = i0; i < i1; ++i) w[ks][i] = __builtin_amdgcn_raw_buffer_load_b32(rw, vw + (i & 3) * 1024, sw0 + ks * 32768 + (i >> 2) * 4096, 0);
      __builtin_amdgcn_sched_barrier(0);
    };
    auto sort = [&](int ks) __attribute__((always_inline)) {
#pragma unroll
      for (int i = 0; i < 4; ++i) {
        a[ks][0][i] = __builtin_amdgcn_perm(w[ks][2 * i + 1], w[ks][2 * i], 0x05040100u);
        a[ks][1][i] = __builtin_amdgcn_perm(w[ks][2 * i + 1], w[ks][2 * i], 0x07060302u);
      }
      asm volatile("" : "+v"(a[ks][0]), "+v"(a[ks][1]));
      __builtin_amdgcn_sched_barrier(0);
    };
    auto rows = [&](int ks) __attribute__((always_inline)) {
      b[ks][0] = *(const bf16x8*)(xb + ks * 64);
      b[ks][1] = *(const bf16x8*)(xb + ks * 64 + 16 * XS_STRIDE);
      __builtin_amdgcn_sched_barrier(0);
    };
    auto step = [&](int ks, int i) __attribute__((always_inline)) {
      acc[i & 1][i >> 1] = __builtin_amdgcn_mfma_f32_16x16x32_bf16(__builtin_bit_cast(bf16x8, a[ks][i & 1]), b[ks][i >> 1], acc[i & 1][i >> 1], 0, 0, 0);
      asm volatile("" : "+v"(acc[i & 1][i >> 1]));
      __builtin_amdgcn_sched_barrier(0);
    };
    fetch(0, 0, 8);
    fetch(1, 0, 8);
    fetch(2, 0, 8);
    fetch(3, 0, 8);
    fetch(4, 0, 8);
    fetch(5, 0, 8);
    sort(0);
    asm volatile("s_waitcnt lgkmcnt(0)" ::"v"(xv[0]), "v"(xv[1]), "v"(xv[2]), "v"(xv[3]), "s"(pc0), "s"(pc1));
    fetch(6, 0, 8);
    sort(1);
    fetch(7, 0, 8);
    sort(2);
    fetch(8, 0, 8);
    sort(3);
    fetch(9, 0, 8);
    sort(4);
    fetch(10, 0, 8);
    flag_wait(a_f, 0xff, tag);
    rows(0);
    fetch(11, 0, 1);
    rows(1);
    fetch(11, 1, 2);
    rows(2);
    fetch(11, 2, 3);
    rows(3);
    fetch(11, 3, 4);
    step(0, 0);
    fetch(11, 4, 5);
    step(0, 1);
    fetch(11, 5, 6);
    step(0, 2);
    fetch(11, 6, 7);
    step(0, 3);
    fetch(11, 7, 8);
    rows(4);
    step(1, 0);
    fetch(12, 0, 1);
    step(1, 1);
    fetch(12, 1, 2);
    step(1, 2);
    fetch(12, 2, 3);
    step(1, 3);
    fetch(12, 3, 4);
    rows(5);
    step(2, 0);
    fetch(12, 4, 5);
    step(2, 1);
    fetch(12, 5, 6);
    step(2, 2);
    fetch(12, 6, 7);
    step(2, 3);
    fetch(12, 7, 8);
    rows(6);
    step(3, 0);
    fetch(13, 0, 1);
    step(3, 1);
    fetch(13, 1, 2);
    step(3, 2);
    fetch(13, 2, 3);
    step(3, 3);
    fetch(13, 3, 4);
    rows(7);
    step(4, 0);
    fetch(13, 4, 5);
    step(4, 1);
    fetch(13, 5, 6);
    step(4, 2);
    fetch(13, 6, 7);
    step(4, 3);
    fetch(13, 7, 8);
    flag_up(a_f0 + (F_ST + sw) * 8, tag);
    rows(8);
    sort(5);
    step(5, 0);
    fetch(14, 0, 1);
    step(5, 1);
    fetch(14, 1, 2);
    step(5, 2);
    fetch(14, 2, 3);
    step(5, 3);
    fetch(14, 3, 4);
    rows(9);
    sort(6);
    step(6, 0);
    fetch(14, 4, 5);
    step(6, 1);
    fetch(14, 5, 6);
    step(6, 2);
    fetch(14, 6, 7);
    step(6, 3);
    fetch(14, 7, 8);
    rows(10);
    sort(7);
    step(7, 0);
    fetch(15, 0, 1);
    step(7, 1);
    fetch(15, 1, 2);
    step(7, 2);
    fetch(15, 2, 3);
    step(7, 3);
    fetch(15, 3, 4);
    rows(11);
    sort(8);
    step(8, 0);
    fetch(15, 4, 5);
    step(8, 1);
    fetch(15, 5, 6);
    step(8, 2);
    fetch(15, 6, 7);
    step(8, 3);
    fetch(15, 7, 8);
    fv = __builtin_amdgcn_raw_buffer_load_b64(rws, v_f, WS_G, 16);
    asm volatile("" : "+v"(v_kv), "+v"(v_k), "+v"(a_q));
    __builtin_amdgcn_sched_barrier(0);
    rows(12);
    sort(9);
    step(9, 0);
    step(9, 1);
    step(9, 2);
    step(9, 3);
    rows(13);
    sort(10);
    step(10, 0);
    step(10, 1);
    step(10, 2);
    step(10, 3);
    rows(14);
    sort(11);
    step(11, 0);
    step(11, 1);
    step(11, 2);
    step(11, 3);
    rows(15);
    sort(12);
    step(12, 0);
    step(12, 1);
    step(12, 2);
    step(12, 3);
    sort(13);
    step(13, 0);
    step(13, 1);
    step(13, 2);
    step(13, 3);
    sort(14);
    step(14, 0);
    step(14, 1);
    step(14, 2);
    step(14, 3);
    sort(15);
    step(15, 0);
    step(15, 1);
    step(15, 2);
    step(15, 3);
    const float sc = 0.25f * 1.4426950408889634f;
#pragma unroll
    for (int m = 0; m < 2; ++m) {
#pragma unroll
      for (int i = 0; i < 4; ++i)
        qo[m][i] = pack2(f32x2{rbf1(acc[0][m][i]), rbf1(acc[1][m][i])} * f32x2{sc, sc});
    }
    }
#pragma unroll
    for (int i = 0; i < 4; ++i) *LDSP(u32x2, a_q + (i >> 1) * 256 + (i & 1) * 512) = u32x2{qo[i >> 1][(i & 1) * 2], qo[i >> 1][(i & 1) * 2 + 1]};
    asm volatile("s_waitcnt lgkmcnt(0)\n s_barrier" ::: "memory");
    __builtin_amdgcn_s_setprio(3);
    {
      int spin = 0;
      while (__any((fv[0] ^ tag0) > 0xffu || fv[1] != tag1) && ++spin < SPIN_MAX) {
        asm volatile("s_sleep 2" ::: "memory");
        fv = __builtin_amdgcn_raw_buffer_load_b64(rws, v_f, WS_G, 16);
      }
    }
    asm volatile("" ::: "memory");
    u32x4 cv;
    asm volatile("buffer_load_dwordx4 %0, %1, %2, %3 offen sc1" : "=v"(cv) : "v"(v_k), "s"(rws), "s"((unsigned)WS_R) : "memory");
    {
      unsigned so = ((list0(sw) + tb) & 63) * 1024, lo = lds0 + list0(sw) * 1024;
#pragma unroll
      for (int i = 0; i < 27; ++i) {
        if (i == QPOS || i == QPOS + 1) continue;
        if (i == QPOS - 1) blds16w<3072>(rws, v_kv, so, lo);
        else blds16w<1024>(rws, v_kv, so, lo);
      }
      if (sw >= 2) blds16w<1024>(rws, v_kv, so, lo);
      so = ((list0(sw) + QPOS + tb) & 63) * 1024, lo = lds0 + (list0(sw) + QPOS) * 1024;
      lds_wait(a_c1, 0xf << 20, 8 << 20);
      blds16w<1024>(rws, v_kv, so, lo);
      blds16w<1024>(rws, v_kv, so, lo);
    }
    __builtin_amdgcn_s_setprio(0);
    unsigned l16 = lane * 16;
    const int fs = (blockIdx.x & 7) * 22 + (blockIdx.x >> 3);
    const bool ffn = blockIdx.x < 176 && fs < NFFN;
    const unsigned stg = lds0 + L_STG + sw * 6144;
    unsigned a_gu = stg + (p >> 1) * 2048 + (8 * g + q) * 16 + (p & 1) * 8;
    unsigned a_dn = stg + 4096 + q * 256 + (16 * g + 4 * p) * 2;
    unsigned a_rw = lds0 + L_RED + (n & 7) * 32 + sw * 8 + (n >> 3) * 4;
    unsigned a_rr = lds0 + L_RED + n * 32;
    unsigned a_hw = lds0 + L_H + sw * 32 + n * 2;
    const unsigned hrow = lds0 + L_H + sw * 32 + (n >> 3) * 16, zrow = lds0 + L_CST + 16;
    unsigned a_h0 = (p == g && (q & 1) == 0) ? hrow : zrow, a_h1 = (p == g && (q & 1) == 1) ? hrow : zrow;
    unsigned a_cnt = lds0 + L_CNT, one = 1, odd = lane & 1, even = (lane & 1) ^ 1, g16 = g * 16;
    unsigned v_w = (lane >> 2) * 1024 + (lane & 3) * 16;
    unsigned a_tw = stg + lane * 16, a_tr = stg + (4 * h + q) * 64 + (g & 1) * 32 + p * 8, a_x = lds0 + lane * 16;
    asm volatile("" : "+v"(a_tw), "+v"(a_tr), "+v"(a_x));
    unsigned v_x = r * 1024 + h * 8, v_s = lds0 + L_ST + r * 8;
    unsigned v_y = lane * 4096, v_y2 = lane < NFFN - 128 ? lane * 4096 + 128 * 4096 : 0x7ffffff0u, v_c = n * 256, v_e = n < 12 ? 11 : 10;
    asm volatile("" : "+v"(l16), "+v"(a_gu), "+v"(a_dn), "+v"(a_rw), "+v"(a_rr), "+v"(a_hw), "+v"(a_h0), "+v"(a_h1),
                 "+v"(a_cnt), "+v"(one), "+v"(odd), "+v"(even), "+v"(g16));
    unsigned vg = lane * (F_ * 2), vd = g * 1024 + n * 16;
    asm volatile("" : "+v"(v_w), "+v"(v_x), "+v"(v_s), "+v"(v_y), "+v"(v_y2), "+v"(v_c), "+v"(v_e), "+v"(vg), "+v"(vd));
    const CONST_AS unsigned long long* ka = (const CONST_AS unsigned long long*)__builtin_amdgcn_kernarg_segment_ptr();
    asm volatile("" : "+s"(ka));
    const unsigned long long px = ka[1], pg = ka[6], pu = ka[7], pd = ka[8], po = ka[9], pout = ka[10];
    asm volatile("s_waitcnt vmcnt(0)\n s_barrier" : "+v"(cv)::"memory");
    __builtin_amdgcn_s_setprio(3);
    {
      int spin = 0;
      while (__any(cv[1] != tagr || cv[3] != ~tagr) && ++spin < SPIN_MAX) {
        asm volatile("s_sleep 2" ::: "memory");
        cv = __builtin_amdgcn_raw_buffer_load_b128(rws, v_k, WS_R, 16);
      }
    }
    unsigned k1 = cv[0], k2 = cv[2];
    top2_wave(k1, k2, 64);
    k1 = __builtin_amdgcn_readfirstlane(k1);
    k2 = __builtin_amdgcn_readfirstlane(k2);
    asm volatile("" : "+s"(k1), "+s"(k2));
    __builtin_amdgcn_s_setprio(0);
    if (ffn) {
      const __amdgpu_buffer_rsrc_t rg = __builtin_amdgcn_make_buffer_rsrc((void*)pg, 0, 512 * F_ * 2, 0x00020000);
      const __amdgpu_buffer_rsrc_t ru = __builtin_amdgcn_make_buffer_rsrc((void*)pu, 0, 512 * F_ * 2, 0x00020000);
      const __amdgpu_buffer_rsrc_t rd = __builtin_amdgcn_make_buffer_rsrc((void*)pd, 0, 512 * F_ * 2, 0x00020000);
      const unsigned so = (sw * 128 * F_ + fs * 8) * 2, sd = (fs * 8 * 512 + sw * 128) * 2;
      blds16(rg, vg, so, stg);
      blds16(rg, vg, so + 64 * F_ * 2, stg + 1024);
      blds16(ru, vg, so, stg + 2048);
      blds16(ru, vg, so + 64 * F_ * 2, stg + 3072);
      blds16(rd, vd, sd, stg + 4096);
      blds16(rd, vd, sd + 4096, stg + 5120);
    }
    if (ffn) {
      u32x4 xf[4];
      __builtin_amdgcn_s_setprio(3);
      unsigned vx = __umul24(even, key_row(k1) * 1024) + g16;
      vx += __umul24(odd, key_row(k2) * 1024);
      asm volatile("" : "+v"(vx));
      __builtin_amdgcn_s_setprio(0);
#pragma unroll
      for (int i = 0; i < 4; ++i) xf[i] = __builtin_amdgcn_raw_buffer_load_b128(rws, vx + i * 64, WS_CX + sw * 256, 16);
      asm volatile("s_waitcnt vmcnt(0)" ::: "memory");
      bf16x8 wf[4], df[2];
#pragma unroll
      for (int i = 0; i < 4; ++i) wf[i] = tr_read8(a_gu + i * 512, a_gu + i * 512 + 64);
#pragma unroll
      for (int i = 0; i < 2; ++i) df[i] = tr_read8(a_dn + i * 128, a_dn + i * 128 + 1024);
      asm volatile("s_waitcnt lgkmcnt(0)" ::: "memory");
      __builtin_amdgcn_s_setprio(3);
      f32x4 gu = {0.f, 0.f, 0.f, 0.f};
#pragma unroll
      for (int i = 0; i < 4; ++i) gu = __builtin_amdgcn_mfma_f32_16x16x32_bf16(__builtin_bit_cast(bf16x8, xf[i]), wf[i], gu, 0, 0, 0);
      *LDSP(float, a_rw) = gu[0];
      *LDSP(float, a_rw + 256) = gu[1];
      asm volatile("s_mov_b64 exec, 1\n ds_add_u32 %0, %1\n s_mov_b64 exec, -1" ::"v"(a_cnt), "v"(one) : "memory");
      while (*(volatile LDS_AS unsigned*)LDSP(unsigned, a_cnt) < 4) __builtin_amdgcn_s_sleep(1);
      asm volatile("" ::: "memory");
      const f32x4 r0 = *LDSP(f32x4, a_rr), r1 = *LDSP(f32x4, a_rr + 16);
      const f32x2 gs = unpack2(pack2((f32x2{r0[0], r0[1]} + f32x2{r0[2], r0[3]}) + (f32x2{r1[0], r1[1]} + f32x2{r1[2], r1[3]})));
      const float hv = rbf(gs.x * __builtin_amdgcn_rcpf(1.f + __expf(-gs.x))) * gs.y;
      *LDSP(bf16, a_hw) = (bf16)hv;
      asm volatile("" ::: "memory");
      const bf16x8 h0 = *LDSP(bf16x8, a_h0), h1 = *LDSP(bf16x8, a_h1);
      f32x4 y = {0.f, 0.f, 0.f, 0.f};
      y = __builtin_amdgcn_mfma_f32_16x16x32_bf16(h0, df[0], y, 0, 0, 0);
      y = __builtin_amdgcn_mfma_f32_16x16x32_bf16(h1, df[1], y, 0, 0, 0);
      __builtin_amdgcn_raw_buffer_store_b128(__builtin_bit_cast(u32x4, y), rws, l16, WS_YP + fs * 4096 + sw * 1024, 16);
      __builtin_amdgcn_s_setprio(0);
      asm volatile("s_waitcnt vmcnt(0)" ::: "memory");
      count_up(rws, one, WS_CNT + (sw * 16 + (fs & 15)) * 256);
    }
    const unsigned so_w = (cb * 128 + sw * 32) * 2, so_x = tok0 * 1024 + so_w;
    const __amdgpu_buffer_rsrc_t rout = __builtin_amdgcn_make_buffer_rsrc((void*)pout, 0, S_ * D_ * 2, 0x00020000);
    u32x4 wf[NWR];
    u32x2 xw[4], nv[4], sxr;
    {
      const __amdgpu_buffer_rsrc_t rw = __builtin_amdgcn_make_buffer_rsrc((void*)po, 0, 512 * 1024, 0x00020000);
      const __amdgpu_buffer_rsrc_t rx = __builtin_amdgcn_make_buffer_rsrc((void*)px, 0, S_ * D_ * 2, 0x00020000);
#pragma unroll
      for (int i = 0; i < NWR; ++i) wf[i] = __builtin_amdgcn_raw_buffer_load_b128(rw, v_w, i * 16384 + so_w, 0);
#pragma unroll
      for (int i = 0; i < 4; ++i) xw[i] = __builtin_amdgcn_raw_buffer_load_b64(rx, v_x + i * 16, so_x, 0);
      sxr = *LDSP(u32x2, v_s);
      if (wave == 11) {
        const unsigned t = __builtin_amdgcn_raw_buffer_load_b32(rout, 0, so_x, 0);
        const unsigned t2 = __builtin_amdgcn_raw_buffer_load_b32(rws, v_c, WS_CL + tb * 1024, 16);
        asm volatile("" ::"v"(t), "v"(t2));
      }
#pragma unroll
      for (int i = 0; i < NWR; ++i) {
        *LDSP(u32x4, a_tw + (i % 6) * 1024) = wf[i];
        wf[i] = __builtin_bit_cast(u32x4, tr_read8(a_tr + (i % 6) * 1024, a_tr + (i % 6) * 1024 + 512));
      }
#pragma unroll
      for (int i = 0; i < NWR; ++i) asm volatile("" : "+v"(wf[i]));
#pragma unroll
      for (int i = NWR; i < 32; ++i) blds16(rw, v_w, i * 16384 + so_w, stg + (i - NWR) * 1024);
    }
    if (cb == 3) {
      const int sel = (blockIdx.x >> 3) & 1, cq = blockIdx.x & 7, nn = (blockIdx.x >> 4 & 3) * 4 + sw;
      __builtin_amdgcn_s_setprio(3);
      int spin = 0;
      while (__any(__builtin_amdgcn_raw_buffer_load_b32(rws, v_c, WS_CNT + (cq >> 1) * 4096, 16) < v_e) && ++spin < SPIN_MAX)
        asm volatile("s_sleep 8" ::: "memory");
      __builtin_amdgcn_s_setprio(0);
      asm volatile("" ::: "memory");
      const unsigned so = WS_YP + (cq >> 1) * 1024 + (2 * sel + (cq & 1)) * 256 + nn * 16;
      const u32x4 t0 = __builtin_amdgcn_raw_buffer_load_b128(rws, v_y, so, 16);
      const u32x4 t1 = __builtin_amdgcn_raw_buffer_load_b128(rws, v_y, so + 64 * 4096, 16);
      const u32x4 t2 = __builtin_amdgcn_raw_buffer_load_b128(rws, v_y2, so, 16);
      __builtin_amdgcn_s_setprio(3);
      const float gate = rbf(__builtin_amdgcn_rcpf(1.f + __expf(-key_val(sel ? k2 : k1))));
      f32x4 a = (__builtin_bit_cast(f32x4, t0) + __builtin_bit_cast(f32x4, t1)) + __builtin_bit_cast(f32x4, t2);
#pragma unroll
      for (int i = 0; i < 4; ++i) a[i] = sum16(a[i]);
      asm volatile("" : "+v"(a));
      float u = p == 0 ? a[0] : p == 1 ? a[1] : p == 2 ? a[2] : a[3];
      u += __shfl_xor(u, 16);
      u += __shfl_xor(u, 32);
      u = rbf(rbf(u) * gate);
      if (lane < 4) __builtin_amdgcn_raw_buffer_store_b32(__float_as_uint(u), rws, lane * 64, WS_UPD + (sel * 512 + cq * 64 + nn) * 4, 16);
      __builtin_amdgcn_s_setprio(0);
      asm volatile("s_waitcnt vmcnt(0)" ::: "memory");
      count_up(rws, one, WS_FLG + (sel * 8 + cq) * 256);
    }
    __builtin_amdgcn_s_setprio(3);
    {
      const f32x2 mb2 = {__uint_as_float(sxr[0]), __uint_as_float(sxr[0])}, ri2 = {__uint_as_float(sxr[1]), __uint_as_float(sxr[1])};
#pragma unroll
      for (int i = 0; i < 4; ++i) {
#pragma unroll
        for (int jj = 0; jj < 2; ++jj) nv[i][jj] = ln2(unpack2(xw[i][jj]), mb2, ri2);
      }
    }
    if ((key_tok(k1) >> 5) == tb || (key_tok(k2) >> 5) == tb) {
      const int tok = tok0 + r;
      const int rsel = tok == key_tok(k1) ? 0 : tok == key_tok(k2) ? 1 : -1;
      if (rsel >= 0) {
        int spin = 0;
        while (__builtin_amdgcn_raw_buffer_load_b32(rws, rsel * 2048, WS_FLG + (so_w >> 7) * 256, 16) < 16 && ++spin < SPIN_MAX)
          asm volatile("s_sleep 4" ::: "memory");
        asm volatile("" ::: "memory");
#pragma unroll
        for (int i = 0; i < 4; ++i) {
          const f32x4 u = __builtin_bit_cast(f32x4, __builtin_amdgcn_raw_buffer_load_b128(rws, (rsel * 512 + 8 * i + 4 * h) * 4, WS_UPD + so_w * 2, 16));
          nv[i][0] = pack2(unpack2(nv[i][0]) + f32x2{u[0], u[1]});
          nv[i][1] = pack2(unpack2(nv[i][1]) + f32x2{u[2], u[3]});
        }
      }
    }
#pragma unroll
    for (int i = 0; i < 4; ++i) asm volatile("" : "+v"(nv[i]), "+v"(xw[i]));
    __builtin_amdgcn_s_setprio(0);
    asm volatile("s_waitcnt vmcnt(0)\n s_barrier" ::: "memory");
    f32x16 acc;
#pragma unroll
    for (int i = 0; i < 16; ++i) acc[i] = 0.f;
    unsigned a_ow = lds0 + 32768 + sw * 4096 + r * 80 + h * 8, a_or = lds0 + 32768 + sw * 4096 + (lane >> 2) * 80 + p * 16;
    unsigned v_o = (lane >> 2) * 1024 + p * 16;
    asm volatile("" : "+v"(acc), "+v"(a_ow), "+v"(a_or), "+v"(v_o));
    asm volatile("s_barrier" ::: "memory");
    {
      bf16x8 b[32], wl[32 - NWR];
#pragma unroll
      for (int ks = 0; ks < 32 + GP; ++ks) {
        if (ks < 32) b[ks] = *LDSP(bf16x8, a_x + ks * 1024);
        if (ks >= NWR && ks < 32) wl[ks - NWR] = tr_read8(a_tr + (ks - NWR) * 1024, a_tr + (ks - NWR) * 1024 + 512);
        if (ks >= GP) {
          const int k = ks - GP;
          acc = __builtin_amdgcn_mfma_f32_32x32x16_bf16(k >= NWR ? wl[k - NWR] : __builtin_bit_cast(bf16x8, wf[k]), b[k], acc, 0, 0, 0);
          asm volatile("" : "+v"(acc));
        }
        __builtin_amdgcn_sched_barrier(0);
      }
    }
#pragma unroll
    for (int g = 0; g < 4; ++g) {
      u32x2 o;
#pragma unroll
      for (int jj = 0; jj < 2; ++jj) {
        const unsigned xr = xw[g][jj], nr = nv[g][jj];
        const f32x2 a = unpack2(pack2(f32x2{acc[4 * g + 2 * jj], acc[4 * g + 2 * jj + 1]}));
        o[jj] = pack2(unpack2(pack2(unpack2(xr) + a)) + unpack2(nr));
      }
      *LDSP(u32x2, a_ow + g * 16) = o;
    }
    asm volatile("" ::: "memory");
    {
      const u32x4 o0 = *LDSP(u32x4, a_or), o1 = *LDSP(u32x4, a_or + 16 * 80);
      __builtin_amdgcn_raw_buffer_store_b128(o0, rout, v_o, so_x, 18);
      __builtin_amdgcn_raw_buffer_store_b128(o1, rout, v_o, so_x + 16384, 18);
    }
    return;
  }

  int tl = ((wave + 4 * half) & 7) * 4 + g;
  unsigned pf = 0;
  unsigned long long pv0 = 0, pv1 = 0, pv2 = 0;
  if (wave == 7) {
    __builtin_amdgcn_s_setprio(3);
    const __amdgpu_buffer_rsrc_t rkv = __builtin_amdgcn_make_buffer_rsrc((void*)(GLB_AS void*)PTR(pkv0, pkv1), 0, 16384, 0x00020000);
#pragma unroll
    for (int i = 0; i < 4; ++i) blds16x4(rkv, lane * 16 + i * 4096, lds0 + (i & 1) * 4096 + (i >> 1) * 65536);
    unsigned o0 = WS_CL + tb * 1024 + cb * 256, o1 = blockIdx.x < NCNT ? WS_CNT + blockIdx.x * 256 : 0x7ffffff0u;
    asm volatile("" : "+s"(o0), "+s"(o1));
    __builtin_amdgcn_raw_buffer_store_b32(0u, rws, lane == 0 ? o0 : lane == 1 ? o1 : 0x7ffffff0u, 0, 16);
    unsigned wa = lds0 + (8 * g + q) * 32 + p * 8, wb = wa + 65536, xb = lds0 + K1_X + (16 * half + n) * XS_STRIDE + 16 * g;
    unsigned off = 65536 + ((half * 2 + (g >> 1)) * 16 + n) * 16 + (g & 1) * 8, offk = ((g & 1) * 32 + 16 * half + n) * 16 + (g >> 1) * 8;
    asm volatile("" : "+v"(off), "+v"(offk));
    off = (cb & 1) ? off : offk;
    asm volatile("s_waitcnt vmcnt(1)" : "+v"(wa), "+v"(wb), "+v"(xb), "+v"(off) : "v"(xv[0]), "v"(xv[1]), "v"(xv[2]), "v"(xv[3]) : "memory");
    bf16x8 wf[16], xf[16];
#pragma unroll
    for (int i = 0; i < 16; ++i) wf[i] = tr_read8((i < 8 ? wa : wb) + (i & 7) * 1024, (i < 8 ? wa : wb) + (i & 7) * 1024 + 128);
    f32x4 c0 = {0.f, 0.f, 0.f, 0.f}, c1 = c0;
#pragma unroll
    for (int i = 0; i < 16; ++i) asm volatile("" : "+v"(wf[i]));
    flag_wait(a_f, 0xf, tag);
#pragma unroll
    for (int i = 0; i < 8; ++i) xf[i] = *LDSP(bf16x8, xb + i * 64);
    if (cb & 1) {
#pragma unroll
      for (int i = 0; i < 16; i += 2) {
        if (i < 8) xf[i + 8] = *LDSP(bf16x8, xb + i * 64 + 512), xf[i + 9] = *LDSP(bf16x8, xb + i * 64 + 576);
        c0 = __builtin_amdgcn_mfma_f32_16x16x32_bf16(xf[i], wf[i], c0, 0, 0, 0);
        c1 = __builtin_amdgcn_mfma_f32_16x16x32_bf16(xf[i + 1], wf[i + 1], c1, 0, 0, 0);
        asm volatile("" : "+v"(c0), "+v"(c1));
        __builtin_amdgcn_sched_barrier(0);
      }
    } else {
#pragma unroll
      for (int i = 0; i < 16; i += 2) {
        if (i < 8) xf[i + 8] = *LDSP(bf16x8, xb + i * 64 + 512), xf[i + 9] = *LDSP(bf16x8, xb + i * 64 + 576);
        c0 = __builtin_amdgcn_mfma_f32_16x16x32_bf16(wf[i], xf[i], c0, 0, 0, 0);
        c1 = __builtin_amdgcn_mfma_f32_16x16x32_bf16(wf[i + 1], xf[i + 1], c1, 0, 0, 0);
        asm volatile("" : "+v"(c0), "+v"(c1));
        __builtin_amdgcn_sched_barrier(0);
      }
    }
    c0 += c1;
    __builtin_amdgcn_raw_buffer_store_b64(u32x2{pack2(f32x2{c0[0], c0[1]}), pack2(f32x2{c0[2], c0[3]})}, rws, off, WS_K + tb * 1024, 16);
    __builtin_amdgcn_sched_barrier(0);
    unsigned tagv;
    {
      f32x4 gm = {0.f, 0.f, 0.f, 0.f};
#pragma unroll
      for (int i = 0; i < 16; ++i) gm = __builtin_amdgcn_mfma_f32_16x16x32_bf16(wf[i], wf[i], gm, 0, 0, 0);
      unsigned e = max(max(__float_as_uint(fabsf(gm[0])), __float_as_uint(fabsf(gm[1]))), max(__float_as_uint(fabsf(gm[2])), __float_as_uint(fabsf(gm[3]))));
      e = max(e, (unsigned)__builtin_amdgcn_update_dpp(0, e, 0xB1, 0xf, 0xf, true));
      e = max(e, (unsigned)__builtin_amdgcn_update_dpp(0, e, 0x4E, 0xf, 0xf, true));
      e = max(e, (unsigned)__builtin_amdgcn_update_dpp(0, e, 0x141, 0xf, 0xf, true));
      e = max(e, (unsigned)__builtin_amdgcn_update_dpp(0, e, 0x140, 0xf, 0xf, true));
      e = max(max(__builtin_amdgcn_readlane(e, 0), __builtin_amdgcn_readlane(e, 16)), max(__builtin_amdgcn_readlane(e, 32), __builtin_amdgcn_readlane(e, 48)));
      e >>= 23;
      tagv = (tag0 & ~0xffu) | (e == 255 ? 255u : max((e + 1) >> 1, 60u) - 33u);
      asm volatile("" : "+s"(tagv));
    }
    asm volatile("s_waitcnt lgkmcnt(0)" ::"s"(pc0), "s"(pc1));
    unsigned v_g = lane < NREP ? lane * 2048 : 0x7ffffff0u, so_g = WS_G + (cb & 1) * 1024 + tb * 16 + half * 8;
    u32x2 tv = {tagv, tag1};
    asm volatile("s_waitcnt vmcnt(0)" : "+v"(v_g), "+s"(so_g), "+v"(tv)::"memory");
    __builtin_amdgcn_raw_buffer_store_b64(tv, rws, v_g, so_g, 16);
    {
      const unsigned v_f = r < 20 - 4 * h ? (((lane >> 1 & 15) + tb) & 63) * 16 + (lane & 1) * 8 + h * 1024 + rep : tb * 16 + rep;
      const unsigned long long want = __builtin_bit_cast(unsigned long long, tag) >> 8;
      unsigned long long seen;
      flag_wait(a_f, 0xf << F_ST, tag);
#define LOOK(v) "buffer_load_dwordx2 " v ", %6, %7, %8 offen sc1\n s_sleep 2\n"
#define SEEN(v) "s_waitcnt vmcnt(2)\n v_lshrrev_b64 %3, 8, " v "\n v_lshlrev_b64 %4, 55, " v "\n v_cmp_ne_u64 vcc, %9, %3\n s_cbranch_vccz 2f\n"
      unsigned long long tmp;
      unsigned spin = SPIN_MAX;
      asm volatile(LOOK("%0") LOOK("%1") "1:\n" LOOK("%2") SEEN("%0") LOOK("%0") SEEN("%1") LOOK("%1") SEEN("%2")
                   "s_sub_u32 %5, %5, 1\n s_cbranch_scc0 1b\n 2:"
                   : "=&v"(pv0), "=&v"(pv1), "=&v"(pv2), "=&v"(tmp), "=&v"(seen), "+s"(spin)
                   : "v"(v_f), "s"(rws), "s"((unsigned)WS_G), "s"(want) : "memory", "vcc", "scc");
      flag_up(a_f0 + F_POLL * 8, tag);
      if (lane == 32) *(unsigned*)(smem + L_LOW) = (unsigned)(seen >> 32) & 0x7f800000u;
    }
  } else {
  auto ln_rows = [&](const u32x4* xv, int tl, unsigned a_fl, float z) __attribute__((always_inline)) {
  float s[4] = {z, z, z, z};
#pragma unroll
  for (int i = 0; i < 16; ++i) s[i & 3] = dot2(xv[i >> 2][i & 3], 0x3f803f80u, s[i & 3]);
  const float ssum = (s[0] + s[1]) + (s[2] + s[3]);
  const float mean = sum16(ssum) * (1.f / 512.f);
  const float mb = rbf1(mean), nmb = -mb;
  f32x2 t[16], v2[2] = {{0.f, 0.f}, {0.f, 0.f}};
#pragma unroll
  for (int i = 0; i < 4; ++i) unpack_add(xv[i], nmb, t + 4 * i);
#pragma unroll
  for (int i = 0; i < 16; ++i) v2[i & 1] += t[i] * t[i];
  const float dm = mean - mb;
  const float var = fmaxf(sum16((v2[0].x + v2[0].y) + (v2[1].x + v2[1].y)) * (1.f / 512.f) - dm * dm, 0.f);
#pragma unroll
  for (int i = 0; i < 16; ++i) t[i] = f32x2{rbf1(t[i].x), rbf1(t[i].y)};
  const float den = rbf1(sqrtf(rbf1(rbf1(var) + 1e-5f)));
  const float rinv = __builtin_amdgcn_rcpf(den);
  const f32x2 rinv2 = {rinv, rinv};
  u32x4 o[4];
#pragma unroll
  for (int i = 0; i < 4; ++i) {
#pragma unroll
    for (int j = 0; j < 4; ++j) o[i][j] = pack2(t[4 * i + j] * rinv2);
    *(u32x4*)(ximg + tl * XS_STRIDE + (n + 16 * i) * 16) = o[i];
  }
  flag_up(a_fl, tag);
  if (n == 0) *(f32x2*)(smem + L_ST + tl * 8) = f32x2{mb, rinv};
  };
  u32x4 xw[4];
  float z;
  if (wave < 4) __builtin_amdgcn_s_setprio(3);
  if (wave == 3) {
    asm volatile("global_load_dwordx4 %0, %4, %5\n global_load_dwordx4 %1, %4, %5 offset:256\n"
                 "global_load_dwordx4 %2, %4, %5 offset:512\n global_load_dwordx4 %3, %4, %5 offset:768\n s_waitcnt vmcnt(4)"
                 : "=&v"(xw[0]), "=&v"(xw[1]), "=&v"(xw[2]), "=&v"(xw[3]) : "v"((tok0 + (tl ^ 16)) * 1024 + n * 16), "s"(xbase) : "memory");
  } else {
    wait_vm<0>();
  }
  asm volatile("v_mov_b32 %0, 0" : "=v"(z)::"memory");
  ln_rows(xv, tl, a_f0 + wave * 8, z);
  if (wave == 3) {
    __builtin_amdgcn_s_setprio(2);
    asm volatile("s_waitcnt vmcnt(0)\n v_mov_b32 %0, 0" : "=v"(z)::"memory");
    ln_rows(xw, tl ^ 16, a_f0 + 56, z);
  }
  asm volatile("s_waitcnt lgkmcnt(0)" ::"s"(pc0), "s"(pc1));
  __builtin_amdgcn_s_setprio(0);
  if (cb == 3 && wave < 5 && wave != 3) {
    const int w = min(wave, 3);
    u32x4 wrv[4], o[8];
#pragma unroll
    for (int i = 0; i < 4; ++i) wrv[i] = *(const u32x4*)(wr + i * 128 + n * 8);
    flag_wait(a_f, 17 << w, tag);
#pragma unroll
    for (int i = 0; i < 8; ++i) o[i] = *(const u32x4*)(ximg + (w * 4 + g + (i >> 2) * 16) * XS_STRIDE + (n + 16 * (i & 3)) * 16);
    float rd = 0.f, rd2 = 0.f;
#pragma unroll
    for (int i = 0; i < 16; ++i) {
      rd = dot2(o[i >> 2][i & 3], wrv[i >> 2][i & 3], rd);
      rd2 = dot2(o[4 + (i >> 2)][i & 3], wrv[i >> 2][i & 3], rd2);
    }
    rd = sum16(rd);
    rd2 = sum16(rd2);
    if (n == 0) {
      rs[w * 4 + g] = rbf1(rd);
      rs[w * 4 + g + 16] = rbf1(rd2);
    }
    flag_up(a_f0 + (F_RS + w) * 8, tag);
    if (wave == 4) {
    flag_wait(a_f, 0xf << F_RS, tag);
    unsigned k1 = route_key(rs[r], tok0 + r), k2 = 0;
    top2_wave(k1, k2, 32);
    const u32x4 a = *(const u32x4*)(ximg + (key_tok(k1) & 31) * XS_STRIDE + lane * 16);
    const u32x4 b = *(const u32x4*)(ximg + (key_tok(k2) & 31) * XS_STRIDE + lane * 16);
    __builtin_amdgcn_raw_buffer_store_b128(a, rws, lane * 16, WS_CX + tb * 2048, 16);
    __builtin_amdgcn_raw_buffer_store_b128(b, rws, lane * 16, WS_CX + tb * 2048 + 1024, 16);
    asm volatile("s_waitcnt vmcnt(0)" ::: "memory");
    if (lane < 2) __builtin_amdgcn_raw_buffer_store_b64(lane ? u32x2{k2 | 1u, ~tagr} : u32x2{k1, tagr}, rws, lane * 8, WS_R + tb * 64, 16);
    }
  } else if (wave == 6) {
    touch(pf, ws + WS_O + tb * 32768 + cb * 8192);
  }
  }
  __builtin_amdgcn_s_setprio(0);
  const int qs = list0(wave >> 1) + QPOS + (wave & 1);
  {
    if (wave != 7) flag_wait(a_f, 1 << F_POLL, tag);
    const unsigned o = ((wave + tb) & 63) * 1024;
    blds16c(rws, lane * 16, WS_K + o, lds0 + wave * 1024);
    blds16c(rws, lane * 16, WS_V + o, lds0 + 65536 + wave * 1024);
    if (wave < 2) blds16c(rws, lane * 16, WS_K + ((8 + wave + tb) & 63) * 1024, lds0 + (8 + wave) * 1024);
  }
  int vo = (r < 16) ? 65536 + (h * 16 + r) * 16 : L_CST + ((r & 11) == 0 ? 0 : 16);
  int vstep = (r < 16) ? 4096 : 0;
  int sel = 0xdddd;
  f32x16 zero;
#pragma unroll
  for (int i = 0; i < 16; ++i) zero[i] = 0.f;
  f32x16 acc = zero;
  asm volatile("" : "+v"(sel), "+v"(acc), "+v"(vstep), "+v"(vo));
  const char* kp0 = kl + lane * 16;
  const char* vp0 = smem + vo;
  asm volatile("s_waitcnt vmcnt(0) lgkmcnt(0)\n s_barrier" ::"v"(pf), "v"(pv0), "v"(pv1), "v"(pv2) : "memory");
  bf16x8 qf = *(const bf16x8*)(smem + qs * 1024 + lane * 16);
  unsigned low = *(const unsigned*)(smem + L_LOW);

  constexpr float HEAD = 90.f;
  f32x16 negm, xs;
  bf16x8 kfn;
  {
    bf16x8 kf[4];
    f32x16 s0[4];
#pragma unroll
    for (int t = 0; t < 4; ++t) kf[t] = *(const bf16x8*)(kp0 + t * 1024);
    asm volatile("" : "+v"(qf), "+v"(low));
    lds_add(a_c1, 1 << 20);
#pragma unroll
    for (int t = 0; t < 4; ++t) {
      s0[t] = __builtin_amdgcn_mfma_f32_32x32x16_bf16(kf[t], qf, zero, 0, 0, 0);
      asm volatile("" : "+v"(s0[t]));
    }
    __builtin_amdgcn_sched_barrier(0);
    float mx = -INFINITY;
#pragma unroll
    for (int t = 0; t < 4; ++t) {
#pragma unroll
      for (int i = 0; i < 16; i += 2) mx = fmaxf(fmaxf(mx, s0[t][i]), s0[t][i + 1]);
    }
    const float nm = -(floorf(max32(mx)) + HEAD);
#pragma unroll
    for (int i = 0; i < 16; ++i) {
      xs[i] = s0[0][i] + nm;
      negm[i] = nm;
    }
    kfn = kf[1];
  }
  asm volatile("" : "+v"(negm));
  {
    const char* kp = kp0;
    const char* vp = vp0;
    f32x16 x = xs;
    if (wave < 4) __builtin_amdgcn_s_setprio(2);
    else __builtin_amdgcn_s_setprio(1);
    for (int c = 0; c < NC; ++c) {
      bool grown;
#pragma unroll
      for (int t = 0; t < NT; ++t) {
        const bf16x8 kf = kfn;
        kfn = *(const bf16x8*)(kp + (t + 2) * 1024);
        const bf16x8 vf0 = *(const bf16x8*)(vp + ((t & 3) * 2) * 512);
        const bf16x8 vf1 = *(const bf16x8*)(vp + ((t & 3) * 2 + 1) * 512);
        if ((t & 3) == 3) vp += vstep;
        f32x8 p0, p1;
#pragma unroll
        for (int j = 0; j < 4; ++j) p0[j] = __builtin_amdgcn_exp2f(x[j]);
        __builtin_amdgcn_sched_barrier(0);
        f32x16 xn = __builtin_amdgcn_mfma_f32_32x32x16_bf16(kf, qf, negm, 0, 0, 0);
        asm volatile("" : "+v"(xn));
        __builtin_amdgcn_sched_barrier(0);
#pragma unroll
        for (int j = 4; j < 8; ++j) p0[j] = __builtin_amdgcn_exp2f(x[j]);
        if (t == NT - 1) grown = __any(!(acc[8] <= 4.3e9f));
        acc = pv_mfma(vf0, p0, acc, sel);
#pragma unroll
        for (int j = 0; j < 8; ++j) p1[j] = __builtin_amdgcn_exp2f(x[8 + j]);
        acc = pv_mfma(vf1, p1, acc, sel);
        if (t == 7 && c == 0) asm volatile("s_barrier" ::: "memory");
        if (c * NT + t == PSW && wave < 4) __builtin_amdgcn_s_setprio(0);
        __builtin_amdgcn_sched_barrier(0);
        x = xn;
      }
      kp += NT * 1024;
      if (grown) {
        const int e = max(__builtin_amdgcn_frexp_expf(acc[8]) - 1 + (int)HEAD, 0);
#pragma unroll
        for (int i = 0; i < 9; ++i) acc[i] = __builtin_amdgcn_ldexpf(acc[i], -e);
        const float ef = (float)e;
#pragma unroll
        for (int i = 0; i < 16; ++i) {
          negm[i] -= ef;
          x[i] -= ef;
        }
        asm volatile("" : "+v"(negm));
      }
    }
  }
  {
    float chk = ((fabsf(acc[0]) + fabsf(acc[1])) + (fabsf(acc[2]) + fabsf(acc[3]))) +
                ((fabsf(acc[4]) + fabsf(acc[5])) + (fabsf(acc[6]) + fabsf(acc[7])));
    chk = __builtin_fmaf(acc[8], 0.f, chk);
    if (__any(__float_as_uint(chk) - low >= 0x7f800000u - low)) {
      f32x16 oacc = zero;
      float mrun = -INFINITY;
      const char* kp = kp0;
      const char* vp = vp0;
      for (int t = 0; t < 64; ++t) {
        bf16x8 kf = *(const bf16x8*)(kp);
        f32x16 x = __builtin_amdgcn_mfma_f32_32x32x16_bf16(kf, qf, zero, 0, 0, 0);
        float mx = x[0];
#pragma unroll
        for (int i = 1; i < 16; ++i) mx = fmaxf(mx, x[i]);
        const float mn = fmaxf(mrun, max32(mx));
        const float al = __builtin_amdgcn_exp2f(mrun - mn);
        mrun = mn;
#pragma unroll
        for (int i = 0; i < 16; ++i) oacc[i] *= al;
#pragma unroll
        for (int s = 0; s < 2; ++s) {
          f32x8 pf;
#pragma unroll
          for (int j = 0; j < 8; ++j) pf[j] = __builtin_amdgcn_exp2f(x[8 * s + j] - mn);
          bf16x8 vf = *(const bf16x8*)(vp + ((t & 3) * 2 + s) * 512);
          oacc = pv_mfma(vf, pf, oacc, sel);
        }
        kp += 1024;
        if ((t & 3) == 3) vp += vstep;
      }
      acc = oacc;
    }
  }
  const float inv = 1.f / acc[8];
  {
    const u32x4 o = {pack2(f32x2{acc[0] * inv, acc[1] * inv}), pack2(f32x2{acc[2] * inv, acc[3] * inv}),
                     pack2(f32x2{acc[4] * inv, acc[5] * inv}), pack2(f32x2{acc[6] * inv, acc[7] * inv})};
    const unsigned l16 = lane * 16, so_o = WS_O + tb * 32768, so_f = WS_CL + tb * 1024;
    __builtin_amdgcn_raw_buffer_store_b128(o, rws, l16, so_o + (cb * 8 + wave) * 1024, 16);
    const int src = (cb + 1 + (wave >> 1)) & 3;
    const unsigned piece = src * 8192 + (wave & 1) * 4096;
    unsigned f = __builtin_amdgcn_raw_buffer_load_b32(rws, 0, so_f + src * 256, 16);
    asm volatile("s_waitcnt vmcnt(0)" : "+v"(f)::"memory");
    const bool early = __builtin_amdgcn_readfirstlane(f) != 0;
    u32x4 t[4];
#pragma unroll
    for (int i = 0; i < 4; ++i) asm volatile("" : "=v"(t[i]));
    if (early) fetch4(t, l16, rws, so_o + piece);
    asm volatile("s_barrier" ::: "memory");
    if (wave == 7) {
      if (lane == 0) __builtin_amdgcn_raw_buffer_store_b32(1u, rws, 0, so_f + cb * 256, 16);
    }
    *(u32x4*)(smem + (cb * 8 + wave) * 1024 + lane * 16) = o;
    if (wave < 6) {
      if (!early) {
        int spin = 0;
        while (__builtin_amdgcn_readfirstlane(__builtin_amdgcn_raw_buffer_load_b32(rws, 0, so_f + src * 256, 16)) == 0 && ++spin < SPIN_MAX)
          asm volatile("s_sleep 1" ::: "memory");
        asm volatile("" ::: "memory");
        fetch4(t, l16, rws, so_o + piece);
      }
      char* dst = smem + piece + lane * 16;
      wait_vm<3>(); asm volatile("" : "+v"(t[0])); *(u32x4*)(dst) = t[0];
      wait_vm<2>(); asm volatile("" : "+v"(t[1])); *(u32x4*)(dst + 1024) = t[1];
      wait_vm<1>(); asm volatile("" : "+v"(t[2])); *(u32x4*)(dst + 2048) = t[2];
      wait_vm<0>(); asm volatile("" : "+v"(t[3])); *(u32x4*)(dst + 3072) = t[3];
    }
  }
  asm volatile("s_waitcnt lgkmcnt(0)" ::: "memory");
}

extern "C" void kernel_launch(void* const* d_in, const int* in_sizes, int n_in, void* d_out, int out_size,
                              void* d_ws, size_t ws_size, hipStream_t stream) {
  const bf16* x = (const bf16*)d_in[0];
  const bf16* wq = (const bf16*)d_in[1];
  const bf16* wk = (const bf16*)d_in[2];
  const bf16* wv = (const bf16*)d_in[3];
  const bf16* wo = (const bf16*)d_in[4];
  const bf16* wr = (const bf16*)d_in[6];
  const bf16* wg = (const bf16*)d_in[7];
  const bf16* wu = (const bf16*)d_in[8];
  const bf16* wd = (const bf16*)d_in[9];
  (void)hipFuncSetAttribute((const void*)k_all, hipFuncAttributeMaxDynamicSharedMemorySize, ALL_LDS);
  k_all<<<256, 768, ALL_LDS, stream>>>((char*)d_ws, x, wq, wk, wv, wr, wg, wu, wd, wo, (bf16*)d_out);
}
```
